```python
import torch

def _softmax(x, axis=-1):
    return torch.softmax(x, dim=axis)

def _gelu(x):
    return torch.nn.functional.gelu(x, approximate='tanh')

def _silu(x):
    return torch.nn.functional.silu(x)

def _sigmoid(x):
    return torch.sigmoid(x)

def _layernorm(x, axis=-1, eps=1e-05):
    m = x.mean(dim=axis, keepdim=True)
    v = x.var(dim=axis, keepdim=True, unbiased=False)
    return (x - m) / torch.sqrt(v + eps)

def _scatter_add(tgt, idx, src, axis=0):
    return tgt.clone().index_add_(axis, idx, src)

def _segment_sum(src, seg, n):
    out = torch.zeros((n,) + src.shape[1:], dtype=src.dtype)
    return out.index_add_(0, seg, src)

def _pad(x, pw):
    flat = tuple((v for pair in reversed(pw) for v in pair))
    return torch.nn.functional.pad(x, flat)
kernel_name = 'adaLN_cond__compressed_kv_cla__glu_gated_geglu__decode'

def setup_inputs():

    _w = lambda a: (lambda t: t.to(torch.bfloat16) if t.is_floating_point() else t)(torch.as_tensor(a))

    def _A_setup():
        B, H, Sq, Sk, D, M = (1, 32, 1, 32768, 128, 512)
        Dc = 4 * D
        return {'x': _w((lambda *s: torch.randn(*s, dtype=torch.bfloat16) * 0.1)(B, Sq, M)), 'c_kv': _w((lambda *s: torch.randn(*s, dtype=torch.bfloat16) * 3.2)(B, Sk, Dc)), 'w_q': _w((lambda *s: torch.randn(*s, dtype=torch.bfloat16) * 0.8)(M, H, D) * M ** (-0.5)), 'w_uk': _w((lambda *s: torch.randn(*s, dtype=torch.bfloat16) * 0.8)(Dc, H, D) * Dc ** (-0.5)), 'w_uv': _w((lambda *s: torch.randn(*s, dtype=torch.bfloat16) * 3.2)(Dc, H, D) * Dc ** (-0.5)), 'w_o': _w((lambda *s: torch.randn(*s, dtype=torch.bfloat16) * 3.2)(H, D, M) * (H * D) ** (-0.5))}
    _A_in = _A_setup()

    def _B_setup():
        B, S, Dm, Df = (1, 1, 512, 1792)
        return {'x': _w((lambda *s: torch.randn(*s, dtype=torch.bfloat16) * 0.1)(B, S, Dm)), 'w_gate': _w((lambda *s: torch.randn(*s, dtype=torch.bfloat16) * 1.6)(Dm, Df) * Dm ** (-0.5)), 'w_up': _w((lambda *s: torch.randn(*s, dtype=torch.bfloat16) * 1.6)(Dm, Df) * Dm ** (-0.5)), 'w_down': _w((lambda *s: torch.randn(*s, dtype=torch.bfloat16) * 1.6)(Df, Dm) * Df ** (-0.5))}
    _B_in = _B_setup()
    return {'A_x': _A_in['x'], 'A_c_kv': _A_in['c_kv'], 'A_w_q': _A_in['w_q'], 'A_w_uk': _A_in['w_uk'], 'A_w_uv': _A_in['w_uv'], 'A_w_o': _A_in['w_o'], 'B_x': _B_in['x'], 'B_w_gate': _B_in['w_gate'], 'B_w_up': _B_in['w_up'], 'B_w_down': _B_in['w_down'], 'c': _w((lambda *s: torch.randn(*s, dtype=torch.bfloat16) * 0.1)(1, 512) * 8.0), 'w_mod': _w((lambda *s: torch.randn(*s, dtype=torch.bfloat16) * 0.1)(512, 6 * 512) * 512 ** (-0.5) * 8.0)}

def reference(A_x, A_c_kv, A_w_q, A_w_uk, A_w_uv, A_w_o, B_x, B_w_gate, B_w_up, B_w_down, c, w_mod):

    def _A_ref(x, c_kv, w_q, w_uk, w_uv, w_o):
        B, Sq, M = (x.shape[0], x.shape[1], x.shape[2])
        Sk = c_kv.shape[1]
        H, D = (w_q.shape[1], w_q.shape[2])
        q = torch.einsum('bqm,mhd->bhqd', x, w_q)
        k = torch.einsum('bkc,chd->bhkd', c_kv, w_uk)
        v = torch.einsum('bkc,chd->bhkd', c_kv, w_uv)
        scores = torch.einsum('bhqd,bhkd->bhqk', q, k) * D ** (-0.5)
        off = Sk - Sq
        qi = torch.arange(Sq)[:, None]
        kj = torch.arange(Sk)[None, :]
        cmask = kj <= qi + off
        scores = torch.where(cmask[None, None], scores, -1000000000.0)
        p = _softmax(scores, axis=-1)
        ctx = torch.einsum('bhqk,bhkd->bhqd', p, v)
        return torch.einsum('bhqd,hdm->bqm', ctx, w_o)

    def _B_ref(x, w_gate, w_up, w_down):
        g = torch.einsum('bsd,df->bsf', x, w_gate)
        u = torch.einsum('bsd,df->bsf', x, w_up)
        h = _gelu(g) * u
        return torch.einsum('bsf,fd->bsd', h, w_down)
    x = A_x
    mod = (_silu(c) @ w_mod).reshape(c.shape[0], 6, 1, -1)
    sa, ca, ga, sb, cb, gb = (mod[:, 0].clone(), mod[:, 1].clone(), mod[:, 2].clone(), mod[:, 3].clone(), mod[:, 4].clone(), mod[:, 5].clone())
    h = _layernorm(x, axis=-1) * (1 + ca) + sa
    x = x + ga * _A_ref(h, A_c_kv, A_w_q, A_w_uk, A_w_uv, A_w_o)
    h = _layernorm(x, axis=-1) * (1 + cb) + sb
    x = x + gb * _B_ref(h, B_w_gate, B_w_up, B_w_down)
    return x
```

```cpp
#include <hip/hip_runtime.h>
#include <hip/hip_bf16.h>
#include <stdint.h>

typedef unsigned short u16;
typedef unsigned int u32;
typedef unsigned long long u64;
typedef float f32x2 __attribute__((ext_vector_type(2)));
typedef float f32x4 __attribute__((ext_vector_type(4)));
typedef float f32x16 __attribute__((ext_vector_type(16)));
typedef u32 u32x2 __attribute__((ext_vector_type(2)));
typedef u32 u32x4 __attribute__((ext_vector_type(4)));
typedef u32 u32x16 __attribute__((ext_vector_type(16)));
typedef short bf16x8 __attribute__((ext_vector_type(8)));
typedef short s16x4 __attribute__((ext_vector_type(4)));
#define LAS __attribute__((address_space(3)))
#define DI __device__ __forceinline__

constexpr int DF = 1792;
constexpr int NWG = 256;
constexpr int AUX_SC1 = 16;

constexpr int WS_EPOCH = 0;
constexpr int WS_SFLAG = 4096;
constexpr int WS_QABS = 32768;
constexpr int WS_LSE = WS_QABS + 65536;
constexpr int WS_PCTX = WS_LSE + 65536;
constexpr int WS_PATTN = WS_PCTX + 65536;
constexpr int WS_PDOWN = WS_PATTN + 32768;
constexpr int WS_MODB = WS_PDOWN + 114688;
constexpr int WS_SC = WS_MODB + 32768;
constexpr int WS_PROBE = WS_SC + 32768;
constexpr int WS_VOTE = WS_PROBE + 262144;
constexpr int WS_PART = 2097152;
constexpr int NPR = 2;
constexpr int PART_BYTES = 32 * 8 * 256 * 128;
constexpr int WS_TOTAL = WS_PART + NPR * PART_BYTES;

constexpr int il(int o, int hf) { return (o >> 13) * 16384 + hf * 8192 + (o & 8191); }
constexpr int il21(int o, int hf) { return il(o, hf ^ ((o >> 20) & 1) ^ 1); }
constexpr u32 SPIN_CAP = 1u << 16;
#define WA 8
#define WA0 5
#define WB 8
#define WB0 6
#define WC 8
#define WC0 6
#define WINT 4
#define QF_T 300
#define E1_T 280
#define E3_T 515
#define MB_T 520
#define GU_T 600
#define DN_T 600
#define E4_T 0
#define E4_S2 12
#define E4_S1 6
#define E4_S0 2
#define WO_T 0
#define KVNT 1
#define KVPOL " nt"
#define WNT true
#define Q_SLP 2
#define WING 2
#define E6_T 1200
#define TAU 32.f
#define QW 4
#define WINP 8
#define PSLP 0
#define GUNT false
#define WV_MODE 0
#define T4_DELAY 12
#define VCLIP 40
#define T0_ADJ 35
#define PF_LEN 36900
#define G_OF(v) ((v) > 24576.f ? 8192u : 0u)
#define VOTE_STAMP(v)
#define STAMP_ARG
#define STAMP_PASS
#define STAMP_DECL
#define STAMP(i)
#define STAMP_FLUSH()

DI float bf2f(u32 b) { return __uint_as_float(b << 16); }
DI float rbf(float f) { return (float)(__bf16)f; }
DI u32 f2bf(float f) { return (u32)__builtin_bit_cast(u16, (__bf16)f); }
typedef __bf16 bf16x2_t __attribute__((ext_vector_type(2)));
DI u32 pk2bf(float a, float b) { return __builtin_bit_cast(u32, __builtin_convertvector(f32x2{a, b}, bf16x2_t)); }
DI float lo16(u32 v) { return __uint_as_float(v << 16); }
DI float hi16(u32 v) { return __uint_as_float(v & 0xFFFF0000u); }
DI u32x4 ldnt(const u16* p) { return WNT ? __builtin_nontemporal_load((const u32x4*)p) : *(const u32x4*)p; }

DI float silu_bf(float a) { return rbf(a * __builtin_amdgcn_rcpf(1.f + __expf(-a))); }

template <int CTRL> DI float dppf(float v) {
  return __int_as_float(__builtin_amdgcn_update_dpp(0, __float_as_int(v), CTRL, 0xF, 0xF, true));
}
DI float quad_sum(float v) { v += dppf<0xB1>(v); v += dppf<0x4E>(v); return v; }
DI float b23_sum(float v) { v += dppf<0x128>(v); v += dppf<0x124>(v); return v; }
DI float b3_sum(float v) { return v + dppf<0x128>(v); }
DI float x16_sum(float v) {
  const auto r = __builtin_amdgcn_permlane16_swap(__float_as_uint(v), __float_as_uint(v), false, false);
  return __uint_as_float(r[0]) + __uint_as_float(r[1]);
}
DI float x32_sum(float v) {
  const auto r = __builtin_amdgcn_permlane32_swap(__float_as_uint(v), __float_as_uint(v), false, false);
  return __uint_as_float(r[0]) + __uint_as_float(r[1]);
}
DI float rs32(float a, float b) {
  const auto r = __builtin_amdgcn_permlane32_swap(__float_as_uint(a), __float_as_uint(b), false, false);
  return __uint_as_float(r[0]) + __uint_as_float(r[1]);
}
DI float rs16(float a, float b) {
  const auto r = __builtin_amdgcn_permlane16_swap(__float_as_uint(a), __float_as_uint(b), false, false);
  return __uint_as_float(r[0]) + __uint_as_float(r[1]);
}
DI float row16_sum(float v) { return b23_sum(quad_sum(v)); }
DI float wave_sum(float v) { return x32_sum(x16_sum(row16_sum(v))); }
DI void row16_sum4(float& a, float& b, float& c, float& d) {
#define R4(C) "v_add_f32_dpp %0, %0, %0 " C " row_mask:0xf bank_mask:0xf\n v_add_f32_dpp %1, %1, %1 " C " row_mask:0xf bank_mask:0xf\n" \
              "v_add_f32_dpp %2, %2, %2 " C " row_mask:0xf bank_mask:0xf\n v_add_f32_dpp %3, %3, %3 " C " row_mask:0xf bank_mask:0xf\n"
  asm("s_nop 1\n" R4("quad_perm:[1,0,3,2]") R4("quad_perm:[2,3,0,1]") R4("row_ror:8") R4("row_ror:4") : "+v"(a), "+v"(b), "+v"(c), "+v"(d));
#undef R4
}
DI float row16_max(float v) {
  v = fmaxf(v, dppf<0xB1>(v)); v = fmaxf(v, dppf<0x4E>(v));
  v = fmaxf(v, dppf<0x128>(v)); v = fmaxf(v, dppf<0x124>(v));
  return v;
}
DI float x16_max(float v) {
  const auto r = __builtin_amdgcn_permlane16_swap(__float_as_uint(v), __float_as_uint(v), false, false);
  return fmaxf(__uint_as_float(r[0]), __uint_as_float(r[1]));
}
DI float x32_max(float v) {
  const auto r = __builtin_amdgcn_permlane32_swap(__float_as_uint(v), __float_as_uint(v), false, false);
  return fmaxf(__uint_as_float(r[0]), __uint_as_float(r[1]));
}
DI float wave_max(float v) { return x32_max(x16_max(row16_max(v))); }

DI float dot2(u32 a, u32 b, float c) {
  return __builtin_amdgcn_fdot2_f32_bf16(__builtin_bit_cast(bf16x2_t, a), __builtin_bit_cast(bf16x2_t, b), c, false);
}
DI void fma8b(float* acc, u32 sbits, u32x4 w) {
  const u32 sh = sbits << 16;
  acc[0] = dot2(w.x, sbits, acc[0]); acc[1] = dot2(w.x, sh, acc[1]);
  acc[2] = dot2(w.y, sbits, acc[2]); acc[3] = dot2(w.y, sh, acc[3]);
  acc[4] = dot2(w.z, sbits, acc[4]); acc[5] = dot2(w.z, sh, acc[5]);
  acc[6] = dot2(w.w, sbits, acc[6]); acc[7] = dot2(w.w, sh, acc[7]);
}
DI void fma8(float* acc, float s, u32x4 w) {
  acc[0] += s * lo16(w.x); acc[1] += s * hi16(w.x);
  acc[2] += s * lo16(w.y); acc[3] += s * hi16(w.y);
  acc[4] += s * lo16(w.z); acc[5] += s * hi16(w.z);
  acc[6] += s * lo16(w.w); acc[7] += s * hi16(w.w);
}

DI unsigned fsw(unsigned row) { return ((row & 3) << 2) | ((row >> 2) & 3); }
DI unsigned off_b(unsigned row, unsigned ch) { return 256u * row + 16u * (ch ^ fsw(row)); }
DI unsigned tr_addr(unsigned lane, unsigned c, unsigned ks, unsigned t) {
  const unsigned h = lane >> 5, blk = (lane >> 4) & 1, q = (lane & 15) >> 2, p = lane & 3;
  return off_b(16 * ks + 8 * h + 4 * t + q, 4 * c + 2 * blk + (p >> 1)) + 8 * (p & 1);
}

typedef __amdgpu_buffer_rsrc_t rsrc_t;
DI u32x4 ld128(rsrc_t rs, int off) { return __builtin_amdgcn_raw_buffer_load_b128(rs, off, 0, AUX_SC1); }
DI u32x2 ld64(rsrc_t rs, int off) { return __builtin_amdgcn_raw_buffer_load_b64(rs, off, 0, AUX_SC1); }
DI void st64(rsrc_t rs, int off, u32 lo, u32 hi) { u32x2 v; v.x = lo; v.y = hi; __builtin_amdgcn_raw_buffer_store_b64(v, rs, off, 0, AUX_SC1); }
DI void st128(rsrc_t rs, int off, u32x4 v) { __builtin_amdgcn_raw_buffer_store_b128(v, rs, off, 0, AUX_SC1); }
#define BAR() do { asm volatile("s_waitcnt lgkmcnt(0)" ::: "memory"); __builtin_amdgcn_s_barrier(); asm volatile("" ::: "memory"); } while (0)
#define BAR0() do { __builtin_amdgcn_sched_barrier(0); asm volatile("" ::: "memory"); __builtin_amdgcn_s_barrier(); asm volatile("" ::: "memory"); __builtin_amdgcn_sched_barrier(0); } while (0)
#define PIN(x) asm volatile("" : "+v"(x))
constexpr u32 POISON = 0xFFFFFFFFu;
DI bool pois4(u32x4 v) { return max(max(v.x, v.y), max(v.z, v.w)) == POISON; }
DI u32 nopois(u32 v) { return min(v, 0xFFFFFFFEu); }
DI u32 fbits(float v) { return nopois(__float_as_uint(v)); }

DI u32x2 mk_gran(float v, u32 tlo, u32 thi) {
  u32 b = __float_as_uint(v);
  if ((b & 0x7F800000u) != 0x7F800000u) b += 0x80u;
  u32x2 g; g.x = (b >> 8) | (tlo << 24); g.y = (tlo >> 8) | (thi << 24);
  return g;
}
DI void st_gran2(rsrc_t rs, int off, float v0, float v1, u32 tlo, u32 thi) {
  const u32x2 a = mk_gran(v0, tlo, thi), b = mk_gran(v1, tlo, thi);
  u32x4 v; v.x = a.x; v.y = a.y; v.z = b.x; v.w = b.y;
  st128(rs, off, v);
}
DI bool gran_ok(u32 lo, u32 hi, u32 tlo, u32 thi) { return (((lo >> 24) ^ (tlo & 0xFFu)) | (hi ^ ((tlo >> 8) | (thi << 24)))) == 0u; }
DI float gran_val(u32 lo) { return __uint_as_float(lo << 8); }

template <int N, int SLP = 1, bool PRE = false> DI void gather128(rsrc_t rs, const int (&off)[N], u32x4 (&v)[N]) {
#pragma unroll
  for (int i = 0; i < N; ++i) if (!PRE) v[i] = ld128(rs, off[i]);
  _Pragma("unroll 1") for (u32 spins = 0; spins < SPIN_CAP; ++spins) {
    bool need[N], bad = false;
#pragma unroll
    for (int i = 0; i < N; ++i) { need[i] = pois4(v[i]); bad = bad || need[i]; }
    if (!__any(bad)) break;
    __builtin_amdgcn_s_sleep(SLP);
    asm volatile("" ::: "memory");
    __builtin_amdgcn_sched_barrier(0);
#pragma unroll
    for (int i = 0; i < N; ++i)
      if (need[i]) v[i] = ld128(rs, off[i]);
  }
}
template <int N> DI void waitvm() { asm volatile("s_waitcnt vmcnt(%0)" ::"n"(N) : "memory"); }
constexpr u32 LSPIN_CAP = 1u << 22;
DI void lds_wait(volatile LAS u32* p, u32 val) {
  _Pragma("unroll 1") for (u32 n = 0; *p < val && n < LSPIN_CAP; ++n) __builtin_amdgcn_s_sleep(1);
  asm volatile("" ::: "memory");
}
#define WAVE_LDS_SYNC() do { __builtin_amdgcn_fence(__ATOMIC_RELEASE, "wavefront"); __builtin_amdgcn_wave_barrier(); __builtin_amdgcn_fence(__ATOMIC_ACQUIRE, "wavefront"); } while (0)
#define SB() __builtin_amdgcn_sched_barrier(0)
#define RARE(c) __builtin_expect(!!(c), 0)

constexpr int PS = 28672;
constexpr int L_QI = 4 * PS;
constexpr int L_P = L_QI + 32768;
constexpr int L_REF = L_P + 11264;
constexpr int L_MX = L_REF + 1408;
constexpr int L_ALW = L_MX + 128;
constexpr int L_DUMP = L_ALW + 512;
constexpr int L_QOFF = 131072;
constexpr int L_H1RED = L_QOFF;
constexpr int L_RED = L_QOFF + 16384;
constexpr int L_Z = L_RED, L_HH = L_RED + 7 * 1088, L_ZERO = L_HH + 64, L_CF = L_ZERO + 64;
constexpr int L_MISC = L_QOFF + 30720;
constexpr int L_CTR = L_MISC;
constexpr int L_WMAX = L_MISC + 256;
constexpr int L_ML = L_MISC + 512;

#define Z4 (u32x4{0u, 0u, 0u, 0u})
DI float max5(f32x4 v, float e) { return fmaxf(fmaxf(fmaxf(fmaxf(v.x, v.y), v.z), v.w), e); }

template <int OFF, bool NT> DI void dma(u32 m, u32 voff, const void* sb) {
  if (NT) asm volatile("s_mov_b32 m0, %0\n s_nop 0\n global_load_lds_dwordx4 %1, %2 offset:%3 nt" ::"s"(m), "v"(voff), "s"(sb), "n"(OFF) : "memory");
  else asm volatile("s_mov_b32 m0, %0\n s_nop 0\n global_load_lds_dwordx4 %1, %2 offset:%3" ::"s"(m), "v"(voff), "s"(sb), "n"(OFF) : "memory");
}
DI void dma_sc1(u32 m, u32 voff, const void* sb) {
  asm volatile("s_mov_b32 m0, %0\n s_nop 0\n global_load_lds_dwordx4 %1, %2 sc1" ::"s"(m), "v"(voff), "s"(sb) : "memory");
}
DI void lds_addv(unsigned char* lds, int idx, u32 v) {
  SB();
  asm volatile("s_mov_b64 exec, 1\n ds_add_u32 %0, %1 offset:%2\n s_mov_b64 exec, -1" ::"v"((u32)L_CTR), "v"(v), "n"(idx * 4) : "memory");
  SB();
}
DI void lds_add(unsigned char* lds, int idx, int lane) { lds_addv(lds, idx, 1u); }
DI void lds_arrive(unsigned char* lds, int idx, int lane) {
  asm volatile("s_waitcnt lgkmcnt(0)" ::: "memory");
  lds_add(lds, idx, lane);
}
DI u32 spin_at(u32 a, u32 val) {
  u32 t, c, m, cap;
  asm volatile("s_mov_b32 %3, %6\n 1: ds_read_b32 %0, %4\n s_waitcnt lgkmcnt(0)\n v_readfirstlane_b32 %1, %0\n s_and_b32 %2, %1, 255\n s_cmp_ge_u32 %2, %5\n s_cbranch_scc1 2f\n"
               "s_sub_u32 %3, %3, 1\n s_cbranch_scc0 1b\n 2:"
               : "=&v"(t), "=&s"(c), "=&s"(m), "=&s"(cap) : "v"(a), "s"(val), "n"(LSPIN_CAP) : "memory", "scc");
  return c;
}
DI void lds_spin(volatile LAS u32* p, u32 val) { spin_at((u32)(uintptr_t)p, val); }
enum { C_SOK = 0, C_BH = 4, C_TLO = 8, C_THI = 9, C_TAG = 11, C_PD = 12, C_H1 = 18, C_PAR = 19, C_VOTE = 20, C_FREE = 22, C_QRDY = 23, C_WV = 24, C_MXC = 25, C_SD = 26, C_GX = 27, C_MK = 32, C_P = 44 };

DI u32 kv_mark(int nm, int skip) { return L_CTR + 4 * C_MK + __builtin_amdgcn_readfirstlane(4 * nm + (nm >= skip ? 4 : 0)); }
#define KV_TS
#define KV_TSO
#define KV_TSA
#define KV_TSP
#define KV_TRY(l)                                                                                                        \
  "7: s_getreg_b32 %[h], hwreg(HW_REG_IB_STS, 0, 4)\n s_cmp_gt_i32 %[h], %[k4]\n s_cbranch_scc1 " l "\n"                  \
  "s_waitcnt vmcnt(4)\n s_cmp_eq_u32 %[k4], 4\n s_cbranch_scc1 8f\n s_waitcnt vmcnt(0)\n"                              \
  "8: v_subrev_u32 %[t], %[k4], %[ml]\n s_mov_b64 exec, 1\n ds_add_u32 %[t], %[one] offset:12\n s_mov_b64 exec, -1\n"   \
  "s_sub_i32 %[k4], %[k4], 4\n" KV_TS "s_branch 7b\n"
#define STR2(x) #x
#define STR(x) STR2(x)
#define SPIN_SLEEP ""
DI void kv_try(int& k4, u32 ml, u32 one KV_TSA) {
  u32 t, h;
  asm volatile(KV_TRY("9f") "9:" : [t] "=&v"(t), [h] "=&s"(h), [k4] "+s"(k4) KV_TSO : [ml] "v"(ml), [one] "v"(one) : "memory", "scc");
}
DI u32 spin_try(int idx, u32 val, int& k4, u32 ml, u32 one KV_TSA) {
  u32 t, h, c, cap;
  asm volatile("s_mov_b32 %[cap], %[n]\n 1: ds_read_b32 %[t], %[ml] offset:%[o]\n s_waitcnt lgkmcnt(0)\n v_readfirstlane_b32 %[c], %[t]\n s_and_b32 %[h], %[c], 255\n"
               "s_cmp_ge_u32 %[h], %[v]\n s_cbranch_scc1 2f\n" KV_TRY("9f") "9: " SPIN_SLEEP "s_sub_u32 %[cap], %[cap], 1\n s_cbranch_scc0 1b\n 2:"
               : [t] "=&v"(t), [h] "=&s"(h), [c] "=&s"(c), [cap] "=&s"(cap), [k4] "+s"(k4) KV_TSO
               : [ml] "v"(ml), [one] "v"(one), [o] "n"(4 * idx), [v] "n"(val), [n] "n"(LSPIN_CAP) : "memory", "scc");
  return c;
}
template <int W> DI void kv_load1(const u16* ckv, u32 vl, int lw, u32 g0, u32 g1, u32 m, int n0, int n1, int& nm, int skip) {
#pragma unroll 1
  for (int n = n0; n < n1; ++n) {
#define KV_POST(k) ".if (%[wm] | 4) == " #k "\n s_cmp_gt_i32 %[nm], %[n2]\n s_cbranch_scc1 20f\n s_mov_b64 exec, 1\n ds_add_u32 %[ma], %[one]\n s_mov_b64 exec, -1\n s_add_i32 %[nm], %[nm], 1\n 20:\n .endif\n"
#define KV_W(o) "s_waitcnt vmcnt(%[wm])\n global_load_lds_dwordx4 %[vo], %[p] offset:" #o KVPOL "\n"
#define KV_M "s_add_u32 m0, m0, %[ms]\n"
    asm volatile("s_mov_b32 m0, %[m]\n" KV_W(0) KV_POST(4) KV_M KV_W(256) KV_POST(5) KV_M KV_W(512) KV_POST(6) KV_M KV_W(768) KV_POST(7)
                 : [nm] "+s"(nm)
                 : [m] "s"(m), [vo] "v"(vl + g0 + ((lw + n) & 3) * 4096), [p] "s"(ckv), [ma] "v"(kv_mark(nm, skip)),
                   [one] "v"(1u), [n2] "s"(__builtin_amdgcn_readfirstlane(n - (W > 4 ? 2 : 1))), [wm] "n"(W - 1), [ms] "n"(PS - 256)
                 : "memory", "scc");
    g0 = g1;
    g1 += 192 * 16384;
    m += n + 1 == skip ? 8192 : 4096;
  }
}
template <int W, int W0> DI void kv_load(const u16* ckv, u32 vl, int lw, u32 g0, u32 g1, u32 m, int n0, int n1, int& nm, int skip) {
  if (W != W0 && skip == 4) kv_load1<W0>(ckv, vl, lw, g0, g1, m, n0, n1, nm, skip); else kv_load1<W>(ckv, vl, lw, g0, g1, m, n0, n1, nm, skip);
}
DI void until(u32 t0, u32 dt) {
  while ((u32)__builtin_amdgcn_s_memrealtime() - t0 < dt) __builtin_amdgcn_s_sleep(2);
}
DI void att_done(rsrc_t rs, int lane, int sp, int gx) {
  u32 old;
  u32x2 ml;
  asm volatile("s_mov_b64 exec, 1\n ds_add_rtn_u32 %0, %2, %3 offset:%5\n s_mov_b64 exec, -1\n ds_read_b64 %1, %4\n s_waitcnt lgkmcnt(0)"
               : "=&v"(old), "=&v"(ml) : "v"((u32)L_CTR), "v"(1u), "v"(L_WMAX + (lane & 31) * 8), "n"(4 * C_PD) : "memory");
  old = __builtin_amdgcn_readfirstlane(old);
  if (old == 4u && lane < 32) st64(rs, (WS_LSE + il(((lane >> 1) * 256 + sp) * 8, lane & 1)) ^ gx, ml.x, ml.y);
}

DI bf16x8 tr_frag(unsigned char* lds, u32 a0, u32 a1) {
  const s16x4 b0 = __builtin_amdgcn_ds_read_tr16_b64_v4i16((LAS s16x4*)(lds + a0));
  const s16x4 b1 = __builtin_amdgcn_ds_read_tr16_b64_v4i16((LAS s16x4*)(lds + a1));
  bf16x8 v;
  v[0] = b0[0]; v[1] = b0[1]; v[2] = b0[2]; v[3] = b0[3]; v[4] = b1[0]; v[5] = b1[1]; v[6] = b1[2]; v[7] = b1[3];
  return v;
}
DI float sum4(f32x4 v) { const f32x2 t = f32x2{v.x, v.y} + f32x2{v.z, v.w}; return t.x + t.y; }

__global__ __launch_bounds__(512, 2) void k_main(const u16* __restrict__ x, const u16* __restrict__ ckv,
                                                 const u16* __restrict__ w_q, const u16* __restrict__ w_uk,
                                                 const u16* __restrict__ w_uv, const u16* __restrict__ w_o,
                                                 const u16* __restrict__ w_gate, const u16* __restrict__ w_up,
                                                 const u16* __restrict__ w_down, const u16* __restrict__ cvec,
                                                 const u16* __restrict__ w_mod, u16* __restrict__ out,
                                                 unsigned char* ws) {
  __shared__ __attribute__((aligned(16))) unsigned char lds[163840];
  const int w = __builtin_amdgcn_readfirstlane(threadIdx.x >> 6), j = blockIdx.x;
  int lane = threadIdx.x & 63;
  const int xc = j & 7, ii = j >> 3;
  STAMP_DECL;
  STAMP(0);
  u32x2 ev;
  u32 xm, vh;
  {
    u64 p, pc, pw;
    u32 a, b, c, d, f, t;
    asm volatile("s_lshr_b32 %[f], %[j], 6\n s_cmp_eq_u32 %[f], 1\n s_cselect_b32 %[a], 0x50, 16\n"
                 "s_load_dwordx2 %[p], %[ka], %[a]\n s_load_dwordx2 %[pw], %[ka], 0x60\n"
                 "s_lshr_b32 %[d], %[j], 3\n s_cmp_lg_u32 %[w], 7\n s_cbranch_scc1 2f\n"
                 "s_and_b32 %[a], %[d], 15\n s_lshl_b32 %[a], %[a], 12\n v_lshl_add_u32 %[t], %[lane], 6, %[a]\n v_min_u32 %[t], %[pfl], %[t]\n"
                 "s_getpc_b64 %[pc]\n s_mov_b32 m0, %[dump]\n s_nop 0\n global_load_lds_dword %[t], %[pc]\n"
                 "2: s_load_dwordx2 %[pc], %[ka], 0x48\n s_and_b32 %[c], %[j], 7\n s_lshl_b32 %[c], %[c], 10\n s_cmp_eq_u32 %[f], 0\n s_cbranch_scc0 3f\n"
                 "s_and_b32 %[a], %[d], 1\n s_lshl1_add_u32 %[a], %[w], %[a]\n s_lshl_b32 %[a], %[a], 18\n s_add_u32 %[c], %[c], %[a]\n"
                 "s_lshr_b32 %[a], %[d], 1\n s_lshl_b32 %[a], %[a], 8\n s_add_u32 %[c], %[c], %[a]\n s_lshl_b32 m0, %[w], 14\n"
                 "v_lshrrev_b32 %[t], 4, %[lane]\n v_mul_u32_u24 %[t], 0x204, %[t]\n v_and_b32 %[vh], 15, %[lane]\n v_xor_b32 %[vh], %[vh], %[t]\n"
                 "v_lshl_add_u32 %[vh], %[vh], 4, %[c]\n v_xor_b32 %[t], 0x8010, %[vh]\n s_waitcnt lgkmcnt(0)\n"
                 "global_load_lds_dwordx4 %[vh], %[p]\n s_add_u32 m0, m0, 0x400\n s_nop 0\n"
                 "global_load_lds_dwordx4 %[t], %[p]\n s_add_u32 m0, m0, 0x400\n v_xor_b32 %[t], 0x10020, %[vh]\n"
                 "global_load_lds_dwordx4 %[t], %[p]\n s_add_u32 m0, m0, 0x400\n v_xor_b32 %[t], 0x18030, %[vh]\n"
                 "global_load_lds_dwordx4 %[t], %[p]\n s_branch 1f\n"
                 "3: s_lshl2_add_u32 %[d], %[d], %[w]\n s_sub_u32 %[d], %[d], 68\n s_lshl_b32 %[d], %[d], 16\n s_add_u32 %[d], %[d], %[c]\n"
                 "s_and_b32 %[a], %[j], 3\n s_lshl2_add_u32 %[b], %[a], %[w]\n s_mul_i32 %[b], %[b], 0x30000\n s_lshl_b32 %[a], %[a], 8\n"
                 "v_lshl_add_u32 %[xm], %[lane], 2, %[a]\n"
                 "s_and_b32 %[a], %[j], 60\n s_lshl_b32 %[a], %[a], 5\n s_add_u32 %[b], %[b], %[a]\n"
                 "s_lshl_b32 %[c], %[w], 12\n s_add_u32 %[c], %[c], %[mq]\n s_lshl_b32 %[a], %[w], 10\n s_add_u32 %[a], %[a], %[mh]\n"
                 "s_cmp_eq_u32 %[f], 1\n s_cselect_b32 %[d], %[b], %[d]\n s_cselect_b32 m0, %[c], %[a]\n s_cselect_b32 %[a], 48, 1\n s_cselect_b32 %[c], 0, 4\n"
                 "v_lshrrev_b32 %[t], 3, %[lane]\n v_mul_u32_u24 %[t], %[a], %[t]\n v_and_b32 %[vh], 7, %[lane]\n v_lshl_add_u32 %[vh], %[t], 3, %[vh]\n"
                 "v_lshl_add_u32 %[vh], %[vh], 4, %[d]\n"
                 "s_cselect_b32 %[b], 6, 1\n s_cselect_b32 %[a], 0x400, 0\n s_lshl_b32 %[b], %[b], 13\n"
                 "s_xor_b32 %[c], %[c], %[w]\n s_bitcmp1_b32 %[c], 2\n v_mov_b32 %[t], 0\n"
                 "s_waitcnt lgkmcnt(0)\n s_cbranch_scc1 1f\n s_cmp_eq_u32 %[f], 1\n s_cbranch_scc0 4f\n"
                 "global_load_dword %[xm], %[xm], %[pc]\n global_load_dwordx2 %[ev], %[t], %[pw] sc1\n"
                 "4: global_load_lds_dwordx4 %[vh], %[p]\n s_add_u32 m0, m0, %[a]\n v_add_u32 %[t], %[b], %[vh]\n"
                 "global_load_lds_dwordx4 %[t], %[p]\n s_add_u32 m0, m0, %[a]\n v_add_u32 %[t], %[b], %[t]\n"
                 "global_load_lds_dwordx4 %[t], %[p]\n s_add_u32 m0, m0, %[a]\n v_add_u32 %[t], %[b], %[t]\n"
                 "global_load_lds_dwordx4 %[t], %[p]\n"
                 "1:\n"
                 : [xm] "=&v"(xm), [ev] "=&v"(ev), [vh] "=&v"(vh), [t] "=&v"(t), [p] "=&s"(p), [pc] "=&s"(pc), [pw] "=&s"(pw), [a] "=&s"(a), [b] "=&s"(b), [c] "=&s"(c),
                   [d] "=&s"(d), [f] "=&s"(f)
                 : [j] "s"(j), [w] "s"(w), [lane] "v"(lane), [ka] "s"(__builtin_amdgcn_kernarg_segment_ptr()), [pfl] "n"(PF_LEN), [dump] "n"(L_DUMP),
                   [mq] "n"(L_QOFF + 8192), [mh] "n"(L_P)
                 : "memory", "scc");
  }
  SB();
  const u32 t0 = (u32)__builtin_amdgcn_s_memrealtime() - T0_ADJ;
  const bool isQ = j < 64, isS0 = j >= 64 && j < 128;
  const int hq = xc * 4 + (ii >> 1), dh = ii & 1, sj = j - 64;
  const int lw = w - 4, myh = xc >> 2;
  int col = lane & 15, g4 = lane >> 4, tq = col >> 2, tp = lane & 3;
  const rsrc_t rs = __builtin_amdgcn_make_buffer_rsrc((void*)ws, 0, WS_TOTAL, 0x00020000);
  volatile LAS u32* ctr = (volatile LAS u32*)(lds + L_CTR);
  SB();
  STAMP(25);
  if (w < 2) *(u32*)(lds + (w == 0 ? L_CTR + lane * 4 : L_MX + (lane & 31) * 4)) = w == 0 ? 0u : 0xFF800000u;
  BAR();
  const bool tl2 = isS0;
  const int t3 = xc < 4 ? (ii < 8 ? xc * 8 + ii : -1) : (ii < 6 ? 8 + xc * 6 + ii : -1);
  const int t4 = (ii >= 8 && ii < 10) ? xc * 2 + (ii - 8) : -1;
  const u32 owno = (w >> 1) * 32768 + 8192 + (w & 1) * 12288;
  const int ks = (ii & 6) + myh, cg = (ii & 1) * 4 + (xc & 3);
  const int h = (j >> 2) & 31, c4 = j & 3;
  u32x4 wv[8];
  u32 tlo, thi;
#define CY(i)
#define CYOUT()
  int gx, pcopy;
#define RELANE() do { asm volatile("v_mbcnt_lo_u32_b32 %0, -1, 0\n v_mbcnt_hi_u32_b32 %0, -1, %0" : "=v"(lane)); col = lane & 15; g4 = lane >> 4; tq = col >> 2; tp = lane & 3; } while (0)
  u32 pa[16], bo, mlo, ro, pv, wa;
  int so1, mo;
  const u32 pb = (w >> 1) * PS + (w & 1) * 6144;
#define T1_SETUP()                                                                                                       \
  do {                                                                                                                   \
    _Pragma("unroll") for (int i = 0; i < 16; ++i) { pa[i] = pb + off_b(8 * (g4 % 3) + 4 * (i & 1) + tq, (i & 14) + (tp >> 1)) + 8 * (tp & 1); PIN(pa[i]); } \
    bo = L_QOFF + col * 272 + g4 * 16; mlo = L_QOFF + 4352 + (col & 7) * 8; ro = L_QOFF + (w + (col & 8)) * 272 + (col & 7) * 32 + g4 * 8; \
    so1 = col < 2 ? (WS_PCTX + h * 2048 + c4 * 512 + (w >> 2) * 256 + (2 * (w & 1) + (g4 >> 1)) * 64 + (((w >> 1) & 1) * 2 + col) * 16 + (g4 & 1) * 8) ^ gx : WS_TOTAL; \
    mo = (WS_LSE + il(((h >> 1) * 256 + 24 * w + min(lane, 23)) * 8, h & 1)) ^ gx;                                       \
    pv = g4 * 256 + ((col ^ (g4 << 2)) << 4);                                                                            \
    wa = tq * PS + 16384 + off_b(g4, 2 * w + (tp >> 1)) + 8 * (tp & 1);                                                  \
    PIN(bo); PIN(mlo); PIN(ro); PIN(so1); PIN(mo); PIN(pv); PIN(wa);                                                     \
  } while (0)
#define TAG_OF(e0, e1)                                                                                                  \
  do {                                                                                                                  \
    const u64 tag = (((u64)(e1) << 32) | (e0)) + 1ull;                                                                  \
    tlo = __builtin_amdgcn_readfirstlane((u32)tag); thi = __builtin_amdgcn_readfirstlane((u32)(tag >> 32));             \
  } while (0)
  const u32x4 P4 = {POISON, POISON, POISON, POISON};
#define POISON_COMMON(j)                                                                                                \
  do {                                                                                                                  \
    int po = WS_PDOWN + (j) * 448 + (lane - 36) * 16;                                                                   \
    po = lane < 36 ? WS_TOTAL : po;                                                                                     \
    po = lane < 32 ? WS_LSE + (j) * 256 + (lane - 16) * 16 : po;                                                        \
    po = lane < 16 ? WS_PCTX + (j) * 256 + lane * 16 : po;                                                              \
    st128(rs, po, P4);                                                                                                  \
  } while (0)
#define START_FLAG()                                                                                                    \
  do {                                                                                                                  \
    if (lane < 8) st64(rs, WS_SFLAG + lane * 2048 + sj * 8, tlo, thi);                                                  \
    if (lane == 0) { ctr[C_TLO] = tlo; ctr[C_THI] = thi; }                                                              \
    lds_add(lds, C_TAG, lane);                                                                                          \
  } while (0)
#define COUNT_VOTES(PRE)                                                                                                \
  do {                                                                                                                  \
    const int vo = min(lane, 47) * 32;                                                                                  \
    u32x4 a_ = Z4, b_ = Z4;                                                                                             \
    if (PRE) { a_ = *(const u32x4*)(lds + L_QI + 16384 + vo); b_ = *(const u32x4*)(lds + L_QI + 16400 + vo); }          \
    _Pragma("unroll 1") for (u32 spins = 0; spins < SPIN_CAP; ++spins) {                                                \
      if (((PRE) || spins) && __all((gran_ok(a_.x, a_.y, tlo, thi) & gran_ok(a_.z, a_.w, tlo, thi)) & (gran_ok(b_.x, b_.y, tlo, thi) & gran_ok(b_.z, b_.w, tlo, thi)))) break; \
      if (spins) __builtin_amdgcn_s_sleep(8);                                                                           \
      asm volatile("" ::: "memory");                                                                                    \
      a_ = ld128(rs, WS_VOTE + xc * 2048 + vo); b_ = ld128(rs, WS_VOTE + xc * 2048 + vo + 16);                          \
    }                                                                                                                   \
    const float vt_ = wave_sum(lane < 48 ? (float)(((a_.x & 1023u) + (a_.z & 1023u)) + ((b_.x & 1023u) + (b_.z & 1023u))) : 0.f); \
    if (lane == 0) ctr[C_PAR] = G_OF(vt_);                                                                              \
    VOTE_STAMP(vt_);                                                                                                    \
    STAMP(8);                                                                                                           \
  } while (0)
#define S_POLL(PRE, NL)                                                                                                 \
  do {                                                                                                                  \
    const int off_ = min(lane, (NL) - 1) * 32;                                                                          \
    u32x4 a_ = Z4, b_ = Z4;                                                                                             \
    if (PRE) { a_ = *(const u32x4*)(lds + L_QI + off_); b_ = *(const u32x4*)(lds + L_QI + 16 + off_); }                 \
    _Pragma("unroll 1") for (u32 spins = 0; spins < SPIN_CAP; ++spins) {                                                \
      const u32 d_ = (((a_.x ^ tlo) | (a_.y ^ thi)) | ((a_.z ^ tlo) | (a_.w ^ thi))) |                                  \
                     (((b_.x ^ tlo) | (b_.y ^ thi)) | ((b_.z ^ tlo) | (b_.w ^ thi)));                                   \
      if (((PRE) || spins) && __all(d_ == 0u)) break;                                                                   \
      if (spins) __builtin_amdgcn_s_sleep(8);                                                                           \
      asm volatile("" ::: "memory");                                                                                    \
      a_ = ld128(rs, WS_SFLAG + xc * 2048 + off_); b_ = ld128(rs, WS_SFLAG + xc * 2048 + off_ + 16);                    \
    }                                                                                                                   \
  } while (0)
#define MB_OFF(i) (WS_MODB + ((lane >> 4) * 2048 + ((i) >> 1) * 512 + 128 * lw + 64 * ((i) & 1) + (lane & 15) * 4) * 4)

  if (!isQ && w >= 4) {
    if (isS0) {
      if (w == 4) {
        const u32x2 e2 = ld64(rs, WS_EPOCH);
        st128(rs, WS_QABS + sj * 1024 + lane * 16, P4);
        st128(rs, lane < 32 ? WS_MODB + sj * 512 + lane * 16 : WS_TOTAL, P4);
        POISON_COMMON(sj);
        POISON_COMMON(j);
        waitvm<0>();
        TAG_OF(e2.x, e2.y);
        lds_add(lds, C_BH, lane);
        START_FLAG();
        STAMP(27);
      }
      lds_wait(ctr + C_H1, 4u);
    } else {
#pragma unroll
      for (int i = 4; i < 16; ++i) { waitvm<WINP - 1>(); dma<0, false>(L_P + w * 1024, vh + (i & 7) * 8192, i < 8 ? w_q : w_uk); }
    }
    const u32 vl = g4 * 1024 + ((col ^ ((g4 << 2) | lw)) << 4);
    int nm = 0, ns = isS0 ? 6 : 7;
    const int skip = isS0 ? 4 : 99;
    kv_load<WA, WA0>(ckv, vl, lw, (isS0 ? sj : 1792 + j) * 16384, (isS0 ? sj + 192 : sj) * 16384, lw * 1024, 0, 2, nm, skip);
    u32 va[8];
#pragma unroll
    for (int i = 0; i < 8; ++i) { va[i] = lw * PS + tr_addr(lane, i >> 1, 0, i & 1); PIN(va[i]); }
    if (!isS0) {
      const rsrc_t rw = __builtin_amdgcn_make_buffer_rsrc((void*)w_uv, 0, 4194304, 0x00020000);
#pragma unroll
      for (int i = 0; i < 8; ++i) { waitvm<WA - 1>(); wv[i] = __builtin_amdgcn_raw_buffer_load_b128(rw, (c4 * 128 + 32 * lw + g4) * 8192 + h * 256 + col * 16, i * 32768, WNT ? 2 : 0); }
      SB();
    }
    int eg[8];
#pragma unroll
    for (int u = 0; u < 8; ++u) eg[u] = WS_PART + il21(((4 * (u >> 1) + 2 * (g4 >> 1) + (u & 1)) * 4 + lw) * 65536 + sj * 256 + col * 16, g4 & 1);
    u32 pr_ = L_P + lane * 16;
    PIN(pr_);
    lds_wait(ctr + C_TAG, 1u);
    pcopy = (__builtin_amdgcn_readfirstlane(ctr[C_TLO]) & (NPR - 1)) * PART_BYTES;
#pragma unroll
    for (int u = 0; u < 8; ++u) { eg[u] += pcopy; PIN(eg[u]); }
    u32 ml = L_CTR + 4 * (C_MK + 7), one = 1u;
    PIN(ml); PIN(one);
    bf16x8 vf[4][4];
#define V_READ(i, slot) do { asm volatile("" ::: "memory"); SB(); _Pragma("unroll") for (int c = 0; c < 4; ++c) vf[i][c] = tr_frag(lds, va[2 * c] + (slot) * 4096, va[2 * c + 1] + (slot) * 4096); SB(); } while (0)
    SB();
    kv_load<WB, WB0>(ckv, vl, lw, (sj + (isS0 ? 384 : 192)) * 16384, (sj + (isS0 ? 576 : 384)) * 16384, lw * 1024 + 8192, 2, ns, nm, skip);
    lds_wait(ctr + C_MK + 3, 4u);
    V_READ(0, 0); V_READ(1, 1); V_READ(2, 2); V_READ(3, 3);
    lds_add(lds, C_FREE, lane);
#pragma unroll
    for (int i = 0; i < 16; ++i) PIN(vf[i >> 2][i & 3]);
    STAMP(2);
    lds_wait(ctr + C_FREE, 8u);
    STAMP(5);
    kv_load<WC, WC0>(ckv, vl, lw, (sj + 6 * 192) * 16384, (sj + 7 * 192) * 16384, lw * 1024, ns, ns + 4, nm, skip);
    int k4 = 4;
    STAMP(3);
    f32x16 acc[4];
#pragma unroll
    for (int c = 0; c < 4; ++c)
#pragma unroll
      for (int i = 0; i < 16; ++i) acc[c][i] = 0.f;
    bf16x8 pf[4], pn[4];
    u32 c_;
#define P_SPIN(g, n) c_ = spin_try(C_P + (g) - C_MK - 7, n, k4, ml, one KV_TSP)
#define TRY() kv_try(k4, ml, one KV_TSP)
#define P_READ(d, i0, i1, T0) _Pragma("unroll") for (int i = (i0); i < (i1); ++i) d[i] = *(volatile LAS bf16x8*)(lds + pr_ + ((T0) + i) * 1024)
#define PV(p, i) do { SB(); _Pragma("unroll") for (int c = 0; c < 4; ++c) acc[c] = __builtin_amdgcn_mfma_f32_32x32x16_bf16(p[i], vf[i][c], acc[c], 0, 0, 0); SB(); } while (0)
#define GX_USE(v) do { gx = __builtin_amdgcn_readfirstlane(v); _Pragma("unroll") for (int u = 0; u < 8; ++u) { eg[u] ^= gx; PIN(eg[u]); } } while (0)
#define MK_WAIT(T) spin_try((T) - 7, 4u, k4, ml, one KV_TSP)
#define PVV(p, i, d, slot)                                                                                               \
  _Pragma("unroll") for (int c = 0; c < 4; ++c) {                                                                        \
    SB();                                                                                                                \
    acc[c] = __builtin_amdgcn_mfma_f32_32x32x16_bf16(p[i], vf[i][c], acc[c], 0, 0, 0);                                   \
    SB();                                                                                                                \
    vf[d][c] = tr_frag(lds, va[2 * c] + (slot) * 4096, va[2 * c + 1] + (slot) * 4096);                                   \
    SB();                                                                                                                \
  }
#define CT(i) (*(volatile LAS u32*)(lds + ml + 4 * ((i) - C_MK - 7)))
    P_SPIN(0, 4u);
    P_READ(pf, 0, 4, 0);
    const u32 k7 = CT(C_MK + 7);
    asm volatile("" ::: "memory");
    STAMP(6);
    PVV(pf, 0, 0, 4); TRY();
    PVV(pf, 1, 1, 5); TRY();
    PVV(pf, 2, 2, 6);
    u32 cv = CT(C_P + 1);
    P_READ(pn, 0, 4, 4);
    PVV(pf, 3, 3, 0);
    if (RARE(__builtin_amdgcn_readfirstlane(k7) < 4u)) { MK_WAIT(7); V_READ(0, 4); V_READ(1, 5); V_READ(2, 6); V_READ(3, 0); }
    STAMP(9);
    c_ = __builtin_amdgcn_readfirstlane(cv);
    if (RARE((c_ & 255u) != 4u)) { P_SPIN(1, 4u); P_READ(pn, 0, 4, 4); }
    int slow = -1;
    if (RARE(c_ > 255u)) slow = 4;
    else {
      if (!isS0) PV(pn, 0);
      TRY();
      const u32 k9 = CT(C_MK + 9);
      asm volatile("" ::: "memory");
      PVV(pn, 1, 0, 1); TRY();
      PVV(pn, 2, 1, 2);
      cv = CT(C_P + 2);
      P_READ(pf, 0, 2, 8);
      const u32 k10 = CT(C_MK + 10);
      asm volatile("" ::: "memory");
      PVV(pn, 3, 2, 3);
      if (RARE(__builtin_amdgcn_readfirstlane(k9) < 4u)) { MK_WAIT(9); V_READ(0, 1); V_READ(1, 2); }
      STAMP(10);
      c_ = __builtin_amdgcn_readfirstlane(cv);
      if (RARE((c_ & 255u) != 4u)) { P_SPIN(2, 4u); P_READ(pf, 0, 2, 8); }
      if (RARE(c_ > 255u)) slow = 8;
      else {
        TRY();
        PV(pf, 0);
        const u32 g0 = ctr[C_GX];
        u32 g1 = ctr[C_PAR];
        cv = CT(C_P + 3);
        P_READ(pf, 2, 3, 8);
        PV(pf, 1);
        STAMP(4);
        if (RARE(__builtin_amdgcn_readfirstlane(k10) < 4u)) { MK_WAIT(10); V_READ(2, 3); }
        if (RARE(__builtin_amdgcn_readfirstlane(g0) == 0u)) { lds_spin(ctr + C_GX, 1u); g1 = ctr[C_PAR]; }
        GX_USE(g1);
        STAMP(11);
        c_ = __builtin_amdgcn_readfirstlane(cv);
        if (RARE((c_ & 255u) != 2u)) { c_ = spin_at(L_CTR + 4 * (C_P + 3), 2u); P_READ(pf, 2, 3, 8); }
        if (RARE(c_ > 255u)) slow = 10;
        else PV(pf, 2);
      }
    }
    if (RARE(slow >= 0)) {
      MK_WAIT(10);
      float R = ceilf(*(const float*)(lds + L_MX + (lane & 31) * 4));
#pragma unroll 1
      for (int T = slow; T < 11; ++T) {
        if (T == 4 && isS0) continue;
        spin_at(L_CTR + 4 * (C_P + (T < 8 ? 1 : T < 10 ? 2 : 3)), T < 10 ? 4u : 2u);
        const float rf = *(const float*)(lds + L_REF + T * 128 + (lane & 31) * 4);
        u32x4 p = *(const u32x4*)(lds + pr_ + T * 1024);
        const u32 so_ = (T < 7 ? T : T - 7) * 4096;
#pragma unroll
        for (int c = 0; c < 4; ++c) vf[0][c] = tr_frag(lds, va[2 * c] + so_, va[2 * c + 1] + so_);
        const float Rn = fmaxf(R, rf), ps = __builtin_amdgcn_exp2f(rf - Rn);
        *(float*)(lds + L_ALW + lw * 128 + (lane & 31) * 4) = __builtin_amdgcn_exp2f(R - Rn);
        R = Rn;
        WAVE_LDS_SYNC();
#pragma unroll
        for (int i = 0; i < 4; ++i) {
          const f32x4 al = *(const f32x4*)(lds + L_ALW + lw * 128 + (lane >> 5) * 16 + i * 32);
#pragma unroll
          for (int c = 0; c < 4; ++c)
#pragma unroll
            for (int r = 0; r < 4; ++r) acc[c][4 * i + r] *= al[r];
          p[i] = pk2bf(lo16(p[i]) * ps, hi16(p[i]) * ps);
        }
        WAVE_LDS_SYNC();
        pf[0] = __builtin_bit_cast(bf16x8, p);
        PV(pf, 0);
      }
      if (slow != 10) { lds_spin(ctr + C_GX, 1u); GX_USE(ctr[C_PAR]); }
    }
    STAMP(30);
#pragma unroll
    for (int u = 0; u < 8; ++u) {
      const auto r0 = __builtin_amdgcn_permlane16_swap(pk2bf(acc[0][2 * u], acc[1][2 * u]), pk2bf(acc[0][2 * u + 1], acc[1][2 * u + 1]), false, false);
      const auto r1 = __builtin_amdgcn_permlane16_swap(pk2bf(acc[2][2 * u], acc[3][2 * u]), pk2bf(acc[2][2 * u + 1], acc[3][2 * u + 1]), false, false);
      st128(rs, eg[u], u32x4{r0[0], r1[0], r0[1], r1[1]});
      SB();
    }
    STAMP(31);
    if (!isS0) {
#pragma unroll
      for (int i = 0; i < 8; ++i) *(u32x4*)(lds + lw * PS + 16384 + off_b(4 * i + g4, col)) = wv[i];
      T1_SETUP();
    }
    waitvm<0>();
    __builtin_amdgcn_s_waitcnt(0x0F70);
    att_done(rs, lane, sj, gx);
    STAMP(13);
  } else if (!isQ) {
    if (isS0) {
      u32 ta = L_QOFF + 8192 + (8 * g4 + tq) * 128 + w * 32 + tp * 8;
      u32 sa = L_H1RED + w * 256;
      int so = col < 2 ? WS_SC + (((sj >> 5) * 4 + (sj & 3)) * 512 + ((sj >> 2) & 7) * 64 + 16 * w + 4 * g4 + 2 * col) * 8 : WS_TOTAL;
      PIN(ta); PIN(so); PIN(sa);
      u32 c2, e0, e1;
      asm volatile("s_waitcnt vmcnt(4) ; HEAD_WAIT\n v_mov_b32 %0, %3\n v_mov_b32 %1, %4\n v_mov_b32 %2, %5" : "=&v"(c2), "=&v"(e0), "=&v"(e1) : "v"(xm), "v"(ev.x), "v"(ev.y) : "memory");
      TAG_OF(e0, e1);
      bf16x8 sf[4];
      u32x2 wt[8];
      asm volatile("ds_write_b32 %4, %5\n"
                   "ds_read_b128 %0, %6\n ds_read_b128 %1, %6 offset:64\n ds_read_b128 %2, %6 offset:128\n ds_read_b128 %3, %6 offset:192\n s_waitcnt lgkmcnt(0)"
                   : "=&v"(sf[0]), "=&v"(sf[1]), "=&v"(sf[2]), "=&v"(sf[3])
                   : "v"(sa + lane * 4), "v"(pk2bf(silu_bf(lo16(c2)), silu_bf(hi16(c2)))), "v"(sa + 16 * g4) : "memory");
      waitvm<0>();
      lds_add(lds, C_BH, lane);
      lds_spin(ctr + C_BH, 5u);
      STAMP(1);
      asm volatile("ds_read_b64_tr_b16 %0, %8\n ds_read_b64_tr_b16 %1, %8 offset:512\n ds_read_b64_tr_b16 %2, %8 offset:4096\n ds_read_b64_tr_b16 %3, %8 offset:4608\n"
                   "ds_read_b64_tr_b16 %4, %8 offset:8192\n ds_read_b64_tr_b16 %5, %8 offset:8704\n ds_read_b64_tr_b16 %6, %8 offset:12288\n ds_read_b64_tr_b16 %7, %8 offset:12800\n"
                   "s_waitcnt lgkmcnt(0)"
                   : "=&v"(wt[0]), "=&v"(wt[1]), "=&v"(wt[2]), "=&v"(wt[3]), "=&v"(wt[4]), "=&v"(wt[5]), "=&v"(wt[6]), "=&v"(wt[7]) : "v"(ta) : "memory");
      f32x4 d = {0.f, 0.f, 0.f, 0.f};
#pragma unroll
      for (int k = 0; k < 4; ++k)
        d = __builtin_amdgcn_mfma_f32_16x16x32_bf16(__builtin_bit_cast(bf16x8, u32x4{wt[2 * k].x, wt[2 * k].y, wt[2 * k + 1].x, wt[2 * k + 1].y}), sf[k], d, 0, 0, 0);
      st_gran2(rs, so, (col & 1) ? d[2] : d[0], (col & 1) ? d[3] : d[1], tlo, thi);
      lds_add(lds, C_H1, lane);
      STAMP(2);
    }
    u32x2 e2;
    if (!isS0) e2 = ld64(rs, WS_EPOCH);
    if (w == 3 && !isS0) {
      POISON_COMMON(j);
      st128(rs, lane < 16 ? WS_PATTN + (j - 128) * 256 + lane * 16 : WS_TOTAL, P4);
    }
    const int pp = __builtin_amdgcn_readfirstlane(WS_PROBE + il((j * 4 + w) * 128, w & 1));
    if (w < 2) {
      st128(rs, lane < 16 ? (pp ^ ((lane & 8) * 1024)) + (lane & 7) * 16 : WS_TOTAL, Z4);
      u32 dm;
      asm volatile("s_load_dword %0, %1, 0x0\n s_waitcnt lgkmcnt(0)" : "=&s"(dm) : "s"(ws) : "memory");
    }
    u32 ab = off_b(col, 4 * g4), qv = WS_QABS + myh * 8192 + g4 * 1024 + w * 256 + ((col ^ (g4 << 2)) << 4);
    const int hb = 16 * (w & 1) + col;
    u32 pw = L_P + (g4 >> 1) * 512 + hb * 16 + (g4 & 1) * 8, mw = hb * 4;
    PIN(ab); PIN(qv); PIN(pw); PIN(mw);
    waitvm<0>();
    __builtin_amdgcn_s_waitcnt(0x0F70);
    STAMP(27);
    if (!isS0) TAG_OF(e2.x, e2.y);
    if (w == 3 && !isS0) START_FLAG();
    if (w < 2) {
      u64 ta, tb, tc;
      u32 dm;
      asm volatile("s_memtime %0\n s_waitcnt lgkmcnt(0)\n s_load_dword %3, %4, %5 glc\n s_waitcnt lgkmcnt(0)\n"
                   "s_memtime %1\n s_waitcnt lgkmcnt(0)\n s_load_dword %3, %4, %6 glc\n s_waitcnt lgkmcnt(0)\n"
                   "s_memtime %2\n s_waitcnt lgkmcnt(0)"
                   : "=&s"(ta), "=&s"(tb), "=&s"(tc), "=&s"(dm) : "s"(ws), "s"(pp), "s"(pp ^ 8192) : "memory");
      int df = min(max(((int)(u32)(tb - ta) - (int)(u32)(tc - tb)) >> 3, -VCLIP), VCLIP);
      df = ((w ^ myh) & 1) ? -df : df;
      u32 old = 0u;
      if (lane == 0) old = __hip_atomic_fetch_add((LAS u32*)(lds + L_CTR) + C_VOTE, (u32)(131072 + 64 + df), __ATOMIC_RELAXED, __HIP_MEMORY_SCOPE_WORKGROUP);
      old = __builtin_amdgcn_readfirstlane(old) + 131072 + 64 + df;
      if (old >= 262144u && lane < 8) st64(rs, WS_VOTE + lane * 2048 + sj * 8, (old & 1023u) | (tlo << 24), (tlo >> 8) | (thi << 24));
    }
    if (w == 2) { until(t0, QF_T); S_POLL(false, 16); lds_add(lds, C_SOK, lane); STAMP(3); }
    bf16x8 af[16], qf[16][2];
    u32 aa[16], qa[32];
#pragma unroll
    for (int i = 0; i < 16; ++i) {
      aa[i] = (ab ^ ((i & 3) << 4)) + (((w + (i >> 2)) & 3) * PS + w * 4096); PIN(aa[i]);
#pragma unroll
      for (int u = 0; u < 2; ++u) { qa[2 * i + u] = (ab ^ ((i & 3) << 4)) + (L_QI + ((w + (i >> 2)) & 3) * 8192 + ((u ^ w) & 1) * 4096); PIN(qa[2 * i + u]); }
    }
    const int sl1 = w == 3 ? 0 : 4 + w, sl2 = 1 + (w >> 1);
#define A_RD(i, o) af[i] = *(const bf16x8*)(lds + aa[i] + (o))
#define A_READ(i0, i1, o) _Pragma("unroll") for (int i = (i0); i < (i1); ++i) A_RD(i, o)
#define A_MOVE(i0, i1, d) _Pragma("unroll") for (int i = (i0); i < (i1); ++i) { aa[i] += (d) * 4096; PIN(aa[i]); }
#define Q_RD(i, u) qf[i][u] = *(const bf16x8*)(lds + qa[2 * (i) + (u)])
#define A_FIRST() do { A_READ(0, 16, 0); lds_add(lds, C_FREE, lane); _Pragma("unroll") for (int i = 0; i < 16; ++i) PIN(af[i]); STAMP(12); } while (0)
    bool have = false;
    _Pragma("unroll 1") while ((u32)__builtin_amdgcn_s_memrealtime() - t0 < E3_T) {
      if (__builtin_amdgcn_readfirstlane(ctr[C_MK + w]) >= 4u) { have = true; break; }
      __builtin_amdgcn_s_sleep(1);
    }
    asm volatile("" ::: "memory");
    if (have) A_FIRST();
    qv ^= (ctr[C_VOTE] & 1023u) > 128u ? 8192u : 0u;
    PIN(qv);
    lds_wait(ctr + C_SOK, 1u);
    until(t0, E3_T);
    pcopy = (tlo & (NPR - 1)) * PART_BYTES;
    STAMP(6);
#define Q_DMA() _Pragma("unroll") for (int i = 0; i < 8; ++i) dma_sc1(L_QI + w * 8192 + i * 1024, (qv ^ ((i & 3) << 4)) + (i >> 1) * 16384 + (i & 1) * 4096, ws)
    Q_DMA();
    if (RARE(!have)) { lds_wait(ctr + C_MK + w, 4u); A_FIRST(); }
    A_MOVE(0, 16, sl1 - w);
    u32 cb = L_CTR, cw = L_CTR + 4 * w, mx0 = mw + L_MX, mx1 = (mw ^ 64) + L_MX, pw0 = pw + w * 1024, pw1 = (pw ^ 256) + w * 1024;
    float ninf = -__builtin_inff();
    u32 one = 1u;
    PIN(ninf); PIN(one); PIN(cb); PIN(cw); PIN(mx0); PIN(mx1); PIN(pw0); PIN(pw1);
#define CTS(b, i) (*(volatile LAS u32*)(lds + (b) + 4 * (i)))
    _Pragma("unroll 1") for (u32 spins = 0; spins < SPIN_CAP; ++spins) {
      waitvm<0>();
#pragma unroll
      for (int i = 0; i < 8; ++i) Q_RD(i >> 1, i & 1);
      u32 mxu[2] = {0u, 0u};
#pragma unroll
      for (int i = 0; i < 8; ++i) { const u32x4 t_ = __builtin_bit_cast(u32x4, qf[i >> 1][i & 1]); mxu[i & 1] = max(max(mxu[i & 1], t_.x), t_.y); mxu[i & 1] = max(max(mxu[i & 1], t_.z), t_.w); }
      if (!RARE(__any(max(mxu[0], mxu[1]) == POISON))) break;
      STAMP(29);
      __builtin_amdgcn_s_sleep(Q_SLP);
      const u64 b0 = __ballot(mxu[w & 1] == POISON), b1 = __ballot(mxu[~w & 1] == POISON);
#pragma unroll 1
      for (int i = 0; i < 8; ++i)
        if (((i < 4 ? b0 : b1) >> (4 * (i & 3))) & 0x000F000F000F000Full) dma_sc1(L_QI + w * 8192 + i * 1024, (qv ^ ((i & 3) << 4)) + (i >> 1) * 16384 + (i & 1) * 4096, ws);
    }
    lds_add(lds, C_QRDY, lane);
    STAMP(7);
    f32x4 sc[2], sn[2];
    sc[0] = sc[1] = sn[0] = sn[1] = f32x4{0.f, 0.f, 0.f, 0.f};
#define QK(d, i0, i1, NU)                                                                                                \
  do {                                                                                                                   \
    SB();                                                                                                                \
    _Pragma("unroll") for (int i = (i0); i < (i1); ++i) _Pragma("unroll") for (int u = 0; u < (NU); ++u)                 \
      d[u] = __builtin_amdgcn_mfma_f32_16x16x32_bf16(af[i], qf[i][u], d[u], 0, 0, 0);                                    \
    SB();                                                                                                                \
  } while (0)
#define MF(d, i, u) do { SB(); d[u] = __builtin_amdgcn_mfma_f32_16x16x32_bf16(af[i], qf[i][u], d[u], 0, 0, 0); SB(); } while (0)
    const bool idle1 = isS0 && w == 0;
    u32 c1;
    {
      const u32 c0 = CTS(cb, C_QRDY);
      QK(sc, 0, 2, 2);
      if (RARE(__builtin_amdgcn_readfirstlane(c0) < 4u)) lds_spin(ctr + C_QRDY, 4u);
      asm volatile("" ::: "memory");
#pragma unroll
      for (int i = 2; i < 4; ++i) { MF(sc, i, 0); Q_RD(2 * i, 0); Q_RD(2 * i, 1); MF(sc, i, 1); Q_RD(2 * i + 1, 0); Q_RD(2 * i + 1, 1); }
#pragma unroll
      for (int i = 4; i < 12; ++i) { MF(sc, i, 0); Q_RD(i + 4, 0); MF(sc, i, 1); Q_RD(i + 4, 1); }
      c1 = CTS(cw, C_MK + 4);
      asm volatile("" ::: "memory");
#pragma unroll
      for (int i = 12; i < 16; ++i) { MF(sc, i, 0); A_RD(2 * i - 24, 0); MF(sc, i, 1); A_RD(2 * i - 23, 0); }
    }
    STAMP(9);
    CY(0);
    float mref[2], lsum[2] = {0.f, 0.f};
#define ADDC(i, v) do { SB(); asm volatile("s_mov_b64 exec, 1\n ds_add_u32 %0, %1 offset:%2\n s_mov_b64 exec, -1" ::"v"(cb), "v"(v), "n"(4 * (i)) : "memory"); SB(); } while (0)
#define SM(d, T, NU, G, P0, P1)                                                                                          \
  do {                                                                                                                   \
    SB();                                                                                                                \
    if (RARE(__any(max5(d[0], (NU) > 1 ? max5(d[1], ninf) : ninf) > TAU))) {                                             \
      sig = 257u;                                                                                                        \
      _Pragma("unroll") for (int u = 0; u < (NU); ++u) {                                                                 \
        const float up = ceilf(x32_max(x16_max(max5(d[u], 0.f))));                                                       \
        lsum[u] *= __builtin_amdgcn_exp2f(-up);                                                                          \
        mref[u] += up;                                                                                                   \
        d[u] -= up;                                                                                                      \
        *(float*)(lds + (mw ^ (u * 64)) + L_REF + (T) * 128) = mref[u];                                                  \
      }                                                                                                                  \
    }                                                                                                                    \
    u32x2 pk[2];                                                                                                         \
    _Pragma("unroll") for (int u = 0; u < (NU); ++u) {                                                                   \
      pk[u].x = pk2bf(__builtin_amdgcn_exp2f(d[u].x), __builtin_amdgcn_exp2f(d[u].y));                                   \
      pk[u].y = pk2bf(__builtin_amdgcn_exp2f(d[u].z), __builtin_amdgcn_exp2f(d[u].w));                                   \
      *(u32x2*)(lds + (u ? (P1) : (P0))) = pk[u];                                                                        \
    }                                                                                                                    \
    ADDC(C_P + (G), sig);                                                                                                \
    _Pragma("unroll") for (int u = 0; u < (NU); ++u) { lsum[u] = dot2(pk[u].x, 0x3F803F80u, dot2(pk[u].y, 0x3F803F80u, lsum[u])); PIN(lsum[u]); } \
    SB();                                                                                                                \
  } while (0)
#define REF_WRITE(T, NU) _Pragma("unroll") for (int u = 0; u < (NU); ++u) *(float*)(lds + (mw ^ (u * 64)) + L_REF + (T) * 128) = mref[u]
    u32 sig = one;
    {
      MF(sn, 0, 0); A_RD(8, 0); MF(sn, 0, 1);
      const float m0 = max5(sc[0], ninf), m1 = max5(sc[1], ninf);
      MF(sn, 1, 0);
      asm volatile("ds_max_f32 %0, %2\n ds_max_f32 %1, %3" ::"v"(mx0), "v"(mx1), "v"(m0), "v"(m1) : "memory");
      ADDC(C_MXC, one);
      MF(sn, 1, 1); A_RD(9, 0);
#pragma unroll
      for (int i = 2; i < 4; ++i) { MF(sn, i, 0); A_RD(2 * i + 6, 0); MF(sn, i, 1); A_RD(2 * i + 7, 0); }
      MF(sn, 4, 0);
      const u32 c0 = CTS(cb, C_MXC);
      mref[0] = *(volatile LAS float*)(lds + mx0);
      mref[1] = *(volatile LAS float*)(lds + mx1);
      MF(sn, 4, 1); MF(sn, 5, 0); A_RD(14, 0); MF(sn, 5, 1); A_RD(15, 0); MF(sn, 6, 0); MF(sn, 6, 1);
      if (RARE(__builtin_amdgcn_readfirstlane(c0) < 4u)) {
        lds_spin(ctr + C_MXC, 4u);
        mref[0] = *(volatile LAS float*)(lds + mx0);
        mref[1] = *(volatile LAS float*)(lds + mx1);
      }
      u32x2 pk[2];
#pragma unroll
      for (int u = 0; u < 2; ++u) {
        MF(sn, 7 + 2 * u, 0);
        mref[u] = ceilf(mref[u]);
        float e0 = __builtin_amdgcn_exp2f(sc[u].x - mref[u]); PIN(e0);
        MF(sn, 7 + 2 * u, 1);
        pk[u].x = pk2bf(e0, __builtin_amdgcn_exp2f(sc[u].y - mref[u])); PIN(pk[u].x);
        MF(sn, 8 + 2 * u, 0);
        e0 = __builtin_amdgcn_exp2f(sc[u].z - mref[u]); PIN(e0);
        MF(sn, 8 + 2 * u, 1);
        pk[u].y = pk2bf(e0, __builtin_amdgcn_exp2f(sc[u].w - mref[u]));
        *(u32x2*)(lds + (u ? pw1 : pw0)) = pk[u];
      }
      ADDC(C_P, one);
      MF(sn, 11, 0);
      lsum[0] = dot2(pk[0].x, 0x3F803F80u, dot2(pk[0].y, 0x3F803F80u, 0.f)); PIN(lsum[0]);
      MF(sn, 11, 1);
      lsum[1] = dot2(pk[1].x, 0x3F803F80u, dot2(pk[1].y, 0x3F803F80u, 0.f)); PIN(lsum[1]);
      MF(sn, 12, 0);
      REF_WRITE(4 + w, 2);
      MF(sn, 12, 1);
      QK(sn, 13, 16, 2);
    }
    STAMP(10);
    CY(1);
#define QK1(N0, slot)                                                                                                    \
  do {                                                                                                                   \
    _Pragma("unroll") for (int i = 0; i < 16; ++i) { MF(sc, i, 0); if (i + (N0) < 16) A_RD(i + (N0), slot); }            \
  } while (0)
    if (RARE(__builtin_amdgcn_readfirstlane(c1) < 4u && !idle1)) {
      lds_spin(ctr + C_MK + 4 + w, 4u);
      A_READ(0, 16, 0);
      sn[0] = sn[1] = f32x4{0.f, 0.f, 0.f, 0.f};
      QK(sn, 0, 16, 2);
    }
    if (RARE(idle1)) lds_add(lds, C_P + 1, lane);
    else { sn[0] -= mref[0]; sn[1] -= mref[1]; SM(sn, 4 + w, 2, 1, pw0 + 4096, pw1 + 4096); }
    CY(2);
    if (RARE(w >= 2)) {
      RELANE();
      const u32 o_ = (w == 2 ? WS_SFLAG : WS_VOTE) + xc * 2048 + lane * 16;
      dma_sc1(L_QI + (w - 2) * 16384, o_, ws);
      dma_sc1(L_QI + (w - 2) * 16384 + 1024, o_ + 1024, ws);
    }
    REF_WRITE(7 + sl2, 1);
    A_MOVE(0, 16, sl2 - sl1);
    pw0 = pw + sl2 * 1024;
    sc[0] = f32x4{-mref[0], -mref[0], -mref[0], -mref[0]};
    PIN(pw0); PIN(sc[0]);
    CY(3);
    lds_spin(ctr + C_MK + 7 + sl2, 4u);
    CY(4);
    A_READ(0, 6, 0);
    STAMP(4);
    QK1(6, 0);
    CY(5);
    SM(sc, 7 + sl2, 1, 2, pw0 + 7168, 0);
    CY(6);
    STAMP(11);
    if (!RARE(w >= 2)) {
      REF_WRITE(10, 1);
      sc[0] = f32x4{-mref[0], -mref[0], -mref[0], -mref[0]};
      PIN(sc[0]);
      lds_spin(ctr + C_MK + 10, 4u);
      STAMP(5);
      A_READ(0, 6, 8192);
      QK1(6, 8192);
      SM(sc, 10, 1, 3, pw0 + 9216, 0);
    } else {
      RELANE();
      waitvm<0>();
      if (w == 2) S_POLL(true, 48);
      else { COUNT_VOTES(true); lds_add(lds, C_GX, lane); }
    }
    STAMP(30);
    CYOUT();
    RELANE();
    {
#pragma unroll
      for (int u = 0; u < 2; ++u) {
        *(float*)(lds + L_ML + (mw ^ (u * 64)) * 4 + w * 4) = mref[u];
        *(float*)(lds + L_ML + 512 + (mw ^ (u * 64)) * 4 + w * 4) = x32_sum(x16_sum(lsum[u]));
      }
      u32 old;
      f32x4 m4, l4;
      asm volatile("s_mov_b64 exec, 1\n ds_add_rtn_u32 %0, %3, %4 offset:%6\n s_mov_b64 exec, -1\n ds_read_b128 %1, %5\n ds_read_b128 %2, %5 offset:512\n s_waitcnt lgkmcnt(0)"
                   : "=&v"(old), "=&v"(m4), "=&v"(l4) : "v"((u32)L_CTR), "v"(1u), "v"(L_ML + (lane & 31) * 16), "n"(4 * C_SD) : "memory");
      if (__builtin_amdgcn_readfirstlane(old) == 3u) {
        const float M = fmaxf(fmaxf(m4.x, m4.y), fmaxf(m4.z, m4.w));
        const float l = (l4.x * __builtin_amdgcn_exp2f(m4.x - M) + l4.y * __builtin_amdgcn_exp2f(m4.y - M)) + (l4.z * __builtin_amdgcn_exp2f(m4.z - M) + l4.w * __builtin_amdgcn_exp2f(m4.w - M));
        *(u32x2*)(lds + L_WMAX + (lane & 31) * 8) = u32x2{fbits(M), fbits(l)};
        att_done(rs, lane, sj, ctr[C_PAR]);
      }
    }
    lds_wait(ctr + C_GX, 1u);
    gx = __builtin_amdgcn_readfirstlane(ctr[C_PAR]);
    if (!isS0) T1_SETUP();
    STAMP(13);
  } else {
    const int mu = 2 * j + (w >> 2);
#define Q_LOAD(i0, i1)                                                                                                   \
  _Pragma("unroll") for (int i = (i0); i < (i1); ++i) {                                                                  \
    waitvm<QW - 1>();                                                                                                    \
    dma<0, false>(w * 16384 + (i & 15) * 1024, (vq ^ ((i & 3) << 4)) + ((i < 8 ? 64 * w + 32 * dh : i < 16 ? 64 * w + 32 - 32 * dh : 256 * dh + 32 * w) + 4 * (i & 7)) * 8192, i < 16 ? w_q : w_uk); \
  }
    const u32 vq = g4 * 8192 + hq * 256 + ((col ^ (g4 << 2)) << 4);
    u32x4 xv;
    u32x2 e2;
    u32 x2_;
    asm volatile("global_load_dwordx4 %0, %3, %5\n global_load_dword %1, %4, %5\n buffer_load_dwordx2 %2, off, %6, 0 sc1"
                 : "=&v"(xv), "=&v"(x2_), "=&v"(e2) : "v"(lane * 16), "v"(128 * w + 4 * (lane & 31)), "s"(x), "s"(rs) : "memory");
    Q_LOAD(4, 8);
    asm volatile("s_waitcnt vmcnt(4)" : "+v"(xv), "+v"(x2_), "+v"(e2)::"memory");
    STAMP(27);
    TAG_OF(e2.x, e2.y);
    Q_LOAD(8, 12);
    float ln0, ln1;
    {
      float xe[8] = {lo16(xv.x), hi16(xv.x), lo16(xv.y), hi16(xv.y), lo16(xv.z), hi16(xv.z), lo16(xv.w), hi16(xv.w)};
      float s = 0.f;
#pragma unroll
      for (int k = 0; k < 8; ++k) s += xe[k];
      const float mean = wave_sum(s) * (1.f / 512.f);
      float d2 = 0.f;
#pragma unroll
      for (int k = 0; k < 8; ++k) d2 += (xe[k] - mean) * (xe[k] - mean);
      const float var = rbf(wave_sum(d2) * (1.f / 512.f));
      const float mb = rbf(mean);
      const float sd = rbf(sqrtf(rbf(var + 1e-5f)));
      ln0 = rbf(rbf(lo16(x2_) - mb) / sd);
      ln1 = rbf(rbf(hi16(x2_) - mb) / sd);
    }
    PIN(ln0); PIN(ln1);
    int so = WS_SC + ((lane >> 5) * 2048 + 64 * w + 2 * (lane & 31)) * 8;
    int qpo = (lane & 7) == 0 ? WS_QABS + il(hq * 1024 + dh * 512 + w * 64 + (lane >> 5) * 32 + (lane & 8) * 2, g4 & 1) : WS_TOTAL;
    u32 hw_ = L_H1RED + w * 128 + (lane & 31) * 4, hr_ = L_H1RED + w * 128 + 16 * g4 + 64 * dh, qw_ = L_H1RED + 1024 + w * 272 + lane * 2, qr_ = L_H1RED + 1024 + col * 272 + 64 * g4;
    PIN(so); PIN(qpo); PIN(hw_); PIN(hr_); PIN(qw_); PIN(qr_);
    bf16x8 wf[16], wk[8];
    const u32 ta0 = w * 16384 + off_b(8 * g4 + tq, tp >> 1) + 8 * (tp & 1), ta1 = w * 16384 + off_b(8 * g4 + 4 + tq, tp >> 1) + 8 * (tp & 1);
    Q_LOAD(12, 16);
    waitvm<8>();
#pragma unroll
    for (int c = 0; c < 8; ++c) wf[c] = tr_frag(lds, ta0 ^ (c << 5), ta1 ^ (c << 5));
#pragma unroll
    for (int c = 0; c < 8; ++c) PIN(wf[c]);
    Q_LOAD(16, 24);
    STAMP(12);
    until(t0, E1_T);
    STAMP(23);
    u32x4 g[4];
    asm volatile("buffer_load_dwordx4 %0, %4, %5, 0 offen sc1\n buffer_load_dwordx4 %1, %4, %5, %6 offen sc1\n"
                 "buffer_load_dwordx4 %2, %4, %5, %7 offen sc1\n buffer_load_dwordx4 %3, %4, %5, %8 offen sc1"
                 : "=&v"(g[0]), "=&v"(g[1]), "=&v"(g[2]), "=&v"(g[3]) : "v"(so), "s"(rs), "s"(4096), "s"(8192), "s"(12288) : "memory");
    waitvm<12>();
#pragma unroll
    for (int c = 0; c < 8; ++c) wf[8 + c] = tr_frag(lds, (ta0 ^ (c << 5)) + 8192, (ta1 ^ (c << 5)) + 8192);
    waitvm<4>();
#pragma unroll
    for (int i = 0; i < 8; ++i) wk[i] = *(const bf16x8*)(lds + w * 16384 + (i >> 2) * 4096 + off_b(col, 4 * g4 + (i & 3)));
#pragma unroll
    for (int c = 0; c < 8; ++c) { PIN(wf[8 + c]); PIN(wk[c]); }
    STAMP(4);
    asm volatile("s_waitcnt vmcnt(0)" : "+v"(g[0]), "+v"(g[1]), "+v"(g[2]), "+v"(g[3])::"memory");
    _Pragma("unroll 1") for (u32 spins = 0; spins < SPIN_CAP; ++spins) {
      bool ok = true;
#pragma unroll
      for (int k = 0; k < 4; ++k) ok = ok & (gran_ok(g[k].x, g[k].y, tlo, thi) & gran_ok(g[k].z, g[k].w, tlo, thi));
      if (__all(ok)) break;
      __builtin_amdgcn_s_sleep(1);
      asm volatile("" ::: "memory");
#pragma unroll
      for (int k = 0; k < 4; ++k) g[k] = ld128(rs, so + k * 4096);
    }
    STAMP(1);
    {
      const float m0 = rbf((gran_val(g[0].x) + gran_val(g[1].x)) + (gran_val(g[2].x) + gran_val(g[3].x)));
      const float m1 = rbf((gran_val(g[0].z) + gran_val(g[1].z)) + (gran_val(g[2].z) + gran_val(g[3].z)));
      const auto s0 = __builtin_amdgcn_permlane32_swap(__float_as_uint(m0), __float_as_uint(m0), false, false);
      const auto s1 = __builtin_amdgcn_permlane32_swap(__float_as_uint(m1), __float_as_uint(m1), false, false);
      *(u32*)(lds + hw_) = pk2bf(rbf(ln0 * rbf(1.f + __uint_as_float(s0[1]))) + __uint_as_float(s0[0]),
                                 rbf(ln1 * rbf(1.f + __uint_as_float(s1[1]))) + __uint_as_float(s1[0]));
    }
    WAVE_LDS_SYNC();
    {
      const bf16x8 h0 = *(const bf16x8*)(lds + hr_), h1 = *(const bf16x8*)(lds + (hr_ ^ 64));
      f32x4 d[8];
#pragma unroll
      for (int c = 0; c < 8; ++c) d[c] = __builtin_amdgcn_mfma_f32_16x16x32_bf16(h0, wf[c], f32x4{0.f, 0.f, 0.f, 0.f}, 0, 0, 0);
#pragma unroll
      for (int c = 0; c < 8; ++c) d[c] = __builtin_amdgcn_mfma_f32_16x16x32_bf16(h1, wf[8 + c], d[c], 0, 0, 0);
#pragma unroll
      for (int k = 0; k < 2; ++k) {
        const float v = (g4 & 2) ? ((g4 & 1) ? d[4 * k + 3][0] : d[4 * k + 2][0]) : ((g4 & 1) ? d[4 * k + 1][0] : d[4 * k][0]);
        const u32 vh = f2bf(v);
        *(u16*)(lds + qw_ + 128 * k) = (u16)vh;
        *(u16*)(lds + qw_ + 128 * k + 8 * 272) = (u16)f2bf(v - bf2f(vh));
      }
    }
    STAMP(2);
    BAR();
    {
      bf16x8 a[4];
#pragma unroll
      for (int s = 0; s < 4; ++s) a[s] = *(const bf16x8*)(lds + qr_ + 16 * s);
      f32x4 d[2] = {f32x4{0.f, 0.f, 0.f, 0.f}, f32x4{0.f, 0.f, 0.f, 0.f}};
#pragma unroll
      for (int s = 0; s < 4; ++s)
#pragma unroll
        for (int c = 0; c < 2; ++c) d[c] = __builtin_amdgcn_mfma_f32_16x16x32_bf16(a[s], wk[4 * c + s], d[c], 0, 0, 0);
      constexpr float SCALE = 0.08838834764831845f * 1.4426950408889634f;
      const float v = SCALE * x16_sum(rs32(sum4(d[0]), sum4(d[1])));
      const float p0 = __uint_as_float(nopois(pk2bf(dppf<0x00>(v), dppf<0x55>(v)))), p1 = __uint_as_float(nopois(pk2bf(dppf<0xAA>(v), dppf<0xFF>(v))));
      st128(rs, qpo, u32x4{__float_as_uint(p0), __float_as_uint(p1), __float_as_uint(dppf<0x104>(p0)), __float_as_uint(dppf<0x104>(p1))});
    }
    STAMP(5);
    SB();
    until(t0, MB_T);
    {
      const int r0 = 128 * (mu & 3) + 32 * (w & 3) + 4 * (lane >> 3);
      const u32x2 cvb = *(const u32x2*)(cvec + r0);
      u32x4 wsc[4];
#pragma unroll
      for (int i = 0; i < 4; ++i) wsc[i] = ldnt(w_mod + (size_t)(r0 + i) * 3072 + 1024 + (mu >> 2) * 64 + (lane & 7) * 8);
      float a[8];
#pragma unroll
      for (int k = 0; k < 8; ++k) a[k] = 0.f;
#pragma unroll
      for (int i = 0; i < 4; ++i) fma8b(a, f2bf(silu_bf((i & 1) ? hi16(i < 2 ? cvb.x : cvb.y) : lo16(i < 2 ? cvb.x : cvb.y))), wsc[i]);
      float r[4];
#pragma unroll
      for (int k = 0; k < 4; ++k) r[k] = rs32(a[k], a[k + 4]);
      const float o0 = b3_sum(rs16(r[0], r[2])), o1 = b3_sum(rs16(r[1], r[3]));
      float* red = (float*)(lds + L_H1RED + 8192);
      if ((lane & 8) == 0) *(f32x2*)(red + w * 64 + (lane & 7) * 8 + (lane >> 5) * 4 + ((lane >> 4) & 1) * 2) = f32x2{o0, o1};
      BAR();
      if ((w & 3) == 3 && lane < 16) {
        red += (w >> 2) * 256 + lane * 4;
        const f32x4 sm = (*(const f32x4*)(red) + *(const f32x4*)(red + 64)) + (*(const f32x4*)(red + 128) + *(const f32x4*)(red + 192));
        st128(rs, WS_MODB + ((mu & 3) * 2048 + (mu >> 2) * 64 + lane * 4) * 4, u32x4{fbits(sm.x), fbits(sm.y), fbits(sm.z), fbits(sm.w)});
      }
    }
    gx = pcopy = 0;
  }

  RELANE();
  const int r16 = lane >> 2, ch = lane & 3;
  if (ii >= 16) {
    BAR();
    STAMP(23);
    until(t0, E4_T);
    u32x2 mt;
    u32 done = 0u;
    _Pragma("unroll 1") for (u32 spins = 0; spins < SPIN_CAP; ++spins) {
      asm volatile("" ::: "memory");
      mt = ld64(rs, mo);
      const u32 ok = ~(u32)__ballot(max(mt.x, mt.y) == POISON);
      u32 nw = ok & (ok >> 1);
      nw = nw & (nw >> 2) & 0x111111u & ~done;
      done |= nw;
      _Pragma("unroll 1") while (nw) {
        const int k = __builtin_ctz(nw);
        nw &= nw - 1;
        dma_sc1(pb + k * 256, (pv ^ ((k & 12) << 2)) + ((WS_PART + pcopy + il21(((h >> 1) * 4 + c4) * 65536 + (24 * w + k) * 256, h & 1)) ^ gx), ws);
      }
      if (spins == 0) STAMP(21);
      if (done == 0x111111u) break;
      const int miss = __popc(~ok & 0xFFFFFFu);
      if (miss > 8) __builtin_amdgcn_s_sleep(E4_S2);
      else if (miss > 2) __builtin_amdgcn_s_sleep(E4_S1);
      else __builtin_amdgcn_s_sleep(E4_S0);
    }
    STAMP(16);
    bf16x8 wk[4];
    {
      const u32 eo = L_QOFF + 4416 + w * 64;
      const float m = __uint_as_float(mt.x);
      const float M = wave_max(m);
      const float e = lane < 24 ? __builtin_amdgcn_exp2f(m - M) : 0.f;
      if (lane < 32) *(u16*)(lds + eo + lane * 2) = (u16)f2bf(e);
      WAVE_LDS_SYNC();
      const u32x4 ev4 = *(const u32x4*)(lds + eo + 16 * g4);
      const float L = wave_sum(e * __uint_as_float(mt.y));
      if (lane == 0) *(f32x2*)(lds + L_QOFF + 4352 + w * 8) = f32x2{M, L};
      bf16x8 wf[8], pt[8];
#pragma unroll
      for (int i = 0; i < 8; ++i) { wf[i] = __builtin_bit_cast(bf16x8, (col & 7) == i ? ev4 : Z4); PIN(wf[i]); }
#pragma unroll
      for (int s = 0; s < 4; ++s) { wk[s] = tr_frag(lds, (wa ^ (s << 4)) + s * 1024, (wa ^ (s << 4)) + s * 1024 + 4096); PIN(wk[s]); }
      waitvm<0>();
      STAMP(17);
#pragma unroll
      for (int i = 0; i < 8; ++i) pt[i] = tr_frag(lds, pa[2 * i], pa[2 * i + 1]);
      SB();
      f32x4 d = {0.f, 0.f, 0.f, 0.f};
#pragma unroll
      for (int i = 0; i < 8; ++i) d = __builtin_amdgcn_mfma_f32_16x16x32_bf16(pt[i], wf[i], d, 0, 0, 0);
      const u32 h0 = pk2bf(d[0], d[1]), h1 = pk2bf(d[2], d[3]);
      const u32 l0 = pk2bf(d[0] - lo16(h0), d[1] - hi16(h0)), l1 = pk2bf(d[2] - lo16(h1), d[3] - hi16(h1));
      *(u32x2*)(lds + ro) = (col & 8) ? u32x2{l0, l1} : u32x2{h0, h1};
    }
    BAR();
    STAMP(14);
    const f32x2 ml = *(const f32x2*)(lds + mlo);
    bf16x8 lf[4];
#pragma unroll
    for (int s = 0; s < 4; ++s) lf[s] = *(const bf16x8*)(lds + bo + 64 * s);
    SB();
    f32x4 d = {0.f, 0.f, 0.f, 0.f};
#pragma unroll
    for (int s = 0; s < 4; ++s) d = __builtin_amdgcn_mfma_f32_16x16x32_bf16(wk[s], lf[s], d, 0, 0, 0);
    SB();
    float Mg = fmaxf(ml.x, dppf<0xB1>(ml.x));
    Mg = fmaxf(Mg, dppf<0x4E>(Mg)); Mg = fmaxf(Mg, dppf<0x141>(Mg));
    const float e = __builtin_amdgcn_exp2f(ml.x - Mg);
    float Lg = quad_sum(e * ml.y);
    Lg += dppf<0x141>(Lg);
    const float sc = e * __builtin_amdgcn_rcpf(Lg);
    {
      float d0 = d[0] * sc, d1 = d[1] * sc, d2 = d[2] * sc, d3 = d[3] * sc;
      row16_sum4(d0, d1, d2, d3);
      d = f32x4{d0, d1, d2, d3};
    }
    const u32 h0 = pk2bf(d[0], d[1]), h1 = pk2bf(d[2], d[3]);
    const u32 l0 = pk2bf(d[0] - lo16(h0), d[1] - hi16(h0)), l1 = pk2bf(d[2] - lo16(h1), d[3] - hi16(h1));
    st64(rs, so1, nopois(col ? l0 : h0), nopois(col ? l1 : h1));
    STAMP(15);
    STAMP_FLUSH();
    return;
  }
  if (tl2) {
    BAR0();
    until(t0, WO_T);
#pragma unroll
    for (int i = 0; i < 8; ++i) dma<0, WNT>(owno + i * 1024, (ks * 512 + 64 * w + 8 * i + (lane >> 3)) * 1024 + cg * 128 + (lane & 7) * 16, w_o);
    waitvm<0>();
    STAMP(26);
    bf16x8 wf[8];
#pragma unroll
    for (int i = 0; i < 8; ++i) {
      const u32 a = owno + (32 * (i >> 2) + 8 * g4 + tq) * 128 + (i & 3) * 32 + tp * 8;
      wf[i] = tr_frag(lds, a, a + 512);
    }
#pragma unroll
    for (int i = 0; i < 8; ++i) PIN(wf[i]);
    STAMP(23);
    int off[1] = {(WS_PCTX + (ks * 4 + (w >> 1)) * 2048 + tq * 512 + (w & 1) * 256 + g4 * 64 + tp * 16) ^ gx};
    u32 rw = L_RED + lane * 32 + w * 4;
    PIN(rw);
    int so2 = tp < 2 ? WS_PATTN + il((ks * 512 + cg * 64 + (lane & 60)) * 4, tp) : WS_TOTAL;
    PIN(so2);
    u32x4 g[1];
    gather128<1, 4>(rs, off, g);
    STAMP(24);
    const bf16x8 a0 = __builtin_bit_cast(bf16x8, (col & 2) ? Z4 : g[0]), a1 = __builtin_bit_cast(bf16x8, (col & 2) ? g[0] : Z4);
    f32x4 d[4];
#pragma unroll
    for (int c = 0; c < 4; ++c) d[c] = __builtin_amdgcn_mfma_f32_16x16x32_bf16(a0, wf[c], f32x4{0.f, 0.f, 0.f, 0.f}, 0, 0, 0);
#pragma unroll
    for (int c = 0; c < 4; ++c) d[c] = __builtin_amdgcn_mfma_f32_16x16x32_bf16(a1, wf[4 + c], d[c], 0, 0, 0);
    *(float*)(lds + rw) = rs16(rs32(sum4(d[0]), sum4(d[2])), rs32(sum4(d[1]), sum4(d[3])));
    BAR();
    if (w == 0) {
      const float v = sum4(*(const f32x4*)(lds + L_RED + lane * 32) + *(const f32x4*)(lds + L_RED + lane * 32 + 16));
      const float vn = __uint_as_float(fbits(v));
      st128(rs, so2, u32x4{__float_as_uint(dppf<0x00>(vn)), __float_as_uint(dppf<0x55>(vn)), __float_as_uint(dppf<0xAA>(vn)), __float_as_uint(dppf<0xFF>(vn))});
    }
    STAMP(17);
  } else if (t3 >= 0) {
    STAMP(20);
    if (w < 4) {
      const int fr = ((128 * w + r16) * DF + t3 * 32) * 2, ca_ = ch ^ ((r16 >> 2) & 2) ^ (t3 & 1);
      const u16* wa = (t3 & 1) ? w_up : w_gate;
      const u16* wb = (t3 & 1) ? w_gate : w_up;
      const u32 d = w * 32768 + 8192;
      until(t0, GU_T);
#pragma unroll
      for (int i = 0; i < 8; ++i) { waitvm<WING - 1>(); dma<0, GUNT>(d + (i >> 2) * 12288 + (t3 & 1) * 4096 + (i & 3) * 1024, fr + ca_ * 16 + 32 * i * DF, wa); }
#pragma unroll
      for (int i = 0; i < 8; ++i) { waitvm<WING - 1>(); dma<0, GUNT>(d + (i >> 2) * 12288 + 4096 - (t3 & 1) * 4096 + (i & 3) * 1024, fr + (ca_ ^ 1) * 16 + 32 * i * DF, wb); }
    } else {
      until(t0, DN_T);
#pragma unroll
      for (int i = 0; i < 8; ++i) {
        waitvm<WING - 1>();
        dma<0, WNT>(lw * 32768 + 16384 + (i >> 2) * 12288 + (i & 3) * 1024, (32 * t3 + 8 * (i & 3) + (lane >> 3)) * 1024 + 256 * lw + 128 * (i >> 2) + (lane & 7) * 16, w_down);
      }
      const u32 x2 = *(const u32*)(x + 128 * lw + 2 * lane);
      u32x4 m6[6];
      int mo[6];
#pragma unroll
      for (int i = 0; i < 6; ++i) mo[i] = MB_OFF(i);
      until(t0, MB_T + 200);
      gather128<6, 8>(rs, mo, m6);
#pragma unroll
      for (int i = 0; i < 6; ++i) {
        const f32x4 f = __builtin_bit_cast(f32x4, m6[i]);
        const float v = rbf(rs16(rs32(f.x, f.z), rs32(f.y, f.w)));
        *(u16*)(lds + L_Z + (i < 2 ? 5 : i < 4 ? 3 : 2) * 1088 + (128 * lw + 64 * (i & 1) + 4 * col + g4) * 2) = (u16)f2bf(i < 4 ? v : 1.f + v);
      }
      *(u32*)(lds + L_Z + 6 * 1088 + lw * 256 + lane * 4) = x2;
      if (w == 4 && lane < 32) *(u32*)(lds + L_ZERO + lane * 4) = lane < 16 ? 0u : POISON;
      if (w == 5) S_POLL(false, 48);
      if (w == 6) COUNT_VOTES(false);
    }
    waitvm<0>();
    STAMP(26);
    BAR();
    gx = __builtin_amdgcn_readfirstlane(ctr[C_PAR]);
    u32 zw = L_Z + (64 * w + 4 * col + g4) * 2;
    u32 hr[4];
#pragma unroll
    for (int i = 0; i < 4; ++i) { hr[i] = (col == i ? L_HH : L_ZERO) + 16 * g4; PIN(hr[i]); }
    int off[2];
#pragma unroll
    for (int i = 0; i < 2; ++i) { off[i] = (WS_PATTN + il(((4 * i + g4) * 512 + 64 * w + col * 4) * 4, myh)) ^ gx; PIN(off[i]); }
    int so = col < 4 ? (WS_PDOWN + il((t3 * 256 + 64 * (w & 3) + 16 * col + 4 * g4) * 4, w >> 2)) ^ gx : WS_TOTAL;
    u32 zr = L_Z + tp * 1088 + 16 * g4, yr = L_Z + 4 * 1088 + lane * 16, hw = L_HH + (w & 3) * 16 + (g4 & 1) * 8 + tp * 2, cr = L_CF + (w & 3) * 16 + tp * 4;
    PIN(cr); PIN(so); PIN(zw); PIN(zr); PIN(yr); PIN(hw);
    float ga = bf2f(*(const u16*)(lds + zw + 5 * 1088)), cb1 = bf2f(*(const u16*)(lds + zw + 2 * 1088)), xe = bf2f(*(const u16*)(lds + zw + 6 * 1088));
    bf16x8 wf[16], wd[4];
    if (w < 4) {
#pragma unroll
      for (int k = 0; k < 16; ++k) {
        const u32 a = (k >> 2) * 32768 + 8192 + ((k >> 1) & 1) * 12288 + (tp >> 1) * 4096 + ((k & 1) * 32 + 8 * g4 + tq) * 64 + (w ^ ((g4 & 1) * 2) ^ (tp >> 1)) * 16 + (tp & 1) * 8;
        wf[k] = tr_frag(lds, a, a + 256);
      }
#pragma unroll
      for (int k = 0; k < 16; ++k) PIN(wf[k]);
    }
#pragma unroll
    for (int i = 0; i < 4; ++i) {
      const u32 a = owno + 8192 + (8 * g4 + tq) * 128 + i * 32 + tp * 8;
      wd[i] = tr_frag(lds, a, a + 512);
    }
#pragma unroll
    for (int i = 0; i < 4; ++i) PIN(wd[i]);
    PIN(ga); PIN(cb1); PIN(xe);
    STAMP(23);
    until(t0, E6_T);
    u32x4 pa2[2];
    gather128<2, 4>(rs, off, pa2);
    STAMP(18);
    {
      const f32x4 s4 = __builtin_bit_cast(f32x4, pa2[0]) + __builtin_bit_cast(f32x4, pa2[1]);
      const u32 yb = f2bf(xe + rbf(ga * rbf(rs16(rs32(s4.x, s4.z), rs32(s4.y, s4.w)))));
      const float z = bf2f(yb) * cb1;
      const u32 zh = f2bf(z);
      *(u16*)(lds + zw + 4 * 1088) = (u16)yb;
      *(u16*)(lds + zw) = (u16)zh;
      *(u16*)(lds + zw + 1088) = (u16)f2bf(z - bf2f(zh));
    }
    BAR();
    if (w < 4) {
      bf16x8 zf[16];
#pragma unroll
      for (int k = 0; k < 16; ++k) zf[k] = *(const bf16x8*)(lds + zr + 64 * k);
      __builtin_amdgcn_sched_barrier(0);
      f32x4 d = {0.f, 0.f, 0.f, 0.f};
#pragma unroll
      for (int k = 0; k < 16; ++k) d = __builtin_amdgcn_mfma_f32_16x16x32_bf16(wf[k], zf[k], d, 0, 0, 0);
      PIN(d);
      u32 cfb;
      do { cfb = *(volatile LAS u32*)(lds + cr); } while (__any(cfb == POISON));
      d *= __uint_as_float(cfb);
      const float a0 = (tp & 1) ? d[1] : d[0], b0 = (tp & 1) ? d[0] : d[1], a1 = (tp & 1) ? d[3] : d[2], b1 = (tp & 1) ? d[2] : d[3];
      const float e0 = a0 + dppf<0xB1>(b0), e1 = a1 + dppf<0xB1>(b1);
      const float v = rbf(((tp & 2) ? e1 : e0) + dppf<0x4E>((tp & 2) ? e0 : e1));
      const auto sw = __builtin_amdgcn_permlane32_swap(__float_as_uint(v), __float_as_uint(v), false, false);
      const float g = __uint_as_float(sw[0]), u = __uint_as_float(sw[1]);
      const float e2 = __builtin_amdgcn_exp2f(g * (-2.3022081981f - 0.1029432396f * g * g));
      *(u16*)(lds + hw) = (u16)f2bf(rbf(g * __builtin_amdgcn_rcpf(e2 + 1.f)) * u);
    } else {
      const u32x4 yv = *(const u32x4*)(lds + yr);
      const float a1 = dot2(yv.x, 0x3F803F80u, dot2(yv.y, 0x3F803F80u, 0.f)) + dot2(yv.z, 0x3F803F80u, dot2(yv.w, 0x3F803F80u, 0.f));
      const float a2 = dot2(yv.x, yv.x, dot2(yv.y, yv.y, 0.f)) + dot2(yv.z, yv.z, dot2(yv.w, yv.w, 0.f));
      const float sr = x16_sum(row16_sum(rs32(a1, a2))) * (1.f / 512.f);
      const float s1 = __uint_as_float(__builtin_amdgcn_readlane(__float_as_uint(sr), 0)), s2 = __uint_as_float(__builtin_amdgcn_readlane(__float_as_uint(sr), 32));
      const float var = rbf(fmaxf(s2 - s1 * s1, 0.f));
      const float mb = rbf(s1);
      const float sd = rbf(__builtin_amdgcn_sqrtf(rbf(var + 1e-5f)));
      float rsd = __builtin_amdgcn_rcpf(sd);
      rsd = rsd * (2.f - sd * rsd);
      *(u32*)(lds + cr) = fbits(tp == 3 ? 1.f : rsd * (tp == 2 ? -mb : 1.f));
    }
    BAR();
    {
      bf16x8 hf[4];
#pragma unroll
      for (int i = 0; i < 4; ++i) hf[i] = *(const bf16x8*)(lds + hr[i]);
      __builtin_amdgcn_sched_barrier(0);
      f32x4 d = {0.f, 0.f, 0.f, 0.f};
#pragma unroll
      for (int i = 0; i < 4; ++i) d = __builtin_amdgcn_mfma_f32_16x16x32_bf16(wd[i], hf[i], d, 0, 0, 0);
      st128(rs, so, u32x4{fbits(d[0]), fbits(d[1]), fbits(d[2]), fbits(d[3])});
    }
    STAMP(19);
  }
  if (t4 >= 0 && lw == 0) {
#define RED8(f) b3_sum(rs16(rs32(f.x, f.z), rs32(f.y, f.w)))
    const int oc = t4 * 32 + (lane & 7) * 4 + g4;
    const u32 xb = x[oc];
    float x1, gb;
    {
      int off[2];
      off[0] = lane < 32 ? WS_MODB + ((lane >> 3) * 2048 + t4 * 32 + (lane & 7) * 4) * 4 : WS_TOTAL;
      off[1] = lane < 32 ? WS_MODB + ((lane >> 3) * 2048 + 1536 + t4 * 32 + (lane & 7) * 4) * 4 : WS_TOTAL;
      u32x4 mg[2];
      gather128<2>(rs, off, mg);
      const f32x4 fa = __builtin_bit_cast(f32x4, mg[0]), fb = __builtin_bit_cast(f32x4, mg[1]);
      const float ga = rbf(RED8(fa));
      gb = rbf(RED8(fb));
      int of1[1] = {(WS_PATTN + il(((lane >> 3) * 512 + t4 * 32 + (lane & 7) * 4) * 4, myh)) ^ gx};
      u32x4 g[1];
      gather128<1, 4>(rs, of1, g);
      const f32x4 gf = __builtin_bit_cast(f32x4, g[0]);
      x1 = rbf(bf2f(xb) + rbf(ga * rbf(RED8(gf))));
    }
    STAMP(20);
    int off[7];
#pragma unroll
    for (int i = 0; i < 7; ++i) off[i] = (WS_PDOWN + il(((7 * (lane >> 3) + i) * 256 + (t4 & 7) * 32 + (lane & 7) * 4) * 4, myh)) ^ gx;
    const rsrc_t ro = __builtin_amdgcn_make_buffer_rsrc((void*)out, 0, 1024, 0x00020000);
    int oo = (lane & 8) ? 1024 : oc * 2;
    PIN(x1); PIN(gb); PIN(oo);
    u32x4 g[7];
    __builtin_amdgcn_s_sleep(T4_DELAY);
    gather128<7>(rs, off, g);
    STAMP(21);
#define F4(i) __builtin_bit_cast(f32x4, g[i])
    const f32x4 s = ((F4(0) + F4(1)) + (F4(2) + F4(3))) + ((F4(4) + F4(5)) + F4(6));
    __builtin_amdgcn_raw_buffer_store_b16((u16)f2bf(x1 + rbf(gb * rbf(RED8(s)))), ro, oo, 0, 0);
    if (t4 == 0 && lane == 0) st64(rs, WS_EPOCH, ctr[C_TLO], ctr[C_THI]);
    STAMP(22);
  }
  STAMP_FLUSH();
}

extern "C" void kernel_launch(void* const* d_in, const int* in_sizes, int n_in, void* d_out, int out_size,
                              void* d_ws, size_t ws_size, hipStream_t stream) {
  k_main<<<NWG, 512, 0, stream>>>((const u16*)d_in[0], (const u16*)d_in[1], (const u16*)d_in[2], (const u16*)d_in[3],
                                  (const u16*)d_in[4], (const u16*)d_in[5], (const u16*)d_in[7], (const u16*)d_in[8],
                                  (const u16*)d_in[9], (const u16*)d_in[10], (const u16*)d_in[11], (u16*)d_out,
                                  (unsigned char*)d_ws);
}
```

```cpp
#include <hip/hip_runtime.h>
#include <hip/hip_bf16.h>
#include <stdint.h>

typedef unsigned short u16;
typedef unsigned int u32;
typedef unsigned long long u64;
typedef float f32x2 __attribute__((ext_vector_type(2)));
typedef float f32x4 __attribute__((ext_vector_type(4)));
typedef float f32x16 __attribute__((ext_vector_type(16)));
typedef u32 u32x2 __attribute__((ext_vector_type(2)));
typedef u32 u32x4 __attribute__((ext_vector_type(4)));
typedef u32 u32x16 __attribute__((ext_vector_type(16)));
typedef short bf16x8 __attribute__((ext_vector_type(8)));
typedef short s16x4 __attribute__((ext_vector_type(4)));
#define LAS __attribute__((address_space(3)))
#define DI __device__ __forceinline__

constexpr int DF = 1792;
constexpr int NWG = 256;
constexpr int AUX_SC1 = 16;

constexpr int WS_EPOCH = 0;
constexpr int WS_SFLAG = 4096;
constexpr int WS_QABS = 32768;
constexpr int WS_LSE = WS_QABS + 65536;
constexpr int WS_PCTX = WS_LSE + 65536;
constexpr int WS_PATTN = WS_PCTX + 65536;
constexpr int WS_PDOWN = WS_PATTN + 32768;
constexpr int WS_MODB = WS_PDOWN + 114688;
constexpr int WS_SC = WS_MODB + 32768;
constexpr int WS_PROBE = WS_SC + 32768;
constexpr int WS_VOTE = WS_PROBE + 262144;
constexpr int WS_PART = 2097152;
constexpr int NPR = 2;
constexpr int PART_BYTES = 32 * 8 * 256 * 128;
constexpr int WS_TOTAL = WS_PART + NPR * PART_BYTES;

constexpr int il(int o, int hf) { return (o >> 13) * 16384 + hf * 8192 + (o & 8191); }
constexpr int il21(int o, int hf) { return il(o, hf ^ ((o >> 20) & 1) ^ 1); }
constexpr u32 SPIN_CAP = 1u << 16;
#define WA 8
#define WA0 4
#define WB 8
#define WB0 5
#define WC 8
#define WC0 5
#define WINT 4
#define QF_T 300
#define E1_T 270
#define E3_T 515
#define MB_T 520
#define GU_T 600
#define DN_T 600
#define E4_T 0
#define E4_S2 12
#define E4_S1 6
#define E4_S0 2
#define WO_T 0
#define KVNT 1
#define KVPOL " nt"
#define WNT true
#define Q_SLP 2
#define E1_BAR 0
#define P_LEN 0
#define HNT 0
#define HPOL ""
#define EARLY 0
#define LSET 1
#define WING 2
#define E6_T 1200
#define TAU 32.f
#define QW 4
#define WINP 8
#define PSLP 0
#define GUNT false
#define WV_MODE 0
#define T4_DELAY 12
#define VCLIP 40
#define T0_ADJ 35
#define PF_LEN 37300
#define G_OF(v) ((v) > 24576.f ? 8192u : 0u)
#define VOTE_STAMP(v)
#define STAMP_ARG
#define STAMP_PASS
#define STAMP_DECL
#define STAMP(i)
#define STAMP_FLUSH()

DI float bf2f(u32 b) { return __uint_as_float(b << 16); }
DI float rbf(float f) { return (float)(__bf16)f; }
DI u32 f2bf(float f) { return (u32)__builtin_bit_cast(u16, (__bf16)f); }
typedef __bf16 bf16x2_t __attribute__((ext_vector_type(2)));
DI u32 pk2bf(float a, float b) { return __builtin_bit_cast(u32, __builtin_convertvector(f32x2{a, b}, bf16x2_t)); }
DI float lo16(u32 v) { return __uint_as_float(v << 16); }
DI float hi16(u32 v) { return __uint_as_float(v & 0xFFFF0000u); }
DI u32x4 ldnt(const u16* p) { return WNT ? __builtin_nontemporal_load((const u32x4*)p) : *(const u32x4*)p; }

DI float silu_bf(float a) { return rbf(a * __builtin_amdgcn_rcpf(1.f + __expf(-a))); }

template <int CTRL> DI float dppf(float v) {
  return __int_as_float(__builtin_amdgcn_update_dpp(0, __float_as_int(v), CTRL, 0xF, 0xF, true));
}
DI float quad_sum(float v) { v += dppf<0xB1>(v); v += dppf<0x4E>(v); return v; }
DI float b23_sum(float v) { v += dppf<0x128>(v); v += dppf<0x124>(v); return v; }
DI float b3_sum(float v) { return v + dppf<0x128>(v); }
DI float x16_sum(float v) {
  const auto r = __builtin_amdgcn_permlane16_swap(__float_as_uint(v), __float_as_uint(v), false, false);
  return __uint_as_float(r[0]) + __uint_as_float(r[1]);
}
DI float x32_sum(float v) {
  const auto r = __builtin_amdgcn_permlane32_swap(__float_as_uint(v), __float_as_uint(v), false, false);
  return __uint_as_float(r[0]) + __uint_as_float(r[1]);
}
DI float rs32(float a, float b) {
  const auto r = __builtin_amdgcn_permlane32_swap(__float_as_uint(a), __float_as_uint(b), false, false);
  return __uint_as_float(r[0]) + __uint_as_float(r[1]);
}
DI float rs16(float a, float b) {
  const auto r = __builtin_amdgcn_permlane16_swap(__float_as_uint(a), __float_as_uint(b), false, false);
  return __uint_as_float(r[0]) + __uint_as_float(r[1]);
}
DI float row16_sum(float v) { return b23_sum(quad_sum(v)); }
DI float wave_sum(float v) { return x32_sum(x16_sum(row16_sum(v))); }
DI void row16_sum4(float& a, float& b, float& c, float& d) {
#define R4(C) "v_add_f32_dpp %0, %0, %0 " C " row_mask:0xf bank_mask:0xf\n v_add_f32_dpp %1, %1, %1 " C " row_mask:0xf bank_mask:0xf\n" \
              "v_add_f32_dpp %2, %2, %2 " C " row_mask:0xf bank_mask:0xf\n v_add_f32_dpp %3, %3, %3 " C " row_mask:0xf bank_mask:0xf\n"
  asm("s_nop 1\n" R4("quad_perm:[1,0,3,2]") R4("quad_perm:[2,3,0,1]") R4("row_ror:8") R4("row_ror:4") : "+v"(a), "+v"(b), "+v"(c), "+v"(d));
#undef R4
}
DI float row16_max(float v) {
  v = fmaxf(v, dppf<0xB1>(v)); v = fmaxf(v, dppf<0x4E>(v));
  v = fmaxf(v, dppf<0x128>(v)); v = fmaxf(v, dppf<0x124>(v));
  return v;
}
DI float x16_max(float v) {
  const auto r = __builtin_amdgcn_permlane16_swap(__float_as_uint(v), __float_as_uint(v), false, false);
  return fmaxf(__uint_as_float(r[0]), __uint_as_float(r[1]));
}
DI float x32_max(float v) {
  const auto r = __builtin_amdgcn_permlane32_swap(__float_as_uint(v), __float_as_uint(v), false, false);
  return fmaxf(__uint_as_float(r[0]), __uint_as_float(r[1]));
}
DI float wave_max(float v) { return x32_max(x16_max(row16_max(v))); }

DI float dot2(u32 a, u32 b, float c) {
  return __builtin_amdgcn_fdot2_f32_bf16(__builtin_bit_cast(bf16x2_t, a), __builtin_bit_cast(bf16x2_t, b), c, false);
}
DI void fma8b(float* acc, u32 sbits, u32x4 w) {
  const u32 sh = sbits << 16;
  acc[0] = dot2(w.x, sbits, acc[0]); acc[1] = dot2(w.x, sh, acc[1]);
  acc[2] = dot2(w.y, sbits, acc[2]); acc[3] = dot2(w.y, sh, acc[3]);
  acc[4] = dot2(w.z, sbits, acc[4]); acc[5] = dot2(w.z, sh, acc[5]);
  acc[6] = dot2(w.w, sbits, acc[6]); acc[7] = dot2(w.w, sh, acc[7]);
}
DI void fma8(float* acc, float s, u32x4 w) {
  acc[0] += s * lo16(w.x); acc[1] += s * hi16(w.x);
  acc[2] += s * lo16(w.y); acc[3] += s * hi16(w.y);
  acc[4] += s * lo16(w.z); acc[5] += s * hi16(w.z);
  acc[6] += s * lo16(w.w); acc[7] += s * hi16(w.w);
}

DI unsigned fsw(unsigned row) { return ((row & 3) << 2) | ((row >> 2) & 3); }
DI unsigned off_b(unsigned row, unsigned ch) { return 256u * row + 16u * (ch ^ fsw(row)); }
DI unsigned tr_addr(unsigned lane, unsigned c, unsigned ks, unsigned t) {
  const unsigned h = lane >> 5, blk = (lane >> 4) & 1, q = (lane & 15) >> 2, p = lane & 3;
  return off_b(16 * ks + 8 * h + 4 * t + q, 4 * c + 2 * blk + (p >> 1)) + 8 * (p & 1);
}

typedef __amdgpu_buffer_rsrc_t rsrc_t;
DI u32x4 ld128(rsrc_t rs, int off) { return __builtin_amdgcn_raw_buffer_load_b128(rs, off, 0, AUX_SC1); }
DI u32x2 ld64(rsrc_t rs, int off) { return __builtin_amdgcn_raw_buffer_load_b64(rs, off, 0, AUX_SC1); }
DI void st64(rsrc_t rs, int off, u32 lo, u32 hi) { u32x2 v; v.x = lo; v.y = hi; __builtin_amdgcn_raw_buffer_store_b64(v, rs, off, 0, AUX_SC1); }
DI void st128(rsrc_t rs, int off, u32x4 v) { __builtin_amdgcn_raw_buffer_store_b128(v, rs, off, 0, AUX_SC1); }
#define BAR() do { asm volatile("s_waitcnt lgkmcnt(0)" ::: "memory"); __builtin_amdgcn_s_barrier(); asm volatile("" ::: "memory"); } while (0)
#define BAR0() do { __builtin_amdgcn_sched_barrier(0); asm volatile("" ::: "memory"); __builtin_amdgcn_s_barrier(); asm volatile("" ::: "memory"); __builtin_amdgcn_sched_barrier(0); } while (0)
#define PIN(x) asm volatile("" : "+v"(x))
constexpr u32 POISON = 0xFFFFFFFFu;
DI bool pois4(u32x4 v) { return max(max(v.x, v.y), max(v.z, v.w)) == POISON; }
DI u32 nopois(u32 v) { return min(v, 0xFFFFFFFEu); }
DI u32 fbits(float v) { return nopois(__float_as_uint(v)); }

DI u32x2 mk_gran(float v, u32 tlo, u32 thi) {
  u32 b = __float_as_uint(v);
  if ((b & 0x7F800000u) != 0x7F800000u) b += 0x80u;
  u32x2 g; g.x = (b >> 8) | (tlo << 24); g.y = (tlo >> 8) | (thi << 24);
  return g;
}
DI void st_gran2(rsrc_t rs, int off, float v0, float v1, u32 tlo, u32 thi) {
  const u32x2 a = mk_gran(v0, tlo, thi), b = mk_gran(v1, tlo, thi);
  u32x4 v; v.x = a.x; v.y = a.y; v.z = b.x; v.w = b.y;
  st128(rs, off, v);
}
DI bool gran_ok(u32 lo, u32 hi, u32 tlo, u32 thi) { return (((lo >> 24) ^ (tlo & 0xFFu)) | (hi ^ ((tlo >> 8) | (thi << 24)))) == 0u; }
DI float gran_val(u32 lo) { return __uint_as_float(lo << 8); }

template <int N, int SLP = 1, bool PRE = false> DI void gather128(rsrc_t rs, const int (&off)[N], u32x4 (&v)[N]) {
#pragma unroll
  for (int i = 0; i < N; ++i) if (!PRE) v[i] = ld128(rs, off[i]);
  _Pragma("unroll 1") for (u32 spins = 0; spins < SPIN_CAP; ++spins) {
    bool need[N], bad = false;
#pragma unroll
    for (int i = 0; i < N; ++i) { need[i] = pois4(v[i]); bad = bad || need[i]; }
    if (!__any(bad)) break;
    __builtin_amdgcn_s_sleep(SLP);
    asm volatile("" ::: "memory");
    __builtin_amdgcn_sched_barrier(0);
#pragma unroll
    for (int i = 0; i < N; ++i)
      if (need[i]) v[i] = ld128(rs, off[i]);
  }
}
template <int N> DI void waitvm() { asm volatile("s_waitcnt vmcnt(%0)" ::"n"(N) : "memory"); }
constexpr u32 LSPIN_CAP = 1u << 22;
DI void lds_wait(volatile LAS u32* p, u32 val) {
  _Pragma("unroll 1") for (u32 n = 0; *p < val && n < LSPIN_CAP; ++n) __builtin_amdgcn_s_sleep(1);
  asm volatile("" ::: "memory");
}
#define WAVE_LDS_SYNC() do { __builtin_amdgcn_fence(__ATOMIC_RELEASE, "wavefront"); __builtin_amdgcn_wave_barrier(); __builtin_amdgcn_fence(__ATOMIC_ACQUIRE, "wavefront"); } while (0)
#define SB() __builtin_amdgcn_sched_barrier(0)
#define RARE(c) __builtin_expect(!!(c), 0)

constexpr int PS = 28672;
constexpr int L_QI = 4 * PS;
constexpr int L_P = L_QI + 32768;
constexpr int L_REF = L_P + 11264;
constexpr int L_MX = L_REF + 1408;
constexpr int L_ALW = L_MX + 128;
constexpr int L_DUMP = L_ALW + 512;
constexpr int L_QOFF = 131072;
constexpr int L_H1RED = L_QOFF;
constexpr int L_RED = L_QOFF + 16384;
constexpr int L_Z = L_RED, L_HH = L_RED + 7 * 1088, L_ZERO = L_HH + 64, L_CF = L_ZERO + 64;
constexpr int L_MISC = L_QOFF + 30720;
constexpr int L_CTR = L_MISC;
constexpr int L_WMAX = L_MISC + 256;
constexpr int L_ML = L_MISC + 512;

#define Z4 (u32x4{0u, 0u, 0u, 0u})
DI float max5(f32x4 v, float e) { return fmaxf(fmaxf(fmaxf(fmaxf(v.x, v.y), v.z), v.w), e); }

template <int OFF, bool NT> DI void dma(u32 m, u32 voff, const void* sb) {
  if (NT) asm volatile("s_mov_b32 m0, %0\n s_nop 0\n global_load_lds_dwordx4 %1, %2 offset:%3 nt" ::"s"(m), "v"(voff), "s"(sb), "n"(OFF) : "memory");
  else asm volatile("s_mov_b32 m0, %0\n s_nop 0\n global_load_lds_dwordx4 %1, %2 offset:%3" ::"s"(m), "v"(voff), "s"(sb), "n"(OFF) : "memory");
}
DI void dma_sc1(u32 m, u32 voff, const void* sb) {
  asm volatile("s_mov_b32 m0, %0\n s_nop 0\n global_load_lds_dwordx4 %1, %2 sc1" ::"s"(m), "v"(voff), "s"(sb) : "memory");
}
DI void lds_addv(unsigned char* lds, int idx, u32 v) {
  SB();
  asm volatile("s_mov_b64 exec, 1\n ds_add_u32 %0, %1 offset:%2\n s_mov_b64 exec, -1" ::"v"((u32)L_CTR), "v"(v), "n"(idx * 4) : "memory");
  SB();
}
DI void lds_add(unsigned char* lds, int idx, int lane) { lds_addv(lds, idx, 1u); }
DI void lds_arrive(unsigned char* lds, int idx, int lane) {
  asm volatile("s_waitcnt lgkmcnt(0)" ::: "memory");
  lds_add(lds, idx, lane);
}
DI u32 spin_at(u32 a, u32 val) {
  u32 t, c, m, cap;
  asm volatile("s_mov_b32 %3, %6\n 1: ds_read_b32 %0, %4\n s_waitcnt lgkmcnt(0)\n v_readfirstlane_b32 %1, %0\n s_and_b32 %2, %1, 255\n s_cmp_ge_u32 %2, %5\n s_cbranch_scc1 2f\n"
               "s_sub_u32 %3, %3, 1\n s_cbranch_scc0 1b\n 2:"
               : "=&v"(t), "=&s"(c), "=&s"(m), "=&s"(cap) : "v"(a), "s"(val), "n"(LSPIN_CAP) : "memory", "scc");
  return c;
}
DI void lds_spin(volatile LAS u32* p, u32 val) { spin_at((u32)(uintptr_t)p, val); }
enum { C_SOK = 0, C_BH = 4, C_TLO = 8, C_THI = 9, C_TAG = 11, C_PD = 12, C_H1 = 18, C_PAR = 19, C_VOTE = 20, C_FREE = 22, C_QRDY = 23, C_WV = 24, C_MXC = 25, C_SD = 26, C_GX = 27, C_MK = 32, C_P = 44 };

DI u32 kv_mark(int nm, int skip) { return L_CTR + 4 * C_MK + __builtin_amdgcn_readfirstlane(4 * nm + (nm >= skip ? 4 : 0)); }
#define KV_TS
#define KV_TSO
#define KV_TSA
#define KV_TSP
#define KV_TRY(l)                                                                                                        \
  "7: s_getreg_b32 %[h], hwreg(HW_REG_IB_STS, 0, 4)\n s_cmp_gt_i32 %[h], %[k4]\n s_cbranch_scc1 " l "\n"                  \
  "s_waitcnt vmcnt(4)\n s_cmp_eq_u32 %[k4], 4\n s_cbranch_scc1 8f\n s_waitcnt vmcnt(0)\n"                              \
  "8: v_subrev_u32 %[t], %[k4], %[ml]\n s_mov_b64 exec, 1\n ds_add_u32 %[t], %[one] offset:12\n s_mov_b64 exec, -1\n"   \
  "s_sub_i32 %[k4], %[k4], 4\n" KV_TS "s_branch 7b\n"
#define STR2(x) #x
#define STR(x) STR2(x)
#define SPIN_SLEEP ""
DI void kv_try(int& k4, u32 ml, u32 one KV_TSA) {
  u32 t, h;
  asm volatile(KV_TRY("9f") "9:" : [t] "=&v"(t), [h] "=&s"(h), [k4] "+s"(k4) KV_TSO : [ml] "v"(ml), [one] "v"(one) : "memory", "scc");
}
DI u32 spin_try(int idx, u32 val, int& k4, u32 ml, u32 one KV_TSA) {
  u32 t, h, c, cap;
  asm volatile("s_mov_b32 %[cap], %[n]\n 1: ds_read_b32 %[t], %[ml] offset:%[o]\n s_waitcnt lgkmcnt(0)\n v_readfirstlane_b32 %[c], %[t]\n s_and_b32 %[h], %[c], 255\n"
               "s_cmp_ge_u32 %[h], %[v]\n s_cbranch_scc1 2f\n" KV_TRY("9f") "9: " SPIN_SLEEP "s_sub_u32 %[cap], %[cap], 1\n s_cbranch_scc0 1b\n 2:"
               : [t] "=&v"(t), [h] "=&s"(h), [c] "=&s"(c), [cap] "=&s"(cap), [k4] "+s"(k4) KV_TSO
               : [ml] "v"(ml), [one] "v"(one), [o] "n"(4 * idx), [v] "n"(val), [n] "n"(LSPIN_CAP) : "memory", "scc");
  return c;
}
template <int W> DI void kv_load1(const u16* ckv, u32 vl, int lw, u32 g0, u32 g1, u32 m, int n0, int n1, int& nm, int skip, u32 tp) {
#pragma unroll 1
  for (int n = n0; n < n1; ++n) {
#define KV_POST(k) ".if (%[wm] | 4) == " #k "\n s_cmp_gt_i32 %[nm], %[n2]\n s_cbranch_scc1 20f\n s_mov_b64 exec, 1\n ds_add_u32 %[ma], %[one]\n s_mov_b64 exec, -1\n s_add_i32 %[nm], %[nm], 1\n 20:\n .endif\n"
#define KV_H "s_waitcnt vmcnt(%[wm])\n"
#define KV_W(o) KV_H "global_load_lds_dwordx4 %[vo], %[p] offset:" #o KVPOL "\n"
#define KV_M "s_add_u32 m0, m0, %[ms]\n"
    asm volatile("s_mov_b32 m0, %[m]\n" KV_W(0) KV_POST(4) KV_M KV_W(256) KV_POST(5) KV_M KV_W(512) KV_POST(6) KV_M KV_W(768) KV_POST(7)
                 : [nm] "+s"(nm)
                 : [m] "s"(m), [vo] "v"(vl + g0 + ((lw + n) & 3) * 4096), [p] "s"(ckv), [ma] "v"(kv_mark(nm, skip)),
                   [one] "v"(1u), [n2] "s"(__builtin_amdgcn_readfirstlane(n - (W > 4 ? 2 : 1))), [wm] "n"(W - 1), [ms] "n"(PS - 256), [tp] "s"(tp), [pl] "n"(P_LEN + 8)
                 : "memory", "scc", "vcc");
    g0 = g1;
    g1 += 192 * 16384;
    m += n + 1 == skip ? 8192 : 4096;
  }
}
template <int W, int W0> DI void kv_load(const u16* ckv, u32 vl, int lw, u32 g0, u32 g1, u32 m, int n0, int n1, int& nm, int skip, u32 tp) {
  if (W != W0 && skip == 4) kv_load1<W0>(ckv, vl, lw, g0, g1, m, n0, n1, nm, skip, tp); else kv_load1<W>(ckv, vl, lw, g0, g1, m, n0, n1, nm, skip, tp);
}
DI void until(u32 t0, u32 dt) {
  while ((u32)__builtin_amdgcn_s_memrealtime() - t0 < dt) __builtin_amdgcn_s_sleep(2);
}
DI void att_done(rsrc_t rs, int lane, int sp, int gx) {
  u32 old;
  u32x4 ml;
  asm volatile("s_mov_b64 exec, 1\n ds_add_rtn_u32 %0, %2, %3 offset:%5\n s_mov_b64 exec, -1\n ds_read_b128 %1, %4\n s_waitcnt lgkmcnt(0)"
               : "=&v"(old), "=&v"(ml) : "v"((u32)L_CTR), "v"(1u), "v"(L_WMAX + (lane & 15) * 16), "n"(4 * C_PD) : "memory");
  old = __builtin_amdgcn_readfirstlane(old);
  if (old == 4u && lane < 16) st128(rs, (WS_LSE + il(sp * 128 + (lane & 7) * 16, lane >> 3)) ^ gx, ml);
  if (old == 4u) asm volatile("s_mov_b64 exec, 1\n ds_add_u32 %0, %1 offset:%2\n s_mov_b64 exec, -1" ::"v"((u32)L_CTR), "v"(1u), "n"(4 * C_PD) : "memory");
}

DI bf16x8 tr_frag(unsigned char* lds, u32 a0, u32 a1) {
  const s16x4 b0 = __builtin_amdgcn_ds_read_tr16_b64_v4i16((LAS s16x4*)(lds + a0));
  const s16x4 b1 = __builtin_amdgcn_ds_read_tr16_b64_v4i16((LAS s16x4*)(lds + a1));
  bf16x8 v;
  v[0] = b0[0]; v[1] = b0[1]; v[2] = b0[2]; v[3] = b0[3]; v[4] = b1[0]; v[5] = b1[1]; v[6] = b1[2]; v[7] = b1[3];
  return v;
}
DI float sum4(f32x4 v) { const f32x2 t = f32x2{v.x, v.y} + f32x2{v.z, v.w}; return t.x + t.y; }

__global__ __launch_bounds__(512, 2) void k_main(const u16* __restrict__ x, const u16* __restrict__ ckv,
                                                 const u16* __restrict__ w_q, const u16* __restrict__ w_uk,
                                                 const u16* __restrict__ w_uv, const u16* __restrict__ w_o,
                                                 const u16* __restrict__ w_gate, const u16* __restrict__ w_up,
                                                 const u16* __restrict__ w_down, const u16* __restrict__ cvec,
                                                 const u16* __restrict__ w_mod, u16* __restrict__ out,
                                                 unsigned char* ws) {
  __shared__ __attribute__((aligned(16))) unsigned char lds[163840];
  const int w = __builtin_amdgcn_readfirstlane(threadIdx.x >> 6), j = blockIdx.x;
  int lane = threadIdx.x & 63;
  const int xc = j & 7, ii = j >> 3;
  STAMP_DECL;
  STAMP(0);
  u32x2 ev;
  u32 xm, vh;
  {
    u64 p, pc, pw;
    u32 a, b, c, d, f, t;
    asm volatile("s_lshr_b32 %[f], %[j], 6\n s_cmp_eq_u32 %[f], 1\n s_cselect_b32 %[a], 0x50, 16\n"
                 "s_load_dwordx2 %[p], %[ka], %[a]\n s_load_dwordx2 %[pw], %[ka], 0x60\n"
                 "s_lshr_b32 %[d], %[j], 3\n s_cmp_lg_u32 %[w], 7\n s_cbranch_scc1 2f\n"
                 "s_and_b32 %[a], %[d], 15\n s_lshl_b32 %[a], %[a], 12\n v_lshl_add_u32 %[t], %[lane], 6, %[a]\n v_min_u32 %[t], %[pfl], %[t]\n"
                 "s_getpc_b64 %[pc]\n s_mov_b32 m0, %[dump]\n s_nop 0\n global_load_lds_dword %[t], %[pc]\n"
                 "2: s_load_dwordx2 %[pc], %[ka], 0x48\n s_and_b32 %[c], %[j], 7\n s_lshl_b32 %[c], %[c], 10\n s_cmp_eq_u32 %[f], 0\n s_cbranch_scc0 3f\n"
                 "s_and_b32 %[a], %[d], 1\n s_lshl1_add_u32 %[a], %[w], %[a]\n s_lshl_b32 %[a], %[a], 18\n s_add_u32 %[c], %[c], %[a]\n"
                 "s_lshr_b32 %[a], %[d], 1\n s_lshl_b32 %[a], %[a], 8\n s_add_u32 %[c], %[c], %[a]\n s_lshl_b32 m0, %[w], 14\n"
                 "v_lshrrev_b32 %[t], 4, %[lane]\n v_mul_u32_u24 %[t], 0x204, %[t]\n v_and_b32 %[vh], 15, %[lane]\n v_xor_b32 %[vh], %[vh], %[t]\n"
                 "v_lshl_add_u32 %[vh], %[vh], 4, %[c]\n v_xor_b32 %[t], 0x8010, %[vh]\n s_waitcnt lgkmcnt(0)\n"
                 "global_load_lds_dwordx4 %[vh], %[p]\n s_add_u32 m0, m0, 0x400\n s_nop 0\n"
                 "global_load_lds_dwordx4 %[t], %[p]\n s_add_u32 m0, m0, 0x400\n v_xor_b32 %[t], 0x10020, %[vh]\n"
                 "global_load_lds_dwordx4 %[t], %[p]\n s_add_u32 m0, m0, 0x400\n v_xor_b32 %[t], 0x18030, %[vh]\n"
                 "global_load_lds_dwordx4 %[t], %[p]\n s_branch 1f\n"
                 "3: s_lshl2_add_u32 %[d], %[d], %[w]\n s_sub_u32 %[d], %[d], 68\n s_lshl_b32 %[d], %[d], 16\n s_add_u32 %[d], %[d], %[c]\n"
                 "s_and_b32 %[a], %[j], 3\n s_lshl2_add_u32 %[b], %[a], %[w]\n s_mul_i32 %[b], %[b], 0x30000\n s_lshl_b32 %[a], %[a], 8\n"
                 "v_lshl_add_u32 %[xm], %[lane], 2, %[a]\n"
                 "s_and_b32 %[a], %[j], 60\n s_lshl_b32 %[a], %[a], 5\n s_add_u32 %[b], %[b], %[a]\n"
                 "s_lshl_b32 %[c], %[w], 12\n s_add_u32 %[c], %[c], %[mq]\n s_lshl_b32 %[a], %[w], 10\n s_add_u32 %[a], %[a], %[mh]\n"
                 "s_cmp_eq_u32 %[f], 1\n s_cselect_b32 %[d], %[b], %[d]\n s_cselect_b32 m0, %[c], %[a]\n s_cselect_b32 %[a], 48, 1\n s_cselect_b32 %[c], 0, 4\n"
                 "v_lshrrev_b32 %[t], 3, %[lane]\n v_mul_u32_u24 %[t], %[a], %[t]\n v_and_b32 %[vh], 7, %[lane]\n v_lshl_add_u32 %[vh], %[t], 3, %[vh]\n"
                 "v_lshl_add_u32 %[vh], %[vh], 4, %[d]\n"
                 "s_cselect_b32 %[b], 6, 1\n s_cselect_b32 %[a], 0x400, 0\n s_lshl_b32 %[b], %[b], 13\n"
                 "s_xor_b32 %[c], %[c], %[w]\n s_bitcmp1_b32 %[c], 2\n v_mov_b32 %[t], 0\n"
                 "s_waitcnt lgkmcnt(0)\n s_cbranch_scc1 1f\n s_cmp_eq_u32 %[f], 1\n s_cbranch_scc0 4f\n"
                 "global_load_dword %[xm], %[xm], %[pc]\n global_load_dwordx2 %[ev], %[t], %[pw] sc1\n"
                 "4: global_load_lds_dwordx4 %[vh], %[p]" HPOL "\n s_add_u32 m0, m0, %[a]\n v_add_u32 %[t], %[b], %[vh]\n"
                 "global_load_lds_dwordx4 %[t], %[p]" HPOL "\n s_add_u32 m0, m0, %[a]\n v_add_u32 %[t], %[b], %[t]\n"
                 "global_load_lds_dwordx4 %[t], %[p]" HPOL "\n s_add_u32 m0, m0, %[a]\n v_add_u32 %[t], %[b], %[t]\n"
                 "global_load_lds_dwordx4 %[t], %[p]" HPOL "\n"
                 "1:\n"
                 : [xm] "=&v"(xm), [ev] "=&v"(ev), [vh] "=&v"(vh), [t] "=&v"(t), [p] "=&s"(p), [pc] "=&s"(pc), [pw] "=&s"(pw), [a] "=&s"(a), [b] "=&s"(b), [c] "=&s"(c),
                   [d] "=&s"(d), [f] "=&s"(f)
                 : [j] "s"(j), [w] "s"(w), [lane] "v"(lane), [ka] "s"(__builtin_amdgcn_kernarg_segment_ptr()), [pfl] "n"(PF_LEN), [dump] "n"(L_DUMP),
                   [mq] "n"(L_QOFF + 8192), [mh] "n"(L_P)
                 : "memory", "scc");
  }
  SB();
  const u32 t0 = (u32)__builtin_amdgcn_s_memrealtime() - T0_ADJ;
  const bool isQ = j < 64, isS0 = j >= 64 && j < 128;
  const int hq = xc * 4 + (ii >> 1), dh = ii & 1, sj = j - 64;
  const int lw = w - 4, myh = xc >> 2;
  int col = lane & 15, g4 = lane >> 4, tq = col >> 2, tp = lane & 3;
  const rsrc_t rs = __builtin_amdgcn_make_buffer_rsrc((void*)ws, 0, WS_TOTAL, 0x00020000);
  volatile LAS u32* ctr = (volatile LAS u32*)(lds + L_CTR);
  SB();
  STAMP(25);
  if (w < 2) *(u32*)(lds + (w == 0 ? L_CTR + lane * 4 : L_MX + (lane & 31) * 4)) = w == 0 ? 0u : 0xFF800000u;
  BAR();
  const bool tl2 = isS0;
  const int t3 = xc < 4 ? (ii < 8 ? xc * 8 + ii : -1) : (ii < 6 ? 8 + xc * 6 + ii : -1);
  const int t4 = (ii >= 8 && ii < 10) ? xc * 2 + (ii - 8) : -1;
  const u32 owno = (w >> 1) * 32768 + 8192 + (w & 1) * 12288;
  const int ks = (ii & 6) + myh, cg = (ii & 1) * 4 + (xc & 3);
  const int h = (j >> 2) & 31, c4 = j & 3;
  u32x4 wv[8];
  u32 tlo, thi;
#define CY(i)
#define CYOUT()
  int gx, pcopy;
#define RELANE() do { asm volatile("v_mbcnt_lo_u32_b32 %0, -1, 0\n v_mbcnt_hi_u32_b32 %0, -1, %0" : "=v"(lane)); col = lane & 15; g4 = lane >> 4; tq = col >> 2; tp = lane & 3; } while (0)
  u32 pa[16], bo, mlo, ro, pv, wa;
  int so1, mo;
  u32x2 mt = {POISON, POISON};
  const u32 pb = (w >> 1) * PS + (w & 1) * 6144;
#define T1_SETUP()                                                                                                       \
  do {                                                                                                                   \
    _Pragma("unroll") for (int i = 0; i < 16; ++i) { pa[i] = pb + off_b(8 * (g4 % 3) + 4 * (i & 1) + tq, (i & 14) + (tp >> 1)) + 8 * (tp & 1); PIN(pa[i]); } \
    bo = L_QOFF + col * 272 + g4 * 16; mlo = L_QOFF + 4352 + (col & 7) * 8; ro = L_QOFF + (w + (col & 8)) * 272 + (col & 7) * 32 + g4 * 8; \
    so1 = col < 2 ? (WS_PCTX + h * 2048 + c4 * 512 + (w >> 2) * 256 + (2 * (w & 1) + (g4 >> 1)) * 64 + (((w >> 1) & 1) * 2 + col) * 16 + (g4 & 1) * 8) ^ gx : WS_TOTAL; \
    mo = lane < 24 ? (WS_LSE + il(LSET ? (24 * w + lane) * 128 + (h >> 1) * 8 : ((h >> 1) * 256 + 24 * w + lane) * 8, h & 1)) ^ gx : WS_TOTAL;                                       \
    pv = g4 * 256 + ((col ^ (g4 << 2)) << 4);                                                                            \
    wa = tq * PS + 16384 + off_b(g4, 2 * w + (tp >> 1)) + 8 * (tp & 1);                                                  \
    PIN(bo); PIN(mlo); PIN(ro); PIN(so1); PIN(mo); PIN(pv); PIN(wa);                                                     \
  } while (0)
#define TAG_OF(e0, e1)                                                                                                  \
  do {                                                                                                                  \
    const u64 tag = (((u64)(e1) << 32) | (e0)) + 1ull;                                                                  \
    tlo = __builtin_amdgcn_readfirstlane((u32)tag); thi = __builtin_amdgcn_readfirstlane((u32)(tag >> 32));             \
  } while (0)
  const u32x4 P4 = {POISON, POISON, POISON, POISON};
#define POISON_COMMON(j)                                                                                                \
  do {                                                                                                                  \
    int po = WS_PDOWN + (j) * 448 + (lane - 36) * 16;                                                                   \
    po = lane < 36 ? WS_TOTAL : po;                                                                                     \
    po = lane < 32 ? WS_LSE + (j) * 256 + (lane - 16) * 16 : po;                                                        \
    po = lane < 16 ? WS_PCTX + (j) * 256 + lane * 16 : po;                                                              \
    st128(rs, po, P4);                                                                                                  \
  } while (0)
#define START_FLAG()                                                                                                    \
  do {                                                                                                                  \
    if (lane < 8) st64(rs, WS_SFLAG + lane * 2048 + sj * 8, tlo, thi);                                                  \
    if (lane == 0) { ctr[C_TLO] = tlo; ctr[C_THI] = thi; }                                                              \
    lds_add(lds, C_TAG, lane);                                                                                          \
  } while (0)
#define COUNT_VOTES(PRE)                                                                                                \
  do {                                                                                                                  \
    const int vo = min(lane, 47) * 32;                                                                                  \
    u32x4 a_ = Z4, b_ = Z4;                                                                                             \
    if (PRE) { a_ = *(const u32x4*)(lds + L_QI + 16384 + vo); b_ = *(const u32x4*)(lds + L_QI + 16400 + vo); }          \
    _Pragma("unroll 1") for (u32 spins = 0; spins < SPIN_CAP; ++spins) {                                                \
      if (((PRE) || spins) && __all((gran_ok(a_.x, a_.y, tlo, thi) & gran_ok(a_.z, a_.w, tlo, thi)) & (gran_ok(b_.x, b_.y, tlo, thi) & gran_ok(b_.z, b_.w, tlo, thi)))) break; \
      if (spins) __builtin_amdgcn_s_sleep(8);                                                                           \
      asm volatile("" ::: "memory");                                                                                    \
      a_ = ld128(rs, WS_VOTE + xc * 2048 + vo); b_ = ld128(rs, WS_VOTE + xc * 2048 + vo + 16);                          \
    }                                                                                                                   \
    const float vt_ = wave_sum(lane < 48 ? (float)(((a_.x & 1023u) + (a_.z & 1023u)) + ((b_.x & 1023u) + (b_.z & 1023u))) : 0.f); \
    if (lane == 0) ctr[C_PAR] = G_OF(vt_);                                                                              \
    VOTE_STAMP(vt_);                                                                                                    \
    STAMP(8);                                                                                                           \
  } while (0)
#define S_POLL(PRE, NL)                                                                                                 \
  do {                                                                                                                  \
    const int off_ = min(lane, (NL) - 1) * 32;                                                                          \
    u32x4 a_ = Z4, b_ = Z4;                                                                                             \
    if (PRE) { a_ = *(const u32x4*)(lds + L_QI + off_); b_ = *(const u32x4*)(lds + L_QI + 16 + off_); }                 \
    _Pragma("unroll 1") for (u32 spins = 0; spins < SPIN_CAP; ++spins) {                                                \
      const u32 d_ = (((a_.x ^ tlo) | (a_.y ^ thi)) | ((a_.z ^ tlo) | (a_.w ^ thi))) |                                  \
                     (((b_.x ^ tlo) | (b_.y ^ thi)) | ((b_.z ^ tlo) | (b_.w ^ thi)));                                   \
      if (((PRE) || spins) && __all(d_ == 0u)) break;                                                                   \
      if (spins) __builtin_amdgcn_s_sleep(8);                                                                           \
      asm volatile("" ::: "memory");                                                                                    \
      a_ = ld128(rs, WS_SFLAG + xc * 2048 + off_); b_ = ld128(rs, WS_SFLAG + xc * 2048 + off_ + 16);                    \
    }                                                                                                                   \
  } while (0)
#define MB_OFF(i) (WS_MODB + ((lane >> 4) * 2048 + ((i) >> 1) * 512 + 128 * lw + 64 * ((i) & 1) + (lane & 15) * 4) * 4)

  if (!isQ && w >= 4) {
    if (isS0) {
      if (w == 4) {
        const u32x2 e2 = ld64(rs, WS_EPOCH);
        st128(rs, WS_QABS + sj * 1024 + lane * 16, P4);
        st128(rs, lane < 32 ? WS_MODB + sj * 512 + lane * 16 : WS_TOTAL, P4);
        POISON_COMMON(sj);
        POISON_COMMON(j);
        waitvm<0>();
        TAG_OF(e2.x, e2.y);
        lds_add(lds, C_BH, lane);
        START_FLAG();
        STAMP(27);
      }
      lds_wait(ctr + C_H1, 4u);
    } else {
#pragma unroll
      for (int i = 4; i < 16; ++i) { waitvm<WINP - 1>(); if (i < 8) dma<0, (HNT & 1) != 0>(L_P + w * 1024, vh + (i & 7) * 8192, w_q); else dma<0, (HNT & 2) != 0>(L_P + w * 1024, vh + (i & 7) * 8192, w_uk); }
    }
    const u32 vl = g4 * 1024 + ((col ^ ((g4 << 2) | lw)) << 4);
    int nm = 0, ns = isS0 ? 6 : 7;
    const int skip = isS0 ? 4 : 99;
    kv_load<WA, WA0>(ckv, vl, lw, (isS0 ? sj : 1792 + j) * 16384, (isS0 ? sj + 192 : sj) * 16384, lw * 1024, 0, 2, nm, skip, t0 + E3_T - P_LEN);
    u32 va[8];
#pragma unroll
    for (int i = 0; i < 8; ++i) { va[i] = lw * PS + tr_addr(lane, i >> 1, 0, i & 1); PIN(va[i]); }
    if (!isS0) {
      const rsrc_t rw = __builtin_amdgcn_make_buffer_rsrc((void*)w_uv, 0, 4194304, 0x00020000);
#pragma unroll
      for (int i = 0; i < 8; ++i) { waitvm<WA - 1>(); wv[i] = __builtin_amdgcn_raw_buffer_load_b128(rw, (c4 * 128 + 32 * lw + g4) * 8192 + h * 256 + col * 16, i * 32768, WNT ? 2 : 0); }
      SB();
    }
    int eg[8];
#pragma unroll
    for (int u = 0; u < 8; ++u) eg[u] = WS_PART + il21(((4 * (u >> 1) + 2 * (g4 >> 1) + (u & 1)) * 4 + lw) * 65536 + sj * 256 + col * 16, g4 & 1);
    u32 pr_ = L_P + lane * 16;
    PIN(pr_);
    lds_wait(ctr + C_TAG, 1u);
    pcopy = (__builtin_amdgcn_readfirstlane(ctr[C_TLO]) & (NPR - 1)) * PART_BYTES;
#pragma unroll
    for (int u = 0; u < 8; ++u) { eg[u] += pcopy; PIN(eg[u]); }
    u32 ml = L_CTR + 4 * (C_MK + 7), one = 1u;
    PIN(ml); PIN(one);
    bf16x8 vf[4][4];
#define V_READ(i, slot) do { asm volatile("" ::: "memory"); SB(); _Pragma("unroll") for (int c = 0; c < 4; ++c) vf[i][c] = tr_frag(lds, va[2 * c] + (slot) * 4096, va[2 * c + 1] + (slot) * 4096); SB(); } while (0)
    SB();
    kv_load<WB, WB0>(ckv, vl, lw, (sj + (isS0 ? 384 : 192)) * 16384, (sj + (isS0 ? 576 : 384)) * 16384, lw * 1024 + 8192, 2, ns, nm, skip, t0 + E3_T - P_LEN);
    lds_wait(ctr + C_MK + 3, 4u);
    V_READ(0, 0); V_READ(1, 1); V_READ(2, 2); V_READ(3, 3);
    lds_add(lds, C_FREE, lane);
#pragma unroll
    for (int i = 0; i < 16; ++i) PIN(vf[i >> 2][i & 3]);
    STAMP(2);
    lds_wait(ctr + C_FREE, 8u);
    STAMP(5);
    kv_load<WC, WC0>(ckv, vl, lw, (sj + 6 * 192) * 16384, (sj + 7 * 192) * 16384, lw * 1024, ns, ns + 4, nm, skip, t0 + E3_T - P_LEN);
    int k4 = 4;
    STAMP(3);
    f32x16 acc[4];
#pragma unroll
    for (int c = 0; c < 4; ++c)
#pragma unroll
      for (int i = 0; i < 16; ++i) acc[c][i] = 0.f;
    bf16x8 pf[4], pn[4];
    u32 c_;
#define P_SPIN(g, n) c_ = spin_try(C_P + (g) - C_MK - 7, n, k4, ml, one KV_TSP)
#define TRY() kv_try(k4, ml, one KV_TSP)
#define P_READ(d, i0, i1, T0) _Pragma("unroll") for (int i = (i0); i < (i1); ++i) d[i] = *(volatile LAS bf16x8*)(lds + pr_ + ((T0) + i) * 1024)
#define PV(p, i) do { SB(); _Pragma("unroll") for (int c = 0; c < 4; ++c) acc[c] = __builtin_amdgcn_mfma_f32_32x32x16_bf16(p[i], vf[i][c], acc[c], 0, 0, 0); SB(); } while (0)
#define GX_USE(v) do { gx = __builtin_amdgcn_readfirstlane(v); _Pragma("unroll") for (int u = 0; u < 8; ++u) { eg[u] ^= gx; PIN(eg[u]); } } while (0)
#define MK_WAIT(T) spin_try((T) - 7, 4u, k4, ml, one KV_TSP)
#define PVV(p, i, d, slot)                                                                                               \
  _Pragma("unroll") for (int c = 0; c < 4; ++c) {                                                                        \
    SB();                                                                                                                \
    acc[c] = __builtin_amdgcn_mfma_f32_32x32x16_bf16(p[i], vf[i][c], acc[c], 0, 0, 0);                                   \
    SB();                                                                                                                \
    vf[d][c] = tr_frag(lds, va[2 * c] + (slot) * 4096, va[2 * c + 1] + (slot) * 4096);                                   \
    SB();                                                                                                                \
  }
#define CT(i) (*(volatile LAS u32*)(lds + ml + 4 * ((i) - C_MK - 7)))
    P_SPIN(0, 4u);
    P_READ(pf, 0, 4, 0);
    const u32 k7 = CT(C_MK + 7);
    asm volatile("" ::: "memory");
    STAMP(6);
    PVV(pf, 0, 0, 4); TRY();
    PVV(pf, 1, 1, 5); TRY();
    PVV(pf, 2, 2, 6);
    u32 cv = CT(C_P + 1);
    P_READ(pn, 0, 4, 4);
    PVV(pf, 3, 3, 0);
    if (RARE(__builtin_amdgcn_readfirstlane(k7) < 4u)) { MK_WAIT(7); V_READ(0, 4); V_READ(1, 5); V_READ(2, 6); V_READ(3, 0); }
    STAMP(9);
    c_ = __builtin_amdgcn_readfirstlane(cv);
    if (RARE((c_ & 255u) != 4u)) { P_SPIN(1, 4u); P_READ(pn, 0, 4, 4); }
    int slow = -1;
    if (RARE(c_ > 255u)) slow = 4;
    else {
      if (!isS0) PV(pn, 0);
      TRY();
      const u32 k9 = CT(C_MK + 9);
      asm volatile("" ::: "memory");
      PVV(pn, 1, 0, 1); TRY();
      PVV(pn, 2, 1, 2);
      cv = CT(C_P + 2);
      P_READ(pf, 0, 2, 8);
      const u32 k10 = CT(C_MK + 10);
      asm volatile("" ::: "memory");
      PVV(pn, 3, 2, 3);
      if (RARE(__builtin_amdgcn_readfirstlane(k9) < 4u)) { MK_WAIT(9); V_READ(0, 1); V_READ(1, 2); }
      STAMP(10);
      c_ = __builtin_amdgcn_readfirstlane(cv);
      if (RARE((c_ & 255u) != 4u)) { P_SPIN(2, 4u); P_READ(pf, 0, 2, 8); }
      if (RARE(c_ > 255u)) slow = 8;
      else {
        TRY();
        PV(pf, 0);
        const u32 g0 = ctr[C_GX];
        u32 g1 = ctr[C_PAR];
        cv = CT(C_P + 3);
        P_READ(pf, 2, 3, 8);
        PV(pf, 1);
        STAMP(4);
        if (RARE(__builtin_amdgcn_readfirstlane(k10) < 4u)) { MK_WAIT(10); V_READ(2, 3); }
        if (RARE(__builtin_amdgcn_readfirstlane(g0) == 0u)) { lds_spin(ctr + C_GX, 1u); g1 = ctr[C_PAR]; }
        GX_USE(g1);
        STAMP(11);
        c_ = __builtin_amdgcn_readfirstlane(cv);
        if (RARE((c_ & 255u) != 2u)) { c_ = spin_at(L_CTR + 4 * (C_P + 3), 2u); P_READ(pf, 2, 3, 8); }
        if (RARE(c_ > 255u)) slow = 10;
        else PV(pf, 2);
      }
    }
    if (RARE(slow >= 0)) {
      MK_WAIT(10);
      float R = ceilf(*(const float*)(lds + L_MX + (lane & 31) * 4));
#pragma unroll 1
      for (int T = slow; T < 11; ++T) {
        if (T == 4 && isS0) continue;
        spin_at(L_CTR + 4 * (C_P + (T < 8 ? 1 : T < 10 ? 2 : 3)), T < 10 ? 4u : 2u);
        const float rf = *(const float*)(lds + L_REF + T * 128 + (lane & 31) * 4);
        u32x4 p = *(const u32x4*)(lds + pr_ + T * 1024);
        const u32 so_ = (T < 7 ? T : T - 7) * 4096;
#pragma unroll
        for (int c = 0; c < 4; ++c) vf[0][c] = tr_frag(lds, va[2 * c] + so_, va[2 * c + 1] + so_);
        const float Rn = fmaxf(R, rf), ps = __builtin_amdgcn_exp2f(rf - Rn);
        *(float*)(lds + L_ALW + lw * 128 + (lane & 31) * 4) = __builtin_amdgcn_exp2f(R - Rn);
        R = Rn;
        WAVE_LDS_SYNC();
#pragma unroll
        for (int i = 0; i < 4; ++i) {
          const f32x4 al = *(const f32x4*)(lds + L_ALW + lw * 128 + (lane >> 5) * 16 + i * 32);
#pragma unroll
          for (int c = 0; c < 4; ++c)
#pragma unroll
            for (int r = 0; r < 4; ++r) acc[c][4 * i + r] *= al[r];
          p[i] = pk2bf(lo16(p[i]) * ps, hi16(p[i]) * ps);
        }
        WAVE_LDS_SYNC();
        pf[0] = __builtin_bit_cast(bf16x8, p);
        PV(pf, 0);
      }
      if (slow != 10) { lds_spin(ctr + C_GX, 1u); GX_USE(ctr[C_PAR]); }
    }
    STAMP(30);
#pragma unroll
    for (int u = 0; u < 8; ++u) {
      const auto r0 = __builtin_amdgcn_permlane16_swap(pk2bf(acc[0][2 * u], acc[1][2 * u]), pk2bf(acc[0][2 * u + 1], acc[1][2 * u + 1]), false, false);
      const auto r1 = __builtin_amdgcn_permlane16_swap(pk2bf(acc[2][2 * u], acc[3][2 * u]), pk2bf(acc[2][2 * u + 1], acc[3][2 * u + 1]), false, false);
      st128(rs, eg[u], u32x4{r0[0], r1[0], r0[1], r1[1]});
      SB();
    }
    STAMP(31);
    if (!isS0) {
#pragma unroll
      for (int i = 0; i < 8; ++i) *(u32x4*)(lds + lw * PS + 16384 + off_b(4 * i + g4, col)) = wv[i];
      lds_add(lds, C_WV, lane);
      T1_SETUP();
      if (EARLY & 1) mt = ld64(rs, mo);
    }
    waitvm<0>();
    __builtin_amdgcn_s_waitcnt(0x0F70);
    att_done(rs, lane, sj, gx);
    STAMP(13);
  } else if (!isQ) {
    if (isS0) {
      u32 ta = L_QOFF + 8192 + (8 * g4 + tq) * 128 + w * 32 + tp * 8;
      u32 sa = L_H1RED + w * 256;
      int so = col < 2 ? WS_SC + (((sj >> 5) * 4 + (sj & 3)) * 512 + ((sj >> 2) & 7) * 64 + 16 * w + 4 * g4 + 2 * col) * 8 : WS_TOTAL;
      PIN(ta); PIN(so); PIN(sa);
      u32 c2, e0, e1;
      asm volatile("s_waitcnt vmcnt(4) ; HEAD_WAIT\n v_mov_b32 %0, %3\n v_mov_b32 %1, %4\n v_mov_b32 %2, %5" : "=&v"(c2), "=&v"(e0), "=&v"(e1) : "v"(xm), "v"(ev.x), "v"(ev.y) : "memory");
      TAG_OF(e0, e1);
      bf16x8 sf[4];
      u32x2 wt[8];
      asm volatile("ds_write_b32 %4, %5\n"
                   "ds_read_b128 %0, %6\n ds_read_b128 %1, %6 offset:64\n ds_read_b128 %2, %6 offset:128\n ds_read_b128 %3, %6 offset:192\n s_waitcnt lgkmcnt(0)"
                   : "=&v"(sf[0]), "=&v"(sf[1]), "=&v"(sf[2]), "=&v"(sf[3])
                   : "v"(sa + lane * 4), "v"(pk2bf(silu_bf(lo16(c2)), silu_bf(hi16(c2)))), "v"(sa + 16 * g4) : "memory");
      waitvm<0>();
      lds_add(lds, C_BH, lane);
      lds_spin(ctr + C_BH, 5u);
      STAMP(1);
      asm volatile("ds_read_b64_tr_b16 %0, %8\n ds_read_b64_tr_b16 %1, %8 offset:512\n ds_read_b64_tr_b16 %2, %8 offset:4096\n ds_read_b64_tr_b16 %3, %8 offset:4608\n"
                   "ds_read_b64_tr_b16 %4, %8 offset:8192\n ds_read_b64_tr_b16 %5, %8 offset:8704\n ds_read_b64_tr_b16 %6, %8 offset:12288\n ds_read_b64_tr_b16 %7, %8 offset:12800\n"
                   "s_waitcnt lgkmcnt(0)"
                   : "=&v"(wt[0]), "=&v"(wt[1]), "=&v"(wt[2]), "=&v"(wt[3]), "=&v"(wt[4]), "=&v"(wt[5]), "=&v"(wt[6]), "=&v"(wt[7]) : "v"(ta) : "memory");
      f32x4 d = {0.f, 0.f, 0.f, 0.f};
#pragma unroll
      for (int k = 0; k < 4; ++k)
        d = __builtin_amdgcn_mfma_f32_16x16x32_bf16(__builtin_bit_cast(bf16x8, u32x4{wt[2 * k].x, wt[2 * k].y, wt[2 * k + 1].x, wt[2 * k + 1].y}), sf[k], d, 0, 0, 0);
      st_gran2(rs, so, (col & 1) ? d[2] : d[0], (col & 1) ? d[3] : d[1], tlo, thi);
      lds_add(lds, C_H1, lane);
      STAMP(2);
    }
    u32x2 e2;
    if (!isS0) e2 = ld64(rs, WS_EPOCH);
    if (w == 3 && !isS0) {
      POISON_COMMON(j);
      st128(rs, lane < 16 ? WS_PATTN + (j - 128) * 256 + lane * 16 : WS_TOTAL, P4);
    }
    const int pp = __builtin_amdgcn_readfirstlane(WS_PROBE + il((j * 4 + w) * 128, w & 1));
    if (w < 2) {
      st128(rs, lane < 16 ? (pp ^ ((lane & 8) * 1024)) + (lane & 7) * 16 : WS_TOTAL, Z4);
      u32 dm;
      asm volatile("s_load_dword %0, %1, 0x0\n s_waitcnt lgkmcnt(0)" : "=&s"(dm) : "s"(ws) : "memory");
    }
    u32 ab = off_b(col, 4 * g4), qv = WS_QABS + myh * 8192 + g4 * 1024 + w * 256 + ((col ^ (g4 << 2)) << 4);
    const int hb = 16 * (w & 1) + col;
    u32 pw = L_P + (g4 >> 1) * 512 + hb * 16 + (g4 & 1) * 8, mw = hb * 4;
    PIN(ab); PIN(qv); PIN(pw); PIN(mw);
    waitvm<0>();
    __builtin_amdgcn_s_waitcnt(0x0F70);
    STAMP(27);
    if (!isS0) TAG_OF(e2.x, e2.y);
    if (w == 3 && !isS0) START_FLAG();
    if (w < 2) {
      u64 ta, tb, tc;
      u32 dm;
      asm volatile("s_memtime %0\n s_waitcnt lgkmcnt(0)\n s_load_dword %3, %4, %5 glc\n s_waitcnt lgkmcnt(0)\n"
                   "s_memtime %1\n s_waitcnt lgkmcnt(0)\n s_load_dword %3, %4, %6 glc\n s_waitcnt lgkmcnt(0)\n"
                   "s_memtime %2\n s_waitcnt lgkmcnt(0)"
                   : "=&s"(ta), "=&s"(tb), "=&s"(tc), "=&s"(dm) : "s"(ws), "s"(pp), "s"(pp ^ 8192) : "memory");
      int df = min(max(((int)(u32)(tb - ta) - (int)(u32)(tc - tb)) >> 3, -VCLIP), VCLIP);
      df = ((w ^ myh) & 1) ? -df : df;
      u32 old = 0u;
      if (lane == 0) old = __hip_atomic_fetch_add((LAS u32*)(lds + L_CTR) + C_VOTE, (u32)(131072 + 64 + df), __ATOMIC_RELAXED, __HIP_MEMORY_SCOPE_WORKGROUP);
      old = __builtin_amdgcn_readfirstlane(old) + 131072 + 64 + df;
      if (old >= 262144u && lane < 8) st64(rs, WS_VOTE + lane * 2048 + sj * 8, (old & 1023u) | (tlo << 24), (tlo >> 8) | (thi << 24));
    }
    if (w == 2) { until(t0, QF_T); S_POLL(false, 16); lds_add(lds, C_SOK, lane); STAMP(3); }
    bf16x8 af[16], qf[16][2];
    u32 aa[16], qa[32];
#pragma unroll
    for (int i = 0; i < 16; ++i) {
      aa[i] = (ab ^ ((i & 3) << 4)) + (((w + (i >> 2)) & 3) * PS + w * 4096); PIN(aa[i]);
#pragma unroll
      for (int u = 0; u < 2; ++u) { qa[2 * i + u] = (ab ^ ((i & 3) << 4)) + (L_QI + ((w + (i >> 2)) & 3) * 8192 + ((u ^ w) & 1) * 4096); PIN(qa[2 * i + u]); }
    }
    const int sl1 = w == 3 ? 0 : 4 + w, sl2 = 1 + (w >> 1);
#define A_RD(i, o) af[i] = *(const bf16x8*)(lds + aa[i] + (o))
#define A_READ(i0, i1, o) _Pragma("unroll") for (int i = (i0); i < (i1); ++i) A_RD(i, o)
#define A_MOVE(i0, i1, d) _Pragma("unroll") for (int i = (i0); i < (i1); ++i) { aa[i] += (d) * 4096; PIN(aa[i]); }
#define Q_RD(i, u) qf[i][u] = *(const bf16x8*)(lds + qa[2 * (i) + (u)])
#define A_FIRST() do { A_READ(0, 16, 0); lds_add(lds, C_FREE, lane); _Pragma("unroll") for (int i = 0; i < 16; ++i) PIN(af[i]); STAMP(12); } while (0)
    bool have = false;
    _Pragma("unroll 1") while ((u32)__builtin_amdgcn_s_memrealtime() - t0 < E3_T) {
      if (__builtin_amdgcn_readfirstlane(ctr[C_MK + w]) >= 4u) { have = true; break; }
      __builtin_amdgcn_s_sleep(1);
    }
    asm volatile("" ::: "memory");
    if (have) A_FIRST();
    qv ^= (ctr[C_VOTE] & 1023u) > 128u ? 8192u : 0u;
    PIN(qv);
    lds_wait(ctr + C_SOK, 1u);
    until(t0, E3_T);
    pcopy = (tlo & (NPR - 1)) * PART_BYTES;
    STAMP(6);
#define Q_DMA() _Pragma("unroll") for (int i = 0; i < 8; ++i) dma_sc1(L_QI + w * 8192 + i * 1024, (qv ^ ((i & 3) << 4)) + (i >> 1) * 16384 + (i & 1) * 4096, ws)
    Q_DMA();
    if (RARE(!have)) { lds_wait(ctr + C_MK + w, 4u); A_FIRST(); }
    A_MOVE(0, 16, sl1 - w);
    u32 cb = L_CTR, cw = L_CTR + 4 * w, mx0 = mw + L_MX, mx1 = (mw ^ 64) + L_MX, pw0 = pw + w * 1024, pw1 = (pw ^ 256) + w * 1024;
    float ninf = -__builtin_inff();
    u32 one = 1u;
    PIN(ninf); PIN(one); PIN(cb); PIN(cw); PIN(mx0); PIN(mx1); PIN(pw0); PIN(pw1);
#define CTS(b, i) (*(volatile LAS u32*)(lds + (b) + 4 * (i)))
    _Pragma("unroll 1") for (u32 spins = 0; spins < SPIN_CAP; ++spins) {
      waitvm<0>();
#pragma unroll
      for (int i = 0; i < 8; ++i) Q_RD(i >> 1, i & 1);
      u32 mxu[2] = {0u, 0u};
#pragma unroll
      for (int i = 0; i < 8; ++i) { const u32x4 t_ = __builtin_bit_cast(u32x4, qf[i >> 1][i & 1]); mxu[i & 1] = max(max(mxu[i & 1], t_.x), t_.y); mxu[i & 1] = max(max(mxu[i & 1], t_.z), t_.w); }
      if (!RARE(__any(max(mxu[0], mxu[1]) == POISON))) break;
      STAMP(29);
      __builtin_amdgcn_s_sleep(Q_SLP);
      const u64 b0 = __ballot(mxu[w & 1] == POISON), b1 = __ballot(mxu[~w & 1] == POISON);
#pragma unroll 1
      for (int i = 0; i < 8; ++i)
        if (((i < 4 ? b0 : b1) >> (4 * (i & 3))) & 0x000F000F000F000Full) dma_sc1(L_QI + w * 8192 + i * 1024, (qv ^ ((i & 3) << 4)) + (i >> 1) * 16384 + (i & 1) * 4096, ws);
    }
    lds_add(lds, C_QRDY, lane);
    STAMP(7);
    f32x4 sc[2], sn[2];
    sc[0] = sc[1] = sn[0] = sn[1] = f32x4{0.f, 0.f, 0.f, 0.f};
#define QK(d, i0, i1, NU)                                                                                                \
  do {                                                                                                                   \
    SB();                                                                                                                \
    _Pragma("unroll") for (int i = (i0); i < (i1); ++i) _Pragma("unroll") for (int u = 0; u < (NU); ++u)                 \
      d[u] = __builtin_amdgcn_mfma_f32_16x16x32_bf16(af[i], qf[i][u], d[u], 0, 0, 0);                                    \
    SB();                                                                                                                \
  } while (0)
#define MF(d, i, u) do { SB(); d[u] = __builtin_amdgcn_mfma_f32_16x16x32_bf16(af[i], qf[i][u], d[u], 0, 0, 0); SB(); } while (0)
    const bool idle1 = isS0 && w == 0;
    u32 c1;
    {
      const u32 c0 = CTS(cb, C_QRDY);
      QK(sc, 0, 2, 2);
      if (RARE(__builtin_amdgcn_readfirstlane(c0) < 4u)) lds_spin(ctr + C_QRDY, 4u);
      asm volatile("" ::: "memory");
#pragma unroll
      for (int i = 2; i < 4; ++i) { MF(sc, i, 0); Q_RD(2 * i, 0); Q_RD(2 * i, 1); MF(sc, i, 1); Q_RD(2 * i + 1, 0); Q_RD(2 * i + 1, 1); }
#pragma unroll
      for (int i = 4; i < 12; ++i) { MF(sc, i, 0); Q_RD(i + 4, 0); MF(sc, i, 1); Q_RD(i + 4, 1); }
      c1 = CTS(cw, C_MK + 4);
      asm volatile("" ::: "memory");
#pragma unroll
      for (int i = 12; i < 16; ++i) { MF(sc, i, 0); A_RD(2 * i - 24, 0); MF(sc, i, 1); A_RD(2 * i - 23, 0); }
    }
    STAMP(9);
    CY(0);
    float mref[2], lsum[2] = {0.f, 0.f};
#define ADDC(i, v) do { SB(); asm volatile("s_mov_b64 exec, 1\n ds_add_u32 %0, %1 offset:%2\n s_mov_b64 exec, -1" ::"v"(cb), "v"(v), "n"(4 * (i)) : "memory"); SB(); } while (0)
#define SM(d, T, NU, G, P0, P1)                                                                                          \
  do {                                                                                                                   \
    SB();                                                                                                                \
    if (RARE(__any(max5(d[0], (NU) > 1 ? max5(d[1], ninf) : ninf) > TAU))) {                                             \
      sig = 257u;                                                                                                        \
      _Pragma("unroll") for (int u = 0; u < (NU); ++u) {                                                                 \
        const float up = ceilf(x32_max(x16_max(max5(d[u], 0.f))));                                                       \
        lsum[u] *= __builtin_amdgcn_exp2f(-up);                                                                          \
        mref[u] += up;                                                                                                   \
        d[u] -= up;                                                                                                      \
        *(float*)(lds + (mw ^ (u * 64)) + L_REF + (T) * 128) = mref[u];                                                  \
      }                                                                                                                  \
    }                                                                                                                    \
    u32x2 pk[2];                                                                                                         \
    _Pragma("unroll") for (int u = 0; u < (NU); ++u) {                                                                   \
      pk[u].x = pk2bf(__builtin_amdgcn_exp2f(d[u].x), __builtin_amdgcn_exp2f(d[u].y));                                   \
      pk[u].y = pk2bf(__builtin_amdgcn_exp2f(d[u].z), __builtin_amdgcn_exp2f(d[u].w));                                   \
      *(u32x2*)(lds + (u ? (P1) : (P0))) = pk[u];                                                                        \
    }                                                                                                                    \
    ADDC(C_P + (G), sig);                                                                                                \
    _Pragma("unroll") for (int u = 0; u < (NU); ++u) { lsum[u] = dot2(pk[u].x, 0x3F803F80u, dot2(pk[u].y, 0x3F803F80u, lsum[u])); PIN(lsum[u]); } \
    SB();                                                                                                                \
  } while (0)
#define REF_WRITE(T, NU) _Pragma("unroll") for (int u = 0; u < (NU); ++u) *(float*)(lds + (mw ^ (u * 64)) + L_REF + (T) * 128) = mref[u]
    u32 sig = one;
    {
      MF(sn, 0, 0); A_RD(8, 0); MF(sn, 0, 1);
      const float m0 = max5(sc[0], ninf), m1 = max5(sc[1], ninf);
      MF(sn, 1, 0);
      asm volatile("ds_max_f32 %0, %2\n ds_max_f32 %1, %3" ::"v"(mx0), "v"(mx1), "v"(m0), "v"(m1) : "memory");
      ADDC(C_MXC, one);
      MF(sn, 1, 1); A_RD(9, 0);
#pragma unroll
      for (int i = 2; i < 4; ++i) { MF(sn, i, 0); A_RD(2 * i + 6, 0); MF(sn, i, 1); A_RD(2 * i + 7, 0); }
      MF(sn, 4, 0);
      const u32 c0 = CTS(cb, C_MXC);
      mref[0] = *(volatile LAS float*)(lds + mx0);
      mref[1] = *(volatile LAS float*)(lds + mx1);
      MF(sn, 4, 1); MF(sn, 5, 0); A_RD(14, 0); MF(sn, 5, 1); A_RD(15, 0); MF(sn, 6, 0); MF(sn, 6, 1);
      if (RARE(__builtin_amdgcn_readfirstlane(c0) < 4u)) {
        lds_spin(ctr + C_MXC, 4u);
        mref[0] = *(volatile LAS float*)(lds + mx0);
        mref[1] = *(volatile LAS float*)(lds + mx1);
      }
      u32x2 pk[2];
#pragma unroll
      for (int u = 0; u < 2; ++u) {
        MF(sn, 7 + 2 * u, 0);
        mref[u] = ceilf(mref[u]);
        float e0 = __builtin_amdgcn_exp2f(sc[u].x - mref[u]); PIN(e0);
        MF(sn, 7 + 2 * u, 1);
        pk[u].x = pk2bf(e0, __builtin_amdgcn_exp2f(sc[u].y - mref[u])); PIN(pk[u].x);
        MF(sn, 8 + 2 * u, 0);
        e0 = __builtin_amdgcn_exp2f(sc[u].z - mref[u]); PIN(e0);
        MF(sn, 8 + 2 * u, 1);
        pk[u].y = pk2bf(e0, __builtin_amdgcn_exp2f(sc[u].w - mref[u]));
        *(u32x2*)(lds + (u ? pw1 : pw0)) = pk[u];
      }
      ADDC(C_P, one);
      MF(sn, 11, 0);
      lsum[0] = dot2(pk[0].x, 0x3F803F80u, dot2(pk[0].y, 0x3F803F80u, 0.f)); PIN(lsum[0]);
      MF(sn, 11, 1);
      lsum[1] = dot2(pk[1].x, 0x3F803F80u, dot2(pk[1].y, 0x3F803F80u, 0.f)); PIN(lsum[1]);
      MF(sn, 12, 0);
      REF_WRITE(4 + w, 2);
      MF(sn, 12, 1);
      QK(sn, 13, 16, 2);
    }
    STAMP(10);
    CY(1);
#define QK1(N0, slot)                                                                                                    \
  do {                                                                                                                   \
    _Pragma("unroll") for (int i = 0; i < 16; ++i) { MF(sc, i, 0); if (i + (N0) < 16) A_RD(i + (N0), slot); }            \
  } while (0)
    if (RARE(__builtin_amdgcn_readfirstlane(c1) < 4u && !idle1)) {
      lds_spin(ctr + C_MK + 4 + w, 4u);
      A_READ(0, 16, 0);
      sn[0] = sn[1] = f32x4{0.f, 0.f, 0.f, 0.f};
      QK(sn, 0, 16, 2);
    }
    if (RARE(idle1)) lds_add(lds, C_P + 1, lane);
    else { sn[0] -= mref[0]; sn[1] -= mref[1]; SM(sn, 4 + w, 2, 1, pw0 + 4096, pw1 + 4096); }
    CY(2);
    if (RARE(w >= 2)) {
      RELANE();
      const u32 o_ = (w == 2 ? WS_SFLAG : WS_VOTE) + xc * 2048 + lane * 16;
      dma_sc1(L_QI + (w - 2) * 16384, o_, ws);
      dma_sc1(L_QI + (w - 2) * 16384 + 1024, o_ + 1024, ws);
    }
    REF_WRITE(7 + sl2, 1);
    A_MOVE(0, 16, sl2 - sl1);
    pw0 = pw + sl2 * 1024;
    sc[0] = f32x4{-mref[0], -mref[0], -mref[0], -mref[0]};
    PIN(pw0); PIN(sc[0]);
    CY(3);
    lds_spin(ctr + C_MK + 7 + sl2, 4u);
    CY(4);
    A_READ(0, 6, 0);
    STAMP(4);
    QK1(6, 0);
    CY(5);
    SM(sc, 7 + sl2, 1, 2, pw0 + 7168, 0);
    CY(6);
    STAMP(11);
    if (!RARE(w >= 2)) {
      REF_WRITE(10, 1);
      sc[0] = f32x4{-mref[0], -mref[0], -mref[0], -mref[0]};
      PIN(sc[0]);
      lds_spin(ctr + C_MK + 10, 4u);
      STAMP(5);
      A_READ(0, 6, 8192);
      QK1(6, 8192);
      SM(sc, 10, 1, 3, pw0 + 9216, 0);
    } else {
      RELANE();
      waitvm<0>();
      if (w == 2) S_POLL(true, 48);
      else { COUNT_VOTES(true); lds_add(lds, C_GX, lane); }
    }
    STAMP(30);
    CYOUT();
    RELANE();
    {
#pragma unroll
      for (int u = 0; u < 2; ++u) {
        *(float*)(lds + L_ML + (mw ^ (u * 64)) * 4 + w * 4) = mref[u];
        *(float*)(lds + L_ML + 512 + (mw ^ (u * 64)) * 4 + w * 4) = x32_sum(x16_sum(lsum[u]));
      }
      u32 old;
      f32x4 m4, l4;
      asm volatile("s_mov_b64 exec, 1\n ds_add_rtn_u32 %0, %3, %4 offset:%6\n s_mov_b64 exec, -1\n ds_read_b128 %1, %5\n ds_read_b128 %2, %5 offset:512\n s_waitcnt lgkmcnt(0)"
                   : "=&v"(old), "=&v"(m4), "=&v"(l4) : "v"((u32)L_CTR), "v"(1u), "v"(L_ML + (lane & 31) * 16), "n"(4 * C_SD) : "memory");
      if (__builtin_amdgcn_readfirstlane(old) == 3u) {
        const float M = fmaxf(fmaxf(m4.x, m4.y), fmaxf(m4.z, m4.w));
        const float l = (l4.x * __builtin_amdgcn_exp2f(m4.x - M) + l4.y * __builtin_amdgcn_exp2f(m4.y - M)) + (l4.z * __builtin_amdgcn_exp2f(m4.z - M) + l4.w * __builtin_amdgcn_exp2f(m4.w - M));
        *(u32x2*)(lds + L_WMAX + (lane & 1) * 128 + (lane & 30) * 4) = u32x2{fbits(M), fbits(l)};
        att_done(rs, lane, sj, ctr[C_PAR]);
      }
    }
    lds_wait(ctr + C_GX, 1u);
    gx = __builtin_amdgcn_readfirstlane(ctr[C_PAR]);
    if (!isS0) T1_SETUP();
    STAMP(13);
  } else {
    const int mu = 2 * j + (w >> 2);
#define Q_LOAD(i0, i1)                                                                                                   \
  _Pragma("unroll") for (int i = (i0); i < (i1); ++i) {                                                                  \
    waitvm<QW - 1>();                                                                                                    \
    dma<0, false>(w * 16384 + (i & 15) * 1024, (vq ^ ((i & 3) << 4)) + ((i < 8 ? 64 * w + 32 * dh : i < 16 ? 64 * w + 32 - 32 * dh : 256 * dh + 32 * w) + 4 * (i & 7)) * 8192, i < 16 ? w_q : w_uk); \
  }
    const u32 vq = g4 * 8192 + hq * 256 + ((col ^ (g4 << 2)) << 4);
    u32x4 xv;
    u32x2 e2;
    u32 x2_;
    asm volatile("global_load_dwordx4 %0, %3, %5\n global_load_dword %1, %4, %5\n buffer_load_dwordx2 %2, off, %6, 0 sc1"
                 : "=&v"(xv), "=&v"(x2_), "=&v"(e2) : "v"(lane * 16), "v"(128 * w + 4 * (lane & 31)), "s"(x), "s"(rs) : "memory");
    bf16x8 wf[16], wk[8];
    u32 ta0 = w * 16384 + off_b(8 * g4 + tq, tp >> 1) + 8 * (tp & 1), ta1 = w * 16384 + off_b(8 * g4 + 4 + tq, tp >> 1) + 8 * (tp & 1);
    int so = WS_SC + ((lane >> 5) * 2048 + 64 * w + 2 * (lane & 31)) * 8;
    PIN(ta0); PIN(ta1); PIN(so);
    Q_LOAD(4, 8 + QW);
    waitvm<QW>();
#pragma unroll
    for (int c = 0; c < 8; ++c) wf[c] = tr_frag(lds, ta0 ^ (c << 5), ta1 ^ (c << 5));
    Q_LOAD(8 + QW, 16);
#pragma unroll
    for (int c = 0; c < 8; ++c) PIN(wf[c]);
    Q_LOAD(16, 24);
    STAMP(12);
    if (E1_BAR) BAR0();
    until(t0, E1_T);
    STAMP(23);
    u32x4 g[4];
    asm volatile("buffer_load_dwordx4 %0, %4, %5, 0 offen sc1\n buffer_load_dwordx4 %1, %4, %5, %6 offen sc1\n"
                 "buffer_load_dwordx4 %2, %4, %5, %7 offen sc1\n buffer_load_dwordx4 %3, %4, %5, %8 offen sc1"
                 : "=&v"(g[0]), "=&v"(g[1]), "=&v"(g[2]), "=&v"(g[3]) : "v"(so), "s"(rs), "s"(4096), "s"(8192), "s"(12288) : "memory");
    asm volatile("s_waitcnt vmcnt(12)" : "+v"(xv), "+v"(x2_), "+v"(e2)::"memory");
#pragma unroll
    for (int c = 0; c < 8; ++c) wf[8 + c] = tr_frag(lds, (ta0 ^ (c << 5)) + 8192, (ta1 ^ (c << 5)) + 8192);
    TAG_OF(e2.x, e2.y);
    float ln0, ln1;
    {
      float xe[8] = {lo16(xv.x), hi16(xv.x), lo16(xv.y), hi16(xv.y), lo16(xv.z), hi16(xv.z), lo16(xv.w), hi16(xv.w)};
      float s = 0.f;
#pragma unroll
      for (int k = 0; k < 8; ++k) s += xe[k];
      const float mean = wave_sum(s) * (1.f / 512.f);
      float d2 = 0.f;
#pragma unroll
      for (int k = 0; k < 8; ++k) d2 += (xe[k] - mean) * (xe[k] - mean);
      const float var = rbf(wave_sum(d2) * (1.f / 512.f));
      const float mb = rbf(mean);
      const float sd = rbf(sqrtf(rbf(var + 1e-5f)));
      ln0 = rbf(rbf(lo16(x2_) - mb) / sd);
      ln1 = rbf(rbf(hi16(x2_) - mb) / sd);
    }
    PIN(ln0); PIN(ln1);
    int qpo = (lane & 7) == 0 ? WS_QABS + il(hq * 1024 + dh * 512 + w * 64 + (lane >> 5) * 32 + (lane & 8) * 2, g4 & 1) : WS_TOTAL;
    u32 hw_ = L_H1RED + w * 128 + (lane & 31) * 4, hr_ = L_H1RED + w * 128 + 16 * g4 + 64 * dh, qw_ = L_H1RED + 1024 + w * 272 + lane * 2, qr_ = L_H1RED + 1024 + col * 272 + 64 * g4;
    PIN(qpo); PIN(hw_); PIN(hr_); PIN(qw_); PIN(qr_);
    waitvm<4>();
#pragma unroll
    for (int i = 0; i < 8; ++i) wk[i] = *(const bf16x8*)(lds + w * 16384 + (i >> 2) * 4096 + off_b(col, 4 * g4 + (i & 3)));
#pragma unroll
    for (int c = 0; c < 8; ++c) { PIN(wf[8 + c]); PIN(wk[c]); }
    STAMP(4);
    asm volatile("s_waitcnt vmcnt(0)" : "+v"(g[0]), "+v"(g[1]), "+v"(g[2]), "+v"(g[3])::"memory");
    _Pragma("unroll 1") for (u32 spins = 0; spins < SPIN_CAP; ++spins) {
      bool ok = true;
#pragma unroll
      for (int k = 0; k < 4; ++k) ok = ok & (gran_ok(g[k].x, g[k].y, tlo, thi) & gran_ok(g[k].z, g[k].w, tlo, thi));
      if (!RARE(!__all(ok))) break;
      STAMP(29);
      __builtin_amdgcn_s_sleep(1);
      asm volatile("" ::: "memory");
#pragma unroll
      for (int k = 0; k < 4; ++k) g[k] = ld128(rs, so + k * 4096);
    }
    STAMP(1);
    {
      const float m0 = rbf((gran_val(g[0].x) + gran_val(g[1].x)) + (gran_val(g[2].x) + gran_val(g[3].x)));
      const float m1 = rbf((gran_val(g[0].z) + gran_val(g[1].z)) + (gran_val(g[2].z) + gran_val(g[3].z)));
      const auto s0 = __builtin_amdgcn_permlane32_swap(__float_as_uint(m0), __float_as_uint(m0), false, false);
      const auto s1 = __builtin_amdgcn_permlane32_swap(__float_as_uint(m1), __float_as_uint(m1), false, false);
      *(u32*)(lds + hw_) = pk2bf(rbf(ln0 * rbf(1.f + __uint_as_float(s0[1]))) + __uint_as_float(s0[0]),
                                 rbf(ln1 * rbf(1.f + __uint_as_float(s1[1]))) + __uint_as_float(s1[0]));
    }
    WAVE_LDS_SYNC();
    {
      const bf16x8 h0 = *(const bf16x8*)(lds + hr_), h1 = *(const bf16x8*)(lds + (hr_ ^ 64));
      f32x4 d[8];
#pragma unroll
      for (int c = 0; c < 8; ++c) d[c] = __builtin_amdgcn_mfma_f32_16x16x32_bf16(h0, wf[c], f32x4{0.f, 0.f, 0.f, 0.f}, 0, 0, 0);
#pragma unroll
      for (int c = 0; c < 8; ++c) d[c] = __builtin_amdgcn_mfma_f32_16x16x32_bf16(h1, wf[8 + c], d[c], 0, 0, 0);
#pragma unroll
      for (int k = 0; k < 2; ++k) {
        const float v = (g4 & 2) ? ((g4 & 1) ? d[4 * k + 3][0] : d[4 * k + 2][0]) : ((g4 & 1) ? d[4 * k + 1][0] : d[4 * k][0]);
        const u32 vh = f2bf(v);
        *(u16*)(lds + qw_ + 128 * k) = (u16)vh;
        *(u16*)(lds + qw_ + 128 * k + 8 * 272) = (u16)f2bf(v - bf2f(vh));
      }
    }
    STAMP(2);
    BAR();
    {
      bf16x8 a[4];
#pragma unroll
      for (int s = 0; s < 4; ++s) a[s] = *(const bf16x8*)(lds + qr_ + 16 * s);
      f32x4 d[2] = {f32x4{0.f, 0.f, 0.f, 0.f}, f32x4{0.f, 0.f, 0.f, 0.f}};
#pragma unroll
      for (int s = 0; s < 4; ++s)
#pragma unroll
        for (int c = 0; c < 2; ++c) d[c] = __builtin_amdgcn_mfma_f32_16x16x32_bf16(a[s], wk[4 * c + s], d[c], 0, 0, 0);
      constexpr float SCALE = 0.08838834764831845f * 1.4426950408889634f;
      const float v = SCALE * x16_sum(rs32(sum4(d[0]), sum4(d[1])));
      const float p0 = __uint_as_float(nopois(pk2bf(dppf<0x00>(v), dppf<0x55>(v)))), p1 = __uint_as_float(nopois(pk2bf(dppf<0xAA>(v), dppf<0xFF>(v))));
      st128(rs, qpo, u32x4{__float_as_uint(p0), __float_as_uint(p1), __float_as_uint(dppf<0x104>(p0)), __float_as_uint(dppf<0x104>(p1))});
    }
    STAMP(5);
    SB();
    until(t0, MB_T);
    {
      const int r0 = 128 * (mu & 3) + 32 * (w & 3) + 4 * (lane >> 3);
      const u32x2 cvb = *(const u32x2*)(cvec + r0);
      u32x4 wsc[4];
#pragma unroll
      for (int i = 0; i < 4; ++i) wsc[i] = ldnt(w_mod + (size_t)(r0 + i) * 3072 + 1024 + (mu >> 2) * 64 + (lane & 7) * 8);
      float a[8];
#pragma unroll
      for (int k = 0; k < 8; ++k) a[k] = 0.f;
#pragma unroll
      for (int i = 0; i < 4; ++i) fma8b(a, f2bf(silu_bf((i & 1) ? hi16(i < 2 ? cvb.x : cvb.y) : lo16(i < 2 ? cvb.x : cvb.y))), wsc[i]);
      float r[4];
#pragma unroll
      for (int k = 0; k < 4; ++k) r[k] = rs32(a[k], a[k + 4]);
      const float o0 = b3_sum(rs16(r[0], r[2])), o1 = b3_sum(rs16(r[1], r[3]));
      float* red = (float*)(lds + L_H1RED + 8192);
      if ((lane & 8) == 0) *(f32x2*)(red + w * 64 + (lane & 7) * 8 + (lane >> 5) * 4 + ((lane >> 4) & 1) * 2) = f32x2{o0, o1};
      BAR();
      if ((w & 3) == 3 && lane < 16) {
        red += (w >> 2) * 256 + lane * 4;
        const f32x4 sm = (*(const f32x4*)(red) + *(const f32x4*)(red + 64)) + (*(const f32x4*)(red + 128) + *(const f32x4*)(red + 192));
        st128(rs, WS_MODB + ((mu & 3) * 2048 + (mu >> 2) * 64 + lane * 4) * 4, u32x4{fbits(sm.x), fbits(sm.y), fbits(sm.z), fbits(sm.w)});
      }
    }
    gx = pcopy = 0;
  }

  RELANE();
  const int r16 = lane >> 2, ch = lane & 3;
  if (ii >= 16) {
    lds_wait(ctr + C_WV, 4u);
    if (w >= 4 || !(EARLY & 2)) lds_wait(ctr + C_PD, 6u);
    STAMP(23);
    until(t0, E4_T);
    u32 done = 0u;
    _Pragma("unroll 1") for (u32 spins = 0; spins < SPIN_CAP; ++spins) {
      asm volatile("" ::: "memory");
      if ((spins || w < 4 || !(EARLY & 1)) && max(mt.x, mt.y) == POISON) mt = ld64(rs, mo);
      const u32 ok = ~(u32)__ballot(max(mt.x, mt.y) == POISON);
      u32 nw = ok & (ok >> 1);
      nw = nw & (nw >> 2) & 0x111111u & ~done;
      done |= nw;
      _Pragma("unroll 1") while (nw) {
        const int k = __builtin_ctz(nw);
        nw &= nw - 1;
        dma_sc1(pb + k * 256, (pv ^ ((k & 12) << 2)) + ((WS_PART + pcopy + il21(((h >> 1) * 4 + c4) * 65536 + (24 * w + k) * 256, h & 1)) ^ gx), ws);
      }
      if (spins == 0) STAMP(21);
      if (done == 0x111111u) break;
      const int miss = __popc(~ok & 0xFFFFFFu);
      if (miss > 8) __builtin_amdgcn_s_sleep(E4_S2);
      else if (miss > 2) __builtin_amdgcn_s_sleep(E4_S1);
      else __builtin_amdgcn_s_sleep(E4_S0);
    }
    STAMP(16);
    bf16x8 wk[4];
    {
      const u32 eo = L_QOFF + 4416 + w * 64;
      const float m = lane < 24 ? __uint_as_float(mt.x) : -__builtin_inff();
      const float M = wave_max(m);
      const float e = __builtin_amdgcn_exp2f(m - M);
      if (lane < 32) *(u16*)(lds + eo + lane * 2) = (u16)f2bf(e);
      WAVE_LDS_SYNC();
      const u32x4 ev4 = *(const u32x4*)(lds + eo + 16 * g4);
      const float L = wave_sum(e * __uint_as_float(mt.y));
      if (lane == 0) *(f32x2*)(lds + L_QOFF + 4352 + w * 8) = f32x2{M, L};
      bf16x8 wf[8], pt[8];
#pragma unroll
      for (int i = 0; i < 8; ++i) { wf[i] = __builtin_bit_cast(bf16x8, (col & 7) == i ? ev4 : Z4); PIN(wf[i]); }
#pragma unroll
      for (int s = 0; s < 4; ++s) { wk[s] = tr_frag(lds, (wa ^ (s << 4)) + s * 1024, (wa ^ (s << 4)) + s * 1024 + 4096); PIN(wk[s]); }
      waitvm<0>();
      STAMP(17);
#pragma unroll
      for (int i = 0; i < 8; ++i) pt[i] = tr_frag(lds, pa[2 * i], pa[2 * i + 1]);
      SB();
      f32x4 d = {0.f, 0.f, 0.f, 0.f};
#pragma unroll
      for (int i = 0; i < 8; ++i) d = __builtin_amdgcn_mfma_f32_16x16x32_bf16(pt[i], wf[i], d, 0, 0, 0);
      const u32 h0 = pk2bf(d[0], d[1]), h1 = pk2bf(d[2], d[3]);
      const u32 l0 = pk2bf(d[0] - lo16(h0), d[1] - hi16(h0)), l1 = pk2bf(d[2] - lo16(h1), d[3] - hi16(h1));
      *(u32x2*)(lds + ro) = (col & 8) ? u32x2{l0, l1} : u32x2{h0, h1};
    }
    BAR();
    STAMP(14);
    const f32x2 ml = *(const f32x2*)(lds + mlo);
    bf16x8 lf[4];
#pragma unroll
    for (int s = 0; s < 4; ++s) lf[s] = *(const bf16x8*)(lds + bo + 64 * s);
    SB();
    f32x4 d = {0.f, 0.f, 0.f, 0.f};
#pragma unroll
    for (int s = 0; s < 4; ++s) d = __builtin_amdgcn_mfma_f32_16x16x32_bf16(wk[s], lf[s], d, 0, 0, 0);
    SB();
    float Mg = fmaxf(ml.x, dppf<0xB1>(ml.x));
    Mg = fmaxf(Mg, dppf<0x4E>(Mg)); Mg = fmaxf(Mg, dppf<0x141>(Mg));
    const float e = __builtin_amdgcn_exp2f(ml.x - Mg);
    float Lg = quad_sum(e * ml.y);
    Lg += dppf<0x141>(Lg);
    const float sc = e * __builtin_amdgcn_rcpf(Lg);
    {
      float d0 = d[0] * sc, d1 = d[1] * sc, d2 = d[2] * sc, d3 = d[3] * sc;
      row16_sum4(d0, d1, d2, d3);
      d = f32x4{d0, d1, d2, d3};
    }
    const u32 h0 = pk2bf(d[0], d[1]), h1 = pk2bf(d[2], d[3]);
    const u32 l0 = pk2bf(d[0] - lo16(h0), d[1] - hi16(h0)), l1 = pk2bf(d[2] - lo16(h1), d[3] - hi16(h1));
    st64(rs, so1, nopois(col ? l0 : h0), nopois(col ? l1 : h1));
    STAMP(15);
    STAMP_FLUSH();
    return;
  }
  if (tl2) {
    BAR0();
    until(t0, WO_T);
#pragma unroll
    for (int i = 0; i < 8; ++i) dma<0, WNT>(owno + i * 1024, (ks * 512 + 64 * w + 8 * i + (lane >> 3)) * 1024 + cg * 128 + (lane & 7) * 16, w_o);
    waitvm<0>();
    STAMP(26);
    bf16x8 wf[8];
#pragma unroll
    for (int i = 0; i < 8; ++i) {
      const u32 a = owno + (32 * (i >> 2) + 8 * g4 + tq) * 128 + (i & 3) * 32 + tp * 8;
      wf[i] = tr_frag(lds, a, a + 512);
    }
#pragma unroll
    for (int i = 0; i < 8; ++i) PIN(wf[i]);
    STAMP(23);
    int off[1] = {(WS_PCTX + (ks * 4 + (w >> 1)) * 2048 + tq * 512 + (w & 1) * 256 + g4 * 64 + tp * 16) ^ gx};
    u32 rw = L_RED + lane * 32 + w * 4;
    PIN(rw);
    int so2 = tp < 2 ? WS_PATTN + il((ks * 512 + cg * 64 + (lane & 60)) * 4, tp) : WS_TOTAL;
    PIN(so2);
    u32x4 g[1];
    gather128<1, 4>(rs, off, g);
    STAMP(24);
    const bf16x8 a0 = __builtin_bit_cast(bf16x8, (col & 2) ? Z4 : g[0]), a1 = __builtin_bit_cast(bf16x8, (col & 2) ? g[0] : Z4);
    f32x4 d[4];
#pragma unroll
    for (int c = 0; c < 4; ++c) d[c] = __builtin_amdgcn_mfma_f32_16x16x32_bf16(a0, wf[c], f32x4{0.f, 0.f, 0.f, 0.f}, 0, 0, 0);
#pragma unroll
    for (int c = 0; c < 4; ++c) d[c] = __builtin_amdgcn_mfma_f32_16x16x32_bf16(a1, wf[4 + c], d[c], 0, 0, 0);
    *(float*)(lds + rw) = rs16(rs32(sum4(d[0]), sum4(d[2])), rs32(sum4(d[1]), sum4(d[3])));
    BAR();
    if (w == 0) {
      const float v = sum4(*(const f32x4*)(lds + L_RED + lane * 32) + *(const f32x4*)(lds + L_RED + lane * 32 + 16));
      const float vn = __uint_as_float(fbits(v));
      st128(rs, so2, u32x4{__float_as_uint(dppf<0x00>(vn)), __float_as_uint(dppf<0x55>(vn)), __float_as_uint(dppf<0xAA>(vn)), __float_as_uint(dppf<0xFF>(vn))});
    }
    STAMP(17);
  } else if (t3 >= 0) {
    STAMP(20);
    if (w < 4) {
      const int fr = ((128 * w + r16) * DF + t3 * 32) * 2, ca_ = ch ^ ((r16 >> 2) & 2) ^ (t3 & 1);
      const u16* wa = (t3 & 1) ? w_up : w_gate;
      const u16* wb = (t3 & 1) ? w_gate : w_up;
      const u32 d = w * 32768 + 8192;
      until(t0, GU_T);
#pragma unroll
      for (int i = 0; i < 8; ++i) { waitvm<WING - 1>(); dma<0, GUNT>(d + (i >> 2) * 12288 + (t3 & 1) * 4096 + (i & 3) * 1024, fr + ca_ * 16 + 32 * i * DF, wa); }
#pragma unroll
      for (int i = 0; i < 8; ++i) { waitvm<WING - 1>(); dma<0, GUNT>(d + (i >> 2) * 12288 + 4096 - (t3 & 1) * 4096 + (i & 3) * 1024, fr + (ca_ ^ 1) * 16 + 32 * i * DF, wb); }
    } else {
      until(t0, DN_T);
#pragma unroll
      for (int i = 0; i < 8; ++i) {
        waitvm<WING - 1>();
        dma<0, WNT>(lw * 32768 + 16384 + (i >> 2) * 12288 + (i & 3) * 1024, (32 * t3 + 8 * (i & 3) + (lane >> 3)) * 1024 + 256 * lw + 128 * (i >> 2) + (lane & 7) * 16, w_down);
      }
      const u32 x2 = *(const u32*)(x + 128 * lw + 2 * lane);
      u32x4 m6[6];
      int mo[6];
#pragma unroll
      for (int i = 0; i < 6; ++i) mo[i] = MB_OFF(i);
      until(t0, MB_T + 200);
      gather128<6, 8>(rs, mo, m6);
#pragma unroll
      for (int i = 0; i < 6; ++i) {
        const f32x4 f = __builtin_bit_cast(f32x4, m6[i]);
        const float v = rbf(rs16(rs32(f.x, f.z), rs32(f.y, f.w)));
        *(u16*)(lds + L_Z + (i < 2 ? 5 : i < 4 ? 3 : 2) * 1088 + (128 * lw + 64 * (i & 1) + 4 * col + g4) * 2) = (u16)f2bf(i < 4 ? v : 1.f + v);
      }
      *(u32*)(lds + L_Z + 6 * 1088 + lw * 256 + lane * 4) = x2;
      if (w == 4 && lane < 32) *(u32*)(lds + L_ZERO + lane * 4) = lane < 16 ? 0u : POISON;
      if (w == 5) S_POLL(false, 48);
      if (w == 6) COUNT_VOTES(false);
    }
    waitvm<0>();
    STAMP(26);
    BAR();
    gx = __builtin_amdgcn_readfirstlane(ctr[C_PAR]);
    u32 zw = L_Z + (64 * w + 4 * col + g4) * 2;
    u32 hr[4];
#pragma unroll
    for (int i = 0; i < 4; ++i) { hr[i] = (col == i ? L_HH : L_ZERO) + 16 * g4; PIN(hr[i]); }
    int off[2];
#pragma unroll
    for (int i = 0; i < 2; ++i) { off[i] = (WS_PATTN + il(((4 * i + g4) * 512 + 64 * w + col * 4) * 4, myh)) ^ gx; PIN(off[i]); }
    int so = col < 4 ? (WS_PDOWN + il((t3 * 256 + 64 * (w & 3) + 16 * col + 4 * g4) * 4, w >> 2)) ^ gx : WS_TOTAL;
    u32 zr = L_Z + tp * 1088 + 16 * g4, yr = L_Z + 4 * 1088 + lane * 16, hw = L_HH + (w & 3) * 16 + (g4 & 1) * 8 + tp * 2, cr = L_CF + (w & 3) * 16 + tp * 4;
    PIN(cr); PIN(so); PIN(zw); PIN(zr); PIN(yr); PIN(hw);
    float ga = bf2f(*(const u16*)(lds + zw + 5 * 1088)), cb1 = bf2f(*(const u16*)(lds + zw + 2 * 1088)), xe = bf2f(*(const u16*)(lds + zw + 6 * 1088));
    bf16x8 wf[16], wd[4];
    if (w < 4) {
#pragma unroll
      for (int k = 0; k < 16; ++k) {
        const u32 a = (k >> 2) * 32768 + 8192 + ((k >> 1) & 1) * 12288 + (tp >> 1) * 4096 + ((k & 1) * 32 + 8 * g4 + tq) * 64 + (w ^ ((g4 & 1) * 2) ^ (tp >> 1)) * 16 + (tp & 1) * 8;
        wf[k] = tr_frag(lds, a, a + 256);
      }
#pragma unroll
      for (int k = 0; k < 16; ++k) PIN(wf[k]);
    }
#pragma unroll
    for (int i = 0; i < 4; ++i) {
      const u32 a = owno + 8192 + (8 * g4 + tq) * 128 + i * 32 + tp * 8;
      wd[i] = tr_frag(lds, a, a + 512);
    }
#pragma unroll
    for (int i = 0; i < 4; ++i) PIN(wd[i]);
    PIN(ga); PIN(cb1); PIN(xe);
    STAMP(23);
    until(t0, E6_T);
    u32x4 pa2[2];
    gather128<2, 4>(rs, off, pa2);
    STAMP(18);
    {
      const f32x4 s4 = __builtin_bit_cast(f32x4, pa2[0]) + __builtin_bit_cast(f32x4, pa2[1]);
      const u32 yb = f2bf(xe + rbf(ga * rbf(rs16(rs32(s4.x, s4.z), rs32(s4.y, s4.w)))));
      const float z = bf2f(yb) * cb1;
      const u32 zh = f2bf(z);
      *(u16*)(lds + zw + 4 * 1088) = (u16)yb;
      *(u16*)(lds + zw) = (u16)zh;
      *(u16*)(lds + zw + 1088) = (u16)f2bf(z - bf2f(zh));
    }
    BAR();
    if (w < 4) {
      bf16x8 zf[16];
#pragma unroll
      for (int k = 0; k < 16; ++k) zf[k] = *(const bf16x8*)(lds + zr + 64 * k);
      __builtin_amdgcn_sched_barrier(0);
      f32x4 d = {0.f, 0.f, 0.f, 0.f};
#pragma unroll
      for (int k = 0; k < 16; ++k) d = __builtin_amdgcn_mfma_f32_16x16x32_bf16(wf[k], zf[k], d, 0, 0, 0);
      PIN(d);
      u32 cfb;
      do { cfb = *(volatile LAS u32*)(lds + cr); } while (__any(cfb == POISON));
      d *= __uint_as_float(cfb);
      const float a0 = (tp & 1) ? d[1] : d[0], b0 = (tp & 1) ? d[0] : d[1], a1 = (tp & 1) ? d[3] : d[2], b1 = (tp & 1) ? d[2] : d[3];
      const float e0 = a0 + dppf<0xB1>(b0), e1 = a1 + dppf<0xB1>(b1);
      const float v = rbf(((tp & 2) ? e1 : e0) + dppf<0x4E>((tp & 2) ? e0 : e1));
      const auto sw = __builtin_amdgcn_permlane32_swap(__float_as_uint(v), __float_as_uint(v), false, false);
      const float g = __uint_as_float(sw[0]), u = __uint_as_float(sw[1]);
      const float e2 = __builtin_amdgcn_exp2f(g * (-2.3022081981f - 0.1029432396f * g * g));
      *(u16*)(lds + hw) = (u16)f2bf(rbf(g * __builtin_amdgcn_rcpf(e2 + 1.f)) * u);
    } else {
      const u32x4 yv = *(const u32x4*)(lds + yr);
      const float a1 = dot2(yv.x, 0x3F803F80u, dot2(yv.y, 0x3F803F80u, 0.f)) + dot2(yv.z, 0x3F803F80u, dot2(yv.w, 0x3F803F80u, 0.f));
      const float a2 = dot2(yv.x, yv.x, dot2(yv.y, yv.y, 0.f)) + dot2(yv.z, yv.z, dot2(yv.w, yv.w, 0.f));
      const float sr = x16_sum(row16_sum(rs32(a1, a2))) * (1.f / 512.f);
      const float s1 = __uint_as_float(__builtin_amdgcn_readlane(__float_as_uint(sr), 0)), s2 = __uint_as_float(__builtin_amdgcn_readlane(__float_as_uint(sr), 32));
      const float var = rbf(fmaxf(s2 - s1 * s1, 0.f));
      const float mb = rbf(s1);
      const float sd = rbf(__builtin_amdgcn_sqrtf(rbf(var + 1e-5f)));
      float rsd = __builtin_amdgcn_rcpf(sd);
      rsd = rsd * (2.f - sd * rsd);
      *(u32*)(lds + cr) = fbits(tp == 3 ? 1.f : rsd * (tp == 2 ? -mb : 1.f));
    }
    BAR();
    {
      bf16x8 hf[4];
#pragma unroll
      for (int i = 0; i < 4; ++i) hf[i] = *(const bf16x8*)(lds + hr[i]);
      __builtin_amdgcn_sched_barrier(0);
      f32x4 d = {0.f, 0.f, 0.f, 0.f};
#pragma unroll
      for (int i = 0; i < 4; ++i) d = __builtin_amdgcn_mfma_f32_16x16x32_bf16(wd[i], hf[i], d, 0, 0, 0);
      st128(rs, so, u32x4{fbits(d[0]), fbits(d[1]), fbits(d[2]), fbits(d[3])});
    }
    STAMP(19);
  }
  if (t4 >= 0 && lw == 0) {
#define RED8(f) b3_sum(rs16(rs32(f.x, f.z), rs32(f.y, f.w)))
    const int oc = t4 * 32 + (lane & 7) * 4 + g4;
    const u32 xb = x[oc];
    float x1, gb;
    {
      int off[2];
      off[0] = lane < 32 ? WS_MODB + ((lane >> 3) * 2048 + t4 * 32 + (lane & 7) * 4) * 4 : WS_TOTAL;
      off[1] = lane < 32 ? WS_MODB + ((lane >> 3) * 2048 + 1536 + t4 * 32 + (lane & 7) * 4) * 4 : WS_TOTAL;
      u32x4 mg[2];
      gather128<2>(rs, off, mg);
      const f32x4 fa = __builtin_bit_cast(f32x4, mg[0]), fb = __builtin_bit_cast(f32x4, mg[1]);
      const float ga = rbf(RED8(fa));
      gb = rbf(RED8(fb));
      int of1[1] = {(WS_PATTN + il(((lane >> 3) * 512 + t4 * 32 + (lane & 7) * 4) * 4, myh)) ^ gx};
      u32x4 g[1];
      gather128<1, 4>(rs, of1, g);
      const f32x4 gf = __builtin_bit_cast(f32x4, g[0]);
      x1 = rbf(bf2f(xb) + rbf(ga * rbf(RED8(gf))));
    }
    STAMP(20);
    int off[7];
#pragma unroll
    for (int i = 0; i < 7; ++i) off[i] = (WS_PDOWN + il(((7 * (lane >> 3) + i) * 256 + (t4 & 7) * 32 + (lane & 7) * 4) * 4, myh)) ^ gx;
    const rsrc_t ro = __builtin_amdgcn_make_buffer_rsrc((void*)out, 0, 1024, 0x00020000);
    int oo = (lane & 8) ? 1024 : oc * 2;
    PIN(x1); PIN(gb); PIN(oo);
    u32x4 g[7];
    __builtin_amdgcn_s_sleep(T4_DELAY);
    gather128<7>(rs, off, g);
    STAMP(21);
#define F4(i) __builtin_bit_cast(f32x4, g[i])
    const f32x4 s = ((F4(0) + F4(1)) + (F4(2) + F4(3))) + ((F4(4) + F4(5)) + F4(6));
    __builtin_amdgcn_raw_buffer_store_b16((u16)f2bf(x1 + rbf(gb * rbf(RED8(s)))), ro, oo, 0, 0);
    if (t4 == 0 && lane == 0) st64(rs, WS_EPOCH, ctr[C_TLO], ctr[C_THI]);
    STAMP(22);
  }
  STAMP_FLUSH();
}

extern "C" void kernel_launch(void* const* d_in, const int* in_sizes, int n_in, void* d_out, int out_size,
                              void* d_ws, size_t ws_size, hipStream_t stream) {
  k_main<<<NWG, 512, 0, stream>>>((const u16*)d_in[0], (const u16*)d_in[1], (const u16*)d_in[2], (const u16*)d_in[3],
                                  (const u16*)d_in[4], (const u16*)d_in[5], (const u16*)d_in[7], (const u16*)d_in[8],
                                  (const u16*)d_in[9], (const u16*)d_in[10], (const u16*)d_in[11], (u16*)d_out,
                                  (unsigned char*)d_ws);
}
```

```cpp
#include <hip/hip_runtime.h>
#include <hip/hip_bf16.h>
#include <stdint.h>

typedef unsigned short u16;
typedef unsigned int u32;
typedef unsigned long long u64;
typedef float f32x2 __attribute__((ext_vector_type(2)));
typedef float f32x4 __attribute__((ext_vector_type(4)));
typedef float f32x16 __attribute__((ext_vector_type(16)));
typedef u32 u32x2 __attribute__((ext_vector_type(2)));
typedef u32 u32x4 __attribute__((ext_vector_type(4)));
typedef u32 u32x16 __attribute__((ext_vector_type(16)));
typedef short bf16x8 __attribute__((ext_vector_type(8)));
typedef short s16x4 __attribute__((ext_vector_type(4)));
#define LAS __attribute__((address_space(3)))
#define DI __device__ __forceinline__

constexpr int DF = 1792;
constexpr int NWG = 256;
constexpr int AUX_SC1 = 16;

constexpr int WS_EPOCH = 0;
constexpr int WS_SFLAG = 4096;
constexpr int WS_QABS = 32768;
constexpr int WS_PCTX = WS_QABS + 65536;
constexpr int WS_PATTN = WS_PCTX + 65536;
constexpr int WS_PDOWN = WS_PATTN + 32768;
constexpr int WS_LSE = WS_PDOWN + 114688;
constexpr int WS_MODB = WS_LSE + 196608;
constexpr int WS_SC = WS_MODB + 32768;
constexpr int WS_PROBE = WS_SC + 32768;
constexpr int WS_VOTE = WS_PROBE + 262144;
constexpr int WS_PART = 2097152;
constexpr int PART_BYTES = 32 * 8 * 256 * 128;
constexpr int WS_TOTAL = WS_PART + PART_BYTES;

constexpr int il(int o, int hf) { return (o >> 13) * 16384 + hf * 8192 + (o & 8191); }
constexpr int il21(int o, int hf) { return il(o, hf ^ ((o >> 20) & 1) ^ 1); }
constexpr u32 SPIN_CAP = 1u << 16;
#define WA 8
#define WA0 4
#define WB 8
#define WB0 5
#define WC 8
#define WC0 5
#define WINT 4
#define QF_T 300
#define E1_T 270
#define E3_T 515
#define MB_T 520
#define GU_T 600
#define DN_T 600
#define E4_S2 12
#define E4_S1 6
#define E4_S0 2
#define WO_T 0
#define KVNT 1
#define KVPOL " nt"
#define WNT true
#define Q_SLP 2
#define E1_BAR 0
#define P_LEN 0
#define LSET 2
#define SCP_T 0
#define T2M 1
#define E5_T 0
#define E5_S 4
#define E6_S 4
#define WING 2
#define E6_T 1200
#define TAU 32.f
#define QW 4
#define GUNT false
#define WV_MODE 0
#define T4_DELAY 12
#define VCLIP 40
#define T0_ADJ 35
#define PF_LEN 35100
#define G_OF(v) ((v) > 32768.f ? 8192u : 0u)
#define VOTE_STAMP(v)
#define STAMP_ARG
#define STAMP_PASS
#define STAMP_DECL
#define STAMP(i)
#define STAMP_FLUSH()

DI float bf2f(u32 b) { return __uint_as_float(b << 16); }
DI float rbf(float f) { return (float)(__bf16)f; }
DI u32 f2bf(float f) { return (u32)__builtin_bit_cast(u16, (__bf16)f); }
typedef __bf16 bf16x2_t __attribute__((ext_vector_type(2)));
DI u32 pk2bf(float a, float b) { return __builtin_bit_cast(u32, __builtin_convertvector(f32x2{a, b}, bf16x2_t)); }
DI float lo16(u32 v) { return __uint_as_float(v << 16); }
DI float hi16(u32 v) { return __uint_as_float(v & 0xFFFF0000u); }
DI u32x4 ldnt(const u16* p) { return WNT ? __builtin_nontemporal_load((const u32x4*)p) : *(const u32x4*)p; }

DI float silu_bf(float a) { return rbf(a * __builtin_amdgcn_rcpf(1.f + __expf(-a))); }

template <int CTRL> DI float dppf(float v) {
  return __int_as_float(__builtin_amdgcn_update_dpp(0, __float_as_int(v), CTRL, 0xF, 0xF, true));
}
DI float quad_sum(float v) { v += dppf<0xB1>(v); v += dppf<0x4E>(v); return v; }
DI float b23_sum(float v) { v += dppf<0x128>(v); v += dppf<0x124>(v); return v; }
DI float b3_sum(float v) { return v + dppf<0x128>(v); }
DI float x16_sum(float v) {
  const auto r = __builtin_amdgcn_permlane16_swap(__float_as_uint(v), __float_as_uint(v), false, false);
  return __uint_as_float(r[0]) + __uint_as_float(r[1]);
}
DI float x32_sum(float v) {
  const auto r = __builtin_amdgcn_permlane32_swap(__float_as_uint(v), __float_as_uint(v), false, false);
  return __uint_as_float(r[0]) + __uint_as_float(r[1]);
}
DI float rs32(float a, float b) {
  const auto r = __builtin_amdgcn_permlane32_swap(__float_as_uint(a), __float_as_uint(b), false, false);
  return __uint_as_float(r[0]) + __uint_as_float(r[1]);
}
DI float rs16(float a, float b) {
  const auto r = __builtin_amdgcn_permlane16_swap(__float_as_uint(a), __float_as_uint(b), false, false);
  return __uint_as_float(r[0]) + __uint_as_float(r[1]);
}
DI float row16_sum(float v) { return b23_sum(quad_sum(v)); }
DI float wave_sum(float v) { return x32_sum(x16_sum(row16_sum(v))); }
DI void row16_sum4(float& a, float& b, float& c, float& d) {
#define R4(C) "v_add_f32_dpp %0, %0, %0 " C " row_mask:0xf bank_mask:0xf\n v_add_f32_dpp %1, %1, %1 " C " row_mask:0xf bank_mask:0xf\n" \
              "v_add_f32_dpp %2, %2, %2 " C " row_mask:0xf bank_mask:0xf\n v_add_f32_dpp %3, %3, %3 " C " row_mask:0xf bank_mask:0xf\n"
  asm("s_nop 1\n" R4("quad_perm:[1,0,3,2]") R4("quad_perm:[2,3,0,1]") R4("row_ror:8") R4("row_ror:4") : "+v"(a), "+v"(b), "+v"(c), "+v"(d));
#undef R4
}
DI float row16_max(float v) {
  v = fmaxf(v, dppf<0xB1>(v)); v = fmaxf(v, dppf<0x4E>(v));
  v = fmaxf(v, dppf<0x128>(v)); v = fmaxf(v, dppf<0x124>(v));
  return v;
}
DI float x16_max(float v) {
  const auto r = __builtin_amdgcn_permlane16_swap(__float_as_uint(v), __float_as_uint(v), false, false);
  return fmaxf(__uint_as_float(r[0]), __uint_as_float(r[1]));
}
DI float x32_max(float v) {
  const auto r = __builtin_amdgcn_permlane32_swap(__float_as_uint(v), __float_as_uint(v), false, false);
  return fmaxf(__uint_as_float(r[0]), __uint_as_float(r[1]));
}
DI float wave_max(float v) { return x32_max(x16_max(row16_max(v))); }

DI float dot2(u32 a, u32 b, float c) {
  return __builtin_amdgcn_fdot2_f32_bf16(__builtin_bit_cast(bf16x2_t, a), __builtin_bit_cast(bf16x2_t, b), c, false);
}
DI void fma8b(float* acc, u32 sbits, u32x4 w) {
  const u32 sh = sbits << 16;
  acc[0] = dot2(w.x, sbits, acc[0]); acc[1] = dot2(w.x, sh, acc[1]);
  acc[2] = dot2(w.y, sbits, acc[2]); acc[3] = dot2(w.y, sh, acc[3]);
  acc[4] = dot2(w.z, sbits, acc[4]); acc[5] = dot2(w.z, sh, acc[5]);
  acc[6] = dot2(w.w, sbits, acc[6]); acc[7] = dot2(w.w, sh, acc[7]);
}
DI void fma8(float* acc, float s, u32x4 w) {
  acc[0] += s * lo16(w.x); acc[1] += s * hi16(w.x);
  acc[2] += s * lo16(w.y); acc[3] += s * hi16(w.y);
  acc[4] += s * lo16(w.z); acc[5] += s * hi16(w.z);
  acc[6] += s * lo16(w.w); acc[7] += s * hi16(w.w);
}

DI unsigned fsw(unsigned row) { return ((row & 3) << 2) | ((row >> 2) & 3); }
DI unsigned off_b(unsigned row, unsigned ch) { return 256u * row + 16u * (ch ^ fsw(row)); }
DI unsigned tr_addr(unsigned lane, unsigned c, unsigned ks, unsigned t) {
  const unsigned h = lane >> 5, blk = (lane >> 4) & 1, q = (lane & 15) >> 2, p = lane & 3;
  return off_b(16 * ks + 8 * h + 4 * t + q, 4 * c + 2 * blk + (p >> 1)) + 8 * (p & 1);
}

typedef __amdgpu_buffer_rsrc_t rsrc_t;
DI u32x4 ld128(rsrc_t rs, int off) { return __builtin_amdgcn_raw_buffer_load_b128(rs, off, 0, AUX_SC1); }
DI u32x2 ld64(rsrc_t rs, int off) { return __builtin_amdgcn_raw_buffer_load_b64(rs, off, 0, AUX_SC1); }
DI void st64(rsrc_t rs, int off, u32 lo, u32 hi) { u32x2 v; v.x = lo; v.y = hi; __builtin_amdgcn_raw_buffer_store_b64(v, rs, off, 0, AUX_SC1); }
DI void st128(rsrc_t rs, int off, u32x4 v) { __builtin_amdgcn_raw_buffer_store_b128(v, rs, off, 0, AUX_SC1); }
#define BAR() do { asm volatile("s_waitcnt lgkmcnt(0)" ::: "memory"); __builtin_amdgcn_s_barrier(); asm volatile("" ::: "memory"); } while (0)
#define BAR0() do { __builtin_amdgcn_sched_barrier(0); asm volatile("" ::: "memory"); __builtin_amdgcn_s_barrier(); asm volatile("" ::: "memory"); __builtin_amdgcn_sched_barrier(0); } while (0)
#define PIN(x) asm volatile("" : "+v"(x))
constexpr u32 POISON = 0xFFFFFFFFu;
DI bool pois4(u32x4 v) { return max(max(v.x, v.y), max(v.z, v.w)) == POISON; }
DI u32 nopois(u32 v) { return min(v, 0xFFFFFFFEu); }
DI u32 fbits(float v) { return nopois(__float_as_uint(v)); }

DI u32x2 mk_gran(float v, u32 tlo, u32 thi) {
  u32 b = __float_as_uint(v);
  if ((b & 0x7F800000u) != 0x7F800000u) b += 0x80u;
  u32x2 g; g.x = (b >> 8) | (tlo << 24); g.y = (tlo >> 8) | (thi << 24);
  return g;
}
DI void st_gran2(rsrc_t rs, int off, float v0, float v1, u32 tlo, u32 thi) {
  const u32x2 a = mk_gran(v0, tlo, thi), b = mk_gran(v1, tlo, thi);
  u32x4 v; v.x = a.x; v.y = a.y; v.z = b.x; v.w = b.y;
  st128(rs, off, v);
}
DI bool gran_ok(u32 lo, u32 hi, u32 tlo, u32 thi) { return (((lo >> 24) ^ (tlo & 0xFFu)) | (hi ^ ((tlo >> 8) | (thi << 24)))) == 0u; }
DI float gran_val(u32 lo) { return __uint_as_float(lo << 8); }

template <int N, int SLP = 1, bool PRE = false> DI void gather128(rsrc_t rs, const int (&off)[N], u32x4 (&v)[N]) {
#pragma unroll
  for (int i = 0; i < N; ++i) if (!PRE) v[i] = ld128(rs, off[i]);
  _Pragma("unroll 1") for (u32 spins = 0; spins < SPIN_CAP; ++spins) {
    bool need[N], bad = false;
#pragma unroll
    for (int i = 0; i < N; ++i) { need[i] = pois4(v[i]); bad = bad || need[i]; }
    if (!__any(bad)) break;
    __builtin_amdgcn_s_sleep(SLP);
    asm volatile("" ::: "memory");
    __builtin_amdgcn_sched_barrier(0);
#pragma unroll
    for (int i = 0; i < N; ++i)
      if (need[i]) v[i] = ld128(rs, off[i]);
  }
}
template <int N> DI void waitvm() { asm volatile("s_waitcnt vmcnt(%0)" ::"n"(N) : "memory"); }
constexpr u32 LSPIN_CAP = 1u << 22;
DI void lds_wait(volatile LAS u32* p, u32 val) {
  _Pragma("unroll 1") for (u32 n = 0; *p < val && n < LSPIN_CAP; ++n) __builtin_amdgcn_s_sleep(1);
  asm volatile("" ::: "memory");
}
#define WAVE_LDS_SYNC() do { __builtin_amdgcn_fence(__ATOMIC_RELEASE, "wavefront"); __builtin_amdgcn_wave_barrier(); __builtin_amdgcn_fence(__ATOMIC_ACQUIRE, "wavefront"); } while (0)
#define SB() __builtin_amdgcn_sched_barrier(0)
#define RARE(c) __builtin_expect(!!(c), 0)

constexpr int PS = 28672;
constexpr int L_QI = 4 * PS;
constexpr int L_P = L_QI + 32768;
constexpr int L_REF = L_P + 11264;
constexpr int L_MX = L_REF + 1408;
constexpr int L_ALW = L_MX + 128;
constexpr int L_DUMP = L_ALW + 512;
constexpr int L_QOFF = 131072;
constexpr int L_H1RED = L_QOFF;
constexpr int L_RED = L_QOFF + 16384;
constexpr int L_Z = L_RED, L_HH = L_RED + 7 * 1088, L_ZERO = L_HH + 64, L_CF = L_ZERO + 64;
constexpr int L_MISC = L_QOFF + 30720;
constexpr int L_CTR = L_MISC;
constexpr int L_WMAX = L_MISC + 256;
constexpr int L_ML = L_MISC + 512;

#define Z4 (u32x4{0u, 0u, 0u, 0u})
DI float max5(f32x4 v, float e) { return fmaxf(fmaxf(fmaxf(fmaxf(v.x, v.y), v.z), v.w), e); }

template <int OFF, bool NT> DI void dma(u32 m, u32 voff, const void* sb) {
  if (NT) asm volatile("s_mov_b32 m0, %0\n s_nop 0\n global_load_lds_dwordx4 %1, %2 offset:%3 nt" ::"s"(m), "v"(voff), "s"(sb), "n"(OFF) : "memory");
  else asm volatile("s_mov_b32 m0, %0\n s_nop 0\n global_load_lds_dwordx4 %1, %2 offset:%3" ::"s"(m), "v"(voff), "s"(sb), "n"(OFF) : "memory");
}
DI void dma_sc1(u32 m, u32 voff, const void* sb) {
  asm volatile("s_mov_b32 m0, %0\n s_nop 0\n global_load_lds_dwordx4 %1, %2 sc1" ::"s"(m), "v"(voff), "s"(sb) : "memory");
}
DI void lds_addv(unsigned char* lds, int idx, u32 v) {
  SB();
  asm volatile("s_mov_b64 exec, 1\n ds_add_u32 %0, %1 offset:%2\n s_mov_b64 exec, -1" ::"v"((u32)L_CTR), "v"(v), "n"(idx * 4) : "memory");
  SB();
}
DI void lds_add(unsigned char* lds, int idx, int lane) { lds_addv(lds, idx, 1u); }
DI void lds_arrive(unsigned char* lds, int idx, int lane) {
  asm volatile("s_waitcnt lgkmcnt(0)" ::: "memory");
  lds_add(lds, idx, lane);
}
DI u32 spin_at(u32 a, u32 val) {
  u32 t, c, m, cap;
  asm volatile("s_mov_b32 %3, %6\n 1: ds_read_b32 %0, %4\n s_waitcnt lgkmcnt(0)\n v_readfirstlane_b32 %1, %0\n s_and_b32 %2, %1, 255\n s_cmp_ge_u32 %2, %5\n s_cbranch_scc1 2f\n"
               "s_sub_u32 %3, %3, 1\n s_cbranch_scc0 1b\n 2:"
               : "=&v"(t), "=&s"(c), "=&s"(m), "=&s"(cap) : "v"(a), "s"(val), "n"(LSPIN_CAP) : "memory", "scc");
  return c;
}
DI void lds_spin(volatile LAS u32* p, u32 val) { spin_at((u32)(uintptr_t)p, val); }
enum { C_SOK = 0, C_BH = 4, C_TLO = 8, C_THI = 9, C_PD = 12, C_H1 = 18, C_PAR = 19, C_VOTE = 20, C_FREE = 22, C_QRDY = 23, C_WV = 24, C_MXC = 25, C_SD = 26, C_GX = 27, C_ML = 28, C_MK = 32, C_P = 44 };

DI u32 kv_mark(int nm, int skip) { return L_CTR + 4 * C_MK + __builtin_amdgcn_readfirstlane(4 * nm + (nm >= skip ? 4 : 0)); }
#define KV_TS
#define KV_TSO
#define KV_TSA
#define KV_TSP
#define KV_TRY(l)                                                                                                        \
  "7: s_getreg_b32 %[h], hwreg(HW_REG_IB_STS, 0, 4)\n s_cmp_gt_i32 %[h], %[k4]\n s_cbranch_scc1 " l "\n"                  \
  "s_waitcnt vmcnt(4)\n s_cmp_eq_u32 %[k4], 4\n s_cbranch_scc1 8f\n s_waitcnt vmcnt(0)\n"                              \
  "8: v_subrev_u32 %[t], %[k4], %[ml]\n s_mov_b64 exec, 1\n ds_add_u32 %[t], %[one] offset:12\n s_mov_b64 exec, -1\n"   \
  "s_sub_i32 %[k4], %[k4], 4\n" KV_TS "s_branch 7b\n"
#define STR2(x) #x
#define STR(x) STR2(x)
#define SPIN_SLEEP ""
DI void kv_try(int& k4, u32 ml, u32 one KV_TSA) {
  u32 t, h;
  asm volatile(KV_TRY("9f") "9:" : [t] "=&v"(t), [h] "=&s"(h), [k4] "+s"(k4) KV_TSO : [ml] "v"(ml), [one] "v"(one) : "memory", "scc");
}
DI u32 spin_try(int idx, u32 val, int& k4, u32 ml, u32 one KV_TSA) {
  u32 t, h, c, cap;
  asm volatile("s_mov_b32 %[cap], %[n]\n 1: ds_read_b32 %[t], %[ml] offset:%[o]\n s_waitcnt lgkmcnt(0)\n v_readfirstlane_b32 %[c], %[t]\n s_and_b32 %[h], %[c], 255\n"
               "s_cmp_ge_u32 %[h], %[v]\n s_cbranch_scc1 2f\n" KV_TRY("9f") "9: " SPIN_SLEEP "s_sub_u32 %[cap], %[cap], 1\n s_cbranch_scc0 1b\n 2:"
               : [t] "=&v"(t), [h] "=&s"(h), [c] "=&s"(c), [cap] "=&s"(cap), [k4] "+s"(k4) KV_TSO
               : [ml] "v"(ml), [one] "v"(one), [o] "n"(4 * idx), [v] "n"(val), [n] "n"(LSPIN_CAP) : "memory", "scc");
  return c;
}
template <int W> DI void kv_load1(const u16* ckv, u32 vl, int lw, u32 g0, u32 g1, u32 m, int n0, int n1, int& nm, int skip, u32 tp) {
#pragma unroll 1
  for (int n = n0; n < n1; ++n) {
#define KV_POST(k) ".if (%[wm] | 4) == " #k "\n s_cmp_gt_i32 %[nm], %[n2]\n s_cbranch_scc1 20f\n s_mov_b64 exec, 1\n ds_add_u32 %[ma], %[one]\n s_mov_b64 exec, -1\n s_add_i32 %[nm], %[nm], 1\n 20:\n .endif\n"
#define KV_H "s_waitcnt vmcnt(%[wm])\n"
#define KV_W(o) KV_H "global_load_lds_dwordx4 %[vo], %[p] offset:" #o KVPOL "\n"
#define KV_M "s_add_u32 m0, m0, %[ms]\n"
    asm volatile("s_mov_b32 m0, %[m]\n" KV_W(0) KV_POST(4) KV_M KV_W(256) KV_POST(5) KV_M KV_W(512) KV_POST(6) KV_M KV_W(768) KV_POST(7)
                 : [nm] "+s"(nm)
                 : [m] "s"(m), [vo] "v"(vl + g0 + ((lw + n) & 3) * 4096), [p] "s"(ckv), [ma] "v"(kv_mark(nm, skip)),
                   [one] "v"(1u), [n2] "s"(__builtin_amdgcn_readfirstlane(n - (W > 4 ? 2 : 1))), [wm] "n"(W - 1), [ms] "n"(PS - 256), [tp] "s"(tp), [pl] "n"(P_LEN + 8)
                 : "memory", "scc", "vcc");
    g0 = g1;
    g1 += 192 * 16384;
    m += n + 1 == skip ? 8192 : 4096;
  }
}
template <int W, int W0> DI void kv_load(const u16* ckv, u32 vl, int lw, u32 g0, u32 g1, u32 m, int n0, int n1, int& nm, int skip, u32 tp) {
  if (W != W0 && skip == 4) kv_load1<W0>(ckv, vl, lw, g0, g1, m, n0, n1, nm, skip, tp); else kv_load1<W>(ckv, vl, lw, g0, g1, m, n0, n1, nm, skip, tp);
}
DI void until(u32 t0, u32 dt) {
  while ((u32)__builtin_amdgcn_s_memrealtime() - t0 < dt) __builtin_amdgcn_s_sleep(2);
}
DI bf16x8 tr_frag(unsigned char* lds, u32 a0, u32 a1) {
  const s16x4 b0 = __builtin_amdgcn_ds_read_tr16_b64_v4i16((LAS s16x4*)(lds + a0));
  const s16x4 b1 = __builtin_amdgcn_ds_read_tr16_b64_v4i16((LAS s16x4*)(lds + a1));
  bf16x8 v;
  v[0] = b0[0]; v[1] = b0[1]; v[2] = b0[2]; v[3] = b0[3]; v[4] = b1[0]; v[5] = b1[1]; v[6] = b1[2]; v[7] = b1[3];
  return v;
}
DI float sum4(f32x4 v) { const f32x2 t = f32x2{v.x, v.y} + f32x2{v.z, v.w}; return t.x + t.y; }

__global__ __launch_bounds__(512, 2) void k_main(const u16* __restrict__ x, const u16* __restrict__ ckv,
                                                 const u16* __restrict__ w_q, const u16* __restrict__ w_uk,
                                                 const u16* __restrict__ w_uv, const u16* __restrict__ w_o,
                                                 const u16* __restrict__ w_gate, const u16* __restrict__ w_up,
                                                 const u16* __restrict__ w_down, const u16* __restrict__ cvec,
                                                 const u16* __restrict__ w_mod, u16* __restrict__ out,
                                                 unsigned char* ws) {
  __shared__ __attribute__((aligned(16))) unsigned char lds[163840];
  const int w = __builtin_amdgcn_readfirstlane(threadIdx.x >> 6), j = blockIdx.x;
  int lane = threadIdx.x & 63;
  const int xc = j & 7, ii = j >> 3;
  STAMP_DECL;
  STAMP(0);
  u32x2 ev;
  u32 xm, vh;
  {
    u64 p, pc, pw;
    u32 a, b, c, d, f, t;
    asm volatile("s_lshr_b32 %[f], %[j], 6\n s_cmp_eq_u32 %[f], 1\n s_cselect_b32 %[a], 0x50, 16\n"
                 "s_load_dwordx2 %[p], %[ka], %[a]\n s_load_dwordx2 %[pw], %[ka], 0x60\n"
                 "s_lshr_b32 %[d], %[j], 3\n s_cmp_lg_u32 %[w], 7\n s_cbranch_scc1 2f\n"
                 "s_and_b32 %[a], %[d], 15\n s_lshl_b32 %[a], %[a], 12\n v_lshl_add_u32 %[t], %[lane], 6, %[a]\n v_min_u32 %[t], %[pfl], %[t]\n"
                 "s_getpc_b64 %[pc]\n s_mov_b32 m0, %[dump]\n s_nop 0\n global_load_lds_dword %[t], %[pc]\n"
                 "2: s_load_dwordx2 %[pc], %[ka], 0x48\n s_and_b32 %[c], %[j], 7\n s_lshl_b32 %[c], %[c], 10\n s_cmp_eq_u32 %[f], 0\n s_cbranch_scc0 3f\n"
                 "s_and_b32 %[a], %[d], 1\n s_lshl1_add_u32 %[a], %[w], %[a]\n s_lshl_b32 %[a], %[a], 18\n s_add_u32 %[c], %[c], %[a]\n"
                 "s_lshr_b32 %[a], %[d], 1\n s_lshl_b32 %[a], %[a], 8\n s_add_u32 %[c], %[c], %[a]\n s_lshl_b32 m0, %[w], 14\n"
                 "v_lshrrev_b32 %[t], 4, %[lane]\n v_mul_u32_u24 %[t], 0x204, %[t]\n v_and_b32 %[vh], 15, %[lane]\n v_xor_b32 %[vh], %[vh], %[t]\n"
                 "v_lshl_add_u32 %[vh], %[vh], 4, %[c]\n v_xor_b32 %[t], 0x8010, %[vh]\n s_waitcnt lgkmcnt(0)\n"
                 "global_load_lds_dwordx4 %[vh], %[p]\n s_add_u32 m0, m0, 0x400\n s_nop 0\n"
                 "global_load_lds_dwordx4 %[t], %[p]\n s_add_u32 m0, m0, 0x400\n v_xor_b32 %[t], 0x10020, %[vh]\n"
                 "global_load_lds_dwordx4 %[t], %[p]\n s_add_u32 m0, m0, 0x400\n v_xor_b32 %[t], 0x18030, %[vh]\n"
                 "global_load_lds_dwordx4 %[t], %[p]\n s_branch 1f\n"
                 "3: s_lshl2_add_u32 %[d], %[d], %[w]\n s_sub_u32 %[d], %[d], 68\n s_lshl_b32 %[d], %[d], 16\n s_add_u32 %[d], %[d], %[c]\n"
                 "s_and_b32 %[a], %[j], 3\n s_lshl2_add_u32 %[b], %[a], %[w]\n s_mul_i32 %[b], %[b], 0x30000\n s_lshl_b32 %[a], %[a], 8\n"
                 "v_lshl_add_u32 %[xm], %[lane], 2, %[a]\n"
                 "s_and_b32 %[a], %[j], 60\n s_lshl_b32 %[a], %[a], 5\n s_add_u32 %[b], %[b], %[a]\n"
                 "s_lshl_b32 %[c], %[w], 12\n s_add_u32 %[c], %[c], %[mq]\n s_lshl_b32 %[a], %[w], 10\n s_add_u32 %[a], %[a], %[mh]\n"
                 "s_cmp_eq_u32 %[f], 1\n s_cselect_b32 %[d], %[b], %[d]\n s_cselect_b32 m0, %[c], %[a]\n s_cselect_b32 %[a], 48, 1\n s_cselect_b32 %[c], 0, 4\n"
                 "v_lshrrev_b32 %[t], 3, %[lane]\n v_mul_u32_u24 %[t], %[a], %[t]\n v_and_b32 %[vh], 7, %[lane]\n v_lshl_add_u32 %[vh], %[t], 3, %[vh]\n"
                 "v_lshl_add_u32 %[vh], %[vh], 4, %[d]\n"
                 "s_cselect_b32 %[b], 6, 1\n s_cselect_b32 %[a], 0x400, 0\n s_lshl_b32 %[b], %[b], 13\n"
                 "s_xor_b32 %[c], %[c], %[w]\n s_bitcmp1_b32 %[c], 2\n v_mov_b32 %[t], 0\n"
                 "s_waitcnt lgkmcnt(0)\n s_cbranch_scc1 1f\n s_cmp_eq_u32 %[f], 1\n s_cbranch_scc0 4f\n"
                 "global_load_dword %[xm], %[xm], %[pc]\n global_load_dwordx2 %[ev], %[t], %[pw] sc1\n"
                 "4: global_load_lds_dwordx4 %[vh], %[p]\n s_add_u32 m0, m0, %[a]\n v_add_u32 %[t], %[b], %[vh]\n"
                 "global_load_lds_dwordx4 %[t], %[p]\n s_add_u32 m0, m0, %[a]\n v_add_u32 %[t], %[b], %[t]\n"
                 "global_load_lds_dwordx4 %[t], %[p]\n s_add_u32 m0, m0, %[a]\n v_add_u32 %[t], %[b], %[t]\n"
                 "global_load_lds_dwordx4 %[t], %[p]\n"
                 "1:\n"
                 : [xm] "=&v"(xm), [ev] "=&v"(ev), [vh] "=&v"(vh), [t] "=&v"(t), [p] "=&s"(p), [pc] "=&s"(pc), [pw] "=&s"(pw), [a] "=&s"(a), [b] "=&s"(b), [c] "=&s"(c),
                   [d] "=&s"(d), [f] "=&s"(f)
                 : [j] "s"(j), [w] "s"(w), [lane] "v"(lane), [ka] "s"(__builtin_amdgcn_kernarg_segment_ptr()), [pfl] "n"(PF_LEN), [dump] "n"(L_DUMP),
                   [mq] "n"(L_QOFF + 8192), [mh] "n"(L_P)
                 : "memory", "scc");
  }
  SB();
  const u32 t0 = (u32)__builtin_amdgcn_s_memrealtime() - T0_ADJ;
  const bool isQ = j < 64, isS0 = j >= 64 && j < 128;
  const int hq = xc * 4 + (ii >> 1), dh = ii & 1, sj = j - 64;
  const int lw = w - 4, myh = xc >> 2;
  int col = lane & 15, g4 = lane >> 4, tq = col >> 2, tp = lane & 3;
  const rsrc_t rs = __builtin_amdgcn_make_buffer_rsrc((void*)ws, 0, WS_TOTAL, 0x00020000);
  volatile LAS u32* ctr = (volatile LAS u32*)(lds + L_CTR);
  SB();
  STAMP(25);
  if (w < 2) *(u32*)(lds + (w == 0 ? L_CTR + lane * 4 : L_MX + (lane & 31) * 4)) = w == 0 ? 0u : 0xFF800000u;
  BAR();
  const bool tl2 = isS0;
  const int t3 = xc < 4 ? (ii < 8 ? xc * 8 + ii : -1) : (ii < 6 ? 8 + xc * 6 + ii : -1);
  const int t4 = (ii >= 8 && ii < 10) ? xc * 2 + (ii - 8) : -1;
  const u32 owno = (w >> 1) * 32768 + 8192 + (w & 1) * 12288;
  const int ks = T2M ? (xc & 3) * 2 + myh : (ii & 6) + myh, cg = T2M ? ii & 7 : (ii & 1) * 4 + (xc & 3);
  const int h = (j >> 2) & 31, c4 = j & 3;
  u32x4 wv[8];
  u32 tlo, thi;
#define CY(i)
#define CYOUT()
  int gx;
#define RELANE() do { asm volatile("v_mbcnt_lo_u32_b32 %0, -1, 0\n v_mbcnt_hi_u32_b32 %0, -1, %0" : "=v"(lane)); col = lane & 15; g4 = lane >> 4; tq = col >> 2; tp = lane & 3; } while (0)
#define TAG_OF(e0, e1)                                                                                                  \
  do {                                                                                                                  \
    const u64 tag = (((u64)(e1) << 32) | (e0)) + 1ull;                                                                  \
    tlo = __builtin_amdgcn_readfirstlane((u32)tag); thi = __builtin_amdgcn_readfirstlane((u32)(tag >> 32));             \
  } while (0)
  const u32x4 P4 = {POISON, POISON, POISON, POISON};
#define START_FLAG()                                                                                                    \
  do {                                                                                                                  \
    if (lane < 8) st64(rs, WS_SFLAG + lane * 2048 + sj * 8, tlo, thi);                                                  \
    if (lane == 0) { ctr[C_TLO] = tlo; ctr[C_THI] = thi; }                                                              \
  } while (0)
#define COUNT_VOTES(PRE)                                                                                                \
  do {                                                                                                                  \
    u32x2 a_ = {0u, 0u};                                                                                                \
    if (PRE) a_ = *(const u32x2*)(lds + L_QI + lane * 8);                                                               \
    _Pragma("unroll 1") for (u32 spins = 0; spins < SPIN_CAP; ++spins) {                                                \
      if (((PRE) || spins) && __all(gran_ok(a_.x, a_.y, tlo, thi))) break;                                              \
      if (spins) __builtin_amdgcn_s_sleep(8);                                                                           \
      asm volatile("" ::: "memory");                                                                                    \
      a_ = ld64(rs, WS_VOTE + xc * 2048 + lane * 8);                                                                    \
    }                                                                                                                   \
    const float vt_ = wave_sum((float)(a_.x & 1023u));                                                                  \
    if (lane == 0) ctr[C_PAR] = G_OF(vt_);                                                                              \
    VOTE_STAMP(vt_);                                                                                                    \
    STAMP(8);                                                                                                           \
  } while (0)
#define S_POLL(a_)                                                                                                      \
  _Pragma("unroll 1") for (u32 spins = 0; spins < SPIN_CAP && RARE(__any(((a_.x ^ tlo) | (a_.y ^ thi)) != 0u)); ++spins) { \
    __builtin_amdgcn_s_sleep(8);                                                                                        \
    asm volatile("" ::: "memory");                                                                                      \
    a_ = ld64(rs, WS_SFLAG + xc * 2048 + lane * 8);                                                                     \
  }
#define MB_OFF(i) (WS_MODB + ((lane >> 4) * 2048 + ((i) >> 1) * 512 + 128 * lw + 64 * ((i) & 1) + (lane & 15) * 4) * 4)

  if (!isQ && w >= 4) {
    u32x2 e4;
    if (isS0) {
      if (w == 4) {
        e4 = ld64(rs, WS_EPOCH);
        st128(rs, WS_QABS + sj * 1024 + lane * 16, P4);
        st128(rs, lane < 32 ? WS_MODB + sj * 512 + lane * 16 : WS_TOTAL, P4);
        st128(rs, WS_PROBE + il(sj * 1024 + lane * 16, 0), Z4);
        st128(rs, WS_PROBE + il(sj * 1024 + lane * 16, 1), Z4);
      }
    } else {
#pragma unroll
      for (int i = 4; i < 8; ++i) dma<0, false>(L_P + w * 1024, vh + i * 8192, w_q);
    }
    SB();
    if (isS0) {
      if (w == 4) {
        waitvm<0>();
        TAG_OF(e4.x, e4.y);
        START_FLAG();
        STAMP(27);
      }
      lds_wait(ctr + C_H1, 4u);
    } else {
#pragma unroll
      for (int i = 0; i < 8; ++i) { waitvm<7>(); dma<0, false>(L_P + w * 1024, vh + i * 8192, w_uk); }
    }
    const u32 vl = g4 * 1024 + ((col ^ ((g4 << 2) | lw)) << 4);
    int nm = 0, ns = isS0 ? 6 : 7;
    const int skip = isS0 ? 4 : 99;
    kv_load<WA, WA0>(ckv, vl, lw, (isS0 ? sj : 1792 + j) * 16384, (isS0 ? sj + 192 : sj) * 16384, lw * 1024, 0, 2, nm, skip, t0 + E3_T - P_LEN);
    if (!isS0) {
      const rsrc_t rw = __builtin_amdgcn_make_buffer_rsrc((void*)w_uv, 0, 4194304, 0x00020000);
#pragma unroll
      for (int i = 0; i < 8; ++i) { waitvm<WA - 1>(); wv[i] = __builtin_amdgcn_raw_buffer_load_b128(rw, (c4 * 128 + 32 * lw + g4) * 8192 + h * 256 + col * 16, i * 32768, WNT ? 2 : 0); }
      SB();
    }
    u32 va[8];
#pragma unroll
    for (int i = 0; i < 8; ++i) { va[i] = lw * PS + tr_addr(lane, i >> 1, 0, i & 1); PIN(va[i]); }
    int eg[8];
#pragma unroll
    for (int u = 0; u < 8; ++u) { eg[u] = WS_PART + il21(((4 * (u >> 1) + 2 * (g4 >> 1) + (u & 1)) * 4 + lw) * 65536 + sj * 256 + col * 16, g4 & 1); PIN(eg[u]); }
    u32 pr_ = L_P + lane * 16;
    PIN(pr_);
    u32 ml = L_CTR + 4 * (C_MK + 7), one = 1u;
    PIN(ml); PIN(one);
    bf16x8 vf[4][4];
#define V_READ(i, slot) do { asm volatile("" ::: "memory"); SB(); _Pragma("unroll") for (int c = 0; c < 4; ++c) vf[i][c] = tr_frag(lds, va[2 * c] + (slot) * 4096, va[2 * c + 1] + (slot) * 4096); SB(); } while (0)
    SB();
    kv_load<WB, WB0>(ckv, vl, lw, (sj + (isS0 ? 384 : 192)) * 16384, (sj + (isS0 ? 576 : 384)) * 16384, lw * 1024 + 8192, 2, ns, nm, skip, t0 + E3_T - P_LEN);
    lds_wait(ctr + C_MK + 3, 4u);
    V_READ(0, 0); V_READ(1, 1); V_READ(2, 2); V_READ(3, 3);
    lds_add(lds, C_FREE, lane);
#pragma unroll
    for (int i = 0; i < 16; ++i) PIN(vf[i >> 2][i & 3]);
    STAMP(2);
    lds_wait(ctr + C_FREE, 8u);
    STAMP(5);
    kv_load<WC, WC0>(ckv, vl, lw, (sj + 6 * 192) * 16384, (sj + 7 * 192) * 16384, lw * 1024, ns, ns + 4, nm, skip, t0 + E3_T - P_LEN);
    int k4 = 4;
    STAMP(3);
    f32x16 acc[4];
#pragma unroll
    for (int c = 0; c < 4; ++c)
#pragma unroll
      for (int i = 0; i < 16; ++i) acc[c][i] = 0.f;
    bf16x8 pf[4], pn[4];
    u32 c_;
#define P_SPIN(g, n) c_ = spin_try(C_P + (g) - C_MK - 7, n, k4, ml, one KV_TSP)
#define TRY() kv_try(k4, ml, one KV_TSP)
#define P_READ(d, i0, i1, T0) _Pragma("unroll") for (int i = (i0); i < (i1); ++i) d[i] = *(volatile LAS bf16x8*)(lds + pr_ + ((T0) + i) * 1024)
#define PV(p, i) do { SB(); _Pragma("unroll") for (int c = 0; c < 4; ++c) acc[c] = __builtin_amdgcn_mfma_f32_32x32x16_bf16(p[i], vf[i][c], acc[c], 0, 0, 0); SB(); } while (0)
#define GX_USE(v) do { gx = __builtin_amdgcn_readfirstlane(v); _Pragma("unroll") for (int u = 0; u < 8; ++u) { eg[u] ^= gx; PIN(eg[u]); } } while (0)
#define MK_WAIT(T) spin_try((T) - 7, 4u, k4, ml, one KV_TSP)
#define PVV(p, i, d, slot)                                                                                               \
  _Pragma("unroll") for (int c = 0; c < 4; ++c) {                                                                        \
    SB();                                                                                                                \
    acc[c] = __builtin_amdgcn_mfma_f32_32x32x16_bf16(p[i], vf[i][c], acc[c], 0, 0, 0);                                   \
    SB();                                                                                                                \
    vf[d][c] = tr_frag(lds, va[2 * c] + (slot) * 4096, va[2 * c + 1] + (slot) * 4096);                                   \
    SB();                                                                                                                \
  }
#define CT(i) (*(volatile LAS u32*)(lds + ml + 4 * ((i) - C_MK - 7)))
    P_SPIN(0, 4u);
    P_READ(pf, 0, 4, 0);
    const u32 k7 = CT(C_MK + 7);
    asm volatile("" ::: "memory");
    STAMP(6);
    PVV(pf, 0, 0, 4); TRY();
    PVV(pf, 1, 1, 5); TRY();
    PVV(pf, 2, 2, 6);
    u32 cv = CT(C_P + 1);
    P_READ(pn, 0, 4, 4);
    PVV(pf, 3, 3, 0);
    if (RARE(__builtin_amdgcn_readfirstlane(k7) < 4u)) { MK_WAIT(7); V_READ(0, 4); V_READ(1, 5); V_READ(2, 6); V_READ(3, 0); }
    STAMP(9);
    c_ = __builtin_amdgcn_readfirstlane(cv);
    if (RARE((c_ & 255u) != 4u)) { P_SPIN(1, 4u); P_READ(pn, 0, 4, 4); }
    int slow = -1;
    if (RARE(c_ > 255u)) slow = 4;
    else {
      if (!isS0) PV(pn, 0);
      TRY();
      const u32 k9 = CT(C_MK + 9);
      asm volatile("" ::: "memory");
      PVV(pn, 1, 0, 1); TRY();
      PVV(pn, 2, 1, 2);
      cv = CT(C_P + 2);
      P_READ(pf, 0, 2, 8);
      const u32 k10 = CT(C_MK + 10);
      asm volatile("" ::: "memory");
      PVV(pn, 3, 2, 3);
      if (RARE(__builtin_amdgcn_readfirstlane(k9) < 4u)) { MK_WAIT(9); V_READ(0, 1); V_READ(1, 2); }
      STAMP(10);
      c_ = __builtin_amdgcn_readfirstlane(cv);
      if (RARE((c_ & 255u) != 4u)) { P_SPIN(2, 4u); P_READ(pf, 0, 2, 8); }
      if (RARE(c_ > 255u)) slow = 8;
      else {
        TRY();
        PV(pf, 0);
        const u32 g0 = ctr[C_GX];
        u32 g1 = ctr[C_PAR];
        cv = CT(C_P + 3);
        P_READ(pf, 2, 3, 8);
        PV(pf, 1);
        STAMP(4);
        if (RARE(__builtin_amdgcn_readfirstlane(k10) < 4u)) { waitvm<0>(); TRY(); lds_spin(ctr + C_MK + 10, 4u); V_READ(2, 3); }
        if (RARE(__builtin_amdgcn_readfirstlane(g0) == 0u)) { lds_spin(ctr + C_GX, 1u); g1 = ctr[C_PAR]; }
        GX_USE(g1);
        STAMP(11);
        c_ = __builtin_amdgcn_readfirstlane(cv);
        _Pragma("unroll 1") for (u32 n_ = 0; RARE((c_ & 255u) != 2u) && n_ < LSPIN_CAP; ++n_) { cv = CT(C_P + 3); P_READ(pf, 2, 3, 8); c_ = __builtin_amdgcn_readfirstlane(cv); }
        if (RARE(c_ > 255u)) slow = 10;
        else PV(pf, 2);
      }
    }
    if (RARE(slow >= 0)) {
      MK_WAIT(10);
      float R = ceilf(*(const float*)(lds + L_MX + (lane & 31) * 4));
#pragma unroll 1
      for (int T = slow; T < 11; ++T) {
        if (T == 4 && isS0) continue;
        spin_at(L_CTR + 4 * (C_P + (T < 8 ? 1 : T < 10 ? 2 : 3)), T < 10 ? 4u : 2u);
        const float rf = *(const float*)(lds + L_REF + T * 128 + (lane & 31) * 4);
        u32x4 p = *(const u32x4*)(lds + pr_ + T * 1024);
        const u32 so_ = (T < 7 ? T : T - 7) * 4096;
#pragma unroll
        for (int c = 0; c < 4; ++c) vf[0][c] = tr_frag(lds, va[2 * c] + so_, va[2 * c + 1] + so_);
        const float Rn = fmaxf(R, rf), ps = __builtin_amdgcn_exp2f(rf - Rn);
        *(float*)(lds + L_ALW + lw * 128 + (lane & 31) * 4) = __builtin_amdgcn_exp2f(R - Rn);
        R = Rn;
        WAVE_LDS_SYNC();
#pragma unroll
        for (int i = 0; i < 4; ++i) {
          const f32x4 al = *(const f32x4*)(lds + L_ALW + lw * 128 + (lane >> 5) * 16 + i * 32);
#pragma unroll
          for (int c = 0; c < 4; ++c)
#pragma unroll
            for (int r = 0; r < 4; ++r) acc[c][4 * i + r] *= al[r];
          p[i] = pk2bf(lo16(p[i]) * ps, hi16(p[i]) * ps);
        }
        WAVE_LDS_SYNC();
        pf[0] = __builtin_bit_cast(bf16x8, p);
        PV(pf, 0);
      }
      if (slow != 10) { lds_spin(ctr + C_GX, 1u); GX_USE(ctr[C_PAR]); }
    }
    STAMP(30);
#pragma unroll
    for (int u = 0; u < 8; u += 2) {
      u32 c[8];
#pragma unroll
      for (int k = 0; k < 8; ++k) c[k] = pk2bf(acc[k & 2][2 * u + (k & 1) + (k >> 2) * 2], acc[(k & 2) + 1][2 * u + (k & 1) + (k >> 2) * 2]);
      SB();
#pragma unroll
      for (int k = 0; k < 2; ++k) {
        const auto r0 = __builtin_amdgcn_permlane16_swap(c[4 * k], c[4 * k + 1], false, false);
        const auto r1 = __builtin_amdgcn_permlane16_swap(c[4 * k + 2], c[4 * k + 3], false, false);
        st128(rs, eg[u + k], u32x4{r0[0], r1[0], r0[1], r1[1]});
      }
      SB();
    }
    STAMP(31);
    if (!isS0) {
#pragma unroll
      for (int i = 0; i < 8; ++i) *(u32x4*)(lds + lw * PS + 16384 + off_b(4 * i + g4, col)) = wv[i];
    }
    {
      int fo = lane < 16 ? (WS_LSE + il(sj * 512 + lw * 128 + (lane & 7) * 16, lane >> 3)) ^ gx : WS_TOTAL;
      lds_spin(ctr + C_ML, 1u);
      u32x4 ml4 = *(const u32x4*)(lds + L_WMAX + col * 16);
      PIN(fo); PIN(ml4);
      waitvm<0>();
      __builtin_amdgcn_s_waitcnt(0x0F70);
      st128(rs, fo, ml4);
      lds_add(lds, C_PD, lane);
    }
    STAMP(13);
  } else if (!isQ) {
    if (isS0) {
      u32 ta = L_QOFF + 8192 + (8 * g4 + tq) * 128 + w * 32 + tp * 8;
      u32 sa = L_H1RED + w * 256;
      int so = col < 2 ? WS_SC + (((sj >> 5) * 4 + (sj & 3)) * 512 + ((sj >> 2) & 7) * 64 + 16 * w + 4 * g4 + 2 * col) * 8 : WS_TOTAL;
      PIN(ta); PIN(so); PIN(sa);
      u32 c2, e0, e1;
      asm volatile("s_waitcnt vmcnt(4) ; HEAD_WAIT\n v_mov_b32 %0, %3\n v_mov_b32 %1, %4\n v_mov_b32 %2, %5" : "=&v"(c2), "=&v"(e0), "=&v"(e1) : "v"(xm), "v"(ev.x), "v"(ev.y) : "memory");
      TAG_OF(e0, e1);
      bf16x8 sf[4];
      u32x2 wt[8];
      asm volatile("ds_write_b32 %4, %5\n"
                   "ds_read_b128 %0, %6\n ds_read_b128 %1, %6 offset:64\n ds_read_b128 %2, %6 offset:128\n ds_read_b128 %3, %6 offset:192\n s_waitcnt lgkmcnt(0)"
                   : "=&v"(sf[0]), "=&v"(sf[1]), "=&v"(sf[2]), "=&v"(sf[3])
                   : "v"(sa + lane * 4), "v"(pk2bf(silu_bf(lo16(c2)), silu_bf(hi16(c2)))), "v"(sa + 16 * g4) : "memory");
      waitvm<0>();
      lds_add(lds, C_BH, lane);
      lds_spin(ctr + C_BH, 4u);
      STAMP(1);
      asm volatile("ds_read_b64_tr_b16 %0, %8\n ds_read_b64_tr_b16 %1, %8 offset:512\n ds_read_b64_tr_b16 %2, %8 offset:4096\n ds_read_b64_tr_b16 %3, %8 offset:4608\n"
                   "ds_read_b64_tr_b16 %4, %8 offset:8192\n ds_read_b64_tr_b16 %5, %8 offset:8704\n ds_read_b64_tr_b16 %6, %8 offset:12288\n ds_read_b64_tr_b16 %7, %8 offset:12800\n"
                   "s_waitcnt lgkmcnt(0)"
                   : "=&v"(wt[0]), "=&v"(wt[1]), "=&v"(wt[2]), "=&v"(wt[3]), "=&v"(wt[4]), "=&v"(wt[5]), "=&v"(wt[6]), "=&v"(wt[7]) : "v"(ta) : "memory");
      f32x4 d = {0.f, 0.f, 0.f, 0.f};
#pragma unroll
      for (int k = 0; k < 4; ++k)
        d = __builtin_amdgcn_mfma_f32_16x16x32_bf16(__builtin_bit_cast(bf16x8, u32x4{wt[2 * k].x, wt[2 * k].y, wt[2 * k + 1].x, wt[2 * k + 1].y}), sf[k], d, 0, 0, 0);
      st_gran2(rs, so, (col & 1) ? d[2] : d[0], (col & 1) ? d[3] : d[1], tlo, thi);
      lds_add(lds, C_H1, lane);
      STAMP(2);
      if (SCP_T) { until(t0, SCP_T); dma_sc1(L_P + w * 1024, WS_SC + ((ii & 7) * 4 + w) * 1024 + lane * 16, ws); }
    }
    u32 ab = off_b(col, 4 * g4), qv = WS_QABS + myh * 8192 + g4 * 1024 + w * 256 + ((col ^ (g4 << 2)) << 4);
    const int hb = 16 * (w & 1) + col;
    u32 pw = L_P + (g4 >> 1) * 512 + hb * 16 + (g4 & 1) * 8, mw = hb * 4;
    PIN(ab); PIN(qv); PIN(pw); PIN(mw);
    if (w == 2) {
      until(t0, QF_T);
      const u32x2 e2 = ld64(rs, WS_EPOCH);
      u32x2 f2 = ld64(rs, WS_SFLAG + xc * 2048 + lane * 8);
      TAG_OF(e2.x, e2.y);
      S_POLL(f2);
      lds_add(lds, C_SOK, lane);
      STAMP(3);
    }
    bf16x8 af[16], qf[16][2];
    u32 aa[16], qa[32];
#pragma unroll
    for (int i = 0; i < 16; ++i) {
      aa[i] = (ab ^ ((i & 3) << 4)) + (((w + (i >> 2)) & 3) * PS + w * 4096); PIN(aa[i]);
#pragma unroll
      for (int u = 0; u < 2; ++u) { qa[2 * i + u] = (ab ^ ((i & 3) << 4)) + (L_QI + ((w + (i >> 2)) & 3) * 8192 + ((u ^ w) & 1) * 4096); PIN(qa[2 * i + u]); }
    }
    const int sl1 = w == 3 ? 0 : 4 + w, sl2 = 1 + (w >> 1);
#define A_RD(i, o) af[i] = *(const bf16x8*)(lds + aa[i] + (o))
#define A_READ(i0, i1, o) _Pragma("unroll") for (int i = (i0); i < (i1); ++i) A_RD(i, o)
#define A_MOVE(i0, i1, d) _Pragma("unroll") for (int i = (i0); i < (i1); ++i) { aa[i] += (d) * 4096; PIN(aa[i]); }
#define Q_RD(i, u) qf[i][u] = *(const bf16x8*)(lds + qa[2 * (i) + (u)])
#define A_FIRST() do { A_READ(0, 16, 0); lds_add(lds, C_FREE, lane); _Pragma("unroll") for (int i = 0; i < 16; ++i) PIN(af[i]); STAMP(12); } while (0)
    bool have = false;
    _Pragma("unroll 1") while ((u32)__builtin_amdgcn_s_memrealtime() - t0 < E3_T) {
      if (__builtin_amdgcn_readfirstlane(ctr[C_MK + w]) >= 4u) { have = true; break; }
      __builtin_amdgcn_s_sleep(1);
    }
    asm volatile("" ::: "memory");
    if (have) A_FIRST();
    lds_wait(ctr + C_SOK, 1u);
    until(t0, E3_T);
    STAMP(6);
#define Q_DMA() _Pragma("unroll") for (int i = 0; i < 8; ++i) dma_sc1(L_QI + w * 8192 + i * 1024, (qv ^ ((i & 3) << 4)) + (i >> 1) * 16384 + (i & 1) * 4096, ws)
    Q_DMA();
    if (RARE(!have)) { lds_wait(ctr + C_MK + w, 4u); A_FIRST(); }
    A_MOVE(0, 16, sl1 - w);
    u32 cb = L_CTR, cw = L_CTR + 4 * w, mx0 = mw + L_MX, mx1 = (mw ^ 64) + L_MX, pw0 = pw + w * 1024, pw1 = (pw ^ 256) + w * 1024;
    float ninf = -__builtin_inff();
    u32 one = 1u;
    PIN(ninf); PIN(one); PIN(cb); PIN(cw); PIN(mx0); PIN(mx1); PIN(pw0); PIN(pw1);
#define CTS(b, i) (*(volatile LAS u32*)(lds + (b) + 4 * (i)))
    _Pragma("unroll 1") for (u32 spins = 0; spins < SPIN_CAP; ++spins) {
      waitvm<0>();
#pragma unroll
      for (int i = 0; i < 8; ++i) Q_RD(i >> 1, i & 1);
      u32 mxu[2] = {0u, 0u};
#pragma unroll
      for (int i = 0; i < 8; ++i) { const u32x4 t_ = __builtin_bit_cast(u32x4, qf[i >> 1][i & 1]); mxu[i & 1] = max(max(mxu[i & 1], t_.x), t_.y); mxu[i & 1] = max(max(mxu[i & 1], t_.z), t_.w); }
      if (!RARE(__any(max(mxu[0], mxu[1]) == POISON))) break;
      STAMP(29);
      __builtin_amdgcn_s_sleep(Q_SLP);
      const u64 b0 = __ballot(mxu[w & 1] == POISON), b1 = __ballot(mxu[~w & 1] == POISON);
#pragma unroll 1
      for (int i = 0; i < 8; ++i)
        if (((i < 4 ? b0 : b1) >> (4 * (i & 3))) & 0x000F000F000F000Full) dma_sc1(L_QI + w * 8192 + i * 1024, (qv ^ ((i & 3) << 4)) + (i >> 1) * 16384 + (i & 1) * 4096, ws);
    }
    lds_add(lds, C_QRDY, lane);
    STAMP(7);
    f32x4 sc[2], sn[2];
    sc[0] = sc[1] = sn[0] = sn[1] = f32x4{0.f, 0.f, 0.f, 0.f};
#define QK(d, i0, i1, NU)                                                                                                \
  do {                                                                                                                   \
    SB();                                                                                                                \
    _Pragma("unroll") for (int i = (i0); i < (i1); ++i) _Pragma("unroll") for (int u = 0; u < (NU); ++u)                 \
      d[u] = __builtin_amdgcn_mfma_f32_16x16x32_bf16(af[i], qf[i][u], d[u], 0, 0, 0);                                    \
    SB();                                                                                                                \
  } while (0)
#define MF(d, i, u) do { SB(); d[u] = __builtin_amdgcn_mfma_f32_16x16x32_bf16(af[i], qf[i][u], d[u], 0, 0, 0); SB(); } while (0)
    const bool idle1 = isS0 && w == 0;
    u32 c1;
    {
      const u32 c0 = CTS(cb, C_QRDY);
      QK(sc, 0, 2, 2);
      if (RARE(__builtin_amdgcn_readfirstlane(c0) < 4u)) lds_spin(ctr + C_QRDY, 4u);
      asm volatile("" ::: "memory");
#pragma unroll
      for (int i = 2; i < 4; ++i) { MF(sc, i, 0); Q_RD(2 * i, 0); Q_RD(2 * i, 1); MF(sc, i, 1); Q_RD(2 * i + 1, 0); Q_RD(2 * i + 1, 1); }
#pragma unroll
      for (int i = 4; i < 12; ++i) { MF(sc, i, 0); Q_RD(i + 4, 0); MF(sc, i, 1); Q_RD(i + 4, 1); }
      c1 = CTS(cw, C_MK + 4);
      asm volatile("" ::: "memory");
#pragma unroll
      for (int i = 12; i < 16; ++i) { MF(sc, i, 0); A_RD(2 * i - 24, 0); MF(sc, i, 1); A_RD(2 * i - 23, 0); }
    }
    STAMP(9);
    CY(0);
    float mref[2], lsum[2] = {0.f, 0.f};
#define ADDC(i, v) do { SB(); asm volatile("s_mov_b64 exec, 1\n ds_add_u32 %0, %1 offset:%2\n s_mov_b64 exec, -1" ::"v"(cb), "v"(v), "n"(4 * (i)) : "memory"); SB(); } while (0)
#define SM(d, T, NU, G, P0, P1)                                                                                          \
  do {                                                                                                                   \
    SB();                                                                                                                \
    if (RARE(__any(max5(d[0], (NU) > 1 ? max5(d[1], ninf) : ninf) > TAU))) {                                             \
      sig = 257u;                                                                                                        \
      _Pragma("unroll") for (int u = 0; u < (NU); ++u) {                                                                 \
        const float up = ceilf(x32_max(x16_max(max5(d[u], 0.f))));                                                       \
        lsum[u] *= __builtin_amdgcn_exp2f(-up);                                                                          \
        mref[u] += up;                                                                                                   \
        d[u] -= up;                                                                                                      \
        *(float*)(lds + (mw ^ (u * 64)) + L_REF + (T) * 128) = mref[u];                                                  \
      }                                                                                                                  \
    }                                                                                                                    \
    u32x2 pk[2];                                                                                                         \
    _Pragma("unroll") for (int u = 0; u < (NU); ++u) {                                                                   \
      pk[u].x = pk2bf(__builtin_amdgcn_exp2f(d[u].x), __builtin_amdgcn_exp2f(d[u].y));                                   \
      pk[u].y = pk2bf(__builtin_amdgcn_exp2f(d[u].z), __builtin_amdgcn_exp2f(d[u].w));                                   \
      *(u32x2*)(lds + (u ? (P1) : (P0))) = pk[u];                                                                        \
    }                                                                                                                    \
    ADDC(C_P + (G), sig);                                                                                                \
    _Pragma("unroll") for (int u = 0; u < (NU); ++u) { lsum[u] = dot2(pk[u].x, 0x3F803F80u, dot2(pk[u].y, 0x3F803F80u, lsum[u])); PIN(lsum[u]); } \
    SB();                                                                                                                \
  } while (0)
#define REF_WRITE(T, NU) _Pragma("unroll") for (int u = 0; u < (NU); ++u) *(float*)(lds + (mw ^ (u * 64)) + L_REF + (T) * 128) = mref[u]
    u32 sig = one;
    {
      MF(sn, 0, 0); A_RD(8, 0); MF(sn, 0, 1);
      const float m0 = max5(sc[0], ninf), m1 = max5(sc[1], ninf);
      MF(sn, 1, 0);
      asm volatile("ds_max_f32 %0, %2\n ds_max_f32 %1, %3" ::"v"(mx0), "v"(mx1), "v"(m0), "v"(m1) : "memory");
      ADDC(C_MXC, one);
      MF(sn, 1, 1); A_RD(9, 0);
#pragma unroll
      for (int i = 2; i < 4; ++i) { MF(sn, i, 0); A_RD(2 * i + 6, 0); MF(sn, i, 1); A_RD(2 * i + 7, 0); }
      MF(sn, 4, 0);
      const u32 c0 = CTS(cb, C_MXC);
      mref[0] = *(volatile LAS float*)(lds + mx0);
      mref[1] = *(volatile LAS float*)(lds + mx1);
      MF(sn, 4, 1); MF(sn, 5, 0); A_RD(14, 0); MF(sn, 5, 1); A_RD(15, 0); MF(sn, 6, 0); MF(sn, 6, 1);
      if (RARE(__builtin_amdgcn_readfirstlane(c0) < 4u)) {
        lds_spin(ctr + C_MXC, 4u);
        mref[0] = *(volatile LAS float*)(lds + mx0);
        mref[1] = *(volatile LAS float*)(lds + mx1);
      }
      u32x2 pk[2];
#pragma unroll
      for (int u = 0; u < 2; ++u) {
        MF(sn, 7 + 2 * u, 0);
        mref[u] = ceilf(mref[u]);
        float e0 = __builtin_amdgcn_exp2f(sc[u].x - mref[u]); PIN(e0);
        MF(sn, 7 + 2 * u, 1);
        pk[u].x = pk2bf(e0, __builtin_amdgcn_exp2f(sc[u].y - mref[u])); PIN(pk[u].x);
        MF(sn, 8 + 2 * u, 0);
        e0 = __builtin_amdgcn_exp2f(sc[u].z - mref[u]); PIN(e0);
        MF(sn, 8 + 2 * u, 1);
        pk[u].y = pk2bf(e0, __builtin_amdgcn_exp2f(sc[u].w - mref[u]));
        *(u32x2*)(lds + (u ? pw1 : pw0)) = pk[u];
      }
      ADDC(C_P, one);
      MF(sn, 11, 0);
      lsum[0] = dot2(pk[0].x, 0x3F803F80u, dot2(pk[0].y, 0x3F803F80u, 0.f)); PIN(lsum[0]);
      MF(sn, 11, 1);
      lsum[1] = dot2(pk[1].x, 0x3F803F80u, dot2(pk[1].y, 0x3F803F80u, 0.f)); PIN(lsum[1]);
      MF(sn, 12, 0);
      REF_WRITE(4 + w, 2);
      MF(sn, 12, 1);
      QK(sn, 13, 16, 2);
    }
    STAMP(10);
    CY(1);
#define QK1(N0, slot)                                                                                                    \
  do {                                                                                                                   \
    _Pragma("unroll") for (int i = 0; i < 16; ++i) { MF(sc, i, 0); if (i + (N0) < 16) A_RD(i + (N0), slot); }            \
  } while (0)
    if (RARE(__builtin_amdgcn_readfirstlane(c1) < 4u && !idle1)) {
      lds_spin(ctr + C_MK + 4 + w, 4u);
      A_READ(0, 16, 0);
      sn[0] = sn[1] = f32x4{0.f, 0.f, 0.f, 0.f};
      QK(sn, 0, 16, 2);
    }
    if (RARE(idle1)) lds_add(lds, C_P + 1, lane);
    else { sn[0] -= mref[0]; sn[1] -= mref[1]; SM(sn, 4 + w, 2, 1, pw0 + 4096, pw1 + 4096); }
    CY(2);
    if (RARE(w == 2)) {
      RELANE();
      dma_sc1(L_QI, WS_VOTE + xc * 2048 + lane * 16, ws);
    }
    REF_WRITE(7 + sl2, 1);
    A_MOVE(0, 16, sl2 - sl1);
    pw0 = pw + sl2 * 1024;
    sc[0] = f32x4{-mref[0], -mref[0], -mref[0], -mref[0]};
    PIN(pw0); PIN(sc[0]);
    CY(3);
    lds_spin(ctr + C_MK + 7 + sl2, 4u);
    CY(4);
    A_READ(0, 6, 0);
    STAMP(4);
    QK1(6, 0);
    CY(5);
    SM(sc, 7 + sl2, 1, 2, pw0 + 7168, 0);
    CY(6);
    STAMP(11);
    if (!RARE(w >= 2)) {
      REF_WRITE(10, 1);
      sc[0] = f32x4{-mref[0], -mref[0], -mref[0], -mref[0]};
      PIN(sc[0]);
      _Pragma("unroll 1") for (u32 n_ = 0; n_ < LSPIN_CAP; ++n_) {
        const u32 k_ = CTS(cb, C_MK + 10);
        asm volatile("" ::: "memory");
        A_READ(0, 6, 8192);
        SB();
        if (__builtin_amdgcn_readfirstlane(k_) >= 4u) break;
      }
      STAMP(5);
      QK1(6, 8192);
      SM(sc, 10, 1, 3, pw0 + 9216, 0);
    } else if (w == 2) {
      RELANE();
      waitvm<0>();
      COUNT_VOTES(true);
      lds_add(lds, C_GX, lane);
    }
    STAMP(30);
    CYOUT();
    RELANE();
    {
#pragma unroll
      for (int u = 0; u < 2; ++u) {
        *(float*)(lds + L_ML + (mw ^ (u * 64)) * 4 + w * 4) = mref[u];
        *(float*)(lds + L_ML + 512 + (mw ^ (u * 64)) * 4 + w * 4) = x32_sum(x16_sum(lsum[u]));
      }
      u32 old;
      f32x4 m4, l4;
      asm volatile("s_mov_b64 exec, 1\n ds_add_rtn_u32 %0, %3, %4 offset:%6\n s_mov_b64 exec, -1\n ds_read_b128 %1, %5\n ds_read_b128 %2, %5 offset:512\n s_waitcnt lgkmcnt(0)"
                   : "=&v"(old), "=&v"(m4), "=&v"(l4) : "v"((u32)L_CTR), "v"(1u), "v"(L_ML + (lane & 31) * 16), "n"(4 * C_SD) : "memory");
      if (__builtin_amdgcn_readfirstlane(old) == 3u) {
        const float M = fmaxf(fmaxf(m4.x, m4.y), fmaxf(m4.z, m4.w));
        const float l = (l4.x * __builtin_amdgcn_exp2f(m4.x - M) + l4.y * __builtin_amdgcn_exp2f(m4.y - M)) + (l4.z * __builtin_amdgcn_exp2f(m4.z - M) + l4.w * __builtin_amdgcn_exp2f(m4.w - M));
        *(u32x2*)(lds + L_WMAX + (lane & 1) * 128 + (lane & 30) * 4) = u32x2{fbits(M), fbits(l)};
        lds_add(lds, C_ML, lane);
      }
    }
    lds_wait(ctr + C_GX, 1u);
    gx = __builtin_amdgcn_readfirstlane(ctr[C_PAR]);
    STAMP(13);
  } else {
    const int mu = 2 * j + (w >> 2);
#define Q_LOAD(i0, i1)                                                                                                   \
  _Pragma("unroll") for (int i = (i0); i < (i1); ++i) {                                                                  \
    waitvm<QW - 1>();                                                                                                    \
    dma<0, false>(w * 16384 + (i & 15) * 1024, (vq ^ ((i & 3) << 4)) + ((i < 8 ? 64 * w + 32 * dh : i < 16 ? 64 * w + 32 - 32 * dh : 256 * dh + 32 * w) + 4 * (i & 7)) * 8192, i < 16 ? w_q : w_uk); \
  }
    const u32 vq = g4 * 8192 + hq * 256 + ((col ^ (g4 << 2)) << 4);
    u32x4 xv;
    u32x2 e2;
    u32 x2_;
    asm volatile("global_load_dwordx4 %0, %3, %5\n global_load_dword %1, %4, %5\n buffer_load_dwordx2 %2, off, %6, 0 sc1"
                 : "=&v"(xv), "=&v"(x2_), "=&v"(e2) : "v"(lane * 16), "v"(128 * w + 4 * (lane & 31)), "s"(x), "s"(rs) : "memory");
    bf16x8 wf[16], wk[8];
    u32 ta0 = w * 16384 + off_b(8 * g4 + tq, tp >> 1) + 8 * (tp & 1), ta1 = w * 16384 + off_b(8 * g4 + 4 + tq, tp >> 1) + 8 * (tp & 1);
    int so = WS_SC + ((lane >> 5) * 2048 + 64 * w + 2 * (lane & 31)) * 8;
    int fo = w == 0 ? WS_SFLAG + xc * 2048 + lane * 8 : WS_TOTAL;
    PIN(ta0); PIN(ta1); PIN(so); PIN(fo);
    Q_LOAD(4, 8 + QW);
    waitvm<QW>();
#pragma unroll
    for (int c = 0; c < 8; ++c) wf[c] = tr_frag(lds, ta0 ^ (c << 5), ta1 ^ (c << 5));
    Q_LOAD(8 + QW, 16);
#pragma unroll
    for (int c = 0; c < 8; ++c) PIN(wf[c]);
    Q_LOAD(16, 24);
    STAMP(12);
    if (E1_BAR) BAR0();
    until(t0, E1_T);
    STAMP(23);
    u32x4 g[4];
    asm volatile("buffer_load_dwordx4 %0, %4, %5, 0 offen sc1\n buffer_load_dwordx4 %1, %4, %5, %6 offen sc1\n"
                 "buffer_load_dwordx4 %2, %4, %5, %7 offen sc1\n buffer_load_dwordx4 %3, %4, %5, %8 offen sc1"
                 : "=&v"(g[0]), "=&v"(g[1]), "=&v"(g[2]), "=&v"(g[3]) : "v"(so), "s"(rs), "s"(4096), "s"(8192), "s"(12288) : "memory");
    u32x2 fl;
    asm volatile("buffer_load_dwordx2 %0, %1, %2, 0 offen sc1" : "=&v"(fl) : "v"(fo), "s"(rs) : "memory");
    asm volatile("s_waitcnt vmcnt(13)" : "+v"(xv), "+v"(x2_), "+v"(e2)::"memory");
#pragma unroll
    for (int c = 0; c < 8; ++c) wf[8 + c] = tr_frag(lds, (ta0 ^ (c << 5)) + 8192, (ta1 ^ (c << 5)) + 8192);
    TAG_OF(e2.x, e2.y);
    float ln0, ln1;
    {
      float xe[8] = {lo16(xv.x), hi16(xv.x), lo16(xv.y), hi16(xv.y), lo16(xv.z), hi16(xv.z), lo16(xv.w), hi16(xv.w)};
      float s = 0.f;
#pragma unroll
      for (int k = 0; k < 8; ++k) s += xe[k];
      const float mean = wave_sum(s) * (1.f / 512.f);
      float d2 = 0.f;
#pragma unroll
      for (int k = 0; k < 8; ++k) d2 += (xe[k] - mean) * (xe[k] - mean);
      const float var = rbf(wave_sum(d2) * (1.f / 512.f));
      const float mb = rbf(mean);
      const float sd = rbf(sqrtf(rbf(var + 1e-5f)));
      ln0 = rbf(rbf(lo16(x2_) - mb) / sd);
      ln1 = rbf(rbf(hi16(x2_) - mb) / sd);
    }
    PIN(ln0); PIN(ln1);
    int qpo = (lane & 7) == 0 ? WS_QABS + il(hq * 1024 + dh * 512 + w * 64 + (lane >> 5) * 32 + (lane & 8) * 2, g4 & 1) : WS_TOTAL;
    u32 hw_ = L_H1RED + w * 128 + (lane & 31) * 4, hr_ = L_H1RED + w * 128 + 16 * g4 + 64 * dh, qw_ = L_H1RED + 1024 + w * 272 + lane * 2, qr_ = L_H1RED + 1024 + col * 272 + 64 * g4;
    PIN(qpo); PIN(hw_); PIN(hr_); PIN(qw_); PIN(qr_);
    waitvm<5>();
#pragma unroll
    for (int i = 0; i < 8; ++i) wk[i] = *(const bf16x8*)(lds + w * 16384 + (i >> 2) * 4096 + off_b(col, 4 * g4 + (i & 3)));
#pragma unroll
    for (int c = 0; c < 8; ++c) { PIN(wf[8 + c]); PIN(wk[c]); }
    STAMP(4);
    asm volatile("s_waitcnt vmcnt(0)" : "+v"(g[0]), "+v"(g[1]), "+v"(g[2]), "+v"(g[3]), "+v"(fl)::"memory");
    _Pragma("unroll 1") for (u32 spins = 0; spins < SPIN_CAP; ++spins) {
      bool ok = true;
#pragma unroll
      for (int k = 0; k < 4; ++k) ok = ok & (gran_ok(g[k].x, g[k].y, tlo, thi) & gran_ok(g[k].z, g[k].w, tlo, thi));
      if (!RARE(!__all(ok))) break;
      STAMP(29);
      __builtin_amdgcn_s_sleep(1);
      asm volatile("" ::: "memory");
#pragma unroll
      for (int k = 0; k < 4; ++k) g[k] = ld128(rs, so + k * 4096);
    }
    STAMP(1);
    {
      const float m0 = rbf((gran_val(g[0].x) + gran_val(g[1].x)) + (gran_val(g[2].x) + gran_val(g[3].x)));
      const float m1 = rbf((gran_val(g[0].z) + gran_val(g[1].z)) + (gran_val(g[2].z) + gran_val(g[3].z)));
      const auto s0 = __builtin_amdgcn_permlane32_swap(__float_as_uint(m0), __float_as_uint(m0), false, false);
      const auto s1 = __builtin_amdgcn_permlane32_swap(__float_as_uint(m1), __float_as_uint(m1), false, false);
      *(u32*)(lds + hw_) = pk2bf(rbf(ln0 * rbf(1.f + __uint_as_float(s0[1]))) + __uint_as_float(s0[0]),
                                 rbf(ln1 * rbf(1.f + __uint_as_float(s1[1]))) + __uint_as_float(s1[0]));
    }
    WAVE_LDS_SYNC();
    {
      const bf16x8 h0 = *(const bf16x8*)(lds + hr_), h1 = *(const bf16x8*)(lds + (hr_ ^ 64));
      f32x4 d[8];
#pragma unroll
      for (int c = 0; c < 8; ++c) d[c] = __builtin_amdgcn_mfma_f32_16x16x32_bf16(h0, wf[c], f32x4{0.f, 0.f, 0.f, 0.f}, 0, 0, 0);
#pragma unroll
      for (int c = 0; c < 8; ++c) d[c] = __builtin_amdgcn_mfma_f32_16x16x32_bf16(h1, wf[8 + c], d[c], 0, 0, 0);
#pragma unroll
      for (int k = 0; k < 2; ++k) {
        const float v = (g4 & 2) ? ((g4 & 1) ? d[4 * k + 3][0] : d[4 * k + 2][0]) : ((g4 & 1) ? d[4 * k + 1][0] : d[4 * k][0]);
        const u32 vh = f2bf(v);
        *(u16*)(lds + qw_ + 128 * k) = (u16)vh;
        *(u16*)(lds + qw_ + 128 * k + 8 * 272) = (u16)f2bf(v - bf2f(vh));
      }
    }
    STAMP(2);
    if (w == 0) S_POLL(fl);
    BAR();
    {
      bf16x8 a[4];
#pragma unroll
      for (int s = 0; s < 4; ++s) a[s] = *(const bf16x8*)(lds + qr_ + 16 * s);
      f32x4 d[2] = {f32x4{0.f, 0.f, 0.f, 0.f}, f32x4{0.f, 0.f, 0.f, 0.f}};
#pragma unroll
      for (int s = 0; s < 4; ++s)
#pragma unroll
        for (int c = 0; c < 2; ++c) d[c] = __builtin_amdgcn_mfma_f32_16x16x32_bf16(a[s], wk[4 * c + s], d[c], 0, 0, 0);
      constexpr float SCALE = 0.08838834764831845f * 1.4426950408889634f;
      const float v = SCALE * x16_sum(rs32(sum4(d[0]), sum4(d[1])));
      const float p0 = __uint_as_float(nopois(pk2bf(dppf<0x00>(v), dppf<0x55>(v)))), p1 = __uint_as_float(nopois(pk2bf(dppf<0xAA>(v), dppf<0xFF>(v))));
      st128(rs, qpo, u32x4{__float_as_uint(p0), __float_as_uint(p1), __float_as_uint(dppf<0x104>(p0)), __float_as_uint(dppf<0x104>(p1))});
    }
    STAMP(5);
    SB();
    {
      st128(rs, lane < 50 ? WS_PCTX + (j * 8 + w) * 800 + lane * 16 : WS_TOTAL, P4);
      const int pp = __builtin_amdgcn_readfirstlane(WS_PROBE + il((j * 8 + w) * 128, w & 1));
      u64 ta, tb, tc;
      u32 dm;
      asm volatile("s_load_dword %3, %4, 0x0\n s_waitcnt lgkmcnt(0)\n"
                   "s_memtime %0\n s_waitcnt lgkmcnt(0)\n s_load_dword %3, %4, %5 glc\n s_waitcnt lgkmcnt(0)\n"
                   "s_memtime %1\n s_waitcnt lgkmcnt(0)\n s_load_dword %3, %4, %6 glc\n s_waitcnt lgkmcnt(0)\n"
                   "s_memtime %2\n s_waitcnt vmcnt(0) lgkmcnt(0)"
                   : "=&s"(ta), "=&s"(tb), "=&s"(tc), "=&s"(dm) : "s"(ws), "s"(pp), "s"(pp ^ 8192) : "memory");
      int df = min(max(((int)(u32)(tb - ta) - (int)(u32)(tc - tb)) >> 3, -VCLIP), VCLIP);
      df = ((w ^ myh) & 1) ? -df : df;
      u32 old = 0u;
      if (lane == 0) old = __hip_atomic_fetch_add((LAS u32*)(lds + L_CTR) + C_VOTE, (u32)(65536 + 64 + df), __ATOMIC_RELAXED, __HIP_MEMORY_SCOPE_WORKGROUP);
      old = __builtin_amdgcn_readfirstlane(old) + 65536 + 64 + df;
      if (old >= 524288u && lane < 8) st64(rs, WS_VOTE + lane * 2048 + j * 8, (old & 1023u) | (tlo << 24), (tlo >> 8) | (thi << 24));
      STAMP(6);
    }
    until(t0, MB_T);
    {
      const int r0 = 128 * (mu & 3) + 32 * (w & 3) + 4 * (lane >> 3);
      const u32x2 cvb = *(const u32x2*)(cvec + r0);
      u32x4 wsc[4];
#pragma unroll
      for (int i = 0; i < 4; ++i) wsc[i] = ldnt(w_mod + (size_t)(r0 + i) * 3072 + 1024 + (mu >> 2) * 64 + (lane & 7) * 8);
      float a[8];
#pragma unroll
      for (int k = 0; k < 8; ++k) a[k] = 0.f;
#pragma unroll
      for (int i = 0; i < 4; ++i) fma8b(a, f2bf(silu_bf((i & 1) ? hi16(i < 2 ? cvb.x : cvb.y) : lo16(i < 2 ? cvb.x : cvb.y))), wsc[i]);
      float r[4];
#pragma unroll
      for (int k = 0; k < 4; ++k) r[k] = rs32(a[k], a[k + 4]);
      const float o0 = b3_sum(rs16(r[0], r[2])), o1 = b3_sum(rs16(r[1], r[3]));
      float* red = (float*)(lds + L_H1RED + 8192);
      if ((lane & 8) == 0) *(f32x2*)(red + w * 64 + (lane & 7) * 8 + (lane >> 5) * 4 + ((lane >> 4) & 1) * 2) = f32x2{o0, o1};
      BAR();
      if ((w & 3) == 3 && lane < 16) {
        red += (w >> 2) * 256 + lane * 4;
        const f32x4 sm = (*(const f32x4*)(red) + *(const f32x4*)(red + 64)) + (*(const f32x4*)(red + 128) + *(const f32x4*)(red + 192));
        st128(rs, WS_MODB + ((mu & 3) * 2048 + (mu >> 2) * 64 + lane * 4) * 4, u32x4{fbits(sm.x), fbits(sm.y), fbits(sm.z), fbits(sm.w)});
      }
    }
    gx = 0;
  }

  RELANE();
  const int r16 = lane >> 2, ch = lane & 3;
  if (ii >= 16) {
    int mo = lane < 24 ? (WS_LSE + il((24 * w + lane) * 512 + c4 * 128 + (h >> 1) * 8, h & 1)) ^ gx : WS_TOTAL;
    PIN(mo);
    lds_spin(ctr + C_PD, 4u);
    STAMP(23);
    u32x2 mt = ld64(rs, mo);
    SB();
    const u32 pb = (w >> 1) * PS + (w & 1) * 6144;
    u32 po[6];
#pragma unroll
    for (int k = 0; k < 6; ++k) {
      po[k] = g4 * 256 + ((col ^ (g4 << 2) ^ (k & 3)) << 4) + ((WS_PART + il21(((h >> 1) * 4 + c4) * 65536 + (24 * w + 4 * k) * 256, h & 1)) ^ gx);
      PIN(po[k]);
    }
    bf16x8 wk[4];
    {
      const u32 wa = tq * PS + 16384 + off_b(g4, 2 * w + (tp >> 1)) + 8 * (tp & 1);
#pragma unroll
      for (int s = 0; s < 4; ++s) wk[s] = tr_frag(lds, (wa ^ (s << 4)) + s * 1024, (wa ^ (s << 4)) + s * 1024 + 4096);
    }
    SB();
    u32 done = 0u;
    _Pragma("unroll 1") for (u32 spins = 0; spins < SPIN_CAP; ++spins) {
      asm volatile("" ::: "memory");
      if (spins && max(mt.x, mt.y) == POISON) mt = ld64(rs, mo);
      const u32 ok = ~(u32)__ballot(max(mt.x, mt.y) == POISON);
      u32 nw = ok & (ok >> 1);
      nw = nw & (nw >> 2) & 0x111111u & ~done;
      done |= nw;
#pragma unroll
      for (int k = 0; k < 6; ++k)
        if (nw & (1u << (4 * k))) dma_sc1(pb + k * 1024, po[k], ws);
      if (spins == 0) STAMP(21);
      if (done == 0x111111u) break;
      const int miss = __popc(~ok & 0xFFFFFFu);
      if (miss > 8) __builtin_amdgcn_s_sleep(E4_S2);
      else if (miss > 2) __builtin_amdgcn_s_sleep(E4_S1);
      else __builtin_amdgcn_s_sleep(E4_S0);
    }
    STAMP(16);
    u32 pa[16];
#pragma unroll
    for (int i = 0; i < 16; ++i) { pa[i] = pb + off_b(8 * (g4 % 3) + 4 * (i & 1) + tq, (i & 14) + (tp >> 1)) + 8 * (tp & 1); PIN(pa[i]); }
    u32 bo = L_QOFF + col * 272 + g4 * 16, mlo = L_QOFF + 4352 + (col & 7) * 8, ro = L_QOFF + (w + (col & 8)) * 272 + (col & 7) * 32 + g4 * 8;
    int so1 = col < 2 ? (WS_PCTX + h * 2048 + c4 * 512 + (w >> 2) * 256 + (2 * (w & 1) + (g4 >> 1)) * 64 + (((w >> 1) & 1) * 2 + col) * 16 + (g4 & 1) * 8) ^ gx : WS_TOTAL;
    PIN(bo); PIN(mlo); PIN(ro); PIN(so1);
    {
      const u32 eo = L_QOFF + 4416 + w * 64;
      const float m = lane < 24 ? __uint_as_float(mt.x) : -__builtin_inff();
      const float M = wave_max(m);
      const float e = __builtin_amdgcn_exp2f(m - M);
      if (lane < 32) *(u16*)(lds + eo + lane * 2) = (u16)f2bf(e);
      WAVE_LDS_SYNC();
      const u32x4 ev4 = *(const u32x4*)(lds + eo + 16 * g4);
      const float L = wave_sum(e * __uint_as_float(mt.y));
      if (lane == 0) *(f32x2*)(lds + L_QOFF + 4352 + w * 8) = f32x2{M, L};
      bf16x8 wf[8], pt[8];
#pragma unroll
      for (int i = 0; i < 8; ++i) { wf[i] = __builtin_bit_cast(bf16x8, (col & 7) == i ? ev4 : Z4); PIN(wf[i]); }
      waitvm<0>();
      STAMP(17);
#pragma unroll
      for (int i = 0; i < 8; ++i) pt[i] = tr_frag(lds, pa[2 * i], pa[2 * i + 1]);
      SB();
      f32x4 d = {0.f, 0.f, 0.f, 0.f};
#pragma unroll
      for (int i = 0; i < 8; ++i) d = __builtin_amdgcn_mfma_f32_16x16x32_bf16(pt[i], wf[i], d, 0, 0, 0);
      const u32 h0 = pk2bf(d[0], d[1]), h1 = pk2bf(d[2], d[3]);
      const u32 l0 = pk2bf(d[0] - lo16(h0), d[1] - hi16(h0)), l1 = pk2bf(d[2] - lo16(h1), d[3] - hi16(h1));
      *(u32x2*)(lds + ro) = (col & 8) ? u32x2{l0, l1} : u32x2{h0, h1};
    }
    BAR();
    STAMP(14);
    const f32x2 ml = *(const f32x2*)(lds + mlo);
    bf16x8 lf[4];
#pragma unroll
    for (int s = 0; s < 4; ++s) lf[s] = *(const bf16x8*)(lds + bo + 64 * s);
    SB();
    f32x4 d = {0.f, 0.f, 0.f, 0.f};
#pragma unroll
    for (int s = 0; s < 4; ++s) d = __builtin_amdgcn_mfma_f32_16x16x32_bf16(wk[s], lf[s], d, 0, 0, 0);
    SB();
    float Mg = fmaxf(ml.x, dppf<0xB1>(ml.x));
    Mg = fmaxf(Mg, dppf<0x4E>(Mg)); Mg = fmaxf(Mg, dppf<0x141>(Mg));
    const float e = __builtin_amdgcn_exp2f(ml.x - Mg);
    float Lg = quad_sum(e * ml.y);
    Lg += dppf<0x141>(Lg);
    const float sc = e * __builtin_amdgcn_rcpf(Lg);
    {
      float d0 = d[0] * sc, d1 = d[1] * sc, d2 = d[2] * sc, d3 = d[3] * sc;
      row16_sum4(d0, d1, d2, d3);
      d = f32x4{d0, d1, d2, d3};
    }
    const u32 h0 = pk2bf(d[0], d[1]), h1 = pk2bf(d[2], d[3]);
    const u32 l0 = pk2bf(d[0] - lo16(h0), d[1] - hi16(h0)), l1 = pk2bf(d[2] - lo16(h1), d[3] - hi16(h1));
    st64(rs, so1, nopois(col ? l0 : h0), nopois(col ? l1 : h1));
    STAMP(15);
    STAMP_FLUSH();
    return;
  }
  if (tl2) {
    BAR0();
    until(t0, WO_T);
#pragma unroll
    for (int i = 0; i < 8; ++i) dma<0, WNT>(owno + i * 1024, (ks * 512 + 64 * w + 8 * i + (lane >> 3)) * 1024 + cg * 128 + (lane & 7) * 16, w_o);
    waitvm<0>();
    STAMP(26);
    bf16x8 wf[8];
#pragma unroll
    for (int i = 0; i < 8; ++i) {
      const u32 a = owno + (32 * (i >> 2) + 8 * g4 + tq) * 128 + (i & 3) * 32 + tp * 8;
      wf[i] = tr_frag(lds, a, a + 512);
    }
#pragma unroll
    for (int i = 0; i < 8; ++i) PIN(wf[i]);
    STAMP(23);
    int off[1] = {(WS_PCTX + (ks * 4 + (w >> 1)) * 2048 + tq * 512 + (w & 1) * 256 + g4 * 64 + tp * 16) ^ gx};
    u32 rw = L_RED + lane * 32 + w * 4;
    PIN(rw);
    int so2 = tp < 2 ? WS_PATTN + il((ks * 512 + cg * 64 + (lane & 60)) * 4, tp) : WS_TOTAL;
    PIN(so2);
    u32x4 g[1];
    until(t0, E5_T);
    gather128<1, E5_S>(rs, off, g);
    STAMP(24);
    const bf16x8 a0 = __builtin_bit_cast(bf16x8, (col & 2) ? Z4 : g[0]), a1 = __builtin_bit_cast(bf16x8, (col & 2) ? g[0] : Z4);
    f32x4 d[4];
#pragma unroll
    for (int c = 0; c < 4; ++c) d[c] = __builtin_amdgcn_mfma_f32_16x16x32_bf16(a0, wf[c], f32x4{0.f, 0.f, 0.f, 0.f}, 0, 0, 0);
#pragma unroll
    for (int c = 0; c < 4; ++c) d[c] = __builtin_amdgcn_mfma_f32_16x16x32_bf16(a1, wf[4 + c], d[c], 0, 0, 0);
    *(float*)(lds + rw) = rs16(rs32(sum4(d[0]), sum4(d[2])), rs32(sum4(d[1]), sum4(d[3])));
    BAR();
    if (w == 0) {
      const float v = sum4(*(const f32x4*)(lds + L_RED + lane * 32) + *(const f32x4*)(lds + L_RED + lane * 32 + 16));
      const float vn = __uint_as_float(fbits(v));
      st128(rs, so2, u32x4{__float_as_uint(dppf<0x00>(vn)), __float_as_uint(dppf<0x55>(vn)), __float_as_uint(dppf<0xAA>(vn)), __float_as_uint(dppf<0xFF>(vn))});
    }
    STAMP(17);
  } else if (t3 >= 0) {
    STAMP(20);
    if (w < 4) {
      const int fr = ((128 * w + r16) * DF + t3 * 32) * 2, ca_ = ch ^ ((r16 >> 2) & 2) ^ (t3 & 1);
      const u16* wa = (t3 & 1) ? w_up : w_gate;
      const u16* wb = (t3 & 1) ? w_gate : w_up;
      const u32 d = w * 32768 + 8192;
      until(t0, GU_T);
#pragma unroll
      for (int i = 0; i < 8; ++i) { waitvm<WING - 1>(); dma<0, GUNT>(d + (i >> 2) * 12288 + (t3 & 1) * 4096 + (i & 3) * 1024, fr + ca_ * 16 + 32 * i * DF, wa); }
#pragma unroll
      for (int i = 0; i < 8; ++i) { waitvm<WING - 1>(); dma<0, GUNT>(d + (i >> 2) * 12288 + 4096 - (t3 & 1) * 4096 + (i & 3) * 1024, fr + (ca_ ^ 1) * 16 + 32 * i * DF, wb); }
    } else {
      until(t0, DN_T);
#pragma unroll
      for (int i = 0; i < 8; ++i) {
        waitvm<WING - 1>();
        dma<0, WNT>(lw * 32768 + 16384 + (i >> 2) * 12288 + (i & 3) * 1024, (32 * t3 + 8 * (i & 3) + (lane >> 3)) * 1024 + 256 * lw + 128 * (i >> 2) + (lane & 7) * 16, w_down);
      }
      const u32 x2 = *(const u32*)(x + 128 * lw + 2 * lane);
      u32x4 m6[6];
      int mo[6];
#pragma unroll
      for (int i = 0; i < 6; ++i) mo[i] = MB_OFF(i);
      until(t0, MB_T + 200);
      gather128<6, 8>(rs, mo, m6);
#pragma unroll
      for (int i = 0; i < 6; ++i) {
        const f32x4 f = __builtin_bit_cast(f32x4, m6[i]);
        const float v = rbf(rs16(rs32(f.x, f.z), rs32(f.y, f.w)));
        *(u16*)(lds + L_Z + (i < 2 ? 5 : i < 4 ? 3 : 2) * 1088 + (128 * lw + 64 * (i & 1) + 4 * col + g4) * 2) = (u16)f2bf(i < 4 ? v : 1.f + v);
      }
      *(u32*)(lds + L_Z + 6 * 1088 + lw * 256 + lane * 4) = x2;
      if (w == 4 && lane < 32) *(u32*)(lds + L_ZERO + lane * 4) = lane < 16 ? 0u : POISON;
      if (w == 6) COUNT_VOTES(false);
    }
    waitvm<0>();
    STAMP(26);
    BAR();
    gx = __builtin_amdgcn_readfirstlane(ctr[C_PAR]);
    u32 zw = L_Z + (64 * w + 4 * col + g4) * 2;
    u32 hr[4];
#pragma unroll
    for (int i = 0; i < 4; ++i) { hr[i] = (col == i ? L_HH : L_ZERO) + 16 * g4; PIN(hr[i]); }
    int off[2];
#pragma unroll
    for (int i = 0; i < 2; ++i) { off[i] = (WS_PATTN + il(((4 * i + g4) * 512 + 64 * w + col * 4) * 4, myh)) ^ gx; PIN(off[i]); }
    int so = col < 4 ? (WS_PDOWN + il((t3 * 256 + 64 * (w & 3) + 16 * col + 4 * g4) * 4, w >> 2)) ^ gx : WS_TOTAL;
    u32 zr = L_Z + tp * 1088 + 16 * g4, yr = L_Z + 4 * 1088 + lane * 16, hw = L_HH + (w & 3) * 16 + (g4 & 1) * 8 + tp * 2, cr = L_CF + (w & 3) * 16 + tp * 4;
    PIN(cr); PIN(so); PIN(zw); PIN(zr); PIN(yr); PIN(hw);
    float ga = bf2f(*(const u16*)(lds + zw + 5 * 1088)), cb1 = bf2f(*(const u16*)(lds + zw + 2 * 1088)), xe = bf2f(*(const u16*)(lds + zw + 6 * 1088));
    bf16x8 wf[16], wd[4];
    if (w < 4) {
#pragma unroll
      for (int k = 0; k < 16; ++k) {
        const u32 a = (k >> 2) * 32768 + 8192 + ((k >> 1) & 1) * 12288 + (tp >> 1) * 4096 + ((k & 1) * 32 + 8 * g4 + tq) * 64 + (w ^ ((g4 & 1) * 2) ^ (tp >> 1)) * 16 + (tp & 1) * 8;
        wf[k] = tr_frag(lds, a, a + 256);
      }
#pragma unroll
      for (int k = 0; k < 16; ++k) PIN(wf[k]);
    }
#pragma unroll
    for (int i = 0; i < 4; ++i) {
      const u32 a = owno + 8192 + (8 * g4 + tq) * 128 + i * 32 + tp * 8;
      wd[i] = tr_frag(lds, a, a + 512);
    }
#pragma unroll
    for (int i = 0; i < 4; ++i) PIN(wd[i]);
    PIN(ga); PIN(cb1); PIN(xe);
    STAMP(23);
    until(t0, E6_T);
    u32x4 pa2[2];
    gather128<2, E6_S>(rs, off, pa2);
    STAMP(18);
    {
      const f32x4 s4 = __builtin_bit_cast(f32x4, pa2[0]) + __builtin_bit_cast(f32x4, pa2[1]);
      const u32 yb = f2bf(xe + rbf(ga * rbf(rs16(rs32(s4.x, s4.z), rs32(s4.y, s4.w)))));
      const float z = bf2f(yb) * cb1;
      const u32 zh = f2bf(z);
      *(u16*)(lds + zw + 4 * 1088) = (u16)yb;
      *(u16*)(lds + zw) = (u16)zh;
      *(u16*)(lds + zw + 1088) = (u16)f2bf(z - bf2f(zh));
    }
    BAR();
    if (w < 4) {
      bf16x8 zf[16];
#pragma unroll
      for (int k = 0; k < 16; ++k) zf[k] = *(const bf16x8*)(lds + zr + 64 * k);
      __builtin_amdgcn_sched_barrier(0);
      f32x4 d = {0.f, 0.f, 0.f, 0.f};
#pragma unroll
      for (int k = 0; k < 16; ++k) d = __builtin_amdgcn_mfma_f32_16x16x32_bf16(wf[k], zf[k], d, 0, 0, 0);
      PIN(d);
      u32 cfb;
      do { cfb = *(volatile LAS u32*)(lds + cr); } while (__any(cfb == POISON));
      d *= __uint_as_float(cfb);
      const float a0 = (tp & 1) ? d[1] : d[0], b0 = (tp & 1) ? d[0] : d[1], a1 = (tp & 1) ? d[3] : d[2], b1 = (tp & 1) ? d[2] : d[3];
      const float e0 = a0 + dppf<0xB1>(b0), e1 = a1 + dppf<0xB1>(b1);
      const float v = rbf(((tp & 2) ? e1 : e0) + dppf<0x4E>((tp & 2) ? e0 : e1));
      const auto sw = __builtin_amdgcn_permlane32_swap(__float_as_uint(v), __float_as_uint(v), false, false);
      const float g = __uint_as_float(sw[0]), u = __uint_as_float(sw[1]);
      const float e2 = __builtin_amdgcn_exp2f(g * (-2.3022081981f - 0.1029432396f * g * g));
      *(u16*)(lds + hw) = (u16)f2bf(rbf(g * __builtin_amdgcn_rcpf(e2 + 1.f)) * u);
    } else {
      const u32x4 yv = *(const u32x4*)(lds + yr);
      const float a1 = dot2(yv.x, 0x3F803F80u, dot2(yv.y, 0x3F803F80u, 0.f)) + dot2(yv.z, 0x3F803F80u, dot2(yv.w, 0x3F803F80u, 0.f));
      const float a2 = dot2(yv.x, yv.x, dot2(yv.y, yv.y, 0.f)) + dot2(yv.z, yv.z, dot2(yv.w, yv.w, 0.f));
      const float sr = x16_sum(row16_sum(rs32(a1, a2))) * (1.f / 512.f);
      const float s1 = __uint_as_float(__builtin_amdgcn_readlane(__float_as_uint(sr), 0)), s2 = __uint_as_float(__builtin_amdgcn_readlane(__float_as_uint(sr), 32));
      const float var = rbf(fmaxf(s2 - s1 * s1, 0.f));
      const float mb = rbf(s1);
      const float sd = rbf(__builtin_amdgcn_sqrtf(rbf(var + 1e-5f)));
      float rsd = __builtin_amdgcn_rcpf(sd);
      rsd = rsd * (2.f - sd * rsd);
      *(u32*)(lds + cr) = fbits(tp == 3 ? 1.f : rsd * (tp == 2 ? -mb : 1.f));
    }
    BAR();
    {
      bf16x8 hf[4];
#pragma unroll
      for (int i = 0; i < 4; ++i) hf[i] = *(const bf16x8*)(lds + hr[i]);
      __builtin_amdgcn_sched_barrier(0);
      f32x4 d = {0.f, 0.f, 0.f, 0.f};
#pragma unroll
      for (int i = 0; i < 4; ++i) d = __builtin_amdgcn_mfma_f32_16x16x32_bf16(wd[i], hf[i], d, 0, 0, 0);
      st128(rs, so, u32x4{fbits(d[0]), fbits(d[1]), fbits(d[2]), fbits(d[3])});
    }
    STAMP(19);
  }
  if (t4 >= 0 && lw == 0) {
#define RED8(f) b3_sum(rs16(rs32(f.x, f.z), rs32(f.y, f.w)))
    const int oc = t4 * 32 + (lane & 7) * 4 + g4;
    const u32 xb = x[oc];
    float x1, gb;
    {
      int off[2];
      off[0] = lane < 32 ? WS_MODB + ((lane >> 3) * 2048 + t4 * 32 + (lane & 7) * 4) * 4 : WS_TOTAL;
      off[1] = lane < 32 ? WS_MODB + ((lane >> 3) * 2048 + 1536 + t4 * 32 + (lane & 7) * 4) * 4 : WS_TOTAL;
      u32x4 mg[2];
      gather128<2>(rs, off, mg);
      const f32x4 fa = __builtin_bit_cast(f32x4, mg[0]), fb = __builtin_bit_cast(f32x4, mg[1]);
      const float ga = rbf(RED8(fa));
      gb = rbf(RED8(fb));
      int of1[1] = {(WS_PATTN + il(((lane >> 3) * 512 + t4 * 32 + (lane & 7) * 4) * 4, myh)) ^ gx};
      u32x4 g[1];
      gather128<1, 4>(rs, of1, g);
      const f32x4 gf = __builtin_bit_cast(f32x4, g[0]);
      x1 = rbf(bf2f(xb) + rbf(ga * rbf(RED8(gf))));
    }
    STAMP(20);
    int off[7];
#pragma unroll
    for (int i = 0; i < 7; ++i) off[i] = (WS_PDOWN + il(((7 * (lane >> 3) + i) * 256 + (t4 & 7) * 32 + (lane & 7) * 4) * 4, myh)) ^ gx;
    const rsrc_t ro = __builtin_amdgcn_make_buffer_rsrc((void*)out, 0, 1024, 0x00020000);
    int oo = (lane & 8) ? 1024 : oc * 2;
    PIN(x1); PIN(gb); PIN(oo);
    u32x4 g[7];
    __builtin_amdgcn_s_sleep(T4_DELAY);
    gather128<7>(rs, off, g);
    STAMP(21);
#define F4(i) __builtin_bit_cast(f32x4, g[i])
    const f32x4 s = ((F4(0) + F4(1)) + (F4(2) + F4(3))) + ((F4(4) + F4(5)) + F4(6));
    __builtin_amdgcn_raw_buffer_store_b16((u16)f2bf(x1 + rbf(gb * rbf(RED8(s)))), ro, oo, 0, 0);
    if (t4 == 0 && lane == 0) st64(rs, WS_EPOCH, ctr[C_TLO], ctr[C_THI]);
    STAMP(22);
  }
  STAMP_FLUSH();
}

extern "C" void kernel_launch(void* const* d_in, const int* in_sizes, int n_in, void* d_out, int out_size,
                              void* d_ws, size_t ws_size, hipStream_t stream) {
  k_main<<<NWG, 512, 0, stream>>>((const u16*)d_in[0], (const u16*)d_in[1], (const u16*)d_in[2], (const u16*)d_in[3],
                                  (const u16*)d_in[4], (const u16*)d_in[5], (const u16*)d_in[7], (const u16*)d_in[8],
                                  (const u16*)d_in[9], (const u16*)d_in[10], (const u16*)d_in[11], (u16*)d_out,
                                  (unsigned char*)d_ws);
}
```

```cpp
#include <hip/hip_runtime.h>
#include <hip/hip_bf16.h>
#include <stdint.h>

typedef unsigned short u16;
typedef unsigned int u32;
typedef unsigned long long u64;
typedef float f32x2 __attribute__((ext_vector_type(2)));
typedef float f32x4 __attribute__((ext_vector_type(4)));
typedef float f32x16 __attribute__((ext_vector_type(16)));
typedef u32 u32x2 __attribute__((ext_vector_type(2)));
typedef u32 u32x4 __attribute__((ext_vector_type(4)));
typedef short bf16x8 __attribute__((ext_vector_type(8)));
typedef short s16x4 __attribute__((ext_vector_type(4)));
#define LAS __attribute__((address_space(3)))
#define DI __device__ __forceinline__

constexpr int DF = 1792;
constexpr int NWG = 256;
constexpr int AUX_SC1 = 16;

constexpr int WS_EPOCH = 0;
constexpr int WS_SFLAG = 4096;
constexpr int WS_QABS = 32768;
constexpr int WS_PCTX = WS_QABS + 65536;
constexpr int WS_PATTN = WS_PCTX + 65536;
constexpr int WS_PDOWN = WS_PATTN + 32768;
constexpr int WS_LSE = WS_PDOWN + 114688;
constexpr int WS_MODB = WS_LSE + 196608;
constexpr int WS_SC = WS_MODB + 32768;
constexpr int WS_PROBE = WS_SC + 32768;
constexpr int WS_VOTE = WS_PROBE + 262144;
constexpr int WS_PART = 2097152;
constexpr int PART_BYTES = 32 * 8 * 256 * 128;
constexpr int WS_TOTAL = WS_PART + PART_BYTES;

constexpr int il(int o, int hf) { return (o >> 13) * 16384 + hf * 8192 + (o & 8191); }
constexpr int il21(int o, int hf) { return il(o, hf ^ ((o >> 20) & 1) ^ 1); }
constexpr u32 SPIN_CAP = 1u << 16;
#define WIN 8
#define WA0 4
#define WB0 5
#define QW 4
#define WING 2
#define QF_T 300
#define E1_T 270
#define E3_T 515
#define E6_T 1200
#define T4_DELAY 12
#define TAU 32.f
#define VCLIP 40
#define PF_LEN 34800
#define G_OF(v) ((v) > 32768.f ? 8192u : 0u)
#define VOTE_STAMP(v)
#define STAMP_DECL
#define STAMP(i)
#define STAMP_FLUSH()

DI float bf2f(u32 b) { return __uint_as_float(b << 16); }
DI float rbf(float f) { return (float)(__bf16)f; }
DI u32 f2bf(float f) { return (u32)__builtin_bit_cast(u16, (__bf16)f); }
typedef __bf16 bf16x2_t __attribute__((ext_vector_type(2)));
DI u32 pk2bf(float a, float b) { return __builtin_bit_cast(u32, __builtin_convertvector(f32x2{a, b}, bf16x2_t)); }
DI float lo16(u32 v) { return __uint_as_float(v << 16); }
DI float hi16(u32 v) { return __uint_as_float(v & 0xFFFF0000u); }
DI u32x4 ldnt(const u16* p) { return __builtin_nontemporal_load((const u32x4*)p); }

DI float silu_bf(float a) { return rbf(a * __builtin_amdgcn_rcpf(1.f + __expf(-a))); }

template <int CTRL> DI float dppf(float v) {
  return __int_as_float(__builtin_amdgcn_update_dpp(0, __float_as_int(v), CTRL, 0xF, 0xF, true));
}
DI float quad_sum(float v) { v += dppf<0xB1>(v); v += dppf<0x4E>(v); return v; }
DI float b23_sum(float v) { v += dppf<0x128>(v); v += dppf<0x124>(v); return v; }
DI float b3_sum(float v) { return v + dppf<0x128>(v); }
DI float x16_sum(float v) {
  const auto r = __builtin_amdgcn_permlane16_swap(__float_as_uint(v), __float_as_uint(v), false, false);
  return __uint_as_float(r[0]) + __uint_as_float(r[1]);
}
DI float x32_sum(float v) {
  const auto r = __builtin_amdgcn_permlane32_swap(__float_as_uint(v), __float_as_uint(v), false, false);
  return __uint_as_float(r[0]) + __uint_as_float(r[1]);
}
DI float rs32(float a, float b) {
  const auto r = __builtin_amdgcn_permlane32_swap(__float_as_uint(a), __float_as_uint(b), false, false);
  return __uint_as_float(r[0]) + __uint_as_float(r[1]);
}
DI float rs16(float a, float b) {
  const auto r = __builtin_amdgcn_permlane16_swap(__float_as_uint(a), __float_as_uint(b), false, false);
  return __uint_as_float(r[0]) + __uint_as_float(r[1]);
}
DI float row16_sum(float v) { return b23_sum(quad_sum(v)); }
DI float wave_sum(float v) { return x32_sum(x16_sum(row16_sum(v))); }
DI void row16_sum4(float& a, float& b, float& c, float& d) {
#define R4(C) "v_add_f32_dpp %0, %0, %0 " C " row_mask:0xf bank_mask:0xf\n v_add_f32_dpp %1, %1, %1 " C " row_mask:0xf bank_mask:0xf\n" \
              "v_add_f32_dpp %2, %2, %2 " C " row_mask:0xf bank_mask:0xf\n v_add_f32_dpp %3, %3, %3 " C " row_mask:0xf bank_mask:0xf\n"
  asm("s_nop 1\n" R4("quad_perm:[1,0,3,2]") R4("quad_perm:[2,3,0,1]") R4("row_ror:8") R4("row_ror:4") : "+v"(a), "+v"(b), "+v"(c), "+v"(d));
#undef R4
}
DI float row16_max(float v) {
  v = fmaxf(v, dppf<0xB1>(v)); v = fmaxf(v, dppf<0x4E>(v));
  v = fmaxf(v, dppf<0x128>(v)); v = fmaxf(v, dppf<0x124>(v));
  return v;
}
DI float x16_max(float v) {
  const auto r = __builtin_amdgcn_permlane16_swap(__float_as_uint(v), __float_as_uint(v), false, false);
  return fmaxf(__uint_as_float(r[0]), __uint_as_float(r[1]));
}
DI float x32_max(float v) {
  const auto r = __builtin_amdgcn_permlane32_swap(__float_as_uint(v), __float_as_uint(v), false, false);
  return fmaxf(__uint_as_float(r[0]), __uint_as_float(r[1]));
}
DI float wave_max(float v) { return x32_max(x16_max(row16_max(v))); }

DI float dot2(u32 a, u32 b, float c) {
  return __builtin_amdgcn_fdot2_f32_bf16(__builtin_bit_cast(bf16x2_t, a), __builtin_bit_cast(bf16x2_t, b), c, false);
}
DI void fma8b(float* acc, u32 sbits, u32x4 w) {
  const u32 sh = sbits << 16;
  acc[0] = dot2(w.x, sbits, acc[0]); acc[1] = dot2(w.x, sh, acc[1]);
  acc[2] = dot2(w.y, sbits, acc[2]); acc[3] = dot2(w.y, sh, acc[3]);
  acc[4] = dot2(w.z, sbits, acc[4]); acc[5] = dot2(w.z, sh, acc[5]);
  acc[6] = dot2(w.w, sbits, acc[6]); acc[7] = dot2(w.w, sh, acc[7]);
}

DI unsigned fsw(unsigned row) { return ((row & 3) << 2) | ((row >> 2) & 3); }
DI unsigned off_b(unsigned row, unsigned ch) { return 256u * row + 16u * (ch ^ fsw(row)); }
DI unsigned tr_addr(unsigned lane, unsigned c, unsigned ks, unsigned t) {
  const unsigned h = lane >> 5, blk = (lane >> 4) & 1, q = (lane & 15) >> 2, p = lane & 3;
  return off_b(16 * ks + 8 * h + 4 * t + q, 4 * c + 2 * blk + (p >> 1)) + 8 * (p & 1);
}

typedef __amdgpu_buffer_rsrc_t rsrc_t;
DI u32x4 ld128(rsrc_t rs, int off) { return __builtin_amdgcn_raw_buffer_load_b128(rs, off, 0, AUX_SC1); }
DI u32x2 ld64(rsrc_t rs, int off) { return __builtin_amdgcn_raw_buffer_load_b64(rs, off, 0, AUX_SC1); }
DI void st64(rsrc_t rs, int off, u32 lo, u32 hi) { u32x2 v; v.x = lo; v.y = hi; __builtin_amdgcn_raw_buffer_store_b64(v, rs, off, 0, AUX_SC1); }
DI void st128(rsrc_t rs, int off, u32x4 v) { __builtin_amdgcn_raw_buffer_store_b128(v, rs, off, 0, AUX_SC1); }
#define BAR() do { asm volatile("s_waitcnt lgkmcnt(0)" ::: "memory"); __builtin_amdgcn_s_barrier(); asm volatile("" ::: "memory"); } while (0)
#define BAR0() do { __builtin_amdgcn_sched_barrier(0); asm volatile("" ::: "memory"); __builtin_amdgcn_s_barrier(); asm volatile("" ::: "memory"); __builtin_amdgcn_sched_barrier(0); } while (0)
#define PIN(x) asm volatile("" : "+v"(x))
constexpr u32 POISON = 0xFFFFFFFFu;
DI bool pois4(u32x4 v) { return max(max(v.x, v.y), max(v.z, v.w)) == POISON; }
DI u32 nopois(u32 v) { return min(v, 0xFFFFFFFEu); }
DI u32 fbits(float v) { return nopois(__float_as_uint(v)); }

DI u32x2 mk_gran(float v, u32 tlo, u32 thi) {
  u32 b = __float_as_uint(v);
  if ((b & 0x7F800000u) != 0x7F800000u) b += 0x80u;
  u32x2 g; g.x = (b >> 8) | (tlo << 24); g.y = (tlo >> 8) | (thi << 24);
  return g;
}
DI void st_gran2(rsrc_t rs, int off, float v0, float v1, u32 tlo, u32 thi) {
  const u32x2 a = mk_gran(v0, tlo, thi), b = mk_gran(v1, tlo, thi);
  u32x4 v; v.x = a.x; v.y = a.y; v.z = b.x; v.w = b.y;
  st128(rs, off, v);
}
DI bool gran_ok(u32 lo, u32 hi, u32 tlo, u32 thi) { return (((lo >> 24) ^ (tlo & 0xFFu)) | (hi ^ ((tlo >> 8) | (thi << 24)))) == 0u; }
DI float gran_val(u32 lo) { return __uint_as_float(lo << 8); }

template <int N, int SLP = 1> DI void gather128(rsrc_t rs, const int (&off)[N], u32x4 (&v)[N]) {
#pragma unroll
  for (int i = 0; i < N; ++i) v[i] = ld128(rs, off[i]);
  _Pragma("unroll 1") for (u32 spins = 0; spins < SPIN_CAP; ++spins) {
    bool need[N], bad = false;
#pragma unroll
    for (int i = 0; i < N; ++i) { need[i] = pois4(v[i]); bad = bad || need[i]; }
    if (!__any(bad)) break;
    __builtin_amdgcn_s_sleep(SLP);
    asm volatile("" ::: "memory");
    __builtin_amdgcn_sched_barrier(0);
#pragma unroll
    for (int i = 0; i < N; ++i)
      if (need[i]) v[i] = ld128(rs, off[i]);
  }
}
template <int N> DI void waitvm() { asm volatile("s_waitcnt vmcnt(%0)" ::"n"(N) : "memory"); }
constexpr u32 LSPIN_CAP = 1u << 22;
DI void lds_wait(volatile LAS u32* p, u32 val) {
  _Pragma("unroll 1") for (u32 n = 0; *p < val && n < LSPIN_CAP; ++n) __builtin_amdgcn_s_sleep(1);
  asm volatile("" ::: "memory");
}
#define WAVE_LDS_SYNC() do { __builtin_amdgcn_fence(__ATOMIC_RELEASE, "wavefront"); __builtin_amdgcn_wave_barrier(); __builtin_amdgcn_fence(__ATOMIC_ACQUIRE, "wavefront"); } while (0)
#define SB() __builtin_amdgcn_sched_barrier(0)
#define RARE(c) __builtin_expect(!!(c), 0)

constexpr int PS = 28672;
constexpr int L_QI = 4 * PS;
constexpr int L_P = L_QI + 32768;
constexpr int L_REF = L_P + 11264;
constexpr int L_MX = L_REF + 1408;
constexpr int L_ALW = L_MX + 128;
constexpr int L_DUMP = L_ALW + 512;
constexpr int L_QOFF = 131072;
constexpr int L_H1RED = L_QOFF;
constexpr int L_RED = L_QOFF + 16384;
constexpr int L_Z = L_RED, L_HH = L_RED + 7 * 1088, L_ZERO = L_HH + 64, L_CF = L_ZERO + 64;
constexpr int L_MISC = L_QOFF + 30720;
constexpr int L_CTR = L_MISC;
constexpr int L_WMAX = L_MISC + 256;
constexpr int L_ML = L_MISC + 512;

#define Z4 (u32x4{0u, 0u, 0u, 0u})
DI float max5(f32x4 v, float e) { return fmaxf(fmaxf(fmaxf(fmaxf(v.x, v.y), v.z), v.w), e); }

template <bool NT> DI void dma(u32 m, u32 voff, const void* sb) {
  if (NT) asm volatile("s_mov_b32 m0, %0\n s_nop 0\n global_load_lds_dwordx4 %1, %2 nt" ::"s"(m), "v"(voff), "s"(sb) : "memory");
  else asm volatile("s_mov_b32 m0, %0\n s_nop 0\n global_load_lds_dwordx4 %1, %2" ::"s"(m), "v"(voff), "s"(sb) : "memory");
}
DI void dma_sc1(u32 m, u32 voff, const void* sb) {
  asm volatile("s_mov_b32 m0, %0\n s_nop 0\n global_load_lds_dwordx4 %1, %2 sc1" ::"s"(m), "v"(voff), "s"(sb) : "memory");
}
DI void lds_add(int idx) {
  SB();
  asm volatile("s_mov_b64 exec, 1\n ds_add_u32 %0, %1 offset:%2\n s_mov_b64 exec, -1" ::"v"((u32)L_CTR), "v"(1u), "n"(idx * 4) : "memory");
  SB();
}
DI u32 spin_at(u32 a, u32 val) {
  u32 t, c, m, cap;
  asm volatile("s_mov_b32 %3, %6\n 1: ds_read_b32 %0, %4\n s_waitcnt lgkmcnt(0)\n v_readfirstlane_b32 %1, %0\n s_and_b32 %2, %1, 255\n s_cmp_ge_u32 %2, %5\n s_cbranch_scc1 2f\n"
               "s_sub_u32 %3, %3, 1\n s_cbranch_scc0 1b\n 2:"
               : "=&v"(t), "=&s"(c), "=&s"(m), "=&s"(cap) : "v"(a), "s"(val), "n"(LSPIN_CAP) : "memory", "scc");
  return c;
}
DI void lds_spin(volatile LAS u32* p, u32 val) { spin_at((u32)(uintptr_t)p, val); }
enum { C_SOK = 0, C_BH = 4, C_PD = 12, C_H1 = 18, C_PAR = 19, C_VOTE = 20, C_FREE = 22, C_QRDY = 23, C_MXC = 25, C_SD = 26, C_GX = 27, C_ML = 28, C_MK = 32, C_P = 44 };

DI u32 kv_mark(int nm, int skip) { return L_CTR + 4 * C_MK + __builtin_amdgcn_readfirstlane(4 * nm + (nm >= skip ? 4 : 0)); }
#define KV_TS
#define KV_TSO
#define KV_TSA
#define KV_TSP
#define KV_TRY(l)                                                                                                        \
  "7: s_getreg_b32 %[h], hwreg(HW_REG_IB_STS, 0, 4)\n s_cmp_gt_i32 %[h], %[k4]\n s_cbranch_scc1 " l "\n"                  \
  "s_waitcnt vmcnt(4)\n s_cmp_eq_u32 %[k4], 4\n s_cbranch_scc1 8f\n s_waitcnt vmcnt(0)\n"                              \
  "8: v_subrev_u32 %[t], %[k4], %[ml]\n s_mov_b64 exec, 1\n ds_add_u32 %[t], %[one] offset:12\n s_mov_b64 exec, -1\n"   \
  "s_sub_i32 %[k4], %[k4], 4\n" KV_TS "s_branch 7b\n"
DI void kv_try(int& k4, u32 ml, u32 one KV_TSA) {
  u32 t, h;
  asm volatile(KV_TRY("9f") "9:" : [t] "=&v"(t), [h] "=&s"(h), [k4] "+s"(k4) KV_TSO : [ml] "v"(ml), [one] "v"(one) : "memory", "scc");
}
DI u32 spin_try(int idx, u32 val, int& k4, u32 ml, u32 one KV_TSA) {
  u32 t, h, c, cap;
  asm volatile("s_mov_b32 %[cap], %[n]\n 1: ds_read_b32 %[t], %[ml] offset:%[o]\n s_waitcnt lgkmcnt(0)\n v_readfirstlane_b32 %[c], %[t]\n s_and_b32 %[h], %[c], 255\n"
               "s_cmp_ge_u32 %[h], %[v]\n s_cbranch_scc1 2f\n" KV_TRY("9f") "9: s_sub_u32 %[cap], %[cap], 1\n s_cbranch_scc0 1b\n 2:"
               : [t] "=&v"(t), [h] "=&s"(h), [c] "=&s"(c), [cap] "=&s"(cap), [k4] "+s"(k4) KV_TSO
               : [ml] "v"(ml), [one] "v"(one), [o] "n"(4 * idx), [v] "n"(val), [n] "n"(LSPIN_CAP) : "memory", "scc");
  return c;
}
template <int W> DI void kv_load1(const u16* ckv, u32 vl, int lw, u32 g0, u32 g1, u32 m, int n0, int n1, int& nm, int skip) {
#pragma unroll 1
  for (int n = n0; n < n1; ++n) {
#define KV_POST(k) ".if (%[wm] | 4) == " #k "\n s_cmp_gt_i32 %[nm], %[n2]\n s_cbranch_scc1 20f\n s_mov_b64 exec, 1\n ds_add_u32 %[ma], %[one]\n s_mov_b64 exec, -1\n s_add_i32 %[nm], %[nm], 1\n 20:\n .endif\n"
#define KV_W(o, k) "s_waitcnt vmcnt(%[wm])\n" KV_POST(k) "global_load_lds_dwordx4 %[vo], %[p] offset:" #o " nt\n"
#define KV_M "s_add_u32 m0, m0, %[ms]\n"
    asm volatile("s_mov_b32 m0, %[m]\n" KV_W(0, 4) KV_M KV_W(256, 5) KV_M KV_W(512, 6) KV_M KV_W(768, 7)
                 : [nm] "+s"(nm)
                 : [m] "s"(m), [vo] "v"(vl + g0 + ((lw + n) & 3) * 4096), [p] "s"(ckv), [ma] "v"(kv_mark(nm, skip)),
                   [one] "v"(1u), [n2] "s"(__builtin_amdgcn_readfirstlane(n - (W > 4 ? 2 : 1))), [wm] "n"(W - 1), [ms] "n"(PS - 256)
                 : "memory", "scc");
    g0 = g1;
    g1 += 192 * 16384;
    m += n + 1 == skip ? 8192 : 4096;
  }
}
template <int W, int W0> DI void kv_load(const u16* ckv, u32 vl, int lw, u32 g0, u32 g1, u32 m, int n0, int n1, int& nm, int skip) {
  if (W != W0 && skip == 4) kv_load1<W0>(ckv, vl, lw, g0, g1, m, n0, n1, nm, skip); else kv_load1<W>(ckv, vl, lw, g0, g1, m, n0, n1, nm, skip);
}
DI void until(u32 t0, u32 dt) {
  while ((u32)__builtin_amdgcn_s_memrealtime() - t0 < dt) __builtin_amdgcn_s_sleep(2);
}
DI bf16x8 tr_frag(unsigned char* lds, u32 a0, u32 a1) {
  const s16x4 b0 = __builtin_amdgcn_ds_read_tr16_b64_v4i16((LAS s16x4*)(lds + a0));
  const s16x4 b1 = __builtin_amdgcn_ds_read_tr16_b64_v4i16((LAS s16x4*)(lds + a1));
  bf16x8 v;
  v[0] = b0[0]; v[1] = b0[1]; v[2] = b0[2]; v[3] = b0[3]; v[4] = b1[0]; v[5] = b1[1]; v[6] = b1[2]; v[7] = b1[3];
  return v;
}
DI float sum4(f32x4 v) { const f32x2 t = f32x2{v.x, v.y} + f32x2{v.z, v.w}; return t.x + t.y; }

__global__ __launch_bounds__(512, 2) void k_main(const u16* __restrict__ x, const u16* __restrict__ ckv,
                                                 const u16* __restrict__ w_q, const u16* __restrict__ w_uk,
                                                 const u16* __restrict__ w_uv, const u16* __restrict__ w_o,
                                                 const u16* __restrict__ w_gate, const u16* __restrict__ w_up,
                                                 const u16* __restrict__ w_down, const u16* __restrict__ cvec,
                                                 const u16* __restrict__ w_mod, u16* __restrict__ out,
                                                 unsigned char* ws) {
  __shared__ __attribute__((aligned(16))) unsigned char lds[163840];
  const int w = __builtin_amdgcn_readfirstlane(threadIdx.x >> 6), j = blockIdx.x;
  int lane = threadIdx.x & 63;
  const int xc = j & 7, ii = j >> 3;
  STAMP_DECL;
  STAMP(0);
  u32x2 ev;
  u32 xm, vh;
  {
    u64 p, pc, pw;
    u32 a, b, c, d, f, t;
    asm volatile("s_lshr_b32 %[f], %[j], 6\n s_cmp_eq_u32 %[f], 1\n s_cselect_b32 %[a], 0x50, 16\n"
                 "s_load_dwordx2 %[p], %[ka], %[a]\n s_load_dwordx2 %[pw], %[ka], 0x60\n"
                 "s_lshr_b32 %[d], %[j], 3\n s_cmp_lg_u32 %[w], 7\n s_cbranch_scc1 2f\n"
                 "s_and_b32 %[a], %[d], 15\n s_lshl_b32 %[a], %[a], 12\n v_lshl_add_u32 %[t], %[lane], 6, %[a]\n v_min_u32 %[t], %[pfl], %[t]\n"
                 "s_getpc_b64 %[pc]\n s_mov_b32 m0, %[dump]\n s_nop 0\n global_load_lds_dword %[t], %[pc]\n"
                 "2: s_load_dwordx2 %[pc], %[ka], 0x48\n s_and_b32 %[c], %[j], 7\n s_lshl_b32 %[c], %[c], 10\n s_cmp_eq_u32 %[f], 0\n s_cbranch_scc0 3f\n"
                 "s_and_b32 %[a], %[d], 1\n s_lshl1_add_u32 %[a], %[w], %[a]\n s_lshl_b32 %[a], %[a], 18\n s_add_u32 %[c], %[c], %[a]\n"
                 "s_lshr_b32 %[a], %[d], 1\n s_lshl_b32 %[a], %[a], 8\n s_add_u32 %[c], %[c], %[a]\n s_lshl_b32 m0, %[w], 14\n"
                 "v_lshrrev_b32 %[t], 4, %[lane]\n v_mul_u32_u24 %[t], 0x204, %[t]\n v_and_b32 %[vh], 15, %[lane]\n v_xor_b32 %[vh], %[vh], %[t]\n"
                 "v_lshl_add_u32 %[vh], %[vh], 4, %[c]\n v_xor_b32 %[t], 0x8010, %[vh]\n s_waitcnt lgkmcnt(0)\n"
                 "global_load_lds_dwordx4 %[vh], %[p]\n s_add_u32 m0, m0, 0x400\n s_nop 0\n"
                 "global_load_lds_dwordx4 %[t], %[p]\n s_add_u32 m0, m0, 0x400\n v_xor_b32 %[t], 0x10020, %[vh]\n"
                 "global_load_lds_dwordx4 %[t], %[p]\n s_add_u32 m0, m0, 0x400\n v_xor_b32 %[t], 0x18030, %[vh]\n"
                 "global_load_lds_dwordx4 %[t], %[p]\n s_branch 1f\n"
                 "3: s_lshl2_add_u32 %[d], %[d], %[w]\n s_sub_u32 %[d], %[d], 68\n s_lshl_b32 %[d], %[d], 16\n s_add_u32 %[d], %[d], %[c]\n"
                 "s_and_b32 %[a], %[j], 3\n s_lshl2_add_u32 %[b], %[a], %[w]\n s_mul_i32 %[b], %[b], 0x30000\n s_lshl_b32 %[a], %[a], 8\n"
                 "v_lshl_add_u32 %[xm], %[lane], 2, %[a]\n"
                 "s_and_b32 %[a], %[j], 60\n s_lshl_b32 %[a], %[a], 5\n s_add_u32 %[b], %[b], %[a]\n"
                 "s_lshl_b32 %[c], %[w], 12\n s_add_u32 %[c], %[c], %[mq]\n s_lshl_b32 %[a], %[w], 10\n s_add_u32 %[a], %[a], %[mh]\n"
                 "s_cmp_eq_u32 %[f], 1\n s_cselect_b32 %[d], %[b], %[d]\n s_cselect_b32 m0, %[c], %[a]\n s_cselect_b32 %[a], 48, 1\n s_cselect_b32 %[c], 0, 4\n"
                 "v_lshrrev_b32 %[t], 3, %[lane]\n v_mul_u32_u24 %[t], %[a], %[t]\n v_and_b32 %[vh], 7, %[lane]\n v_lshl_add_u32 %[vh], %[t], 3, %[vh]\n"
                 "v_lshl_add_u32 %[vh], %[vh], 4, %[d]\n"
                 "s_cselect_b32 %[b], 6, 1\n s_cselect_b32 %[a], 0x400, 0\n s_lshl_b32 %[b], %[b], 13\n"
                 "s_xor_b32 %[c], %[c], %[w]\n s_bitcmp1_b32 %[c], 2\n v_mov_b32 %[t], 0\n"
                 "s_waitcnt lgkmcnt(0)\n s_cbranch_scc1 1f\n s_cmp_eq_u32 %[f], 1\n s_cbranch_scc0 4f\n"
                 "global_load_dword %[xm], %[xm], %[pc]\n global_load_dwordx2 %[ev], %[t], %[pw] sc1\n"
                 "4: global_load_lds_dwordx4 %[vh], %[p]\n s_add_u32 m0, m0, %[a]\n v_add_u32 %[t], %[b], %[vh]\n"
                 "global_load_lds_dwordx4 %[t], %[p]\n s_add_u32 m0, m0, %[a]\n v_add_u32 %[t], %[b], %[t]\n"
                 "global_load_lds_dwordx4 %[t], %[p]\n s_add_u32 m0, m0, %[a]\n v_add_u32 %[t], %[b], %[t]\n"
                 "global_load_lds_dwordx4 %[t], %[p]\n"
                 "1:\n"
                 : [xm] "=&v"(xm), [ev] "=&v"(ev), [vh] "=&v"(vh), [t] "=&v"(t), [p] "=&s"(p), [pc] "=&s"(pc), [pw] "=&s"(pw), [a] "=&s"(a), [b] "=&s"(b), [c] "=&s"(c),
                   [d] "=&s"(d), [f] "=&s"(f)
                 : [j] "s"(j), [w] "s"(w), [lane] "v"(lane), [ka] "s"(__builtin_amdgcn_kernarg_segment_ptr()), [pfl] "n"(PF_LEN), [dump] "n"(L_DUMP),
                   [mq] "n"(L_QOFF + 8192), [mh] "n"(L_P)
                 : "memory", "scc");
  }
  SB();
  const u32 t0 = (u32)__builtin_amdgcn_s_memrealtime() - 35u;
  const bool isQ = j < 64, isS0 = j >= 64 && j < 128;
  const int hq = xc * 4 + (ii >> 1), dh = ii & 1, sj = j - 64;
  const int lw = w - 4, myh = xc >> 2;
  int col = lane & 15, g4 = lane >> 4, tq = col >> 2, tp = lane & 3;
  const rsrc_t rs = __builtin_amdgcn_make_buffer_rsrc((void*)ws, 0, WS_TOTAL, 0x00020000);
  volatile LAS u32* ctr = (volatile LAS u32*)(lds + L_CTR);
  SB();
  STAMP(25);
  if (w < 2) *(u32*)(lds + (w == 0 ? L_CTR + lane * 4 : L_MX + (lane & 31) * 4)) = w == 0 ? 0u : 0xFF800000u;
  BAR();
  const bool tl2 = isS0;
  const int t3 = xc < 4 ? (ii < 8 ? xc * 8 + ii : -1) : (ii < 6 ? 8 + xc * 6 + ii : -1);
  const int t4 = (ii >= 8 && ii < 10) ? xc * 2 + (ii - 8) : -1;
  const u32 owno = (w >> 1) * 32768 + 8192 + (w & 1) * 12288;
  const int ks = (xc & 3) * 2 + myh, cg = ii & 7;
  const int h = (j >> 2) & 31, c4 = j & 3;
  u32x4 wv[8];
  u32 tlo, thi;
  int gx;
#define RELANE() do { asm volatile("v_mbcnt_lo_u32_b32 %0, -1, 0\n v_mbcnt_hi_u32_b32 %0, -1, %0" : "=v"(lane)); col = lane & 15; g4 = lane >> 4; tq = col >> 2; tp = lane & 3; } while (0)
#define TAG_OF(e0, e1)                                                                                                  \
  do {                                                                                                                  \
    const u64 tag = (((u64)(e1) << 32) | (e0)) + 1ull;                                                                  \
    tlo = __builtin_amdgcn_readfirstlane((u32)tag); thi = __builtin_amdgcn_readfirstlane((u32)(tag >> 32));             \
  } while (0)
  const u32x4 P4 = {POISON, POISON, POISON, POISON};
#define COUNT_VOTES(PRE)                                                                                                \
  do {                                                                                                                  \
    u32x2 a_ = {0u, 0u};                                                                                                \
    if (PRE) a_ = *(const u32x2*)(lds + L_QI + lane * 8);                                                               \
    _Pragma("unroll 1") for (u32 spins = 0; spins < SPIN_CAP; ++spins) {                                                \
      if (((PRE) || spins) && __all(gran_ok(a_.x, a_.y, tlo, thi))) break;                                              \
      if (spins) __builtin_amdgcn_s_sleep(8);                                                                           \
      asm volatile("" ::: "memory");                                                                                    \
      a_ = ld64(rs, WS_VOTE + xc * 2048 + lane * 8);                                                                    \
    }                                                                                                                   \
    const float vt_ = wave_sum((float)(a_.x & 1023u));                                                                  \
    if (lane == 0) ctr[C_PAR] = G_OF(vt_);                                                                              \
    VOTE_STAMP(vt_);                                                                                                    \
    STAMP(8);                                                                                                           \
  } while (0)
#define S_POLL(a_)                                                                                                      \
  _Pragma("unroll 1") for (u32 spins = 0; spins < SPIN_CAP && RARE(__any(((a_.x ^ tlo) | (a_.y ^ thi)) != 0u)); ++spins) { \
    __builtin_amdgcn_s_sleep(8);                                                                                        \
    asm volatile("" ::: "memory");                                                                                      \
    a_ = ld64(rs, WS_SFLAG + xc * 2048 + lane * 8);                                                                     \
  }
#define MB_OFF(i) (WS_MODB + ((lane >> 4) * 2048 + ((i) >> 1) * 512 + 128 * lw + 64 * ((i) & 1) + (lane & 15) * 4) * 4)

  if (!isQ && w >= 4) {
    if (isS0) {
      if (w == 4) {
        const u32x2 e4 = ld64(rs, WS_EPOCH);
        st128(rs, WS_QABS + sj * 1024 + lane * 16, P4);
        st128(rs, lane < 32 ? WS_MODB + sj * 512 + lane * 16 : WS_TOTAL, P4);
        st128(rs, WS_PROBE + il(sj * 1024 + lane * 16, 0), Z4);
        st128(rs, WS_PROBE + il(sj * 1024 + lane * 16, 1), Z4);
        waitvm<0>();
        TAG_OF(e4.x, e4.y);
        if (lane < 8) st64(rs, WS_SFLAG + lane * 2048 + sj * 8, tlo, thi);
        STAMP(27);
      }
      lds_wait(ctr + C_H1, 4u);
    } else {
#pragma unroll
      for (int i = 4; i < 16; ++i) { waitvm<7>(); dma<false>(L_P + w * 1024, vh + (i & 7) * 8192, i < 8 ? w_q : w_uk); }
    }
    const u32 vl = g4 * 1024 + ((col ^ ((g4 << 2) | lw)) << 4);
    int nm = 0, ns = isS0 ? 6 : 7;
    const int skip = isS0 ? 4 : 99;
    kv_load<WIN, WA0>(ckv, vl, lw, (isS0 ? sj : 1792 + j) * 16384, (isS0 ? sj + 192 : sj) * 16384, lw * 1024, 0, 2, nm, skip);
    if (!isS0) {
      const rsrc_t rw = __builtin_amdgcn_make_buffer_rsrc((void*)w_uv, 0, 4194304, 0x00020000);
#pragma unroll
      for (int i = 0; i < 8; ++i) { waitvm<WIN - 1>(); wv[i] = __builtin_amdgcn_raw_buffer_load_b128(rw, (c4 * 128 + 32 * lw + g4) * 8192 + h * 256 + col * 16, i * 32768, 2); }
      SB();
    }
    u32 va[8];
#pragma unroll
    for (int i = 0; i < 8; ++i) { va[i] = lw * PS + tr_addr(lane, i >> 1, 0, i & 1); PIN(va[i]); }
    int eg[8];
#pragma unroll
    for (int u = 0; u < 8; ++u) { eg[u] = WS_PART + il21(((4 * (u >> 1) + 2 * (g4 >> 1) + (u & 1)) * 4 + lw) * 65536 + sj * 256 + col * 16, g4 & 1); PIN(eg[u]); }
    u32 pr_ = L_P + lane * 16;
    PIN(pr_);
    u32 ml = L_CTR + 4 * (C_MK + 7), one = 1u;
    PIN(ml); PIN(one);
    bf16x8 vf[4][4];
#define V_READ(i, slot) do { asm volatile("" ::: "memory"); SB(); _Pragma("unroll") for (int c = 0; c < 4; ++c) vf[i][c] = tr_frag(lds, va[2 * c] + (slot) * 4096, va[2 * c + 1] + (slot) * 4096); SB(); } while (0)
    SB();
    kv_load<WIN, WB0>(ckv, vl, lw, (sj + (isS0 ? 384 : 192)) * 16384, (sj + (isS0 ? 576 : 384)) * 16384, lw * 1024 + 8192, 2, ns, nm, skip);
    lds_wait(ctr + C_MK + 3, 4u);
    V_READ(0, 0); V_READ(1, 1); V_READ(2, 2); V_READ(3, 3);
    lds_add(C_FREE);
#pragma unroll
    for (int i = 0; i < 16; ++i) PIN(vf[i >> 2][i & 3]);
    STAMP(2);
    lds_wait(ctr + C_FREE, 8u);
    STAMP(5);
    kv_load<WIN, WB0>(ckv, vl, lw, (sj + 6 * 192) * 16384, (sj + 7 * 192) * 16384, lw * 1024, ns, ns + 4, nm, skip);
    int k4 = 4;
    STAMP(3);
    f32x16 acc[4];
#pragma unroll
    for (int c = 0; c < 4; ++c)
#pragma unroll
      for (int i = 0; i < 16; ++i) acc[c][i] = 0.f;
    bf16x8 pf[4], pn[4];
    u32 c_;
#define P_SPIN(g, n) c_ = spin_try(C_P + (g) - C_MK - 7, n, k4, ml, one KV_TSP)
#define TRY() kv_try(k4, ml, one KV_TSP)
#define P_READ(d, i0, i1, T0) _Pragma("unroll") for (int i = (i0); i < (i1); ++i) d[i] = *(volatile LAS bf16x8*)(lds + pr_ + ((T0) + i) * 1024)
#define PV(p, i) do { SB(); _Pragma("unroll") for (int c = 0; c < 4; ++c) acc[c] = __builtin_amdgcn_mfma_f32_32x32x16_bf16(p[i], vf[i][c], acc[c], 0, 0, 0); SB(); } while (0)
#define GX_USE(v) do { gx = __builtin_amdgcn_readfirstlane(v); _Pragma("unroll") for (int u = 0; u < 8; ++u) { eg[u] ^= gx; PIN(eg[u]); } } while (0)
#define MK_WAIT(T) spin_try((T) - 7, 4u, k4, ml, one KV_TSP)
#define PVV(p, i, d, slot)                                                                                               \
  _Pragma("unroll") for (int c = 0; c < 4; ++c) {                                                                        \
    SB();                                                                                                                \
    acc[c] = __builtin_amdgcn_mfma_f32_32x32x16_bf16(p[i], vf[i][c], acc[c], 0, 0, 0);                                   \
    SB();                                                                                                                \
    vf[d][c] = tr_frag(lds, va[2 * c] + (slot) * 4096, va[2 * c + 1] + (slot) * 4096);                                   \
    SB();                                                                                                                \
  }
#define CT(i) (*(volatile LAS u32*)(lds + ml + 4 * ((i) - C_MK - 7)))
    P_SPIN(0, 4u);
    P_READ(pf, 0, 4, 0);
    const u32 k7 = CT(C_MK + 7);
    asm volatile("" ::: "memory");
    STAMP(6);
    PVV(pf, 0, 0, 4); TRY();
    PVV(pf, 1, 1, 5); TRY();
    PVV(pf, 2, 2, 6);
    u32 cv = CT(C_P + 1);
    P_READ(pn, 0, 4, 4);
    PVV(pf, 3, 3, 0);
    if (RARE(__builtin_amdgcn_readfirstlane(k7) < 4u)) { MK_WAIT(7); V_READ(0, 4); V_READ(1, 5); V_READ(2, 6); V_READ(3, 0); }
    STAMP(9);
    c_ = __builtin_amdgcn_readfirstlane(cv);
    if (RARE((c_ & 255u) != 4u)) { P_SPIN(1, 4u); P_READ(pn, 0, 4, 4); }
    int slow = -1;
    if (RARE(c_ > 255u)) slow = 4;
    else {
      if (!isS0) PV(pn, 0);
      TRY();
      const u32 k9 = CT(C_MK + 9);
      asm volatile("" ::: "memory");
      PVV(pn, 1, 0, 1); TRY();
      PVV(pn, 2, 1, 2);
      cv = CT(C_P + 2);
      P_READ(pf, 0, 2, 8);
      const u32 k10 = CT(C_MK + 10);
      asm volatile("" ::: "memory");
      PVV(pn, 3, 2, 3);
      if (RARE(__builtin_amdgcn_readfirstlane(k9) < 4u)) { MK_WAIT(9); V_READ(0, 1); V_READ(1, 2); }
      STAMP(10);
      c_ = __builtin_amdgcn_readfirstlane(cv);
      if (RARE((c_ & 255u) != 4u)) { P_SPIN(2, 4u); P_READ(pf, 0, 2, 8); }
      if (RARE(c_ > 255u)) slow = 8;
      else {
        TRY();
        PV(pf, 0);
        const u32 g0 = ctr[C_GX];
        u32 g1 = ctr[C_PAR];
        cv = CT(C_P + 3);
        P_READ(pf, 2, 3, 8);
        PV(pf, 1);
        STAMP(4);
        if (RARE(__builtin_amdgcn_readfirstlane(k10) < 4u)) { waitvm<0>(); TRY(); lds_spin(ctr + C_MK + 10, 4u); V_READ(2, 3); }
        if (RARE(__builtin_amdgcn_readfirstlane(g0) == 0u)) { lds_spin(ctr + C_GX, 1u); g1 = ctr[C_PAR]; }
        GX_USE(g1);
        STAMP(11);
        c_ = __builtin_amdgcn_readfirstlane(cv);
        _Pragma("unroll 1") for (u32 n_ = 0; RARE((c_ & 255u) != 2u) && n_ < LSPIN_CAP; ++n_) { cv = CT(C_P + 3); P_READ(pf, 2, 3, 8); c_ = __builtin_amdgcn_readfirstlane(cv); }
        if (RARE(c_ > 255u)) slow = 10;
        else PV(pf, 2);
      }
    }
    if (RARE(slow >= 0)) {
      MK_WAIT(10);
      float R = ceilf(*(const float*)(lds + L_MX + (lane & 31) * 4));
#pragma unroll 1
      for (int T = slow; T < 11; ++T) {
        if (T == 4 && isS0) continue;
        spin_at(L_CTR + 4 * (C_P + (T < 8 ? 1 : T < 10 ? 2 : 3)), T < 10 ? 4u : 2u);
        const float rf = *(const float*)(lds + L_REF + T * 128 + (lane & 31) * 4);
        u32x4 p = *(const u32x4*)(lds + pr_ + T * 1024);
        const u32 so_ = (T < 7 ? T : T - 7) * 4096;
#pragma unroll
        for (int c = 0; c < 4; ++c) vf[0][c] = tr_frag(lds, va[2 * c] + so_, va[2 * c + 1] + so_);
        const float Rn = fmaxf(R, rf), ps = __builtin_amdgcn_exp2f(rf - Rn);
        *(float*)(lds + L_ALW + lw * 128 + (lane & 31) * 4) = __builtin_amdgcn_exp2f(R - Rn);
        R = Rn;
        WAVE_LDS_SYNC();
#pragma unroll
        for (int i = 0; i < 4; ++i) {
          const f32x4 al = *(const f32x4*)(lds + L_ALW + lw * 128 + (lane >> 5) * 16 + i * 32);
#pragma unroll
          for (int c = 0; c < 4; ++c)
#pragma unroll
            for (int r = 0; r < 4; ++r) acc[c][4 * i + r] *= al[r];
          p[i] = pk2bf(lo16(p[i]) * ps, hi16(p[i]) * ps);
        }
        WAVE_LDS_SYNC();
        pf[0] = __builtin_bit_cast(bf16x8, p);
        PV(pf, 0);
      }
      if (slow != 10) { lds_spin(ctr + C_GX, 1u); GX_USE(ctr[C_PAR]); }
    }
    STAMP(30);
#pragma unroll
    for (int u = 0; u < 8; u += 2) {
      u32 c[8];
#pragma unroll
      for (int k = 0; k < 8; ++k) c[k] = pk2bf(acc[k & 2][2 * u + (k & 1) + (k >> 2) * 2], acc[(k & 2) + 1][2 * u + (k & 1) + (k >> 2) * 2]);
      SB();
#pragma unroll
      for (int k = 0; k < 2; ++k) {
        const auto r0 = __builtin_amdgcn_permlane16_swap(c[4 * k], c[4 * k + 1], false, false);
        const auto r1 = __builtin_amdgcn_permlane16_swap(c[4 * k + 2], c[4 * k + 3], false, false);
        st128(rs, eg[u + k], u32x4{r0[0], r1[0], r0[1], r1[1]});
      }
      SB();
    }
    STAMP(31);
    if (!isS0) {
#pragma unroll
      for (int i = 0; i < 8; ++i) *(u32x4*)(lds + lw * PS + 16384 + off_b(4 * i + g4, col)) = wv[i];
    }
    {
      int fo = lane < 16 ? (WS_LSE + il(sj * 512 + lw * 128 + (lane & 7) * 16, lane >> 3)) ^ gx : WS_TOTAL;
      lds_spin(ctr + C_ML, 1u);
      u32x4 ml4 = *(const u32x4*)(lds + L_WMAX + col * 16);
      PIN(fo); PIN(ml4);
      waitvm<0>();
      __builtin_amdgcn_s_waitcnt(0x0F70);
      st128(rs, fo, ml4);
      lds_add(C_PD);
    }
    STAMP(13);
  } else if (!isQ) {
    if (isS0) {
      u32 ta = L_QOFF + 8192 + (8 * g4 + tq) * 128 + w * 32 + tp * 8;
      u32 sa = L_H1RED + w * 256;
      int so = col < 2 ? WS_SC + (((sj >> 5) * 4 + (sj & 3)) * 512 + ((sj >> 2) & 7) * 64 + 16 * w + 4 * g4 + 2 * col) * 8 : WS_TOTAL;
      PIN(ta); PIN(so); PIN(sa);
      u32 c2, e0, e1;
      asm volatile("s_waitcnt vmcnt(4) ; HEAD_WAIT\n v_mov_b32 %0, %3\n v_mov_b32 %1, %4\n v_mov_b32 %2, %5" : "=&v"(c2), "=&v"(e0), "=&v"(e1) : "v"(xm), "v"(ev.x), "v"(ev.y) : "memory");
      TAG_OF(e0, e1);
      bf16x8 sf[4];
      u32x2 wt[8];
      asm volatile("ds_write_b32 %4, %5\n"
                   "ds_read_b128 %0, %6\n ds_read_b128 %1, %6 offset:64\n ds_read_b128 %2, %6 offset:128\n ds_read_b128 %3, %6 offset:192\n s_waitcnt lgkmcnt(0)"
                   : "=&v"(sf[0]), "=&v"(sf[1]), "=&v"(sf[2]), "=&v"(sf[3])
                   : "v"(sa + lane * 4), "v"(pk2bf(silu_bf(lo16(c2)), silu_bf(hi16(c2)))), "v"(sa + 16 * g4) : "memory");
      waitvm<0>();
      lds_add(C_BH);
      lds_spin(ctr + C_BH, 4u);
      STAMP(1);
      asm volatile("ds_read_b64_tr_b16 %0, %8\n ds_read_b64_tr_b16 %1, %8 offset:512\n ds_read_b64_tr_b16 %2, %8 offset:4096\n ds_read_b64_tr_b16 %3, %8 offset:4608\n"
                   "ds_read_b64_tr_b16 %4, %8 offset:8192\n ds_read_b64_tr_b16 %5, %8 offset:8704\n ds_read_b64_tr_b16 %6, %8 offset:12288\n ds_read_b64_tr_b16 %7, %8 offset:12800\n"
                   "s_waitcnt lgkmcnt(0)"
                   : "=&v"(wt[0]), "=&v"(wt[1]), "=&v"(wt[2]), "=&v"(wt[3]), "=&v"(wt[4]), "=&v"(wt[5]), "=&v"(wt[6]), "=&v"(wt[7]) : "v"(ta) : "memory");
      f32x4 d = {0.f, 0.f, 0.f, 0.f};
#pragma unroll
      for (int k = 0; k < 4; ++k)
        d = __builtin_amdgcn_mfma_f32_16x16x32_bf16(__builtin_bit_cast(bf16x8, u32x4{wt[2 * k].x, wt[2 * k].y, wt[2 * k + 1].x, wt[2 * k + 1].y}), sf[k], d, 0, 0, 0);
      st_gran2(rs, so, (col & 1) ? d[2] : d[0], (col & 1) ? d[3] : d[1], tlo, thi);
      lds_add(C_H1);
      STAMP(2);
    }
    u32 ab = off_b(col, 4 * g4), qv = WS_QABS + myh * 8192 + g4 * 1024 + w * 256 + ((col ^ (g4 << 2)) << 4);
    const int hb = 16 * (w & 1) + col;
    u32 pw = L_P + (g4 >> 1) * 512 + hb * 16 + (g4 & 1) * 8, mw = hb * 4;
    PIN(ab); PIN(qv); PIN(pw); PIN(mw);
    if (w == 2) {
      until(t0, QF_T);
      const u32x2 e2 = ld64(rs, WS_EPOCH);
      u32x2 f2 = ld64(rs, WS_SFLAG + xc * 2048 + lane * 8);
      TAG_OF(e2.x, e2.y);
      S_POLL(f2);
      lds_add(C_SOK);
      STAMP(3);
    }
    bf16x8 af[16], qf[16][2];
    u32 aa[16], qa[32];
#pragma unroll
    for (int i = 0; i < 16; ++i) {
      aa[i] = (ab ^ ((i & 3) << 4)) + (((w + (i >> 2)) & 3) * PS + w * 4096); PIN(aa[i]);
#pragma unroll
      for (int u = 0; u < 2; ++u) { qa[2 * i + u] = (ab ^ ((i & 3) << 4)) + (L_QI + ((w + (i >> 2)) & 3) * 8192 + ((u ^ w) & 1) * 4096); PIN(qa[2 * i + u]); }
    }
    const int sl1 = w == 3 ? 0 : 4 + w, sl2 = 1 + (w >> 1);
#define A_RD(i, o) af[i] = *(const bf16x8*)(lds + aa[i] + (o))
#define A_READ(i0, i1, o) _Pragma("unroll") for (int i = (i0); i < (i1); ++i) A_RD(i, o)
#define A_MOVE(i0, i1, d) _Pragma("unroll") for (int i = (i0); i < (i1); ++i) { aa[i] += (d) * 4096; PIN(aa[i]); }
#define Q_RD(i, u) qf[i][u] = *(const bf16x8*)(lds + qa[2 * (i) + (u)])
#define A_FIRST() do { A_READ(0, 16, 0); lds_add(C_FREE); _Pragma("unroll") for (int i = 0; i < 16; ++i) PIN(af[i]); STAMP(12); } while (0)
    bool have = false;
    _Pragma("unroll 1") while ((u32)__builtin_amdgcn_s_memrealtime() - t0 < E3_T) {
      if (__builtin_amdgcn_readfirstlane(ctr[C_MK + w]) >= 4u) { have = true; break; }
      __builtin_amdgcn_s_sleep(1);
    }
    asm volatile("" ::: "memory");
    if (have) A_FIRST();
    lds_wait(ctr + C_SOK, 1u);
    if (isS0) lds_wait(ctr + C_H1, 4u);
    until(t0, E3_T);
    STAMP(6);
#define Q_DMA() _Pragma("unroll") for (int i = 0; i < 8; ++i) dma_sc1(L_QI + w * 8192 + i * 1024, (qv ^ ((i & 3) << 4)) + (i >> 1) * 16384 + (i & 1) * 4096, ws)
    Q_DMA();
    if (RARE(!have)) { lds_wait(ctr + C_MK + w, 4u); A_FIRST(); }
    A_MOVE(0, 16, sl1 - w);
    u32 cb = L_CTR, cw = L_CTR + 4 * w, mx0 = mw + L_MX, mx1 = (mw ^ 64) + L_MX, pw0 = pw + w * 1024, pw1 = (pw ^ 256) + w * 1024;
    float ninf = -__builtin_inff();
    u32 one = 1u;
    PIN(ninf); PIN(one); PIN(cb); PIN(cw); PIN(mx0); PIN(mx1); PIN(pw0); PIN(pw1);
#define CTS(b, i) (*(volatile LAS u32*)(lds + (b) + 4 * (i)))
    _Pragma("unroll 1") for (u32 spins = 0; spins < SPIN_CAP; ++spins) {
      waitvm<0>();
#pragma unroll
      for (int i = 0; i < 8; ++i) Q_RD(i >> 1, i & 1);
      u32 mxu[2] = {0u, 0u};
#pragma unroll
      for (int i = 0; i < 8; ++i) { const u32x4 t_ = __builtin_bit_cast(u32x4, qf[i >> 1][i & 1]); mxu[i & 1] = max(max(mxu[i & 1], t_.x), t_.y); mxu[i & 1] = max(max(mxu[i & 1], t_.z), t_.w); }
      if (!RARE(__any(max(mxu[0], mxu[1]) == POISON))) break;
      STAMP(29);
      __builtin_amdgcn_s_sleep(2);
      const u64 b0 = __ballot(mxu[w & 1] == POISON), b1 = __ballot(mxu[~w & 1] == POISON);
#pragma unroll 1
      for (int i = 0; i < 8; ++i)
        if (((i < 4 ? b0 : b1) >> (4 * (i & 3))) & 0x000F000F000F000Full) dma_sc1(L_QI + w * 8192 + i * 1024, (qv ^ ((i & 3) << 4)) + (i >> 1) * 16384 + (i & 1) * 4096, ws);
    }
    lds_add(C_QRDY);
    STAMP(7);
    f32x4 sc[2], sn[2];
    sc[0] = sc[1] = sn[0] = sn[1] = f32x4{0.f, 0.f, 0.f, 0.f};
#define QK(d, i0, i1, NU)                                                                                                \
  do {                                                                                                                   \
    SB();                                                                                                                \
    _Pragma("unroll") for (int i = (i0); i < (i1); ++i) _Pragma("unroll") for (int u = 0; u < (NU); ++u)                 \
      d[u] = __builtin_amdgcn_mfma_f32_16x16x32_bf16(af[i], qf[i][u], d[u], 0, 0, 0);                                    \
    SB();                                                                                                                \
  } while (0)
#define MF(d, i, u) do { SB(); d[u] = __builtin_amdgcn_mfma_f32_16x16x32_bf16(af[i], qf[i][u], d[u], 0, 0, 0); SB(); } while (0)
    const bool idle1 = isS0 && w == 0;
    u32 c1;
    {
      const u32 c0 = CTS(cb, C_QRDY);
      QK(sc, 0, 2, 2);
      if (RARE(__builtin_amdgcn_readfirstlane(c0) < 4u)) lds_spin(ctr + C_QRDY, 4u);
      asm volatile("" ::: "memory");
#pragma unroll
      for (int i = 2; i < 4; ++i) { MF(sc, i, 0); Q_RD(2 * i, 0); Q_RD(2 * i, 1); MF(sc, i, 1); Q_RD(2 * i + 1, 0); Q_RD(2 * i + 1, 1); }
#pragma unroll
      for (int i = 4; i < 12; ++i) { MF(sc, i, 0); Q_RD(i + 4, 0); MF(sc, i, 1); Q_RD(i + 4, 1); }
      c1 = CTS(cw, C_MK + 4);
      asm volatile("" ::: "memory");
#pragma unroll
      for (int i = 12; i < 16; ++i) { MF(sc, i, 0); A_RD(2 * i - 24, 0); MF(sc, i, 1); A_RD(2 * i - 23, 0); }
    }
    STAMP(9);
    float mref[2], lsum[2] = {0.f, 0.f};
#define ADDC(i, v) do { SB(); asm volatile("s_mov_b64 exec, 1\n ds_add_u32 %0, %1 offset:%2\n s_mov_b64 exec, -1" ::"v"(cb), "v"(v), "n"(4 * (i)) : "memory"); SB(); } while (0)
#define SM(d, T, NU, G, P0, P1)                                                                                          \
  do {                                                                                                                   \
    SB();                                                                                                                \
    if (RARE(__any(max5(d[0], (NU) > 1 ? max5(d[1], ninf) : ninf) > TAU))) {                                             \
      sig = 257u;                                                                                                        \
      _Pragma("unroll") for (int u = 0; u < (NU); ++u) {                                                                 \
        const float up = ceilf(x32_max(x16_max(max5(d[u], 0.f))));                                                       \
        lsum[u] *= __builtin_amdgcn_exp2f(-up);                                                                          \
        mref[u] += up;                                                                                                   \
        d[u] -= up;                                                                                                      \
        *(float*)(lds + (mw ^ (u * 64)) + L_REF + (T) * 128) = mref[u];                                                  \
      }                                                                                                                  \
    }                                                                                                                    \
    u32x2 pk[2];                                                                                                         \
    _Pragma("unroll") for (int u = 0; u < (NU); ++u) {                                                                   \
      pk[u].x = pk2bf(__builtin_amdgcn_exp2f(d[u].x), __builtin_amdgcn_exp2f(d[u].y));                                   \
      pk[u].y = pk2bf(__builtin_amdgcn_exp2f(d[u].z), __builtin_amdgcn_exp2f(d[u].w));                                   \
      *(u32x2*)(lds + (u ? (P1) : (P0))) = pk[u];                                                                        \
    }                                                                                                                    \
    ADDC(C_P + (G), sig);                                                                                                \
    _Pragma("unroll") for (int u = 0; u < (NU); ++u) { lsum[u] = dot2(pk[u].x, 0x3F803F80u, dot2(pk[u].y, 0x3F803F80u, lsum[u])); PIN(lsum[u]); } \
    SB();                                                                                                                \
  } while (0)
#define REF_WRITE(T, NU) _Pragma("unroll") for (int u = 0; u < (NU); ++u) *(float*)(lds + (mw ^ (u * 64)) + L_REF + (T) * 128) = mref[u]
    u32 sig = one;
    {
      MF(sn, 0, 0); A_RD(8, 0); MF(sn, 0, 1);
      const float m0 = max5(sc[0], ninf), m1 = max5(sc[1], ninf);
      MF(sn, 1, 0);
      asm volatile("ds_max_f32 %0, %2\n ds_max_f32 %1, %3" ::"v"(mx0), "v"(mx1), "v"(m0), "v"(m1) : "memory");
      ADDC(C_MXC, one);
      MF(sn, 1, 1); A_RD(9, 0);
#pragma unroll
      for (int i = 2; i < 4; ++i) { MF(sn, i, 0); A_RD(2 * i + 6, 0); MF(sn, i, 1); A_RD(2 * i + 7, 0); }
      MF(sn, 4, 0);
      const u32 c0 = CTS(cb, C_MXC);
      mref[0] = *(volatile LAS float*)(lds + mx0);
      mref[1] = *(volatile LAS float*)(lds + mx1);
      MF(sn, 4, 1); MF(sn, 5, 0); A_RD(14, 0); MF(sn, 5, 1); A_RD(15, 0); MF(sn, 6, 0); MF(sn, 6, 1);
      if (RARE(__builtin_amdgcn_readfirstlane(c0) < 4u)) {
        lds_spin(ctr + C_MXC, 4u);
        mref[0] = *(volatile LAS float*)(lds + mx0);
        mref[1] = *(volatile LAS float*)(lds + mx1);
      }
      u32x2 pk[2];
#pragma unroll
      for (int u = 0; u < 2; ++u) {
        MF(sn, 7 + 2 * u, 0);
        mref[u] = ceilf(mref[u]);
        float e0 = __builtin_amdgcn_exp2f(sc[u].x - mref[u]); PIN(e0);
        MF(sn, 7 + 2 * u, 1);
        pk[u].x = pk2bf(e0, __builtin_amdgcn_exp2f(sc[u].y - mref[u])); PIN(pk[u].x);
        MF(sn, 8 + 2 * u, 0);
        e0 = __builtin_amdgcn_exp2f(sc[u].z - mref[u]); PIN(e0);
        MF(sn, 8 + 2 * u, 1);
        pk[u].y = pk2bf(e0, __builtin_amdgcn_exp2f(sc[u].w - mref[u]));
        *(u32x2*)(lds + (u ? pw1 : pw0)) = pk[u];
      }
      ADDC(C_P, one);
      MF(sn, 11, 0);
      lsum[0] = dot2(pk[0].x, 0x3F803F80u, dot2(pk[0].y, 0x3F803F80u, 0.f)); PIN(lsum[0]);
      MF(sn, 11, 1);
      lsum[1] = dot2(pk[1].x, 0x3F803F80u, dot2(pk[1].y, 0x3F803F80u, 0.f)); PIN(lsum[1]);
      MF(sn, 12, 0);
      REF_WRITE(4 + w, 2);
      MF(sn, 12, 1);
      QK(sn, 13, 16, 2);
    }
    STAMP(10);
#define QK1(N0, slot)                                                                                                    \
  do {                                                                                                                   \
    _Pragma("unroll") for (int i = 0; i < 16; ++i) { MF(sc, i, 0); if (i + (N0) < 16) A_RD(i + (N0), slot); }            \
  } while (0)
    if (RARE(__builtin_amdgcn_readfirstlane(c1) < 4u && !idle1)) {
      lds_spin(ctr + C_MK + 4 + w, 4u);
      A_READ(0, 16, 0);
      sn[0] = sn[1] = f32x4{0.f, 0.f, 0.f, 0.f};
      QK(sn, 0, 16, 2);
    }
    if (RARE(idle1)) lds_add(C_P + 1);
    else { sn[0] -= mref[0]; sn[1] -= mref[1]; SM(sn, 4 + w, 2, 1, pw0 + 4096, pw1 + 4096); }
    if (RARE(w == 2)) {
      RELANE();
      dma_sc1(L_QI, WS_VOTE + xc * 2048 + lane * 16, ws);
    }
    REF_WRITE(7 + sl2, 1);
    A_MOVE(0, 16, sl2 - sl1);
    pw0 = pw + sl2 * 1024;
    sc[0] = f32x4{-mref[0], -mref[0], -mref[0], -mref[0]};
    PIN(pw0); PIN(sc[0]);
    lds_spin(ctr + C_MK + 7 + sl2, 4u);
    A_READ(0, 6, 0);
    STAMP(4);
    QK1(6, 0);
    SM(sc, 7 + sl2, 1, 2, pw0 + 7168, 0);
    STAMP(11);
    if (!RARE(w >= 2)) {
      REF_WRITE(10, 1);
      sc[0] = f32x4{-mref[0], -mref[0], -mref[0], -mref[0]};
      PIN(sc[0]);
      _Pragma("unroll 1") for (u32 n_ = 0; n_ < LSPIN_CAP; ++n_) {
        const u32 k_ = CTS(cb, C_MK + 10);
        asm volatile("" ::: "memory");
        A_READ(0, 6, 8192);
        SB();
        if (__builtin_amdgcn_readfirstlane(k_) >= 4u) break;
      }
      STAMP(5);
      QK1(6, 8192);
      SM(sc, 10, 1, 3, pw0 + 9216, 0);
    } else if (w == 2) {
      RELANE();
      waitvm<0>();
      COUNT_VOTES(true);
      lds_add(C_GX);
    }
    STAMP(30);
    RELANE();
    {
#pragma unroll
      for (int u = 0; u < 2; ++u) {
        *(float*)(lds + L_ML + (mw ^ (u * 64)) * 4 + w * 4) = mref[u];
        *(float*)(lds + L_ML + 512 + (mw ^ (u * 64)) * 4 + w * 4) = x32_sum(x16_sum(lsum[u]));
      }
      u32 old;
      f32x4 m4, l4;
      asm volatile("s_mov_b64 exec, 1\n ds_add_rtn_u32 %0, %3, %4 offset:%6\n s_mov_b64 exec, -1\n ds_read_b128 %1, %5\n ds_read_b128 %2, %5 offset:512\n s_waitcnt lgkmcnt(0)"
                   : "=&v"(old), "=&v"(m4), "=&v"(l4) : "v"((u32)L_CTR), "v"(1u), "v"(L_ML + (lane & 31) * 16), "n"(4 * C_SD) : "memory");
      if (__builtin_amdgcn_readfirstlane(old) == 3u) {
        const float M = fmaxf(fmaxf(m4.x, m4.y), fmaxf(m4.z, m4.w));
        const float l = (l4.x * __builtin_amdgcn_exp2f(m4.x - M) + l4.y * __builtin_amdgcn_exp2f(m4.y - M)) + (l4.z * __builtin_amdgcn_exp2f(m4.z - M) + l4.w * __builtin_amdgcn_exp2f(m4.w - M));
        *(u32x2*)(lds + L_WMAX + (lane & 1) * 128 + (lane & 30) * 4) = u32x2{fbits(M), fbits(l)};
        lds_add(C_ML);
      }
    }
    lds_wait(ctr + C_GX, 1u);
    gx = __builtin_amdgcn_readfirstlane(ctr[C_PAR]);
    STAMP(13);
  } else {
    const int mu = 2 * j + (w >> 2);
#define Q_LOAD(i0, i1)                                                                                                   \
  _Pragma("unroll") for (int i = (i0); i < (i1); ++i) {                                                                  \
    waitvm<QW - 1>();                                                                                                    \
    dma<false>(w * 16384 + (i & 15) * 1024, (vq ^ ((i & 3) << 4)) + ((i < 8 ? 64 * w + 32 * dh : i < 16 ? 64 * w + 32 - 32 * dh : 256 * dh + 32 * w) + 4 * (i & 7)) * 8192, i < 16 ? w_q : w_uk); \
  }
    const u32 vq = g4 * 8192 + hq * 256 + ((col ^ (g4 << 2)) << 4);
    u32x4 xv;
    u32x2 e2;
    u32 x2_;
    asm volatile("global_load_dwordx4 %0, %3, %5\n global_load_dword %1, %4, %5\n buffer_load_dwordx2 %2, off, %6, 0 sc1"
                 : "=&v"(xv), "=&v"(x2_), "=&v"(e2) : "v"(lane * 16), "v"(128 * w + 4 * (lane & 31)), "s"(x), "s"(rs) : "memory");
    bf16x8 wf[16], wk[8];
    u32 ta0 = w * 16384 + off_b(8 * g4 + tq, tp >> 1) + 8 * (tp & 1), ta1 = w * 16384 + off_b(8 * g4 + 4 + tq, tp >> 1) + 8 * (tp & 1);
    int so = WS_SC + ((lane >> 5) * 2048 + 64 * w + 2 * (lane & 31)) * 8;
    int fo = w == 0 ? WS_SFLAG + xc * 2048 + lane * 8 : WS_TOTAL;
    PIN(ta0); PIN(ta1); PIN(so); PIN(fo);
    Q_LOAD(4, 8 + QW);
    waitvm<QW>();
#pragma unroll
    for (int c = 0; c < 8; ++c) wf[c] = tr_frag(lds, ta0 ^ (c << 5), ta1 ^ (c << 5));
    Q_LOAD(8 + QW, 16);
#pragma unroll
    for (int c = 0; c < 8; ++c) PIN(wf[c]);
    Q_LOAD(16, 24);
    STAMP(12);
    until(t0, E1_T);
    STAMP(23);
    u32x4 g[4];
    asm volatile("buffer_load_dwordx4 %0, %4, %5, 0 offen sc1\n buffer_load_dwordx4 %1, %4, %5, %6 offen sc1\n"
                 "buffer_load_dwordx4 %2, %4, %5, %7 offen sc1\n buffer_load_dwordx4 %3, %4, %5, %8 offen sc1"
                 : "=&v"(g[0]), "=&v"(g[1]), "=&v"(g[2]), "=&v"(g[3]) : "v"(so), "s"(rs), "s"(4096), "s"(8192), "s"(12288) : "memory");
    u32x2 fl;
    asm volatile("buffer_load_dwordx2 %0, %1, %2, 0 offen sc1" : "=&v"(fl) : "v"(fo), "s"(rs) : "memory");
    asm volatile("s_waitcnt vmcnt(13)" : "+v"(xv), "+v"(x2_), "+v"(e2)::"memory");
#pragma unroll
    for (int c = 0; c < 8; ++c) wf[8 + c] = tr_frag(lds, (ta0 ^ (c << 5)) + 8192, (ta1 ^ (c << 5)) + 8192);
    TAG_OF(e2.x, e2.y);
    float ln0, ln1;
    {
      float xe[8] = {lo16(xv.x), hi16(xv.x), lo16(xv.y), hi16(xv.y), lo16(xv.z), hi16(xv.z), lo16(xv.w), hi16(xv.w)};
      float s = 0.f;
#pragma unroll
      for (int k = 0; k < 8; ++k) s += xe[k];
      const float mean = wave_sum(s) * (1.f / 512.f);
      float d2 = 0.f;
#pragma unroll
      for (int k = 0; k < 8; ++k) d2 += (xe[k] - mean) * (xe[k] - mean);
      const float var = rbf(wave_sum(d2) * (1.f / 512.f));
      const float mb = rbf(mean);
      const float sd = rbf(sqrtf(rbf(var + 1e-5f)));
      ln0 = rbf(rbf(lo16(x2_) - mb) / sd);
      ln1 = rbf(rbf(hi16(x2_) - mb) / sd);
    }
    PIN(ln0); PIN(ln1);
    int qpo = (lane & 7) == 0 ? WS_QABS + il(hq * 1024 + dh * 512 + w * 64 + (lane >> 5) * 32 + (lane & 8) * 2, g4 & 1) : WS_TOTAL;
    u32 hw_ = L_H1RED + w * 128 + (lane & 31) * 4, hr_ = L_H1RED + w * 128 + 16 * g4 + 64 * dh, qw_ = L_H1RED + 1024 + w * 272 + lane * 2, qr_ = L_H1RED + 1024 + col * 272 + 64 * g4;
    PIN(qpo); PIN(hw_); PIN(hr_); PIN(qw_); PIN(qr_);
    waitvm<5>();
#pragma unroll
    for (int i = 0; i < 8; ++i) wk[i] = *(const bf16x8*)(lds + w * 16384 + (i >> 2) * 4096 + off_b(col, 4 * g4 + (i & 3)));
#pragma unroll
    for (int c = 0; c < 8; ++c) { PIN(wf[8 + c]); PIN(wk[c]); }
    STAMP(4);
    asm volatile("s_waitcnt vmcnt(0)" : "+v"(g[0]), "+v"(g[1]), "+v"(g[2]), "+v"(g[3]), "+v"(fl)::"memory");
    _Pragma("unroll 1") for (u32 spins = 0; spins < SPIN_CAP; ++spins) {
      bool ok = true;
#pragma unroll
      for (int k = 0; k < 4; ++k) ok = ok & (gran_ok(g[k].x, g[k].y, tlo, thi) & gran_ok(g[k].z, g[k].w, tlo, thi));
      if (!RARE(!__all(ok))) break;
      STAMP(29);
      __builtin_amdgcn_s_sleep(1);
      asm volatile("" ::: "memory");
#pragma unroll
      for (int k = 0; k < 4; ++k) g[k] = ld128(rs, so + k * 4096);
    }
    STAMP(1);
    {
      const float m0 = rbf((gran_val(g[0].x) + gran_val(g[1].x)) + (gran_val(g[2].x) + gran_val(g[3].x)));
      const float m1 = rbf((gran_val(g[0].z) + gran_val(g[1].z)) + (gran_val(g[2].z) + gran_val(g[3].z)));
      const auto s0 = __builtin_amdgcn_permlane32_swap(__float_as_uint(m0), __float_as_uint(m0), false, false);
      const auto s1 = __builtin_amdgcn_permlane32_swap(__float_as_uint(m1), __float_as_uint(m1), false, false);
      *(u32*)(lds + hw_) = pk2bf(rbf(ln0 * rbf(1.f + __uint_as_float(s0[1]))) + __uint_as_float(s0[0]),
                                 rbf(ln1 * rbf(1.f + __uint_as_float(s1[1]))) + __uint_as_float(s1[0]));
    }
    WAVE_LDS_SYNC();
    {
      const bf16x8 h0 = *(const bf16x8*)(lds + hr_), h1 = *(const bf16x8*)(lds + (hr_ ^ 64));
      f32x4 d[8];
#pragma unroll
      for (int c = 0; c < 8; ++c) d[c] = __builtin_amdgcn_mfma_f32_16x16x32_bf16(h0, wf[c], f32x4{0.f, 0.f, 0.f, 0.f}, 0, 0, 0);
#pragma unroll
      for (int c = 0; c < 8; ++c) d[c] = __builtin_amdgcn_mfma_f32_16x16x32_bf16(h1, wf[8 + c], d[c], 0, 0, 0);
#pragma unroll
      for (int k = 0; k < 2; ++k) {
        const float v = (g4 & 2) ? ((g4 & 1) ? d[4 * k + 3][0] : d[4 * k + 2][0]) : ((g4 & 1) ? d[4 * k + 1][0] : d[4 * k][0]);
        const u32 vh = f2bf(v);
        *(u16*)(lds + qw_ + 128 * k) = (u16)vh;
        *(u16*)(lds + qw_ + 128 * k + 8 * 272) = (u16)f2bf(v - bf2f(vh));
      }
    }
    STAMP(2);
    if (w == 0) S_POLL(fl);
    BAR();
    {
      bf16x8 a[4];
#pragma unroll
      for (int s = 0; s < 4; ++s) a[s] = *(const bf16x8*)(lds + qr_ + 16 * s);
      f32x4 d[2] = {f32x4{0.f, 0.f, 0.f, 0.f}, f32x4{0.f, 0.f, 0.f, 0.f}};
#pragma unroll
      for (int s = 0; s < 4; ++s)
#pragma unroll
        for (int c = 0; c < 2; ++c) d[c] = __builtin_amdgcn_mfma_f32_16x16x32_bf16(a[s], wk[4 * c + s], d[c], 0, 0, 0);
      constexpr float SCALE = 0.08838834764831845f * 1.4426950408889634f;
      const float v = SCALE * x16_sum(rs32(sum4(d[0]), sum4(d[1])));
      const float p0 = __uint_as_float(nopois(pk2bf(dppf<0x00>(v), dppf<0x55>(v)))), p1 = __uint_as_float(nopois(pk2bf(dppf<0xAA>(v), dppf<0xFF>(v))));
      st128(rs, qpo, u32x4{__float_as_uint(p0), __float_as_uint(p1), __float_as_uint(dppf<0x104>(p0)), __float_as_uint(dppf<0x104>(p1))});
    }
    STAMP(5);
    SB();
    {
      st128(rs, lane < 50 ? WS_PCTX + (j * 8 + w) * 800 + lane * 16 : WS_TOTAL, P4);
      const int pp = __builtin_amdgcn_readfirstlane(WS_PROBE + il((j * 8 + w) * 128, w & 1));
      u64 ta, tb, tc;
      u32 dm;
      asm volatile("s_load_dword %3, %4, 0x0\n s_waitcnt lgkmcnt(0)\n"
                   "s_memtime %0\n s_waitcnt lgkmcnt(0)\n s_load_dword %3, %4, %5 glc\n s_waitcnt lgkmcnt(0)\n"
                   "s_memtime %1\n s_waitcnt lgkmcnt(0)\n s_load_dword %3, %4, %6 glc\n s_waitcnt lgkmcnt(0)\n"
                   "s_memtime %2\n s_waitcnt vmcnt(0) lgkmcnt(0)"
                   : "=&s"(ta), "=&s"(tb), "=&s"(tc), "=&s"(dm) : "s"(ws), "s"(pp), "s"(pp ^ 8192) : "memory");
      int df = min(max(((int)(u32)(tb - ta) - (int)(u32)(tc - tb)) >> 3, -VCLIP), VCLIP);
      df = ((w ^ myh) & 1) ? -df : df;
      u32 old = 0u;
      if (lane == 0) old = __hip_atomic_fetch_add((LAS u32*)(lds + L_CTR) + C_VOTE, (u32)(65536 + 64 + df), __ATOMIC_RELAXED, __HIP_MEMORY_SCOPE_WORKGROUP);
      old = __builtin_amdgcn_readfirstlane(old) + 65536 + 64 + df;
      if (old >= 524288u && lane < 8) st64(rs, WS_VOTE + lane * 2048 + j * 8, (old & 1023u) | (tlo << 24), (tlo >> 8) | (thi << 24));
      STAMP(6);
    }
    {
      const int r0 = 128 * (mu & 3) + 32 * (w & 3) + 4 * (lane >> 3);
      const u32x2 cvb = *(const u32x2*)(cvec + r0);
      u32x4 wsc[4];
#pragma unroll
      for (int i = 0; i < 4; ++i) wsc[i] = ldnt(w_mod + (size_t)(r0 + i) * 3072 + 1024 + (mu >> 2) * 64 + (lane & 7) * 8);
      float a[8];
#pragma unroll
      for (int k = 0; k < 8; ++k) a[k] = 0.f;
#pragma unroll
      for (int i = 0; i < 4; ++i) fma8b(a, f2bf(silu_bf((i & 1) ? hi16(i < 2 ? cvb.x : cvb.y) : lo16(i < 2 ? cvb.x : cvb.y))), wsc[i]);
      float r[4];
#pragma unroll
      for (int k = 0; k < 4; ++k) r[k] = rs32(a[k], a[k + 4]);
      const float o0 = b3_sum(rs16(r[0], r[2])), o1 = b3_sum(rs16(r[1], r[3]));
      float* red = (float*)(lds + L_H1RED + 8192);
      if ((lane & 8) == 0) *(f32x2*)(red + w * 64 + (lane & 7) * 8 + (lane >> 5) * 4 + ((lane >> 4) & 1) * 2) = f32x2{o0, o1};
      BAR();
      if ((w & 3) == 3 && lane < 16) {
        red += (w >> 2) * 256 + lane * 4;
        const f32x4 sm = (*(const f32x4*)(red) + *(const f32x4*)(red + 64)) + (*(const f32x4*)(red + 128) + *(const f32x4*)(red + 192));
        st128(rs, WS_MODB + ((mu & 3) * 2048 + (mu >> 2) * 64 + lane * 4) * 4, u32x4{fbits(sm.x), fbits(sm.y), fbits(sm.z), fbits(sm.w)});
      }
    }
    gx = 0;
  }

  RELANE();
  const int r16 = lane >> 2, ch = lane & 3;
  if (ii >= 16) {
    int mo = lane < 24 ? (WS_LSE + il((24 * w + lane) * 512 + c4 * 128 + (h >> 1) * 8, h & 1)) ^ gx : WS_TOTAL;
    PIN(mo);
    lds_spin(ctr + C_PD, 4u);
    STAMP(23);
    u32x2 mt = ld64(rs, mo);
    SB();
    const u32 pb = (w >> 1) * PS + (w & 1) * 6144;
    u32 po[6];
#pragma unroll
    for (int k = 0; k < 6; ++k) {
      po[k] = g4 * 256 + ((col ^ (g4 << 2) ^ (k & 3)) << 4) + ((WS_PART + il21(((h >> 1) * 4 + c4) * 65536 + (24 * w + 4 * k) * 256, h & 1)) ^ gx);
      PIN(po[k]);
    }
    bf16x8 wk[4];
    {
      const u32 wa = tq * PS + 16384 + off_b(g4, 2 * w + (tp >> 1)) + 8 * (tp & 1);
#pragma unroll
      for (int s = 0; s < 4; ++s) wk[s] = tr_frag(lds, (wa ^ (s << 4)) + s * 1024, (wa ^ (s << 4)) + s * 1024 + 4096);
    }
    SB();
    u32 done = 0u;
    _Pragma("unroll 1") for (u32 spins = 0; spins < SPIN_CAP; ++spins) {
      asm volatile("" ::: "memory");
      if (spins && max(mt.x, mt.y) == POISON) mt = ld64(rs, mo);
      const u32 ok = ~(u32)__ballot(max(mt.x, mt.y) == POISON);
      u32 nw = ok & (ok >> 1);
      nw = nw & (nw >> 2) & 0x111111u & ~done;
      done |= nw;
#pragma unroll
      for (int k = 0; k < 6; ++k)
        if (nw & (1u << (4 * k))) dma_sc1(pb + k * 1024, po[k], ws);
      if (spins == 0) STAMP(21);
      if (done == 0x111111u) break;
      __builtin_amdgcn_s_sleep(2);
    }
    STAMP(16);
    u32 pa[16];
#pragma unroll
    for (int i = 0; i < 16; ++i) { pa[i] = pb + off_b(8 * (g4 % 3) + 4 * (i & 1) + tq, (i & 14) + (tp >> 1)) + 8 * (tp & 1); PIN(pa[i]); }
    u32 bo = L_QOFF + col * 272 + g4 * 16, mlo = L_QOFF + 4352 + (col & 7) * 8, ro = L_QOFF + (w + (col & 8)) * 272 + (col & 7) * 32 + g4 * 8;
    int so1 = col < 2 ? (WS_PCTX + h * 2048 + c4 * 512 + (w >> 2) * 256 + (2 * (w & 1) + (g4 >> 1)) * 64 + (((w >> 1) & 1) * 2 + col) * 16 + (g4 & 1) * 8) ^ gx : WS_TOTAL;
    PIN(bo); PIN(mlo); PIN(ro); PIN(so1);
    {
      const u32 eo = L_QOFF + 4416 + w * 64;
      const float m = lane < 24 ? __uint_as_float(mt.x) : -__builtin_inff();
      const float M = wave_max(m);
      const float e = __builtin_amdgcn_exp2f(m - M);
      if (lane < 32) *(u16*)(lds + eo + lane * 2) = (u16)f2bf(e);
      WAVE_LDS_SYNC();
      const u32x4 ev4 = *(const u32x4*)(lds + eo + 16 * g4);
      const float L = wave_sum(e * __uint_as_float(mt.y));
      if (lane == 0) *(f32x2*)(lds + L_QOFF + 4352 + w * 8) = f32x2{M, L};
      bf16x8 wf[8], pt[8];
#pragma unroll
      for (int i = 0; i < 8; ++i) { wf[i] = __builtin_bit_cast(bf16x8, (col & 7) == i ? ev4 : Z4); PIN(wf[i]); }
      waitvm<0>();
      STAMP(17);
#pragma unroll
      for (int i = 0; i < 8; ++i) pt[i] = tr_frag(lds, pa[2 * i], pa[2 * i + 1]);
      SB();
      f32x4 d = {0.f, 0.f, 0.f, 0.f};
#pragma unroll
      for (int i = 0; i < 8; ++i) d = __builtin_amdgcn_mfma_f32_16x16x32_bf16(pt[i], wf[i], d, 0, 0, 0);
      const u32 h0 = pk2bf(d[0], d[1]), h1 = pk2bf(d[2], d[3]);
      const u32 l0 = pk2bf(d[0] - lo16(h0), d[1] - hi16(h0)), l1 = pk2bf(d[2] - lo16(h1), d[3] - hi16(h1));
      *(u32x2*)(lds + ro) = (col & 8) ? u32x2{l0, l1} : u32x2{h0, h1};
    }
    BAR();
    STAMP(14);
    const f32x2 ml = *(const f32x2*)(lds + mlo);
    bf16x8 lf[4];
#pragma unroll
    for (int s = 0; s < 4; ++s) lf[s] = *(const bf16x8*)(lds + bo + 64 * s);
    SB();
    f32x4 d = {0.f, 0.f, 0.f, 0.f};
#pragma unroll
    for (int s = 0; s < 4; ++s) d = __builtin_amdgcn_mfma_f32_16x16x32_bf16(wk[s], lf[s], d, 0, 0, 0);
    SB();
    float Mg = fmaxf(ml.x, dppf<0xB1>(ml.x));
    Mg = fmaxf(Mg, dppf<0x4E>(Mg)); Mg = fmaxf(Mg, dppf<0x141>(Mg));
    const float e = __builtin_amdgcn_exp2f(ml.x - Mg);
    float Lg = quad_sum(e * ml.y);
    Lg += dppf<0x141>(Lg);
    const float sc = e * __builtin_amdgcn_rcpf(Lg);
    {
      float d0 = d[0] * sc, d1 = d[1] * sc, d2 = d[2] * sc, d3 = d[3] * sc;
      row16_sum4(d0, d1, d2, d3);
      d = f32x4{d0, d1, d2, d3};
    }
    const u32 h0 = pk2bf(d[0], d[1]), h1 = pk2bf(d[2], d[3]);
    const u32 l0 = pk2bf(d[0] - lo16(h0), d[1] - hi16(h0)), l1 = pk2bf(d[2] - lo16(h1), d[3] - hi16(h1));
    st64(rs, so1, nopois(col ? l0 : h0), nopois(col ? l1 : h1));
    STAMP(15);
    STAMP_FLUSH();
    return;
  }
  if (tl2) {
    BAR0();
#pragma unroll
    for (int i = 0; i < 8; ++i) dma<true>(owno + i * 1024, (ks * 512 + 64 * w + 8 * i + (lane >> 3)) * 1024 + cg * 128 + (lane & 7) * 16, w_o);
    waitvm<0>();
    STAMP(26);
    bf16x8 wf[8];
#pragma unroll
    for (int i = 0; i < 8; ++i) {
      const u32 a = owno + (32 * (i >> 2) + 8 * g4 + tq) * 128 + (i & 3) * 32 + tp * 8;
      wf[i] = tr_frag(lds, a, a + 512);
    }
#pragma unroll
    for (int i = 0; i < 8; ++i) PIN(wf[i]);
    STAMP(23);
    int off[1] = {(WS_PCTX + (ks * 4 + (w >> 1)) * 2048 + tq * 512 + (w & 1) * 256 + g4 * 64 + tp * 16) ^ gx};
    u32 rw = L_RED + lane * 32 + w * 4;
    PIN(rw);
    int so2 = tp < 2 ? WS_PATTN + il((ks * 512 + cg * 64 + (lane & 60)) * 4, tp) : WS_TOTAL;
    PIN(so2);
    u32x4 g[1];
    gather128<1, 4>(rs, off, g);
    STAMP(24);
    const bf16x8 a0 = __builtin_bit_cast(bf16x8, (col & 2) ? Z4 : g[0]), a1 = __builtin_bit_cast(bf16x8, (col & 2) ? g[0] : Z4);
    f32x4 d[4];
#pragma unroll
    for (int c = 0; c < 4; ++c) d[c] = __builtin_amdgcn_mfma_f32_16x16x32_bf16(a0, wf[c], f32x4{0.f, 0.f, 0.f, 0.f}, 0, 0, 0);
#pragma unroll
    for (int c = 0; c < 4; ++c) d[c] = __builtin_amdgcn_mfma_f32_16x16x32_bf16(a1, wf[4 + c], d[c], 0, 0, 0);
    *(float*)(lds + rw) = rs16(rs32(sum4(d[0]), sum4(d[2])), rs32(sum4(d[1]), sum4(d[3])));
    BAR();
    if (w == 0) {
      const float v = sum4(*(const f32x4*)(lds + L_RED + lane * 32) + *(const f32x4*)(lds + L_RED + lane * 32 + 16));
      const float vn = __uint_as_float(fbits(v));
      st128(rs, so2, u32x4{__float_as_uint(dppf<0x00>(vn)), __float_as_uint(dppf<0x55>(vn)), __float_as_uint(dppf<0xAA>(vn)), __float_as_uint(dppf<0xFF>(vn))});
    }
    STAMP(17);
  } else if (t3 >= 0) {
    STAMP(20);
    if (w < 4) {
      const int fr = ((128 * w + r16) * DF + t3 * 32) * 2, ca_ = ch ^ ((r16 >> 2) & 2) ^ (t3 & 1);
      const u16* wa = (t3 & 1) ? w_up : w_gate;
      const u16* wb = (t3 & 1) ? w_gate : w_up;
      const u32 d = w * 32768 + 8192;
#pragma unroll
      for (int i = 0; i < 8; ++i) { waitvm<WING - 1>(); dma<false>(d + (i >> 2) * 12288 + (t3 & 1) * 4096 + (i & 3) * 1024, fr + ca_ * 16 + 32 * i * DF, wa); }
#pragma unroll
      for (int i = 0; i < 8; ++i) { waitvm<WING - 1>(); dma<false>(d + (i >> 2) * 12288 + 4096 - (t3 & 1) * 4096 + (i & 3) * 1024, fr + (ca_ ^ 1) * 16 + 32 * i * DF, wb); }
    } else {
#pragma unroll
      for (int i = 0; i < 8; ++i) {
        waitvm<WING - 1>();
        dma<true>(lw * 32768 + 16384 + (i >> 2) * 12288 + (i & 3) * 1024, (32 * t3 + 8 * (i & 3) + (lane >> 3)) * 1024 + 256 * lw + 128 * (i >> 2) + (lane & 7) * 16, w_down);
      }
      const u32 x2 = *(const u32*)(x + 128 * lw + 2 * lane);
      u32x4 m6[6];
      int mo[6];
#pragma unroll
      for (int i = 0; i < 6; ++i) mo[i] = MB_OFF(i);
      gather128<6, 8>(rs, mo, m6);
#pragma unroll
      for (int i = 0; i < 6; ++i) {
        const f32x4 f = __builtin_bit_cast(f32x4, m6[i]);
        const float v = rbf(rs16(rs32(f.x, f.z), rs32(f.y, f.w)));
        *(u16*)(lds + L_Z + (i < 2 ? 5 : i < 4 ? 3 : 2) * 1088 + (128 * lw + 64 * (i & 1) + 4 * col + g4) * 2) = (u16)f2bf(i < 4 ? v : 1.f + v);
      }
      *(u32*)(lds + L_Z + 6 * 1088 + lw * 256 + lane * 4) = x2;
      if (w == 4 && lane < 32) *(u32*)(lds + L_ZERO + lane * 4) = lane < 16 ? 0u : POISON;
      if (w == 6) COUNT_VOTES(false);
    }
    waitvm<0>();
    STAMP(26);
    BAR();
    gx = __builtin_amdgcn_readfirstlane(ctr[C_PAR]);
    u32 zw = L_Z + (64 * w + 4 * col + g4) * 2;
    u32 hr[4];
#pragma unroll
    for (int i = 0; i < 4; ++i) { hr[i] = (col == i ? L_HH : L_ZERO) + 16 * g4; PIN(hr[i]); }
    int off[2];
#pragma unroll
    for (int i = 0; i < 2; ++i) { off[i] = (WS_PATTN + il(((4 * i + g4) * 512 + 64 * w + col * 4) * 4, myh)) ^ gx; PIN(off[i]); }
    int so = col < 4 ? (WS_PDOWN + il((t3 * 256 + 64 * (w & 3) + 16 * col + 4 * g4) * 4, w >> 2)) ^ gx : WS_TOTAL;
    u32 zr = L_Z + tp * 1088 + 16 * g4, yr = L_Z + 4 * 1088 + lane * 16, hw = L_HH + (w & 3) * 16 + (g4 & 1) * 8 + tp * 2, cr = L_CF + (w & 3) * 16 + tp * 4;
    PIN(cr); PIN(so); PIN(zw); PIN(zr); PIN(yr); PIN(hw);
    float ga = bf2f(*(const u16*)(lds + zw + 5 * 1088)), cb1 = bf2f(*(const u16*)(lds + zw + 2 * 1088)), xe = bf2f(*(const u16*)(lds + zw + 6 * 1088));
    bf16x8 wf[16], wd[4];
    if (w < 4) {
#pragma unroll
      for (int k = 0; k < 16; ++k) {
        const u32 a = (k >> 2) * 32768 + 8192 + ((k >> 1) & 1) * 12288 + (tp >> 1) * 4096 + ((k & 1) * 32 + 8 * g4 + tq) * 64 + (w ^ ((g4 & 1) * 2) ^ (tp >> 1)) * 16 + (tp & 1) * 8;
        wf[k] = tr_frag(lds, a, a + 256);
      }
#pragma unroll
      for (int k = 0; k < 16; ++k) PIN(wf[k]);
    }
#pragma unroll
    for (int i = 0; i < 4; ++i) {
      const u32 a = owno + 8192 + (8 * g4 + tq) * 128 + i * 32 + tp * 8;
      wd[i] = tr_frag(lds, a, a + 512);
    }
#pragma unroll
    for (int i = 0; i < 4; ++i) PIN(wd[i]);
    PIN(ga); PIN(cb1); PIN(xe);
    STAMP(23);
    until(t0, E6_T);
    u32x4 pa2[2];
    gather128<2, 4>(rs, off, pa2);
    STAMP(18);
    {
      const f32x4 s4 = __builtin_bit_cast(f32x4, pa2[0]) + __builtin_bit_cast(f32x4, pa2[1]);
      const u32 yb = f2bf(xe + rbf(ga * rbf(rs16(rs32(s4.x, s4.z), rs32(s4.y, s4.w)))));
      const float z = bf2f(yb) * cb1;
      const u32 zh = f2bf(z);
      *(u16*)(lds + zw + 4 * 1088) = (u16)yb;
      *(u16*)(lds + zw) = (u16)zh;
      *(u16*)(lds + zw + 1088) = (u16)f2bf(z - bf2f(zh));
    }
    BAR();
    if (w < 4) {
      bf16x8 zf[16];
#pragma unroll
      for (int k = 0; k < 16; ++k) zf[k] = *(const bf16x8*)(lds + zr + 64 * k);
      __builtin_amdgcn_sched_barrier(0);
      f32x4 d = {0.f, 0.f, 0.f, 0.f};
#pragma unroll
      for (int k = 0; k < 16; ++k) d = __builtin_amdgcn_mfma_f32_16x16x32_bf16(wf[k], zf[k], d, 0, 0, 0);
      PIN(d);
      u32 cfb;
      do { cfb = *(volatile LAS u32*)(lds + cr); } while (__any(cfb == POISON));
      d *= __uint_as_float(cfb);
      const float a0 = (tp & 1) ? d[1] : d[0], b0 = (tp & 1) ? d[0] : d[1], a1 = (tp & 1) ? d[3] : d[2], b1 = (tp & 1) ? d[2] : d[3];
      const float e0 = a0 + dppf<0xB1>(b0), e1 = a1 + dppf<0xB1>(b1);
      const float v = rbf(((tp & 2) ? e1 : e0) + dppf<0x4E>((tp & 2) ? e0 : e1));
      const auto sw = __builtin_amdgcn_permlane32_swap(__float_as_uint(v), __float_as_uint(v), false, false);
      const float g = __uint_as_float(sw[0]), u = __uint_as_float(sw[1]);
      const float e2 = __builtin_amdgcn_exp2f(g * (-2.3022081981f - 0.1029432396f * g * g));
      *(u16*)(lds + hw) = (u16)f2bf(rbf(g * __builtin_amdgcn_rcpf(e2 + 1.f)) * u);
    } else {
      const u32x4 yv = *(const u32x4*)(lds + yr);
      const float a1 = dot2(yv.x, 0x3F803F80u, dot2(yv.y, 0x3F803F80u, 0.f)) + dot2(yv.z, 0x3F803F80u, dot2(yv.w, 0x3F803F80u, 0.f));
      const float a2 = dot2(yv.x, yv.x, dot2(yv.y, yv.y, 0.f)) + dot2(yv.z, yv.z, dot2(yv.w, yv.w, 0.f));
      const float sr = x16_sum(row16_sum(rs32(a1, a2))) * (1.f / 512.f);
      const float s1 = __uint_as_float(__builtin_amdgcn_readlane(__float_as_uint(sr), 0)), s2 = __uint_as_float(__builtin_amdgcn_readlane(__float_as_uint(sr), 32));
      const float var = rbf(fmaxf(s2 - s1 * s1, 0.f));
      const float mb = rbf(s1);
      const float sd = rbf(__builtin_amdgcn_sqrtf(rbf(var + 1e-5f)));
      float rsd = __builtin_amdgcn_rcpf(sd);
      rsd = rsd * (2.f - sd * rsd);
      *(u32*)(lds + cr) = fbits(tp == 3 ? 1.f : rsd * (tp == 2 ? -mb : 1.f));
    }
    BAR();
    {
      bf16x8 hf[4];
#pragma unroll
      for (int i = 0; i < 4; ++i) hf[i] = *(const bf16x8*)(lds + hr[i]);
      __builtin_amdgcn_sched_barrier(0);
      f32x4 d = {0.f, 0.f, 0.f, 0.f};
#pragma unroll
      for (int i = 0; i < 4; ++i) d = __builtin_amdgcn_mfma_f32_16x16x32_bf16(wd[i], hf[i], d, 0, 0, 0);
      st128(rs, so, u32x4{fbits(d[0]), fbits(d[1]), fbits(d[2]), fbits(d[3])});
    }
    STAMP(19);
  }
  if (t4 >= 0 && lw == 0) {
#define RED8(f) b3_sum(rs16(rs32(f.x, f.z), rs32(f.y, f.w)))
    const int oc = t4 * 32 + (lane & 7) * 4 + g4;
    const u32 xb = x[oc];
    float x1, gb;
    {
      int off[2];
      off[0] = lane < 32 ? WS_MODB + ((lane >> 3) * 2048 + t4 * 32 + (lane & 7) * 4) * 4 : WS_TOTAL;
      off[1] = lane < 32 ? WS_MODB + ((lane >> 3) * 2048 + 1536 + t4 * 32 + (lane & 7) * 4) * 4 : WS_TOTAL;
      u32x4 mg[2];
      gather128<2>(rs, off, mg);
      const f32x4 fa = __builtin_bit_cast(f32x4, mg[0]), fb = __builtin_bit_cast(f32x4, mg[1]);
      const float ga = rbf(RED8(fa));
      gb = rbf(RED8(fb));
      int of1[1] = {(WS_PATTN + il(((lane >> 3) * 512 + t4 * 32 + (lane & 7) * 4) * 4, myh)) ^ gx};
      u32x4 g[1];
      gather128<1, 4>(rs, of1, g);
      const f32x4 gf = __builtin_bit_cast(f32x4, g[0]);
      x1 = rbf(bf2f(xb) + rbf(ga * rbf(RED8(gf))));
    }
    STAMP(20);
    int off[7];
#pragma unroll
    for (int i = 0; i < 7; ++i) off[i] = (WS_PDOWN + il(((7 * (lane >> 3) + i) * 256 + (t4 & 7) * 32 + (lane & 7) * 4) * 4, myh)) ^ gx;
    const rsrc_t ro = __builtin_amdgcn_make_buffer_rsrc((void*)out, 0, 1024, 0x00020000);
    int oo = (lane & 8) ? 1024 : oc * 2;
    PIN(x1); PIN(gb); PIN(oo);
    u32x4 g[7];
    __builtin_amdgcn_s_sleep(T4_DELAY);
    gather128<7>(rs, off, g);
    STAMP(21);
#define F4(i) __builtin_bit_cast(f32x4, g[i])
    const f32x4 s = ((F4(0) + F4(1)) + (F4(2) + F4(3))) + ((F4(4) + F4(5)) + F4(6));
    __builtin_amdgcn_raw_buffer_store_b16((u16)f2bf(x1 + rbf(gb * rbf(RED8(s)))), ro, oo, 0, 0);
    if (t4 == 0 && lane == 0) st64(rs, WS_EPOCH, tlo, thi);
    STAMP(22);
  }
  STAMP_FLUSH();
}

extern "C" void kernel_launch(void* const* d_in, const int* in_sizes, int n_in, void* d_out, int out_size,
                              void* d_ws, size_t ws_size, hipStream_t stream) {
  k_main<<<NWG, 512, 0, stream>>>((const u16*)d_in[0], (const u16*)d_in[1], (const u16*)d_in[2], (const u16*)d_in[3],
                                  (const u16*)d_in[4], (const u16*)d_in[5], (const u16*)d_in[7], (const u16*)d_in[8],
                                  (const u16*)d_in[9], (const u16*)d_in[10], (const u16*)d_in[11], (u16*)d_out,
                                  (unsigned char*)d_ws);
}
```

```cpp
#include <hip/hip_runtime.h>
#include <hip/hip_bf16.h>
#include <stdint.h>

typedef unsigned short u16;
typedef unsigned int u32;
typedef unsigned long long u64;
typedef float f32x2 __attribute__((ext_vector_type(2)));
typedef float f32x4 __attribute__((ext_vector_type(4)));
typedef float f32x16 __attribute__((ext_vector_type(16)));
typedef u32 u32x2 __attribute__((ext_vector_type(2)));
typedef u32 u32x4 __attribute__((ext_vector_type(4)));
typedef short bf16x8 __attribute__((ext_vector_type(8)));
typedef short s16x4 __attribute__((ext_vector_type(4)));
#define LAS __attribute__((address_space(3)))
#define DI __device__ __forceinline__

constexpr int DF = 1792;
constexpr int NWG = 256;
constexpr int AUX_SC1 = 16;

constexpr int WS_EPOCH = 0;
constexpr int WS_SFLAG = 4096;
constexpr int WS_QABS = 32768;
constexpr int WS_PCTX = WS_QABS + 65536;
constexpr int WS_PATTN = WS_PCTX + 65536;
constexpr int WS_PDOWN = WS_PATTN + 32768;
constexpr int WS_LSE = WS_PDOWN + 114688;
constexpr int WS_MODB = WS_LSE + 196608;
constexpr int WS_SC = WS_MODB + 32768;
constexpr int WS_PROBE = WS_SC + 32768;
constexpr int WS_VOTE = WS_PROBE + 262144;
constexpr int WS_PART = 2097152;
constexpr int PART_BYTES = 32 * 8 * 256 * 128;
constexpr int WS_TOTAL = WS_PART + PART_BYTES;

constexpr int il(int o, int hf) { return (o >> 13) * 16384 + hf * 8192 + (o & 8191); }
constexpr int il21(int o, int hf) { return il(o, hf ^ ((o >> 20) & 1) ^ 1); }
constexpr u32 SPIN_CAP = 1u << 16;
#define WIN 8
#define WA0 5
#define WB0 6
#define QW 4
#define WING 2
#define QF_T 300
#define E1_T 270
#define E3_T 515
#define E6_T 1200
#define T4_DELAY 12
#define TAU 32.f
#define VCLIP 40
#define PF_LEN 35200
#define G_OF(v) ((v) > 32768.f ? 8192u : 0u)
#define VOTE_STAMP(v)
#define STAMP_DECL
#define STAMP(i)
#define STAMP_FLUSH()

DI float bf2f(u32 b) { return __uint_as_float(b << 16); }
DI float rbf(float f) { return (float)(__bf16)f; }
DI u32 f2bf(float f) { return (u32)__builtin_bit_cast(u16, (__bf16)f); }
typedef __bf16 bf16x2_t __attribute__((ext_vector_type(2)));
DI u32 pk2bf(float a, float b) { return __builtin_bit_cast(u32, __builtin_convertvector(f32x2{a, b}, bf16x2_t)); }
DI float lo16(u32 v) { return __uint_as_float(v << 16); }
DI float hi16(u32 v) { return __uint_as_float(v & 0xFFFF0000u); }
DI u32x4 ldnt(const u16* p) { return __builtin_nontemporal_load((const u32x4*)p); }

DI float silu_bf(float a) { return rbf(a * __builtin_amdgcn_rcpf(1.f + __expf(-a))); }

template <int CTRL> DI float dppf(float v) {
  return __int_as_float(__builtin_amdgcn_update_dpp(0, __float_as_int(v), CTRL, 0xF, 0xF, true));
}
DI float quad_sum(float v) { v += dppf<0xB1>(v); v += dppf<0x4E>(v); return v; }
DI float b23_sum(float v) { v += dppf<0x128>(v); v += dppf<0x124>(v); return v; }
DI float b3_sum(float v) { return v + dppf<0x128>(v); }
DI float x16_sum(float v) {
  const auto r = __builtin_amdgcn_permlane16_swap(__float_as_uint(v), __float_as_uint(v), false, false);
  return __uint_as_float(r[0]) + __uint_as_float(r[1]);
}
DI float x32_sum(float v) {
  const auto r = __builtin_amdgcn_permlane32_swap(__float_as_uint(v), __float_as_uint(v), false, false);
  return __uint_as_float(r[0]) + __uint_as_float(r[1]);
}
DI float rs32(float a, float b) {
  const auto r = __builtin_amdgcn_permlane32_swap(__float_as_uint(a), __float_as_uint(b), false, false);
  return __uint_as_float(r[0]) + __uint_as_float(r[1]);
}
DI float rs16(float a, float b) {
  const auto r = __builtin_amdgcn_permlane16_swap(__float_as_uint(a), __float_as_uint(b), false, false);
  return __uint_as_float(r[0]) + __uint_as_float(r[1]);
}
DI float row16_sum(float v) { return b23_sum(quad_sum(v)); }
DI float wave_sum(float v) { return x32_sum(x16_sum(row16_sum(v))); }
DI void row16_sum4(float& a, float& b, float& c, float& d) {
#define R4(C) "v_add_f32_dpp %0, %0, %0 " C " row_mask:0xf bank_mask:0xf\n v_add_f32_dpp %1, %1, %1 " C " row_mask:0xf bank_mask:0xf\n" \
              "v_add_f32_dpp %2, %2, %2 " C " row_mask:0xf bank_mask:0xf\n v_add_f32_dpp %3, %3, %3 " C " row_mask:0xf bank_mask:0xf\n"
  asm("s_nop 1\n" R4("quad_perm:[1,0,3,2]") R4("quad_perm:[2,3,0,1]") R4("row_ror:8") R4("row_ror:4") : "+v"(a), "+v"(b), "+v"(c), "+v"(d));
#undef R4
}
DI float row16_max(float v) {
  v = fmaxf(v, dppf<0xB1>(v)); v = fmaxf(v, dppf<0x4E>(v));
  v = fmaxf(v, dppf<0x128>(v)); v = fmaxf(v, dppf<0x124>(v));
  return v;
}
DI float x16_max(float v) {
  const auto r = __builtin_amdgcn_permlane16_swap(__float_as_uint(v), __float_as_uint(v), false, false);
  return fmaxf(__uint_as_float(r[0]), __uint_as_float(r[1]));
}
DI float x32_max(float v) {
  const auto r = __builtin_amdgcn_permlane32_swap(__float_as_uint(v), __float_as_uint(v), false, false);
  return fmaxf(__uint_as_float(r[0]), __uint_as_float(r[1]));
}
DI float wave_max(float v) { return x32_max(x16_max(row16_max(v))); }

DI float dot2(u32 a, u32 b, float c) {
  return __builtin_amdgcn_fdot2_f32_bf16(__builtin_bit_cast(bf16x2_t, a), __builtin_bit_cast(bf16x2_t, b), c, false);
}
DI void fma8b(float* acc, u32 sbits, u32x4 w) {
  const u32 sh = sbits << 16;
  acc[0] = dot2(w.x, sbits, acc[0]); acc[1] = dot2(w.x, sh, acc[1]);
  acc[2] = dot2(w.y, sbits, acc[2]); acc[3] = dot2(w.y, sh, acc[3]);
  acc[4] = dot2(w.z, sbits, acc[4]); acc[5] = dot2(w.z, sh, acc[5]);
  acc[6] = dot2(w.w, sbits, acc[6]); acc[7] = dot2(w.w, sh, acc[7]);
}

DI unsigned fsw(unsigned row) { return ((row & 3) << 2) | ((row >> 2) & 3); }
DI unsigned off_b(unsigned row, unsigned ch) { return 256u * row + 16u * (ch ^ fsw(row)); }
DI unsigned tr_addr(unsigned lane, unsigned c, unsigned ks, unsigned t) {
  const unsigned h = lane >> 5, blk = (lane >> 4) & 1, q = (lane & 15) >> 2, p = lane & 3;
  return off_b(16 * ks + 8 * h + 4 * t + q, 4 * c + 2 * blk + (p >> 1)) + 8 * (p & 1);
}

typedef __amdgpu_buffer_rsrc_t rsrc_t;
DI u32x4 ld128(rsrc_t rs, int off) { return __builtin_amdgcn_raw_buffer_load_b128(rs, off, 0, AUX_SC1); }
DI u32x2 ld64(rsrc_t rs, int off) { return __builtin_amdgcn_raw_buffer_load_b64(rs, off, 0, AUX_SC1); }
DI void st64(rsrc_t rs, int off, u32 lo, u32 hi) { u32x2 v; v.x = lo; v.y = hi; __builtin_amdgcn_raw_buffer_store_b64(v, rs, off, 0, AUX_SC1); }
DI void st128(rsrc_t rs, int off, u32x4 v) { __builtin_amdgcn_raw_buffer_store_b128(v, rs, off, 0, AUX_SC1); }
#define BAR() do { asm volatile("s_waitcnt lgkmcnt(0)" ::: "memory"); __builtin_amdgcn_s_barrier(); asm volatile("" ::: "memory"); } while (0)
#define BAR0() do { __builtin_amdgcn_sched_barrier(0); asm volatile("" ::: "memory"); __builtin_amdgcn_s_barrier(); asm volatile("" ::: "memory"); __builtin_amdgcn_sched_barrier(0); } while (0)
#define PIN(x) asm volatile("" : "+v"(x))
constexpr u32 POISON = 0xFFFFFFFFu;
DI bool pois4(u32x4 v) { return max(max(v.x, v.y), max(v.z, v.w)) == POISON; }
DI u32 nopois(u32 v) { return min(v, 0xFFFFFFFEu); }
DI u32 fbits(float v) { return nopois(__float_as_uint(v)); }

DI u32x2 mk_gran(float v, u32 tlo, u32 thi) {
  u32 b = __float_as_uint(v);
  if ((b & 0x7F800000u) != 0x7F800000u) b += 0x80u;
  u32x2 g; g.x = (b >> 8) | (tlo << 24); g.y = (tlo >> 8) | (thi << 24);
  return g;
}
DI void st_gran2(rsrc_t rs, int off, float v0, float v1, u32 tlo, u32 thi) {
  const u32x2 a = mk_gran(v0, tlo, thi), b = mk_gran(v1, tlo, thi);
  u32x4 v; v.x = a.x; v.y = a.y; v.z = b.x; v.w = b.y;
  st128(rs, off, v);
}
DI bool gran_ok(u32 lo, u32 hi, u32 tlo, u32 thi) { return (((lo >> 24) ^ (tlo & 0xFFu)) | (hi ^ ((tlo >> 8) | (thi << 24)))) == 0u; }
DI float gran_val(u32 lo) { return __uint_as_float(lo << 8); }

template <int N, int SLP = 1> DI void gather128(rsrc_t rs, const int (&off)[N], u32x4 (&v)[N]) {
#pragma unroll
  for (int i = 0; i < N; ++i) v[i] = ld128(rs, off[i]);
  _Pragma("unroll 1") for (u32 spins = 0; spins < SPIN_CAP; ++spins) {
    bool need[N], bad = false;
#pragma unroll
    for (int i = 0; i < N; ++i) { need[i] = pois4(v[i]); bad = bad || need[i]; }
    if (!__any(bad)) break;
    __builtin_amdgcn_s_sleep(SLP);
    asm volatile("" ::: "memory");
    __builtin_amdgcn_sched_barrier(0);
#pragma unroll
    for (int i = 0; i < N; ++i)
      if (need[i]) v[i] = ld128(rs, off[i]);
  }
}
template <int N> DI void waitvm() { asm volatile("s_waitcnt vmcnt(%0)" ::"n"(N) : "memory"); }
constexpr u32 LSPIN_CAP = 1u << 22;
DI void lds_wait(volatile LAS u32* p, u32 val) {
  _Pragma("unroll 1") for (u32 n = 0; *p < val && n < LSPIN_CAP; ++n) __builtin_amdgcn_s_sleep(1);
  asm volatile("" ::: "memory");
}
#define WAVE_LDS_SYNC() do { __builtin_amdgcn_fence(__ATOMIC_RELEASE, "wavefront"); __builtin_amdgcn_wave_barrier(); __builtin_amdgcn_fence(__ATOMIC_ACQUIRE, "wavefront"); } while (0)
#define SB() __builtin_amdgcn_sched_barrier(0)
#define RARE(c) __builtin_expect(!!(c), 0)

constexpr int PS = 28672;
constexpr int L_QI = 4 * PS;
constexpr int L_P = L_QI + 32768;
constexpr int L_REF = L_P + 11264;
constexpr int L_MX = L_REF + 1408;
constexpr int L_ALW = L_MX + 128;
constexpr int L_DUMP = L_ALW + 512;
constexpr int L_QOFF = 131072;
constexpr int L_H1RED = L_QOFF;
constexpr int L_RED = L_QOFF + 16384;
constexpr int L_Z = L_RED, L_HH = L_RED + 7 * 1088, L_ZERO = L_HH + 64, L_CF = L_ZERO + 64;
constexpr int L_MISC = L_QOFF + 30720;
constexpr int L_CTR = L_MISC;
constexpr int L_WMAX = L_MISC + 256;
constexpr int L_ML = L_MISC + 512;

#define Z4 (u32x4{0u, 0u, 0u, 0u})
DI float max5(f32x4 v, float e) { return fmaxf(fmaxf(fmaxf(fmaxf(v.x, v.y), v.z), v.w), e); }

template <bool NT> DI void dma(u32 m, u32 voff, const void* sb) {
  if (NT) asm volatile("s_mov_b32 m0, %0\n s_nop 0\n global_load_lds_dwordx4 %1, %2 nt" ::"s"(m), "v"(voff), "s"(sb) : "memory");
  else asm volatile("s_mov_b32 m0, %0\n s_nop 0\n global_load_lds_dwordx4 %1, %2" ::"s"(m), "v"(voff), "s"(sb) : "memory");
}
DI void dma_sc1(u32 m, u32 voff, const void* sb) {
  asm volatile("s_mov_b32 m0, %0\n s_nop 0\n global_load_lds_dwordx4 %1, %2 sc1" ::"s"(m), "v"(voff), "s"(sb) : "memory");
}
DI void lds_add(int idx) {
  SB();
  asm volatile("s_mov_b64 exec, 1\n ds_add_u32 %0, %1 offset:%2\n s_mov_b64 exec, -1" ::"v"((u32)L_CTR), "v"(1u), "n"(idx * 4) : "memory");
  SB();
}
DI u32 spin_at(u32 a, u32 val) {
  u32 t, c, m, cap;
  asm volatile("s_mov_b32 %3, %6\n 1: ds_read_b32 %0, %4\n s_waitcnt lgkmcnt(0)\n v_readfirstlane_b32 %1, %0\n s_and_b32 %2, %1, 255\n s_cmp_ge_u32 %2, %5\n s_cbranch_scc1 2f\n"
               "s_sub_u32 %3, %3, 1\n s_cbranch_scc0 1b\n 2:"
               : "=&v"(t), "=&s"(c), "=&s"(m), "=&s"(cap) : "v"(a), "s"(val), "n"(LSPIN_CAP) : "memory", "scc");
  return c;
}
DI void lds_spin(volatile LAS u32* p, u32 val) { spin_at((u32)(uintptr_t)p, val); }
enum { C_SOK = 0, C_BH = 4, C_PD = 12, C_H1 = 18, C_PAR = 19, C_VOTE = 20, C_FREE = 22, C_QRDY = 23, C_MXC = 25, C_SD = 26, C_GX = 27, C_ML = 28, C_MK = 32, C_P = 44 };

DI u32 kv_mark(int nm, int skip) { return L_CTR + 4 * C_MK + __builtin_amdgcn_readfirstlane(4 * nm + (nm >= skip ? 4 : 0)); }
#define KV_TS
#define KV_TSO
#define KV_TSA
#define KV_TSP
#define KV_TRY(l)                                                                                                        \
  "7: s_getreg_b32 %[h], hwreg(HW_REG_IB_STS)\n s_and_b32 %[h], %[h], 0xC0000F\n s_max_i32 %[h], %[h], %[x]\n s_sub_i32 %[h], %[h], %[x]\n s_cmp_gt_i32 %[h], %[k4]\n s_cbranch_scc1 " l "\n" \
  "s_waitcnt vmcnt(%[x4])\n s_cmp_eq_u32 %[k4], 4\n s_cbranch_scc1 8f\n s_waitcnt vmcnt(%[x])\n"                        \
  "8: v_subrev_u32 %[t], %[k4], %[ml]\n s_mov_b64 exec, 1\n ds_add_u32 %[t], %[one] offset:12\n s_mov_b64 exec, -1\n"   \
  "s_sub_i32 %[k4], %[k4], 4\n" KV_TS "s_branch 7b\n"
template <int X> DI void kv_try(int& k4, u32 ml, u32 one KV_TSA) {
  u32 t, h;
  asm volatile(KV_TRY("9f") "9:" : [t] "=&v"(t), [h] "=&s"(h), [k4] "+s"(k4) KV_TSO : [ml] "v"(ml), [one] "v"(one), [x] "n"(X), [x4] "n"(X + 4) : "memory", "scc");
}
template <int X> DI u32 spin_try(int idx, u32 val, int& k4, u32 ml, u32 one KV_TSA) {
  u32 t, h, c, cap;
  asm volatile("s_mov_b32 %[cap], %[n]\n 1: ds_read_b32 %[t], %[ml] offset:%[o]\n s_waitcnt lgkmcnt(0)\n v_readfirstlane_b32 %[c], %[t]\n s_and_b32 %[h], %[c], 255\n"
               "s_cmp_ge_u32 %[h], %[v]\n s_cbranch_scc1 2f\n" KV_TRY("9f") "9: s_sub_u32 %[cap], %[cap], 1\n s_cbranch_scc0 1b\n 2:"
               : [t] "=&v"(t), [h] "=&s"(h), [c] "=&s"(c), [cap] "=&s"(cap), [k4] "+s"(k4) KV_TSO
               : [ml] "v"(ml), [one] "v"(one), [o] "n"(4 * idx), [v] "n"(val), [n] "n"(LSPIN_CAP), [x] "n"(X), [x4] "n"(X + 4) : "memory", "scc");
  return c;
}
template <int W> DI void kv_load1(const u16* ckv, u32 vl, int lw, u32 g0, u32 g1, u32 m, int n0, int n1, int& nm, int skip) {
#pragma unroll 1
  for (int n = n0; n < n1; ++n) {
#define KV_POST(k) ".if (%[wm] | 4) == " #k "\n s_cmp_gt_i32 %[nm], %[n2]\n s_cbranch_scc1 20f\n s_mov_b64 exec, 1\n ds_add_u32 %[ma], %[one]\n s_mov_b64 exec, -1\n s_add_i32 %[nm], %[nm], 1\n 20:\n .endif\n"
#define KV_W(o, k) "s_waitcnt vmcnt(%[wm])\n" KV_POST(k) "global_load_lds_dwordx4 %[vo], %[p] offset:" #o " nt\n"
#define KV_M "s_add_u32 m0, m0, %[ms]\n"
    asm volatile("s_mov_b32 m0, %[m]\n" KV_W(0, 4) KV_M KV_W(256, 5) KV_M KV_W(512, 6) KV_M KV_W(768, 7)
                 : [nm] "+s"(nm)
                 : [m] "s"(m), [vo] "v"(vl + g0 + ((lw + n) & 3) * 4096), [p] "s"(ckv), [ma] "v"(kv_mark(nm, skip)),
                   [one] "v"(1u), [n2] "s"(__builtin_amdgcn_readfirstlane(n - (W > 4 ? 2 : 1))), [wm] "n"(W - 1), [ms] "n"(PS - 256)
                 : "memory", "scc");
    g0 = g1;
    g1 += 192 * 16384;
    m += n + 1 == skip ? 8192 : 4096;
  }
}
template <int W, int W0> DI void kv_load(const u16* ckv, u32 vl, int lw, u32 g0, u32 g1, u32 m, int n0, int n1, int& nm, int skip) {
  if (W != W0 && skip == 4) kv_load1<W0>(ckv, vl, lw, g0, g1, m, n0, n1, nm, skip); else kv_load1<W>(ckv, vl, lw, g0, g1, m, n0, n1, nm, skip);
}
DI void until(u32 t0, u32 dt) {
  while ((u32)__builtin_amdgcn_s_memrealtime() - t0 < dt) __builtin_amdgcn_s_sleep(2);
}
DI bf16x8 tr_frag(unsigned char* lds, u32 a0, u32 a1) {
  const s16x4 b0 = __builtin_amdgcn_ds_read_tr16_b64_v4i16((LAS s16x4*)(lds + a0));
  const s16x4 b1 = __builtin_amdgcn_ds_read_tr16_b64_v4i16((LAS s16x4*)(lds + a1));
  bf16x8 v;
  v[0] = b0[0]; v[1] = b0[1]; v[2] = b0[2]; v[3] = b0[3]; v[4] = b1[0]; v[5] = b1[1]; v[6] = b1[2]; v[7] = b1[3];
  return v;
}
DI float sum4(f32x4 v) { const f32x2 t = f32x2{v.x, v.y} + f32x2{v.z, v.w}; return t.x + t.y; }

__global__ __launch_bounds__(512, 2) void k_main(const u16* __restrict__ x, const u16* __restrict__ ckv,
                                                 const u16* __restrict__ w_q, const u16* __restrict__ w_uk,
                                                 const u16* __restrict__ w_uv, const u16* __restrict__ w_o,
                                                 const u16* __restrict__ w_gate, const u16* __restrict__ w_up,
                                                 const u16* __restrict__ w_down, const u16* __restrict__ cvec,
                                                 const u16* __restrict__ w_mod, u16* __restrict__ out,
                                                 unsigned char* ws) {
  __shared__ __attribute__((aligned(16))) unsigned char lds[163840];
  const int w = __builtin_amdgcn_readfirstlane(threadIdx.x >> 6), j = blockIdx.x;
  int lane = threadIdx.x & 63;
  const int xc = j & 7, ii = j >> 3;
  STAMP_DECL;
  STAMP(0);
  u32x2 ev;
  u32 xm, vh;
  {
    u64 p, pc, pw;
    u32 a, b, c, d, f, t;
    asm volatile("s_lshr_b32 %[f], %[j], 6\n s_cmp_eq_u32 %[f], 1\n s_cselect_b32 %[a], 0x50, 16\n"
                 "s_load_dwordx2 %[p], %[ka], %[a]\n s_load_dwordx2 %[pw], %[ka], 0x60\n"
                 "s_lshr_b32 %[d], %[j], 3\n s_cmp_lg_u32 %[w], 7\n s_cbranch_scc1 2f\n"
                 "s_and_b32 %[a], %[d], 15\n s_lshl_b32 %[a], %[a], 12\n v_lshl_add_u32 %[t], %[lane], 6, %[a]\n v_min_u32 %[t], %[pfl], %[t]\n"
                 "s_getpc_b64 %[pc]\n s_mov_b32 m0, %[dump]\n s_nop 0\n global_load_lds_dword %[t], %[pc]\n"
                 "2: s_load_dwordx2 %[pc], %[ka], 0x48\n s_and_b32 %[c], %[j], 7\n s_lshl_b32 %[c], %[c], 10\n s_cmp_eq_u32 %[f], 0\n s_cbranch_scc0 3f\n"
                 "s_and_b32 %[a], %[d], 1\n s_lshl1_add_u32 %[a], %[w], %[a]\n s_lshl_b32 %[a], %[a], 18\n s_add_u32 %[c], %[c], %[a]\n"
                 "s_lshr_b32 %[a], %[d], 1\n s_lshl_b32 %[a], %[a], 8\n s_add_u32 %[c], %[c], %[a]\n s_lshl_b32 m0, %[w], 14\n"
                 "v_lshrrev_b32 %[t], 4, %[lane]\n v_mul_u32_u24 %[t], 0x204, %[t]\n v_and_b32 %[vh], 15, %[lane]\n v_xor_b32 %[vh], %[vh], %[t]\n"
                 "v_lshl_add_u32 %[vh], %[vh], 4, %[c]\n v_xor_b32 %[t], 0x8010, %[vh]\n s_waitcnt lgkmcnt(0)\n"
                 "global_load_lds_dwordx4 %[vh], %[p]\n s_add_u32 m0, m0, 0x400\n s_nop 0\n"
                 "global_load_lds_dwordx4 %[t], %[p]\n s_add_u32 m0, m0, 0x400\n v_xor_b32 %[t], 0x10020, %[vh]\n"
                 "global_load_lds_dwordx4 %[t], %[p]\n s_add_u32 m0, m0, 0x400\n v_xor_b32 %[t], 0x18030, %[vh]\n"
                 "global_load_lds_dwordx4 %[t], %[p]\n s_branch 1f\n"
                 "3: s_lshl2_add_u32 %[d], %[d], %[w]\n s_sub_u32 %[d], %[d], 68\n s_lshl_b32 %[d], %[d], 16\n s_add_u32 %[d], %[d], %[c]\n"
                 "s_and_b32 %[a], %[j], 3\n s_lshl2_add_u32 %[b], %[a], %[w]\n s_mul_i32 %[b], %[b], 0x30000\n s_lshl_b32 %[a], %[a], 8\n"
                 "v_lshl_add_u32 %[xm], %[lane], 2, %[a]\n"
                 "s_and_b32 %[a], %[j], 60\n s_lshl_b32 %[a], %[a], 5\n s_add_u32 %[b], %[b], %[a]\n"
                 "s_lshl_b32 %[c], %[w], 12\n s_add_u32 %[c], %[c], %[mq]\n s_lshl_b32 %[a], %[w], 10\n s_add_u32 %[a], %[a], %[mh]\n"
                 "s_cmp_eq_u32 %[f], 1\n s_cselect_b32 %[d], %[b], %[d]\n s_cselect_b32 m0, %[c], %[a]\n s_cselect_b32 %[a], 48, 1\n s_cselect_b32 %[c], 0, 4\n"
                 "v_lshrrev_b32 %[t], 3, %[lane]\n v_mul_u32_u24 %[t], %[a], %[t]\n v_and_b32 %[vh], 7, %[lane]\n v_lshl_add_u32 %[vh], %[t], 3, %[vh]\n"
                 "v_lshl_add_u32 %[vh], %[vh], 4, %[d]\n"
                 "s_cselect_b32 %[b], 6, 1\n s_cselect_b32 %[a], 0x400, 0\n s_lshl_b32 %[b], %[b], 13\n"
                 "s_xor_b32 %[c], %[c], %[w]\n s_bitcmp1_b32 %[c], 2\n v_mov_b32 %[t], 0\n"
                 "s_waitcnt lgkmcnt(0)\n s_cbranch_scc1 1f\n s_cmp_eq_u32 %[f], 1\n s_cbranch_scc0 4f\n"
                 "global_load_dword %[xm], %[xm], %[pc]\n global_load_dwordx2 %[ev], %[t], %[pw] sc1\n"
                 "4: global_load_lds_dwordx4 %[vh], %[p]\n s_add_u32 m0, m0, %[a]\n v_add_u32 %[t], %[b], %[vh]\n"
                 "global_load_lds_dwordx4 %[t], %[p]\n s_add_u32 m0, m0, %[a]\n v_add_u32 %[t], %[b], %[t]\n"
                 "global_load_lds_dwordx4 %[t], %[p]\n s_add_u32 m0, m0, %[a]\n v_add_u32 %[t], %[b], %[t]\n"
                 "global_load_lds_dwordx4 %[t], %[p]\n"
                 "1:\n"
                 : [xm] "=&v"(xm), [ev] "=&v"(ev), [vh] "=&v"(vh), [t] "=&v"(t), [p] "=&s"(p), [pc] "=&s"(pc), [pw] "=&s"(pw), [a] "=&s"(a), [b] "=&s"(b), [c] "=&s"(c),
                   [d] "=&s"(d), [f] "=&s"(f)
                 : [j] "s"(j), [w] "s"(w), [lane] "v"(lane), [ka] "s"(__builtin_amdgcn_kernarg_segment_ptr()), [pfl] "n"(PF_LEN), [dump] "n"(L_DUMP),
                   [mq] "n"(L_QOFF + 8192), [mh] "n"(L_P)
                 : "memory", "scc");
  }
  SB();
  const u32 t0 = (u32)__builtin_amdgcn_s_memrealtime() - 35u;
  const bool isQ = j < 64, isS0 = j >= 64 && j < 128;
  const int hq = xc * 4 + (ii >> 1), dh = ii & 1, sj = j - 64;
  const int lw = w - 4, myh = xc >> 2;
  int col = lane & 15, g4 = lane >> 4, tq = col >> 2, tp = lane & 3;
  const rsrc_t rs = __builtin_amdgcn_make_buffer_rsrc((void*)ws, 0, WS_TOTAL, 0x00020000);
  volatile LAS u32* ctr = (volatile LAS u32*)(lds + L_CTR);
  SB();
  STAMP(25);
  if (w < 2) *(u32*)(lds + (w == 0 ? L_CTR + lane * 4 : L_MX + (lane & 31) * 4)) = w == 0 ? 0u : 0xFF800000u;
  BAR();
  const bool tl2 = isS0;
  const int t3 = xc < 4 ? (ii < 8 ? xc * 8 + ii : -1) : (ii < 6 ? 8 + xc * 6 + ii : -1);
  const int t4 = (ii >= 8 && ii < 10) ? xc * 2 + (ii - 8) : -1;
  const u32 owno = (w >> 1) * 32768 + 8192 + (w & 1) * 12288;
  const int ks = (xc & 3) * 2 + myh, cg = ii & 7;
  const int h = (j >> 2) & 31, c4 = j & 3;
  u32x4 wv[8];
  u32 tlo, thi;
  int gx;
#define RELANE() do { asm volatile("v_mbcnt_lo_u32_b32 %0, -1, 0\n v_mbcnt_hi_u32_b32 %0, -1, %0" : "=v"(lane)); col = lane & 15; g4 = lane >> 4; tq = col >> 2; tp = lane & 3; } while (0)
#define TAG_OF(e0, e1)                                                                                                  \
  do {                                                                                                                  \
    const u64 tag = (((u64)(e1) << 32) | (e0)) + 1ull;                                                                  \
    tlo = __builtin_amdgcn_readfirstlane((u32)tag); thi = __builtin_amdgcn_readfirstlane((u32)(tag >> 32));             \
  } while (0)
  const u32x4 P4 = {POISON, POISON, POISON, POISON};
#define COUNT_VOTES(PRE)                                                                                                \
  do {                                                                                                                  \
    u32x2 a_ = {0u, 0u};                                                                                                \
    if (PRE) a_ = *(const u32x2*)(lds + L_QI + lane * 8);                                                               \
    _Pragma("unroll 1") for (u32 spins = 0; spins < SPIN_CAP; ++spins) {                                                \
      if (((PRE) || spins) && __all(gran_ok(a_.x, a_.y, tlo, thi))) break;                                              \
      if (spins) __builtin_amdgcn_s_sleep(8);                                                                           \
      asm volatile("" ::: "memory");                                                                                    \
      a_ = ld64(rs, WS_VOTE + xc * 2048 + lane * 8);                                                                    \
    }                                                                                                                   \
    const float vt_ = wave_sum((float)(a_.x & 1023u));                                                                  \
    if (lane == 0) ctr[C_PAR] = G_OF(vt_);                                                                              \
    VOTE_STAMP(vt_);                                                                                                    \
    STAMP(8);                                                                                                           \
  } while (0)
#define S_POLL(a_)                                                                                                      \
  _Pragma("unroll 1") for (u32 spins = 0; spins < SPIN_CAP && RARE(__any(((a_.x ^ tlo) | (a_.y ^ thi)) != 0u)); ++spins) { \
    __builtin_amdgcn_s_sleep(8);                                                                                        \
    asm volatile("" ::: "memory");                                                                                      \
    a_ = ld64(rs, WS_SFLAG + xc * 2048 + lane * 8);                                                                     \
  }
#define MB_OFF(i) (WS_MODB + ((lane >> 4) * 2048 + ((i) >> 1) * 512 + 128 * lw + 64 * ((i) & 1) + (lane & 15) * 4) * 4)

  if (!isQ && w >= 4) {
    if (isS0) {
      if (w == 4) {
        const u32x2 e4 = ld64(rs, WS_EPOCH);
        st128(rs, WS_QABS + sj * 1024 + lane * 16, P4);
        st128(rs, lane < 32 ? WS_MODB + sj * 512 + lane * 16 : WS_TOTAL, P4);
        st128(rs, WS_PROBE + il(sj * 1024 + lane * 16, 0), Z4);
        st128(rs, WS_PROBE + il(sj * 1024 + lane * 16, 1), Z4);
        waitvm<0>();
        TAG_OF(e4.x, e4.y);
        if (lane < 8) st64(rs, WS_SFLAG + lane * 2048 + sj * 8, tlo, thi);
        STAMP(27);
      }
      lds_wait(ctr + C_H1, 4u);
    } else {
#pragma unroll
      for (int i = 4; i < 16; ++i) { waitvm<7>(); dma<false>(L_P + w * 1024, vh + (i & 7) * 8192, i < 8 ? w_q : w_uk); }
    }
    const u32 vl = g4 * 1024 + ((col ^ ((g4 << 2) | lw)) << 4);
    int nm = 0, ns = isS0 ? 6 : 7;
    const int skip = isS0 ? 4 : 99;
    kv_load<WIN, WA0>(ckv, vl, lw, (isS0 ? sj : 1792 + j) * 16384, (isS0 ? sj + 192 : sj) * 16384, lw * 1024, 0, 2, nm, skip);
    const rsrc_t rw = __builtin_amdgcn_make_buffer_rsrc((void*)(isS0 ? w_o : w_uv), 0, 4194304, 0x00020000);
    int wo = isS0 ? (ks * 512 + 64 * w + (lane >> 3)) * 1024 + cg * 128 + (lane & 7) * 16 : (c4 * 128 + 32 * lw + g4) * 8192 + h * 256 + col * 16;
    const int wst = isS0 ? 8192 : 32768;
    PIN(wo);
#define WV_LD(i) do { SB(); wv[i] = __builtin_amdgcn_raw_buffer_load_b128(rw, wo, (i) * wst, 2); wv[(i) + 1] = __builtin_amdgcn_raw_buffer_load_b128(rw, wo, ((i) + 1) * wst, 2); SB(); } while (0)
    u32 va[8];
#pragma unroll
    for (int i = 0; i < 8; ++i) { va[i] = lw * PS + tr_addr(lane, i >> 1, 0, i & 1); PIN(va[i]); }
    int eg[8];
#pragma unroll
    for (int u = 0; u < 8; ++u) { eg[u] = WS_PART + il21(((4 * (u >> 1) + 2 * (g4 >> 1) + (u & 1)) * 4 + lw) * 65536 + sj * 256 + col * 16, g4 & 1); PIN(eg[u]); }
    u32 pr_ = L_P + lane * 16;
    PIN(pr_);
    u32 ml = L_CTR + 4 * (C_MK + 7), one = 1u;
    PIN(ml); PIN(one);
    bf16x8 vf[4][4];
#define V_READ(i, slot) do { asm volatile("" ::: "memory"); SB(); _Pragma("unroll") for (int c = 0; c < 4; ++c) vf[i][c] = tr_frag(lds, va[2 * c] + (slot) * 4096, va[2 * c + 1] + (slot) * 4096); SB(); } while (0)
    SB();
    kv_load<WIN, WB0>(ckv, vl, lw, (sj + (isS0 ? 384 : 192)) * 16384, (sj + (isS0 ? 576 : 384)) * 16384, lw * 1024 + 8192, 2, ns, nm, skip);
    lds_wait(ctr + C_MK + 3, 4u);
    V_READ(0, 0); V_READ(1, 1); V_READ(2, 2); V_READ(3, 3);
    lds_add(C_FREE);
#pragma unroll
    for (int i = 0; i < 16; ++i) PIN(vf[i >> 2][i & 3]);
    STAMP(2);
    lds_wait(ctr + C_FREE, 8u);
    STAMP(5);
    kv_load<WIN, WB0>(ckv, vl, lw, (sj + 6 * 192) * 16384, (sj + 7 * 192) * 16384, lw * 1024, ns, ns + 4, nm, skip);
    int k4 = 4;
    STAMP(3);
    f32x16 acc[4];
#pragma unroll
    for (int c = 0; c < 4; ++c)
#pragma unroll
      for (int i = 0; i < 16; ++i) acc[c][i] = 0.f;
    bf16x8 pf[4], pn[4];
    u32 c_;
#define P_SPIN(g, n) c_ = spin_try<(g) ? 8 : 0>(C_P + (g) - C_MK - 7, n, k4, ml, one KV_TSP)
#define TRY() kv_try<8>(k4, ml, one KV_TSP)
#define P_READ(d, i0, i1, T0) _Pragma("unroll") for (int i = (i0); i < (i1); ++i) d[i] = *(volatile LAS bf16x8*)(lds + pr_ + ((T0) + i) * 1024)
#define PV(p, i) do { SB(); _Pragma("unroll") for (int c = 0; c < 4; ++c) acc[c] = __builtin_amdgcn_mfma_f32_32x32x16_bf16(p[i], vf[i][c], acc[c], 0, 0, 0); SB(); } while (0)
#define GX_USE(v) do { gx = __builtin_amdgcn_readfirstlane(v); _Pragma("unroll") for (int u = 0; u < 8; ++u) { eg[u] ^= gx; PIN(eg[u]); } } while (0)
#define MK_WAIT(T) spin_try<8>((T) - 7, 4u, k4, ml, one KV_TSP)
#define PVV(p, i, d, slot)                                                                                               \
  _Pragma("unroll") for (int c = 0; c < 4; ++c) {                                                                        \
    SB();                                                                                                                \
    acc[c] = __builtin_amdgcn_mfma_f32_32x32x16_bf16(p[i], vf[i][c], acc[c], 0, 0, 0);                                   \
    SB();                                                                                                                \
    vf[d][c] = tr_frag(lds, va[2 * c] + (slot) * 4096, va[2 * c + 1] + (slot) * 4096);                                   \
    SB();                                                                                                                \
  }
#define CT(i) (*(volatile LAS u32*)(lds + ml + 4 * ((i) - C_MK - 7)))
    P_SPIN(0, 4u);
    P_READ(pf, 0, 4, 0);
    const u32 k7 = CT(C_MK + 7);
    asm volatile("" ::: "memory");
    STAMP(6);
    PVV(pf, 0, 0, 4); kv_try<0>(k4, ml, one KV_TSP); WV_LD(0);
    PVV(pf, 1, 1, 5); kv_try<2>(k4, ml, one KV_TSP); WV_LD(2);
    PVV(pf, 2, 2, 6); WV_LD(4);
    u32 cv = CT(C_P + 1);
    P_READ(pn, 0, 4, 4);
    PVV(pf, 3, 3, 0); WV_LD(6);
    if (RARE(__builtin_amdgcn_readfirstlane(k7) < 4u)) { MK_WAIT(7); V_READ(0, 4); V_READ(1, 5); V_READ(2, 6); V_READ(3, 0); }
    STAMP(9);
    c_ = __builtin_amdgcn_readfirstlane(cv);
    if (RARE((c_ & 255u) != 4u)) { P_SPIN(1, 4u); P_READ(pn, 0, 4, 4); }
    int slow = -1;
    if (RARE(c_ > 255u)) slow = 4;
    else {
      if (!isS0) PV(pn, 0);
      TRY();
      const u32 k9 = CT(C_MK + 9);
      asm volatile("" ::: "memory");
      PVV(pn, 1, 0, 1); TRY();
      PVV(pn, 2, 1, 2);
      cv = CT(C_P + 2);
      P_READ(pf, 0, 2, 8);
      const u32 k10 = CT(C_MK + 10);
      asm volatile("" ::: "memory");
      PVV(pn, 3, 2, 3);
      if (RARE(__builtin_amdgcn_readfirstlane(k9) < 4u)) { MK_WAIT(9); V_READ(0, 1); V_READ(1, 2); }
      STAMP(10);
      c_ = __builtin_amdgcn_readfirstlane(cv);
      if (RARE((c_ & 255u) != 4u)) { P_SPIN(2, 4u); P_READ(pf, 0, 2, 8); }
      if (RARE(c_ > 255u)) slow = 8;
      else {
        TRY();
        PV(pf, 0);
        const u32 g0 = ctr[C_GX];
        u32 g1 = ctr[C_PAR];
        cv = CT(C_P + 3);
        P_READ(pf, 2, 3, 8);
        PV(pf, 1);
        STAMP(4);
        if (RARE(__builtin_amdgcn_readfirstlane(k10) < 4u)) { waitvm<8>(); TRY(); lds_spin(ctr + C_MK + 10, 4u); V_READ(2, 3); }
        if (RARE(__builtin_amdgcn_readfirstlane(g0) == 0u)) { lds_spin(ctr + C_GX, 1u); g1 = ctr[C_PAR]; }
        GX_USE(g1);
        STAMP(11);
        c_ = __builtin_amdgcn_readfirstlane(cv);
        _Pragma("unroll 1") for (u32 n_ = 0; RARE((c_ & 255u) != 2u) && n_ < LSPIN_CAP; ++n_) { cv = CT(C_P + 3); P_READ(pf, 2, 3, 8); c_ = __builtin_amdgcn_readfirstlane(cv); }
        if (RARE(c_ > 255u)) slow = 10;
        else PV(pf, 2);
      }
    }
    if (RARE(slow >= 0)) {
      MK_WAIT(10);
      float R = ceilf(*(const float*)(lds + L_MX + (lane & 31) * 4));
#pragma unroll 1
      for (int T = slow; T < 11; ++T) {
        if (T == 4 && isS0) continue;
        spin_at(L_CTR + 4 * (C_P + (T < 8 ? 1 : T < 10 ? 2 : 3)), T < 10 ? 4u : 2u);
        const float rf = *(const float*)(lds + L_REF + T * 128 + (lane & 31) * 4);
        u32x4 p = *(const u32x4*)(lds + pr_ + T * 1024);
        const u32 so_ = (T < 7 ? T : T - 7) * 4096;
#pragma unroll
        for (int c = 0; c < 4; ++c) vf[0][c] = tr_frag(lds, va[2 * c] + so_, va[2 * c + 1] + so_);
        const float Rn = fmaxf(R, rf), ps = __builtin_amdgcn_exp2f(rf - Rn);
        *(float*)(lds + L_ALW + lw * 128 + (lane & 31) * 4) = __builtin_amdgcn_exp2f(R - Rn);
        R = Rn;
        WAVE_LDS_SYNC();
#pragma unroll
        for (int i = 0; i < 4; ++i) {
          const f32x4 al = *(const f32x4*)(lds + L_ALW + lw * 128 + (lane >> 5) * 16 + i * 32);
#pragma unroll
          for (int c = 0; c < 4; ++c)
#pragma unroll
            for (int r = 0; r < 4; ++r) acc[c][4 * i + r] *= al[r];
          p[i] = pk2bf(lo16(p[i]) * ps, hi16(p[i]) * ps);
        }
        WAVE_LDS_SYNC();
        pf[0] = __builtin_bit_cast(bf16x8, p);
        PV(pf, 0);
      }
      if (slow != 10) { lds_spin(ctr + C_GX, 1u); GX_USE(ctr[C_PAR]); }
    }
    STAMP(30);
#pragma unroll
    for (int u = 0; u < 8; u += 2) {
      u32 c[8];
#pragma unroll
      for (int k = 0; k < 8; ++k) c[k] = pk2bf(acc[k & 2][2 * u + (k & 1) + (k >> 2) * 2], acc[(k & 2) + 1][2 * u + (k & 1) + (k >> 2) * 2]);
      SB();
#pragma unroll
      for (int k = 0; k < 2; ++k) {
        const auto r0 = __builtin_amdgcn_permlane16_swap(c[4 * k], c[4 * k + 1], false, false);
        const auto r1 = __builtin_amdgcn_permlane16_swap(c[4 * k + 2], c[4 * k + 3], false, false);
        st128(rs, eg[u + k], u32x4{r0[0], r1[0], r0[1], r1[1]});
      }
      SB();
    }
    STAMP(31);
    if (!isS0) {
#pragma unroll
      for (int i = 0; i < 8; ++i) *(u32x4*)(lds + lw * PS + 16384 + off_b(4 * i + g4, col)) = wv[i];
    }
    {
      int fo = lane < 16 ? (WS_LSE + il(sj * 512 + lw * 128 + (lane & 7) * 16, lane >> 3)) ^ gx : WS_TOTAL;
      lds_spin(ctr + C_ML, 1u);
      u32x4 ml4 = *(const u32x4*)(lds + L_WMAX + col * 16);
      PIN(fo); PIN(ml4);
      waitvm<0>();
      __builtin_amdgcn_s_waitcnt(0x0F70);
      st128(rs, fo, ml4);
      lds_add(C_PD);
    }
    STAMP(13);
  } else if (!isQ) {
    if (isS0) {
      u32 ta = L_QOFF + 8192 + (8 * g4 + tq) * 128 + w * 32 + tp * 8;
      u32 sa = L_H1RED + w * 256;
      int so = col < 2 ? WS_SC + (((sj >> 5) * 4 + (sj & 3)) * 512 + ((sj >> 2) & 7) * 64 + 16 * w + 4 * g4 + 2 * col) * 8 : WS_TOTAL;
      PIN(ta); PIN(so); PIN(sa);
      u32 c2, e0, e1;
      asm volatile("s_waitcnt vmcnt(4) ; HEAD_WAIT\n v_mov_b32 %0, %3\n v_mov_b32 %1, %4\n v_mov_b32 %2, %5" : "=&v"(c2), "=&v"(e0), "=&v"(e1) : "v"(xm), "v"(ev.x), "v"(ev.y) : "memory");
      TAG_OF(e0, e1);
      bf16x8 sf[4];
      u32x2 wt[8];
      asm volatile("ds_write_b32 %4, %5\n"
                   "ds_read_b128 %0, %6\n ds_read_b128 %1, %6 offset:64\n ds_read_b128 %2, %6 offset:128\n ds_read_b128 %3, %6 offset:192\n s_waitcnt lgkmcnt(0)"
                   : "=&v"(sf[0]), "=&v"(sf[1]), "=&v"(sf[2]), "=&v"(sf[3])
                   : "v"(sa + lane * 4), "v"(pk2bf(silu_bf(lo16(c2)), silu_bf(hi16(c2)))), "v"(sa + 16 * g4) : "memory");
      waitvm<0>();
      lds_add(C_BH);
      lds_spin(ctr + C_BH, 4u);
      STAMP(1);
      asm volatile("ds_read_b64_tr_b16 %0, %8\n ds_read_b64_tr_b16 %1, %8 offset:512\n ds_read_b64_tr_b16 %2, %8 offset:4096\n ds_read_b64_tr_b16 %3, %8 offset:4608\n"
                   "ds_read_b64_tr_b16 %4, %8 offset:8192\n ds_read_b64_tr_b16 %5, %8 offset:8704\n ds_read_b64_tr_b16 %6, %8 offset:12288\n ds_read_b64_tr_b16 %7, %8 offset:12800\n"
                   "s_waitcnt lgkmcnt(0)"
                   : "=&v"(wt[0]), "=&v"(wt[1]), "=&v"(wt[2]), "=&v"(wt[3]), "=&v"(wt[4]), "=&v"(wt[5]), "=&v"(wt[6]), "=&v"(wt[7]) : "v"(ta) : "memory");
      f32x4 d = {0.f, 0.f, 0.f, 0.f};
#pragma unroll
      for (int k = 0; k < 4; ++k)
        d = __builtin_amdgcn_mfma_f32_16x16x32_bf16(__builtin_bit_cast(bf16x8, u32x4{wt[2 * k].x, wt[2 * k].y, wt[2 * k + 1].x, wt[2 * k + 1].y}), sf[k], d, 0, 0, 0);
      st_gran2(rs, so, (col & 1) ? d[2] : d[0], (col & 1) ? d[3] : d[1], tlo, thi);
      lds_add(C_H1);
      STAMP(2);
    }
    u32 ab = off_b(col, 4 * g4), qv = WS_QABS + myh * 8192 + g4 * 1024 + w * 256 + ((col ^ (g4 << 2)) << 4);
    const int hb = 16 * (w & 1) + col;
    u32 pw = L_P + (g4 >> 1) * 512 + hb * 16 + (g4 & 1) * 8, mw = hb * 4;
    PIN(ab); PIN(qv); PIN(pw); PIN(mw);
    if (w == 2) {
      until(t0, QF_T);
      const u32x2 e2 = ld64(rs, WS_EPOCH);
      u32x2 f2 = ld64(rs, WS_SFLAG + xc * 2048 + lane * 8);
      TAG_OF(e2.x, e2.y);
      S_POLL(f2);
      lds_add(C_SOK);
      STAMP(3);
    }
    bf16x8 af[16], qf[16][2];
    u32 aa[16], qa[32];
#pragma unroll
    for (int i = 0; i < 16; ++i) {
      aa[i] = (ab ^ ((i & 3) << 4)) + (((w + (i >> 2)) & 3) * PS + w * 4096); PIN(aa[i]);
#pragma unroll
      for (int u = 0; u < 2; ++u) { qa[2 * i + u] = (ab ^ ((i & 3) << 4)) + (L_QI + ((w + (i >> 2)) & 3) * 8192 + ((u ^ w) & 1) * 4096); PIN(qa[2 * i + u]); }
    }
    const int sl1 = w == 3 ? 0 : 4 + w, sl2 = 1 + (w >> 1);
#define A_RD(i, o) af[i] = *(const bf16x8*)(lds + aa[i] + (o))
#define A_READ(i0, i1, o) _Pragma("unroll") for (int i = (i0); i < (i1); ++i) A_RD(i, o)
#define A_MOVE(i0, i1, d) _Pragma("unroll") for (int i = (i0); i < (i1); ++i) { aa[i] += (d) * 4096; PIN(aa[i]); }
#define Q_RD(i, u) qf[i][u] = *(const bf16x8*)(lds + qa[2 * (i) + (u)])
#define A_FIRST() do { A_READ(0, 16, 0); lds_add(C_FREE); _Pragma("unroll") for (int i = 0; i < 16; ++i) PIN(af[i]); STAMP(12); } while (0)
    bool have = false;
    _Pragma("unroll 1") while ((u32)__builtin_amdgcn_s_memrealtime() - t0 < E3_T) {
      if (__builtin_amdgcn_readfirstlane(ctr[C_MK + w]) >= 4u) { have = true; break; }
      __builtin_amdgcn_s_sleep(1);
    }
    asm volatile("" ::: "memory");
    if (have) A_FIRST();
    lds_wait(ctr + C_SOK, 1u);
    if (isS0) lds_wait(ctr + C_H1, 4u);
    until(t0, E3_T);
    STAMP(6);
#define Q_DMA() _Pragma("unroll") for (int i = 0; i < 8; ++i) dma_sc1(L_QI + w * 8192 + i * 1024, (qv ^ ((i & 3) << 4)) + (i >> 1) * 16384 + (i & 1) * 4096, ws)
    Q_DMA();
    if (RARE(!have)) { lds_wait(ctr + C_MK + w, 4u); A_FIRST(); }
    A_MOVE(0, 16, sl1 - w);
    u32 cb = L_CTR, cw = L_CTR + 4 * w, mx0 = mw + L_MX, mx1 = (mw ^ 64) + L_MX, pw0 = pw + w * 1024, pw1 = (pw ^ 256) + w * 1024;
    float ninf = -__builtin_inff();
    u32 one = 1u;
    PIN(ninf); PIN(one); PIN(cb); PIN(cw); PIN(mx0); PIN(mx1); PIN(pw0); PIN(pw1);
#define CTS(b, i) (*(volatile LAS u32*)(lds + (b) + 4 * (i)))
    _Pragma("unroll 1") for (u32 spins = 0; spins < SPIN_CAP; ++spins) {
      waitvm<0>();
#pragma unroll
      for (int i = 0; i < 8; ++i) Q_RD(i >> 1, i & 1);
      u32 mxu[2] = {0u, 0u};
#pragma unroll
      for (int i = 0; i < 8; ++i) { const u32x4 t_ = __builtin_bit_cast(u32x4, qf[i >> 1][i & 1]); mxu[i & 1] = max(max(mxu[i & 1], t_.x), t_.y); mxu[i & 1] = max(max(mxu[i & 1], t_.z), t_.w); }
      if (!RARE(__any(max(mxu[0], mxu[1]) == POISON))) break;
      STAMP(29);
      __builtin_amdgcn_s_sleep(2);
      const u64 b0 = __ballot(mxu[w & 1] == POISON), b1 = __ballot(mxu[~w & 1] == POISON);
#pragma unroll 1
      for (int i = 0; i < 8; ++i)
        if (((i < 4 ? b0 : b1) >> (4 * (i & 3))) & 0x000F000F000F000Full) dma_sc1(L_QI + w * 8192 + i * 1024, (qv ^ ((i & 3) << 4)) + (i >> 1) * 16384 + (i & 1) * 4096, ws);
    }
    lds_add(C_QRDY);
    STAMP(7);
    f32x4 sc[2], sn[2];
    sc[0] = sc[1] = sn[0] = sn[1] = f32x4{0.f, 0.f, 0.f, 0.f};
#define QK(d, i0, i1, NU)                                                                                                \
  do {                                                                                                                   \
    SB();                                                                                                                \
    _Pragma("unroll") for (int i = (i0); i < (i1); ++i) _Pragma("unroll") for (int u = 0; u < (NU); ++u)                 \
      d[u] = __builtin_amdgcn_mfma_f32_16x16x32_bf16(af[i], qf[i][u], d[u], 0, 0, 0);                                    \
    SB();                                                                                                                \
  } while (0)
#define MF(d, i, u) do { SB(); d[u] = __builtin_amdgcn_mfma_f32_16x16x32_bf16(af[i], qf[i][u], d[u], 0, 0, 0); SB(); } while (0)
    const bool idle1 = isS0 && w == 0;
    u32 c1;
    {
      const u32 c0 = CTS(cb, C_QRDY);
      QK(sc, 0, 2, 2);
      if (RARE(__builtin_amdgcn_readfirstlane(c0) < 4u)) lds_spin(ctr + C_QRDY, 4u);
      asm volatile("" ::: "memory");
#pragma unroll
      for (int i = 2; i < 4; ++i) { MF(sc, i, 0); Q_RD(2 * i, 0); Q_RD(2 * i, 1); MF(sc, i, 1); Q_RD(2 * i + 1, 0); Q_RD(2 * i + 1, 1); }
#pragma unroll
      for (int i = 4; i < 12; ++i) { MF(sc, i, 0); Q_RD(i + 4, 0); MF(sc, i, 1); Q_RD(i + 4, 1); }
      c1 = CTS(cw, C_MK + 4);
      asm volatile("" ::: "memory");
#pragma unroll
      for (int i = 12; i < 16; ++i) { MF(sc, i, 0); A_RD(2 * i - 24, 0); MF(sc, i, 1); A_RD(2 * i - 23, 0); }
    }
    STAMP(9);
    float mref[2], lsum[2] = {0.f, 0.f};
#define ADDC(i, v) do { SB(); asm volatile("s_mov_b64 exec, 1\n ds_add_u32 %0, %1 offset:%2\n s_mov_b64 exec, -1" ::"v"(cb), "v"(v), "n"(4 * (i)) : "memory"); SB(); } while (0)
#define SM(d, T, NU, G, P0, P1)                                                                                          \
  do {                                                                                                                   \
    SB();                                                                                                                \
    if (RARE(__any(max5(d[0], (NU) > 1 ? max5(d[1], ninf) : ninf) > TAU))) {                                             \
      sig = 257u;                                                                                                        \
      _Pragma("unroll") for (int u = 0; u < (NU); ++u) {                                                                 \
        const float up = ceilf(x32_max(x16_max(max5(d[u], 0.f))));                                                       \
        lsum[u] *= __builtin_amdgcn_exp2f(-up);                                                                          \
        mref[u] += up;                                                                                                   \
        d[u] -= up;                                                                                                      \
        *(float*)(lds + (mw ^ (u * 64)) + L_REF + (T) * 128) = mref[u];                                                  \
      }                                                                                                                  \
    }                                                                                                                    \
    u32x2 pk[2];                                                                                                         \
    _Pragma("unroll") for (int u = 0; u < (NU); ++u) {                                                                   \
      pk[u].x = pk2bf(__builtin_amdgcn_exp2f(d[u].x), __builtin_amdgcn_exp2f(d[u].y));                                   \
      pk[u].y = pk2bf(__builtin_amdgcn_exp2f(d[u].z), __builtin_amdgcn_exp2f(d[u].w));                                   \
      *(u32x2*)(lds + (u ? (P1) : (P0))) = pk[u];                                                                        \
    }                                                                                                                    \
    ADDC(C_P + (G), sig);                                                                                                \
    _Pragma("unroll") for (int u = 0; u < (NU); ++u) { lsum[u] = dot2(pk[u].x, 0x3F803F80u, dot2(pk[u].y, 0x3F803F80u, lsum[u])); PIN(lsum[u]); } \
    SB();                                                                                                                \
  } while (0)
#define REF_WRITE(T, NU) _Pragma("unroll") for (int u = 0; u < (NU); ++u) *(float*)(lds + (mw ^ (u * 64)) + L_REF + (T) * 128) = mref[u]
    u32 sig = one;
    {
      MF(sn, 0, 0); A_RD(8, 0); MF(sn, 0, 1);
      const float m0 = max5(sc[0], ninf), m1 = max5(sc[1], ninf);
      MF(sn, 1, 0);
      asm volatile("ds_max_f32 %0, %2\n ds_max_f32 %1, %3" ::"v"(mx0), "v"(mx1), "v"(m0), "v"(m1) : "memory");
      ADDC(C_MXC, one);
      MF(sn, 1, 1); A_RD(9, 0);
#pragma unroll
      for (int i = 2; i < 4; ++i) { MF(sn, i, 0); A_RD(2 * i + 6, 0); MF(sn, i, 1); A_RD(2 * i + 7, 0); }
      MF(sn, 4, 0);
      const u32 c0 = CTS(cb, C_MXC);
      mref[0] = *(volatile LAS float*)(lds + mx0);
      mref[1] = *(volatile LAS float*)(lds + mx1);
      MF(sn, 4, 1); MF(sn, 5, 0); A_RD(14, 0); MF(sn, 5, 1); A_RD(15, 0); MF(sn, 6, 0); MF(sn, 6, 1);
      if (RARE(__builtin_amdgcn_readfirstlane(c0) < 4u)) {
        lds_spin(ctr + C_MXC, 4u);
        mref[0] = *(volatile LAS float*)(lds + mx0);
        mref[1] = *(volatile LAS float*)(lds + mx1);
      }
      u32x2 pk[2];
#pragma unroll
      for (int u = 0; u < 2; ++u) {
        MF(sn, 7 + 2 * u, 0);
        mref[u] = ceilf(mref[u]);
        float e0 = __builtin_amdgcn_exp2f(sc[u].x - mref[u]); PIN(e0);
        MF(sn, 7 + 2 * u, 1);
        pk[u].x = pk2bf(e0, __builtin_amdgcn_exp2f(sc[u].y - mref[u])); PIN(pk[u].x);
        MF(sn, 8 + 2 * u, 0);
        e0 = __builtin_amdgcn_exp2f(sc[u].z - mref[u]); PIN(e0);
        MF(sn, 8 + 2 * u, 1);
        pk[u].y = pk2bf(e0, __builtin_amdgcn_exp2f(sc[u].w - mref[u]));
        *(u32x2*)(lds + (u ? pw1 : pw0)) = pk[u];
      }
      ADDC(C_P, one);
      MF(sn, 11, 0);
      lsum[0] = dot2(pk[0].x, 0x3F803F80u, dot2(pk[0].y, 0x3F803F80u, 0.f)); PIN(lsum[0]);
      MF(sn, 11, 1);
      lsum[1] = dot2(pk[1].x, 0x3F803F80u, dot2(pk[1].y, 0x3F803F80u, 0.f)); PIN(lsum[1]);
      MF(sn, 12, 0);
      REF_WRITE(4 + w, 2);
      MF(sn, 12, 1);
      QK(sn, 13, 16, 2);
    }
    STAMP(10);
#define QK1(N0, slot)                                                                                                    \
  do {                                                                                                                   \
    _Pragma("unroll") for (int i = 0; i < 16; ++i) { MF(sc, i, 0); if (i + (N0) < 16) A_RD(i + (N0), slot); }            \
  } while (0)
    if (RARE(__builtin_amdgcn_readfirstlane(c1) < 4u && !idle1)) {
      lds_spin(ctr + C_MK + 4 + w, 4u);
      A_READ(0, 16, 0);
      sn[0] = sn[1] = f32x4{0.f, 0.f, 0.f, 0.f};
      QK(sn, 0, 16, 2);
    }
    if (RARE(idle1)) lds_add(C_P + 1);
    else { sn[0] -= mref[0]; sn[1] -= mref[1]; SM(sn, 4 + w, 2, 1, pw0 + 4096, pw1 + 4096); }
    if (RARE(w == 2)) {
      RELANE();
      dma_sc1(L_QI, WS_VOTE + xc * 2048 + lane * 16, ws);
    }
    REF_WRITE(7 + sl2, 1);
    A_MOVE(0, 16, sl2 - sl1);
    pw0 = pw + sl2 * 1024;
    sc[0] = f32x4{-mref[0], -mref[0], -mref[0], -mref[0]};
    PIN(pw0); PIN(sc[0]);
    lds_spin(ctr + C_MK + 7 + sl2, 4u);
    A_READ(0, 6, 0);
    STAMP(4);
    QK1(6, 0);
    SM(sc, 7 + sl2, 1, 2, pw0 + 7168, 0);
    STAMP(11);
    if (!RARE(w >= 2)) {
      REF_WRITE(10, 1);
      sc[0] = f32x4{-mref[0], -mref[0], -mref[0], -mref[0]};
      PIN(sc[0]);
      _Pragma("unroll 1") for (u32 n_ = 0; n_ < LSPIN_CAP; ++n_) {
        const u32 k_ = CTS(cb, C_MK + 10);
        asm volatile("" ::: "memory");
        A_READ(0, 6, 8192);
        SB();
        if (__builtin_amdgcn_readfirstlane(k_) >= 4u) break;
      }
      STAMP(5);
      QK1(6, 8192);
      SM(sc, 10, 1, 3, pw0 + 9216, 0);
    } else if (w == 2) {
      RELANE();
      waitvm<0>();
      COUNT_VOTES(true);
      lds_add(C_GX);
    }
    STAMP(30);
    RELANE();
    {
#pragma unroll
      for (int u = 0; u < 2; ++u) {
        *(float*)(lds + L_ML + (mw ^ (u * 64)) * 4 + w * 4) = mref[u];
        *(float*)(lds + L_ML + 512 + (mw ^ (u * 64)) * 4 + w * 4) = x32_sum(x16_sum(lsum[u]));
      }
      u32 old;
      f32x4 m4, l4;
      asm volatile("s_mov_b64 exec, 1\n ds_add_rtn_u32 %0, %3, %4 offset:%6\n s_mov_b64 exec, -1\n ds_read_b128 %1, %5\n ds_read_b128 %2, %5 offset:512\n s_waitcnt lgkmcnt(0)"
                   : "=&v"(old), "=&v"(m4), "=&v"(l4) : "v"((u32)L_CTR), "v"(1u), "v"(L_ML + (lane & 31) * 16), "n"(4 * C_SD) : "memory");
      if (__builtin_amdgcn_readfirstlane(old) == 3u) {
        const float M = fmaxf(fmaxf(m4.x, m4.y), fmaxf(m4.z, m4.w));
        const float l = (l4.x * __builtin_amdgcn_exp2f(m4.x - M) + l4.y * __builtin_amdgcn_exp2f(m4.y - M)) + (l4.z * __builtin_amdgcn_exp2f(m4.z - M) + l4.w * __builtin_amdgcn_exp2f(m4.w - M));
        *(u32x2*)(lds + L_WMAX + (lane & 1) * 128 + (lane & 30) * 4) = u32x2{fbits(M), fbits(l)};
        lds_add(C_ML);
      }
    }
    lds_wait(ctr + C_GX, 1u);
    gx = __builtin_amdgcn_readfirstlane(ctr[C_PAR]);
    STAMP(13);
  } else {
    const int mu = 2 * j + (w >> 2);
#define Q_LOAD(i0, i1)                                                                                                   \
  _Pragma("unroll") for (int i = (i0); i < (i1); ++i) {                                                                  \
    waitvm<QW - 1>();                                                                                                    \
    dma<false>(w * 16384 + (i & 15) * 1024, (vq ^ ((i & 3) << 4)) + ((i < 8 ? 64 * w + 32 * dh : i < 16 ? 64 * w + 32 - 32 * dh : 256 * dh + 32 * w) + 4 * (i & 7)) * 8192, i < 16 ? w_q : w_uk); \
  }
    const u32 vq = g4 * 8192 + hq * 256 + ((col ^ (g4 << 2)) << 4);
    u32x4 xv;
    u32x2 e2;
    u32 x2_;
    asm volatile("global_load_dwordx4 %0, %3, %5\n global_load_dword %1, %4, %5\n buffer_load_dwordx2 %2, off, %6, 0 sc1"
                 : "=&v"(xv), "=&v"(x2_), "=&v"(e2) : "v"(lane * 16), "v"(128 * w + 4 * (lane & 31)), "s"(x), "s"(rs) : "memory");
    bf16x8 wf[16], wk[8];
    u32 ta0 = w * 16384 + off_b(8 * g4 + tq, tp >> 1) + 8 * (tp & 1), ta1 = w * 16384 + off_b(8 * g4 + 4 + tq, tp >> 1) + 8 * (tp & 1);
    int so = WS_SC + ((lane >> 5) * 2048 + 64 * w + 2 * (lane & 31)) * 8;
    int fo = w == 0 ? WS_SFLAG + xc * 2048 + lane * 8 : WS_TOTAL;
    PIN(ta0); PIN(ta1); PIN(so); PIN(fo);
    Q_LOAD(4, 8 + QW);
    waitvm<QW>();
#pragma unroll
    for (int c = 0; c < 8; ++c) wf[c] = tr_frag(lds, ta0 ^ (c << 5), ta1 ^ (c << 5));
    Q_LOAD(8 + QW, 16);
#pragma unroll
    for (int c = 0; c < 8; ++c) PIN(wf[c]);
    Q_LOAD(16, 24);
    STAMP(12);
    until(t0, E1_T);
    STAMP(23);
    u32x4 g[4];
    asm volatile("buffer_load_dwordx4 %0, %4, %5, 0 offen sc1\n buffer_load_dwordx4 %1, %4, %5, %6 offen sc1\n"
                 "buffer_load_dwordx4 %2, %4, %5, %7 offen sc1\n buffer_load_dwordx4 %3, %4, %5, %8 offen sc1"
                 : "=&v"(g[0]), "=&v"(g[1]), "=&v"(g[2]), "=&v"(g[3]) : "v"(so), "s"(rs), "s"(4096), "s"(8192), "s"(12288) : "memory");
    u32x2 fl;
    asm volatile("buffer_load_dwordx2 %0, %1, %2, 0 offen sc1" : "=&v"(fl) : "v"(fo), "s"(rs) : "memory");
    asm volatile("s_waitcnt vmcnt(13)" : "+v"(xv), "+v"(x2_), "+v"(e2)::"memory");
#pragma unroll
    for (int c = 0; c < 8; ++c) wf[8 + c] = tr_frag(lds, (ta0 ^ (c << 5)) + 8192, (ta1 ^ (c << 5)) + 8192);
    TAG_OF(e2.x, e2.y);
    float ln0, ln1;
    {
      float xe[8] = {lo16(xv.x), hi16(xv.x), lo16(xv.y), hi16(xv.y), lo16(xv.z), hi16(xv.z), lo16(xv.w), hi16(xv.w)};
      float s = 0.f;
#pragma unroll
      for (int k = 0; k < 8; ++k) s += xe[k];
      const float mean = wave_sum(s) * (1.f / 512.f);
      float d2 = 0.f;
#pragma unroll
      for (int k = 0; k < 8; ++k) d2 += (xe[k] - mean) * (xe[k] - mean);
      const float var = rbf(wave_sum(d2) * (1.f / 512.f));
      const float mb = rbf(mean);
      const float sd = rbf(sqrtf(rbf(var + 1e-5f)));
      ln0 = rbf(rbf(lo16(x2_) - mb) / sd);
      ln1 = rbf(rbf(hi16(x2_) - mb) / sd);
    }
    PIN(ln0); PIN(ln1);
    int qpo = (lane & 7) == 0 ? WS_QABS + il(hq * 1024 + dh * 512 + w * 64 + (lane >> 5) * 32 + (lane & 8) * 2, g4 & 1) : WS_TOTAL;
    u32 hw_ = L_H1RED + w * 128 + (lane & 31) * 4, hr_ = L_H1RED + w * 128 + 16 * g4 + 64 * dh, qw_ = L_H1RED + 1024 + w * 272 + lane * 2, qr_ = L_H1RED + 1024 + col * 272 + 64 * g4;
    PIN(qpo); PIN(hw_); PIN(hr_); PIN(qw_); PIN(qr_);
    waitvm<5>();
#pragma unroll
    for (int i = 0; i < 8; ++i) wk[i] = *(const bf16x8*)(lds + w * 16384 + (i >> 2) * 4096 + off_b(col, 4 * g4 + (i & 3)));
#pragma unroll
    for (int c = 0; c < 8; ++c) { PIN(wf[8 + c]); PIN(wk[c]); }
    STAMP(4);
    asm volatile("s_waitcnt vmcnt(0)" : "+v"(g[0]), "+v"(g[1]), "+v"(g[2]), "+v"(g[3]), "+v"(fl)::"memory");
    _Pragma("unroll 1") for (u32 spins = 0; spins < SPIN_CAP; ++spins) {
      bool ok = true;
#pragma unroll
      for (int k = 0; k < 4; ++k) ok = ok & (gran_ok(g[k].x, g[k].y, tlo, thi) & gran_ok(g[k].z, g[k].w, tlo, thi));
      if (!RARE(!__all(ok))) break;
      STAMP(29);
      __builtin_amdgcn_s_sleep(1);
      asm volatile("" ::: "memory");
#pragma unroll
      for (int k = 0; k < 4; ++k) g[k] = ld128(rs, so + k * 4096);
    }
    STAMP(1);
    {
      const float m0 = rbf((gran_val(g[0].x) + gran_val(g[1].x)) + (gran_val(g[2].x) + gran_val(g[3].x)));
      const float m1 = rbf((gran_val(g[0].z) + gran_val(g[1].z)) + (gran_val(g[2].z) + gran_val(g[3].z)));
      const auto s0 = __builtin_amdgcn_permlane32_swap(__float_as_uint(m0), __float_as_uint(m0), false, false);
      const auto s1 = __builtin_amdgcn_permlane32_swap(__float_as_uint(m1), __float_as_uint(m1), false, false);
      *(u32*)(lds + hw_) = pk2bf(rbf(ln0 * rbf(1.f + __uint_as_float(s0[1]))) + __uint_as_float(s0[0]),
                                 rbf(ln1 * rbf(1.f + __uint_as_float(s1[1]))) + __uint_as_float(s1[0]));
    }
    WAVE_LDS_SYNC();
    {
      const bf16x8 h0 = *(const bf16x8*)(lds + hr_), h1 = *(const bf16x8*)(lds + (hr_ ^ 64));
      f32x4 d[8];
#pragma unroll
      for (int c = 0; c < 8; ++c) d[c] = __builtin_amdgcn_mfma_f32_16x16x32_bf16(h0, wf[c], f32x4{0.f, 0.f, 0.f, 0.f}, 0, 0, 0);
#pragma unroll
      for (int c = 0; c < 8; ++c) d[c] = __builtin_amdgcn_mfma_f32_16x16x32_bf16(h1, wf[8 + c], d[c], 0, 0, 0);
#pragma unroll
      for (int k = 0; k < 2; ++k) {
        const float v = (g4 & 2) ? ((g4 & 1) ? d[4 * k + 3][0] : d[4 * k + 2][0]) : ((g4 & 1) ? d[4 * k + 1][0] : d[4 * k][0]);
        const u32 vh = f2bf(v);
        *(u16*)(lds + qw_ + 128 * k) = (u16)vh;
        *(u16*)(lds + qw_ + 128 * k + 8 * 272) = (u16)f2bf(v - bf2f(vh));
      }
    }
    STAMP(2);
    if (w == 0) S_POLL(fl);
    BAR();
    {
      bf16x8 a[4];
#pragma unroll
      for (int s = 0; s < 4; ++s) a[s] = *(const bf16x8*)(lds + qr_ + 16 * s);
      f32x4 d[2] = {f32x4{0.f, 0.f, 0.f, 0.f}, f32x4{0.f, 0.f, 0.f, 0.f}};
#pragma unroll
      for (int s = 0; s < 4; ++s)
#pragma unroll
        for (int c = 0; c < 2; ++c) d[c] = __builtin_amdgcn_mfma_f32_16x16x32_bf16(a[s], wk[4 * c + s], d[c], 0, 0, 0);
      constexpr float SCALE = 0.08838834764831845f * 1.4426950408889634f;
      const float v = SCALE * x16_sum(rs32(sum4(d[0]), sum4(d[1])));
      const float p0 = __uint_as_float(nopois(pk2bf(dppf<0x00>(v), dppf<0x55>(v)))), p1 = __uint_as_float(nopois(pk2bf(dppf<0xAA>(v), dppf<0xFF>(v))));
      st128(rs, qpo, u32x4{__float_as_uint(p0), __float_as_uint(p1), __float_as_uint(dppf<0x104>(p0)), __float_as_uint(dppf<0x104>(p1))});
    }
    STAMP(5);
    SB();
    {
      st128(rs, lane < 50 ? WS_PCTX + (j * 8 + w) * 800 + lane * 16 : WS_TOTAL, P4);
      const int pp = __builtin_amdgcn_readfirstlane(WS_PROBE + il((j * 8 + w) * 128, w & 1));
      u64 ta, tb, tc;
      u32 dm;
      asm volatile("s_load_dword %3, %4, 0x0\n s_waitcnt lgkmcnt(0)\n"
                   "s_memtime %0\n s_waitcnt lgkmcnt(0)\n s_load_dword %3, %4, %5 glc\n s_waitcnt lgkmcnt(0)\n"
                   "s_memtime %1\n s_waitcnt lgkmcnt(0)\n s_load_dword %3, %4, %6 glc\n s_waitcnt lgkmcnt(0)\n"
                   "s_memtime %2\n s_waitcnt vmcnt(0) lgkmcnt(0)"
                   : "=&s"(ta), "=&s"(tb), "=&s"(tc), "=&s"(dm) : "s"(ws), "s"(pp), "s"(pp ^ 8192) : "memory");
      int df = min(max(((int)(u32)(tb - ta) - (int)(u32)(tc - tb)) >> 3, -VCLIP), VCLIP);
      df = ((w ^ myh) & 1) ? -df : df;
      u32 old = 0u;
      if (lane == 0) old = __hip_atomic_fetch_add((LAS u32*)(lds + L_CTR) + C_VOTE, (u32)(65536 + 64 + df), __ATOMIC_RELAXED, __HIP_MEMORY_SCOPE_WORKGROUP);
      old = __builtin_amdgcn_readfirstlane(old) + 65536 + 64 + df;
      if (old >= 524288u && lane < 8) st64(rs, WS_VOTE + lane * 2048 + j * 8, (old & 1023u) | (tlo << 24), (tlo >> 8) | (thi << 24));
      STAMP(6);
    }
    {
      const int r0 = 128 * (mu & 3) + 32 * (w & 3) + 4 * (lane >> 3);
      const u32x2 cvb = *(const u32x2*)(cvec + r0);
      u32x4 wsc[4];
#pragma unroll
      for (int i = 0; i < 4; ++i) wsc[i] = ldnt(w_mod + (size_t)(r0 + i) * 3072 + 1024 + (mu >> 2) * 64 + (lane & 7) * 8);
      float a[8];
#pragma unroll
      for (int k = 0; k < 8; ++k) a[k] = 0.f;
#pragma unroll
      for (int i = 0; i < 4; ++i) fma8b(a, f2bf(silu_bf((i & 1) ? hi16(i < 2 ? cvb.x : cvb.y) : lo16(i < 2 ? cvb.x : cvb.y))), wsc[i]);
      float r[4];
#pragma unroll
      for (int k = 0; k < 4; ++k) r[k] = rs32(a[k], a[k + 4]);
      const float o0 = b3_sum(rs16(r[0], r[2])), o1 = b3_sum(rs16(r[1], r[3]));
      float* red = (float*)(lds + L_H1RED + 8192);
      if ((lane & 8) == 0) *(f32x2*)(red + w * 64 + (lane & 7) * 8 + (lane >> 5) * 4 + ((lane >> 4) & 1) * 2) = f32x2{o0, o1};
      BAR();
      if ((w & 3) == 3 && lane < 16) {
        red += (w >> 2) * 256 + lane * 4;
        const f32x4 sm = (*(const f32x4*)(red) + *(const f32x4*)(red + 64)) + (*(const f32x4*)(red + 128) + *(const f32x4*)(red + 192));
        st128(rs, WS_MODB + ((mu & 3) * 2048 + (mu >> 2) * 64 + lane * 4) * 4, u32x4{fbits(sm.x), fbits(sm.y), fbits(sm.z), fbits(sm.w)});
      }
    }
    gx = 0;
  }

  RELANE();
  const int r16 = lane >> 2, ch = lane & 3;
  if (ii >= 16) {
    int mo = lane < 24 ? (WS_LSE + il((24 * w + lane) * 512 + c4 * 128 + (h >> 1) * 8, h & 1)) ^ gx : WS_TOTAL;
    PIN(mo);
    lds_spin(ctr + C_PD, 4u);
    STAMP(23);
    u32x2 mt = ld64(rs, mo);
    SB();
    const u32 pb = (w >> 1) * PS + (w & 1) * 6144;
    u32 po[6];
#pragma unroll
    for (int k = 0; k < 6; ++k) {
      po[k] = g4 * 256 + ((col ^ (g4 << 2) ^ (k & 3)) << 4) + ((WS_PART + il21(((h >> 1) * 4 + c4) * 65536 + (24 * w + 4 * k) * 256, h & 1)) ^ gx);
      PIN(po[k]);
    }
    bf16x8 wk[4];
    {
      const u32 wa = tq * PS + 16384 + off_b(g4, 2 * w + (tp >> 1)) + 8 * (tp & 1);
#pragma unroll
      for (int s = 0; s < 4; ++s) wk[s] = tr_frag(lds, (wa ^ (s << 4)) + s * 1024, (wa ^ (s << 4)) + s * 1024 + 4096);
    }
    SB();
    u32 done = 0u;
    _Pragma("unroll 1") for (u32 spins = 0; spins < SPIN_CAP; ++spins) {
      asm volatile("" ::: "memory");
      if (spins && max(mt.x, mt.y) == POISON) mt = ld64(rs, mo);
      const u32 ok = ~(u32)__ballot(max(mt.x, mt.y) == POISON);
      u32 nw = ok & (ok >> 1);
      nw = nw & (nw >> 2) & 0x111111u & ~done;
      done |= nw;
#pragma unroll
      for (int k = 0; k < 6; ++k)
        if (nw & (1u << (4 * k))) dma_sc1(pb + k * 1024, po[k], ws);
      if (spins == 0) STAMP(21);
      if (done == 0x111111u) break;
      __builtin_amdgcn_s_sleep(2);
    }
    STAMP(16);
    u32 pa[16];
#pragma unroll
    for (int i = 0; i < 16; ++i) { pa[i] = pb + off_b(8 * (g4 % 3) + 4 * (i & 1) + tq, (i & 14) + (tp >> 1)) + 8 * (tp & 1); PIN(pa[i]); }
    u32 bo = L_QOFF + col * 272 + g4 * 16, mlo = L_QOFF + 4352 + (col & 7) * 8, ro = L_QOFF + (w + (col & 8)) * 272 + (col & 7) * 32 + g4 * 8;
    int so1 = col < 2 ? (WS_PCTX + h * 2048 + c4 * 512 + (w >> 2) * 256 + (2 * (w & 1) + (g4 >> 1)) * 64 + (((w >> 1) & 1) * 2 + col) * 16 + (g4 & 1) * 8) ^ gx : WS_TOTAL;
    PIN(bo); PIN(mlo); PIN(ro); PIN(so1);
    {
      const u32 eo = L_QOFF + 4416 + w * 64;
      const float m = lane < 24 ? __uint_as_float(mt.x) : -__builtin_inff();
      const float M = wave_max(m);
      const float e = __builtin_amdgcn_exp2f(m - M);
      if (lane < 32) *(u16*)(lds + eo + lane * 2) = (u16)f2bf(e);
      WAVE_LDS_SYNC();
      const u32x4 ev4 = *(const u32x4*)(lds + eo + 16 * g4);
      const float L = wave_sum(e * __uint_as_float(mt.y));
      if (lane == 0) *(f32x2*)(lds + L_QOFF + 4352 + w * 8) = f32x2{M, L};
      bf16x8 wf[8], pt[8];
#pragma unroll
      for (int i = 0; i < 8; ++i) { wf[i] = __builtin_bit_cast(bf16x8, (col & 7) == i ? ev4 : Z4); PIN(wf[i]); }
      waitvm<0>();
      STAMP(17);
#pragma unroll
      for (int i = 0; i < 8; ++i) pt[i] = tr_frag(lds, pa[2 * i], pa[2 * i + 1]);
      SB();
      f32x4 d = {0.f, 0.f, 0.f, 0.f};
#pragma unroll
      for (int i = 0; i < 8; ++i) d = __builtin_amdgcn_mfma_f32_16x16x32_bf16(pt[i], wf[i], d, 0, 0, 0);
      const u32 h0 = pk2bf(d[0], d[1]), h1 = pk2bf(d[2], d[3]);
      const u32 l0 = pk2bf(d[0] - lo16(h0), d[1] - hi16(h0)), l1 = pk2bf(d[2] - lo16(h1), d[3] - hi16(h1));
      *(u32x2*)(lds + ro) = (col & 8) ? u32x2{l0, l1} : u32x2{h0, h1};
    }
    BAR();
    STAMP(14);
    const f32x2 ml = *(const f32x2*)(lds + mlo);
    bf16x8 lf[4];
#pragma unroll
    for (int s = 0; s < 4; ++s) lf[s] = *(const bf16x8*)(lds + bo + 64 * s);
    SB();
    f32x4 d = {0.f, 0.f, 0.f, 0.f};
#pragma unroll
    for (int s = 0; s < 4; ++s) d = __builtin_amdgcn_mfma_f32_16x16x32_bf16(wk[s], lf[s], d, 0, 0, 0);
    SB();
    float Mg = fmaxf(ml.x, dppf<0xB1>(ml.x));
    Mg = fmaxf(Mg, dppf<0x4E>(Mg)); Mg = fmaxf(Mg, dppf<0x141>(Mg));
    const float e = __builtin_amdgcn_exp2f(ml.x - Mg);
    float Lg = quad_sum(e * ml.y);
    Lg += dppf<0x141>(Lg);
    const float sc = e * __builtin_amdgcn_rcpf(Lg);
    {
      float d0 = d[0] * sc, d1 = d[1] * sc, d2 = d[2] * sc, d3 = d[3] * sc;
      row16_sum4(d0, d1, d2, d3);
      d = f32x4{d0, d1, d2, d3};
    }
    const u32 h0 = pk2bf(d[0], d[1]), h1 = pk2bf(d[2], d[3]);
    const u32 l0 = pk2bf(d[0] - lo16(h0), d[1] - hi16(h0)), l1 = pk2bf(d[2] - lo16(h1), d[3] - hi16(h1));
    st64(rs, so1, nopois(col ? l0 : h0), nopois(col ? l1 : h1));
    STAMP(15);
    STAMP_FLUSH();
    return;
  }
  if (tl2) {
    BAR0();
    if (w < 4) {
#pragma unroll
      for (int i = 0; i < 8; ++i) dma<true>(owno + i * 1024, (ks * 512 + 64 * w + 8 * i + (lane >> 3)) * 1024 + cg * 128 + (lane & 7) * 16, w_o);
      waitvm<0>();
    } else {
#pragma unroll
      for (int i = 0; i < 8; ++i) *(u32x4*)(lds + owno + i * 1024 + lane * 16) = wv[i];
      WAVE_LDS_SYNC();
    }
    STAMP(26);
    bf16x8 wf[8];
#pragma unroll
    for (int i = 0; i < 8; ++i) {
      const u32 a = owno + (32 * (i >> 2) + 8 * g4 + tq) * 128 + (i & 3) * 32 + tp * 8;
      wf[i] = tr_frag(lds, a, a + 512);
    }
#pragma unroll
    for (int i = 0; i < 8; ++i) PIN(wf[i]);
    STAMP(23);
    int off[1] = {(WS_PCTX + (ks * 4 + (w >> 1)) * 2048 + tq * 512 + (w & 1) * 256 + g4 * 64 + tp * 16) ^ gx};
    u32 rw = L_RED + lane * 32 + w * 4;
    PIN(rw);
    int so2 = tp < 2 ? WS_PATTN + il((ks * 512 + cg * 64 + (lane & 60)) * 4, tp) : WS_TOTAL;
    PIN(so2);
    u32x4 g[1];
    gather128<1, 4>(rs, off, g);
    STAMP(24);
    const bf16x8 a0 = __builtin_bit_cast(bf16x8, (col & 2) ? Z4 : g[0]), a1 = __builtin_bit_cast(bf16x8, (col & 2) ? g[0] : Z4);
    f32x4 d[4];
#pragma unroll
    for (int c = 0; c < 4; ++c) d[c] = __builtin_amdgcn_mfma_f32_16x16x32_bf16(a0, wf[c], f32x4{0.f, 0.f, 0.f, 0.f}, 0, 0, 0);
#pragma unroll
    for (int c = 0; c < 4; ++c) d[c] = __builtin_amdgcn_mfma_f32_16x16x32_bf16(a1, wf[4 + c], d[c], 0, 0, 0);
    *(float*)(lds + rw) = rs16(rs32(sum4(d[0]), sum4(d[2])), rs32(sum4(d[1]), sum4(d[3])));
    BAR();
    if (w == 0) {
      const float v = sum4(*(const f32x4*)(lds + L_RED + lane * 32) + *(const f32x4*)(lds + L_RED + lane * 32 + 16));
      const float vn = __uint_as_float(fbits(v));
      st128(rs, so2, u32x4{__float_as_uint(dppf<0x00>(vn)), __float_as_uint(dppf<0x55>(vn)), __float_as_uint(dppf<0xAA>(vn)), __float_as_uint(dppf<0xFF>(vn))});
    }
    STAMP(17);
  } else if (t3 >= 0) {
    STAMP(20);
    if (w < 4) {
      const int fr = ((128 * w + r16) * DF + t3 * 32) * 2, ca_ = ch ^ ((r16 >> 2) & 2) ^ (t3 & 1);
      const u16* wa = (t3 & 1) ? w_up : w_gate;
      const u16* wb = (t3 & 1) ? w_gate : w_up;
      const u32 d = w * 32768 + 8192;
#pragma unroll
      for (int i = 0; i < 8; ++i) { waitvm<WING - 1>(); dma<false>(d + (i >> 2) * 12288 + (t3 & 1) * 4096 + (i & 3) * 1024, fr + ca_ * 16 + 32 * i * DF, wa); }
#pragma unroll
      for (int i = 0; i < 8; ++i) { waitvm<WING - 1>(); dma<false>(d + (i >> 2) * 12288 + 4096 - (t3 & 1) * 4096 + (i & 3) * 1024, fr + (ca_ ^ 1) * 16 + 32 * i * DF, wb); }
    } else {
#pragma unroll
      for (int i = 0; i < 8; ++i) {
        waitvm<WING - 1>();
        dma<true>(lw * 32768 + 16384 + (i >> 2) * 12288 + (i & 3) * 1024, (32 * t3 + 8 * (i & 3) + (lane >> 3)) * 1024 + 256 * lw + 128 * (i >> 2) + (lane & 7) * 16, w_down);
      }
      const u32 x2 = *(const u32*)(x + 128 * lw + 2 * lane);
      u32x4 m6[6];
      int mo[6];
#pragma unroll
      for (int i = 0; i < 6; ++i) mo[i] = MB_OFF(i);
      gather128<6, 8>(rs, mo, m6);
#pragma unroll
      for (int i = 0; i < 6; ++i) {
        const f32x4 f = __builtin_bit_cast(f32x4, m6[i]);
        const float v = rbf(rs16(rs32(f.x, f.z), rs32(f.y, f.w)));
        *(u16*)(lds + L_Z + (i < 2 ? 5 : i < 4 ? 3 : 2) * 1088 + (128 * lw + 64 * (i & 1) + 4 * col + g4) * 2) = (u16)f2bf(i < 4 ? v : 1.f + v);
      }
      *(u32*)(lds + L_Z + 6 * 1088 + lw * 256 + lane * 4) = x2;
      if (w == 4 && lane < 32) *(u32*)(lds + L_ZERO + lane * 4) = lane < 16 ? 0u : POISON;
      if (w == 6) COUNT_VOTES(false);
    }
    waitvm<0>();
    STAMP(26);
    BAR();
    gx = __builtin_amdgcn_readfirstlane(ctr[C_PAR]);
    u32 zw = L_Z + (64 * w + 4 * col + g4) * 2;
    u32 hr[4];
#pragma unroll
    for (int i = 0; i < 4; ++i) { hr[i] = (col == i ? L_HH : L_ZERO) + 16 * g4; PIN(hr[i]); }
    int off[2];
#pragma unroll
    for (int i = 0; i < 2; ++i) { off[i] = (WS_PATTN + il(((4 * i + g4) * 512 + 64 * w + col * 4) * 4, myh)) ^ gx; PIN(off[i]); }
    int so = col < 4 ? (WS_PDOWN + il((t3 * 256 + 64 * (w & 3) + 16 * col + 4 * g4) * 4, w >> 2)) ^ gx : WS_TOTAL;
    u32 zr = L_Z + tp * 1088 + 16 * g4, yr = L_Z + 4 * 1088 + lane * 16, hw = L_HH + (w & 3) * 16 + (g4 & 1) * 8 + tp * 2, cr = L_CF + (w & 3) * 16 + tp * 4;
    PIN(cr); PIN(so); PIN(zw); PIN(zr); PIN(yr); PIN(hw);
    float ga = bf2f(*(const u16*)(lds + zw + 5 * 1088)), cb1 = bf2f(*(const u16*)(lds + zw + 2 * 1088)), xe = bf2f(*(const u16*)(lds + zw + 6 * 1088));
    bf16x8 wf[16], wd[4];
    if (w < 4) {
#pragma unroll
      for (int k = 0; k < 16; ++k) {
        const u32 a = (k >> 2) * 32768 + 8192 + ((k >> 1) & 1) * 12288 + (tp >> 1) * 4096 + ((k & 1) * 32 + 8 * g4 + tq) * 64 + (w ^ ((g4 & 1) * 2) ^ (tp >> 1)) * 16 + (tp & 1) * 8;
        wf[k] = tr_frag(lds, a, a + 256);
      }
#pragma unroll
      for (int k = 0; k < 16; ++k) PIN(wf[k]);
    }
#pragma unroll
    for (int i = 0; i < 4; ++i) {
      const u32 a = owno + 8192 + (8 * g4 + tq) * 128 + i * 32 + tp * 8;
      wd[i] = tr_frag(lds, a, a + 512);
    }
#pragma unroll
    for (int i = 0; i < 4; ++i) PIN(wd[i]);
    PIN(ga); PIN(cb1); PIN(xe);
    STAMP(23);
    until(t0, E6_T);
    u32x4 pa2[2];
    gather128<2, 4>(rs, off, pa2);
    STAMP(18);
    {
      const f32x4 s4 = __builtin_bit_cast(f32x4, pa2[0]) + __builtin_bit_cast(f32x4, pa2[1]);
      const u32 yb = f2bf(xe + rbf(ga * rbf(rs16(rs32(s4.x, s4.z), rs32(s4.y, s4.w)))));
      const float z = bf2f(yb) * cb1;
      const u32 zh = f2bf(z);
      *(u16*)(lds + zw + 4 * 1088) = (u16)yb;
      *(u16*)(lds + zw) = (u16)zh;
      *(u16*)(lds + zw + 1088) = (u16)f2bf(z - bf2f(zh));
    }
    BAR();
    if (w < 4) {
      bf16x8 zf[16];
#pragma unroll
      for (int k = 0; k < 16; ++k) zf[k] = *(const bf16x8*)(lds + zr + 64 * k);
      __builtin_amdgcn_sched_barrier(0);
      f32x4 d = {0.f, 0.f, 0.f, 0.f};
#pragma unroll
      for (int k = 0; k < 16; ++k) d = __builtin_amdgcn_mfma_f32_16x16x32_bf16(wf[k], zf[k], d, 0, 0, 0);
      PIN(d);
      u32 cfb;
      do { cfb = *(volatile LAS u32*)(lds + cr); } while (__any(cfb == POISON));
      d *= __uint_as_float(cfb);
      const float a0 = (tp & 1) ? d[1] : d[0], b0 = (tp & 1) ? d[0] : d[1], a1 = (tp & 1) ? d[3] : d[2], b1 = (tp & 1) ? d[2] : d[3];
      const float e0 = a0 + dppf<0xB1>(b0), e1 = a1 + dppf<0xB1>(b1);
      const float v = rbf(((tp & 2) ? e1 : e0) + dppf<0x4E>((tp & 2) ? e0 : e1));
      const auto sw = __builtin_amdgcn_permlane32_swap(__float_as_uint(v), __float_as_uint(v), false, false);
      const float g = __uint_as_float(sw[0]), u = __uint_as_float(sw[1]);
      const float e2 = __builtin_amdgcn_exp2f(g * (-2.3022081981f - 0.1029432396f * g * g));
      *(u16*)(lds + hw) = (u16)f2bf(rbf(g * __builtin_amdgcn_rcpf(e2 + 1.f)) * u);
    } else {
      const u32x4 yv = *(const u32x4*)(lds + yr);
      const float a1 = dot2(yv.x, 0x3F803F80u, dot2(yv.y, 0x3F803F80u, 0.f)) + dot2(yv.z, 0x3F803F80u, dot2(yv.w, 0x3F803F80u, 0.f));
      const float a2 = dot2(yv.x, yv.x, dot2(yv.y, yv.y, 0.f)) + dot2(yv.z, yv.z, dot2(yv.w, yv.w, 0.f));
      const float sr = x16_sum(row16_sum(rs32(a1, a2))) * (1.f / 512.f);
      const float s1 = __uint_as_float(__builtin_amdgcn_readlane(__float_as_uint(sr), 0)), s2 = __uint_as_float(__builtin_amdgcn_readlane(__float_as_uint(sr), 32));
      const float var = rbf(fmaxf(s2 - s1 * s1, 0.f));
      const float mb = rbf(s1);
      const float sd = rbf(__builtin_amdgcn_sqrtf(rbf(var + 1e-5f)));
      float rsd = __builtin_amdgcn_rcpf(sd);
      rsd = rsd * (2.f - sd * rsd);
      *(u32*)(lds + cr) = fbits(tp == 3 ? 1.f : rsd * (tp == 2 ? -mb : 1.f));
    }
    BAR();
    {
      bf16x8 hf[4];
#pragma unroll
      for (int i = 0; i < 4; ++i) hf[i] = *(const bf16x8*)(lds + hr[i]);
      __builtin_amdgcn_sched_barrier(0);
      f32x4 d = {0.f, 0.f, 0.f, 0.f};
#pragma unroll
      for (int i = 0; i < 4; ++i) d = __builtin_amdgcn_mfma_f32_16x16x32_bf16(wd[i], hf[i], d, 0, 0, 0);
      st128(rs, so, u32x4{fbits(d[0]), fbits(d[1]), fbits(d[2]), fbits(d[3])});
    }
    STAMP(19);
  }
  if (t4 >= 0 && lw == 0) {
#define RED8(f) b3_sum(rs16(rs32(f.x, f.z), rs32(f.y, f.w)))
    const int oc = t4 * 32 + (lane & 7) * 4 + g4;
    const u32 xb = x[oc];
    float x1, gb;
    {
      int off[2];
      off[0] = lane < 32 ? WS_MODB + ((lane >> 3) * 2048 + t4 * 32 + (lane & 7) * 4) * 4 : WS_TOTAL;
      off[1] = lane < 32 ? WS_MODB + ((lane >> 3) * 2048 + 1536 + t4 * 32 + (lane & 7) * 4) * 4 : WS_TOTAL;
      u32x4 mg[2];
      gather128<2>(rs, off, mg);
      const f32x4 fa = __builtin_bit_cast(f32x4, mg[0]), fb = __builtin_bit_cast(f32x4, mg[1]);
      const float ga = rbf(RED8(fa));
      gb = rbf(RED8(fb));
      int of1[1] = {(WS_PATTN + il(((lane >> 3) * 512 + t4 * 32 + (lane & 7) * 4) * 4, myh)) ^ gx};
      u32x4 g[1];
      gather128<1, 4>(rs, of1, g);
      const f32x4 gf = __builtin_bit_cast(f32x4, g[0]);
      x1 = rbf(bf2f(xb) + rbf(ga * rbf(RED8(gf))));
    }
    STAMP(20);
    int off[7];
#pragma unroll
    for (int i = 0; i < 7; ++i) off[i] = (WS_PDOWN + il(((7 * (lane >> 3) + i) * 256 + (t4 & 7) * 32 + (lane & 7) * 4) * 4, myh)) ^ gx;
    const rsrc_t ro = __builtin_amdgcn_make_buffer_rsrc((void*)out, 0, 1024, 0x00020000);
    int oo = (lane & 8) ? 1024 : oc * 2;
    PIN(x1); PIN(gb); PIN(oo);
    u32x4 g[7];
    __builtin_amdgcn_s_sleep(T4_DELAY);
    gather128<7>(rs, off, g);
    STAMP(21);
#define F4(i) __builtin_bit_cast(f32x4, g[i])
    const f32x4 s = ((F4(0) + F4(1)) + (F4(2) + F4(3))) + ((F4(4) + F4(5)) + F4(6));
    __builtin_amdgcn_raw_buffer_store_b16((u16)f2bf(x1 + rbf(gb * rbf(RED8(s)))), ro, oo, 0, 0);
    if (t4 == 0 && lane == 0) st64(rs, WS_EPOCH, tlo, thi);
    STAMP(22);
  }
  STAMP_FLUSH();
}

extern "C" void kernel_launch(void* const* d_in, const int* in_sizes, int n_in, void* d_out, int out_size,
                              void* d_ws, size_t ws_size, hipStream_t stream) {
  k_main<<<NWG, 512, 0, stream>>>((const u16*)d_in[0], (const u16*)d_in[1], (const u16*)d_in[2], (const u16*)d_in[3],
                                  (const u16*)d_in[4], (const u16*)d_in[5], (const u16*)d_in[7], (const u16*)d_in[8],
                                  (const u16*)d_in[9], (const u16*)d_in[10], (const u16*)d_in[11], (u16*)d_out,
                                  (unsigned char*)d_ws);
}
```

```cpp
#include <hip/hip_runtime.h>
#include <hip/hip_bf16.h>
#include <stdint.h>

typedef unsigned short u16;
typedef unsigned int u32;
typedef unsigned long long u64;
typedef float f32x2 __attribute__((ext_vector_type(2)));
typedef float f32x4 __attribute__((ext_vector_type(4)));
typedef float f32x16 __attribute__((ext_vector_type(16)));
typedef u32 u32x2 __attribute__((ext_vector_type(2)));
typedef u32 u32x4 __attribute__((ext_vector_type(4)));
typedef short bf16x8 __attribute__((ext_vector_type(8)));
typedef short s16x4 __attribute__((ext_vector_type(4)));
#define LAS __attribute__((address_space(3)))
#define DI __device__ __forceinline__

constexpr int DF = 1792;
constexpr int NWG = 256;
constexpr int AUX_SC1 = 16;

constexpr int WS_EPOCH = 0;
constexpr int WS_SFLAG = 4096;
constexpr int WS_QABS = 32768;
constexpr int WS_PCTX = WS_QABS + 65536;
constexpr int WS_PATTN = WS_PCTX + 65536;
constexpr int WS_PDOWN = WS_PATTN + 32768;
constexpr int WS_LSE = WS_PDOWN + 114688;
constexpr int WS_MODB = WS_LSE + 196608;
constexpr int WS_SC = WS_MODB + 32768;
constexpr int WS_PROBE = WS_SC + 32768;
constexpr int WS_VOTE = WS_PROBE + 262144;
constexpr int WS_PART = 2097152;
constexpr int PART_BYTES = 32 * 8 * 256 * 128;
constexpr int WS_TOTAL = WS_PART + PART_BYTES;

constexpr int il(int o, int hf) { return (o >> 13) * 16384 + hf * 8192 + (o & 8191); }
constexpr int il21(int o, int hf) { return il(o, hf ^ ((o >> 20) & 1) ^ 1); }
constexpr u32 SPIN_CAP = 1u << 16;
#define WIN 8
#define WA0 5
#define WB0 6
#define QW 4
#define WING 2
#define QF_T 300
#define E1_T 270
#define E3_T 515
#define E6_T 1200
#define T4_DELAY 12
#define TAU 32.f
#define VCLIP 40
#define PF_LEN 34200
#define G_OF(v) ((v) > 32768.f ? 8192u : 0u)
#define VOTE_STAMP(v)
#define STAMP_DECL
#define STAMP(i)
#define STAMP_FLUSH()

DI float bf2f(u32 b) { return __uint_as_float(b << 16); }
DI float rbf(float f) { return (float)(__bf16)f; }
DI u32 f2bf(float f) { return (u32)__builtin_bit_cast(u16, (__bf16)f); }
typedef __bf16 bf16x2_t __attribute__((ext_vector_type(2)));
DI u32 pk2bf(float a, float b) { return __builtin_bit_cast(u32, __builtin_convertvector(f32x2{a, b}, bf16x2_t)); }
DI float lo16(u32 v) { return __uint_as_float(v << 16); }
DI float hi16(u32 v) { return __uint_as_float(v & 0xFFFF0000u); }
DI u32x4 ldnt(const u16* p) { return __builtin_nontemporal_load((const u32x4*)p); }

DI float silu_bf(float a) { return rbf(a * __builtin_amdgcn_rcpf(1.f + __expf(-a))); }

template <int CTRL> DI float dppf(float v) {
  return __int_as_float(__builtin_amdgcn_update_dpp(0, __float_as_int(v), CTRL, 0xF, 0xF, true));
}
DI float quad_sum(float v) { v += dppf<0xB1>(v); v += dppf<0x4E>(v); return v; }
DI float b23_sum(float v) { v += dppf<0x128>(v); v += dppf<0x124>(v); return v; }
DI float b3_sum(float v) { return v + dppf<0x128>(v); }
DI float x16_sum(float v) {
  const auto r = __builtin_amdgcn_permlane16_swap(__float_as_uint(v), __float_as_uint(v), false, false);
  return __uint_as_float(r[0]) + __uint_as_float(r[1]);
}
DI float x32_sum(float v) {
  const auto r = __builtin_amdgcn_permlane32_swap(__float_as_uint(v), __float_as_uint(v), false, false);
  return __uint_as_float(r[0]) + __uint_as_float(r[1]);
}
DI float rs32(float a, float b) {
  const auto r = __builtin_amdgcn_permlane32_swap(__float_as_uint(a), __float_as_uint(b), false, false);
  return __uint_as_float(r[0]) + __uint_as_float(r[1]);
}
DI float rs16(float a, float b) {
  const auto r = __builtin_amdgcn_permlane16_swap(__float_as_uint(a), __float_as_uint(b), false, false);
  return __uint_as_float(r[0]) + __uint_as_float(r[1]);
}
DI float row16_sum(float v) { return b23_sum(quad_sum(v)); }
DI float wave_sum(float v) { return x32_sum(x16_sum(row16_sum(v))); }
DI void row16_sum4(float& a, float& b, float& c, float& d) {
#define R4(C) "v_add_f32_dpp %0, %0, %0 " C " row_mask:0xf bank_mask:0xf\n v_add_f32_dpp %1, %1, %1 " C " row_mask:0xf bank_mask:0xf\n" \
              "v_add_f32_dpp %2, %2, %2 " C " row_mask:0xf bank_mask:0xf\n v_add_f32_dpp %3, %3, %3 " C " row_mask:0xf bank_mask:0xf\n"
  asm("s_nop 1\n" R4("quad_perm:[1,0,3,2]") R4("quad_perm:[2,3,0,1]") R4("row_ror:8") R4("row_ror:4") : "+v"(a), "+v"(b), "+v"(c), "+v"(d));
#undef R4
}
DI float row16_max(float v) {
  v = fmaxf(v, dppf<0xB1>(v)); v = fmaxf(v, dppf<0x4E>(v));
  v = fmaxf(v, dppf<0x128>(v)); v = fmaxf(v, dppf<0x124>(v));
  return v;
}
DI float x16_max(float v) {
  const auto r = __builtin_amdgcn_permlane16_swap(__float_as_uint(v), __float_as_uint(v), false, false);
  return fmaxf(__uint_as_float(r[0]), __uint_as_float(r[1]));
}
DI float x32_max(float v) {
  const auto r = __builtin_amdgcn_permlane32_swap(__float_as_uint(v), __float_as_uint(v), false, false);
  return fmaxf(__uint_as_float(r[0]), __uint_as_float(r[1]));
}
DI float wave_max(float v) { return x32_max(x16_max(row16_max(v))); }

DI float dot2(u32 a, u32 b, float c) {
  return __builtin_amdgcn_fdot2_f32_bf16(__builtin_bit_cast(bf16x2_t, a), __builtin_bit_cast(bf16x2_t, b), c, false);
}
DI void fma8b(float* acc, u32 sbits, u32x4 w) {
  const u32 sh = sbits << 16;
  acc[0] = dot2(w.x, sbits, acc[0]); acc[1] = dot2(w.x, sh, acc[1]);
  acc[2] = dot2(w.y, sbits, acc[2]); acc[3] = dot2(w.y, sh, acc[3]);
  acc[4] = dot2(w.z, sbits, acc[4]); acc[5] = dot2(w.z, sh, acc[5]);
  acc[6] = dot2(w.w, sbits, acc[6]); acc[7] = dot2(w.w, sh, acc[7]);
}

DI unsigned fsw(unsigned row) { return ((row & 3) << 2) | ((row >> 2) & 3); }
DI unsigned off_b(unsigned row, unsigned ch) { return 256u * row + 16u * (ch ^ fsw(row)); }
DI unsigned tr_addr(unsigned lane, unsigned c, unsigned ks, unsigned t) {
  const unsigned h = lane >> 5, blk = (lane >> 4) & 1, q = (lane & 15) >> 2, p = lane & 3;
  return off_b(16 * ks + 8 * h + 4 * t + q, 4 * c + 2 * blk + (p >> 1)) + 8 * (p & 1);
}

typedef __amdgpu_buffer_rsrc_t rsrc_t;
DI u32x4 ld128(rsrc_t rs, int off) { return __builtin_amdgcn_raw_buffer_load_b128(rs, off, 0, AUX_SC1); }
DI u32x2 ld64(rsrc_t rs, int off) { return __builtin_amdgcn_raw_buffer_load_b64(rs, off, 0, AUX_SC1); }
DI void st64(rsrc_t rs, int off, u32 lo, u32 hi) { u32x2 v; v.x = lo; v.y = hi; __builtin_amdgcn_raw_buffer_store_b64(v, rs, off, 0, AUX_SC1); }
DI void st128(rsrc_t rs, int off, u32x4 v) { __builtin_amdgcn_raw_buffer_store_b128(v, rs, off, 0, AUX_SC1); }
#define BAR() do { asm volatile("s_waitcnt lgkmcnt(0)" ::: "memory"); __builtin_amdgcn_s_barrier(); asm volatile("" ::: "memory"); } while (0)
#define BAR0() do { __builtin_amdgcn_sched_barrier(0); asm volatile("" ::: "memory"); __builtin_amdgcn_s_barrier(); asm volatile("" ::: "memory"); __builtin_amdgcn_sched_barrier(0); } while (0)
#define PIN(x) asm volatile("" : "+v"(x))
constexpr u32 POISON = 0xFFFFFFFFu;
DI bool pois4(u32x4 v) { return max(max(v.x, v.y), max(v.z, v.w)) == POISON; }
DI u32 nopois(u32 v) { return min(v, 0xFFFFFFFEu); }
DI u32 fbits(float v) { return nopois(__float_as_uint(v)); }

DI u32x2 mk_gran(float v, u32 tlo, u32 thi) {
  u32 b = __float_as_uint(v);
  if ((b & 0x7F800000u) != 0x7F800000u) b += 0x80u;
  u32x2 g; g.x = (b >> 8) | (tlo << 24); g.y = (tlo >> 8) | (thi << 24);
  return g;
}
DI void st_gran2(rsrc_t rs, int off, float v0, float v1, u32 tlo, u32 thi) {
  const u32x2 a = mk_gran(v0, tlo, thi), b = mk_gran(v1, tlo, thi);
  u32x4 v; v.x = a.x; v.y = a.y; v.z = b.x; v.w = b.y;
  st128(rs, off, v);
}
DI bool gran_ok(u32 lo, u32 hi, u32 tlo, u32 thi) { return (((lo >> 24) ^ (tlo & 0xFFu)) | (hi ^ ((tlo >> 8) | (thi << 24)))) == 0u; }
DI float gran_val(u32 lo) { return __uint_as_float(lo << 8); }

template <int N, int SLP = 1> DI void gather128(rsrc_t rs, const int (&off)[N], u32x4 (&v)[N]) {
#pragma unroll
  for (int i = 0; i < N; ++i) v[i] = ld128(rs, off[i]);
  _Pragma("unroll 1") for (u32 spins = 0; spins < SPIN_CAP; ++spins) {
    bool need[N], bad = false;
#pragma unroll
    for (int i = 0; i < N; ++i) { need[i] = pois4(v[i]); bad = bad || need[i]; }
    if (!__any(bad)) break;
    __builtin_amdgcn_s_sleep(SLP);
    asm volatile("" ::: "memory");
    __builtin_amdgcn_sched_barrier(0);
#pragma unroll
    for (int i = 0; i < N; ++i)
      if (need[i]) v[i] = ld128(rs, off[i]);
  }
}
template <int N> DI void waitvm() { asm volatile("s_waitcnt vmcnt(%0)" ::"n"(N) : "memory"); }
constexpr u32 LSPIN_CAP = 1u << 22;
DI void lds_wait(volatile LAS u32* p, u32 val) {
  _Pragma("unroll 1") for (u32 n = 0; *p < val && n < LSPIN_CAP; ++n) __builtin_amdgcn_s_sleep(1);
  asm volatile("" ::: "memory");
}
#define WAVE_LDS_SYNC() do { __builtin_amdgcn_fence(__ATOMIC_RELEASE, "wavefront"); __builtin_amdgcn_wave_barrier(); __builtin_amdgcn_fence(__ATOMIC_ACQUIRE, "wavefront"); } while (0)
#define SB() __builtin_amdgcn_sched_barrier(0)
#define RARE(c) __builtin_expect(!!(c), 0)

constexpr int PS = 28672;
constexpr int L_QI = 4 * PS;
constexpr int L_P = L_QI + 32768;
constexpr int L_REF = L_P + 11264;
constexpr int L_MX = L_REF + 1408;
constexpr int L_ALW = L_MX + 128;
constexpr int L_DUMP = L_ALW + 512;
constexpr int L_QOFF = 131072;
constexpr int L_H1RED = L_QOFF;
constexpr int L_RED = L_QOFF + 16384;
constexpr int L_Z = L_RED, L_HH = L_RED + 7 * 1088, L_ZERO = L_HH + 64, L_CF = L_ZERO + 64;
constexpr int L_MISC = L_QOFF + 30720;
constexpr int L_CTR = L_MISC;
constexpr int L_WMAX = L_MISC + 256;
constexpr int L_ML = L_MISC + 512;

#define Z4 (u32x4{0u, 0u, 0u, 0u})
DI float max5(f32x4 v, float e) { return fmaxf(fmaxf(fmaxf(fmaxf(v.x, v.y), v.z), v.w), e); }

template <bool NT> DI void dma(u32 m, u32 voff, const void* sb) {
  if (NT) asm volatile("s_mov_b32 m0, %0\n s_nop 0\n global_load_lds_dwordx4 %1, %2 nt" ::"s"(m), "v"(voff), "s"(sb) : "memory");
  else asm volatile("s_mov_b32 m0, %0\n s_nop 0\n global_load_lds_dwordx4 %1, %2" ::"s"(m), "v"(voff), "s"(sb) : "memory");
}
DI void dma_sc1(u32 m, u32 voff, const void* sb) {
  asm volatile("s_mov_b32 m0, %0\n s_nop 0\n global_load_lds_dwordx4 %1, %2 sc1" ::"s"(m), "v"(voff), "s"(sb) : "memory");
}
DI void lds_add(int idx) {
  SB();
  asm volatile("s_mov_b64 exec, 1\n ds_add_u32 %0, %1 offset:%2\n s_mov_b64 exec, -1" ::"v"((u32)L_CTR), "v"(1u), "n"(idx * 4) : "memory");
  SB();
}
DI u32 spin_at(u32 a, u32 val) {
  u32 t, c, m, cap;
  asm volatile("s_mov_b32 %3, %6\n 1: ds_read_b32 %0, %4\n s_waitcnt lgkmcnt(0)\n v_readfirstlane_b32 %1, %0\n s_and_b32 %2, %1, 255\n s_cmp_ge_u32 %2, %5\n s_cbranch_scc1 2f\n"
               "s_sub_u32 %3, %3, 1\n s_cbranch_scc0 1b\n 2:"
               : "=&v"(t), "=&s"(c), "=&s"(m), "=&s"(cap) : "v"(a), "s"(val), "n"(LSPIN_CAP) : "memory", "scc");
  return c;
}
DI void lds_spin(volatile LAS u32* p, u32 val) { spin_at((u32)(uintptr_t)p, val); }
enum { C_SOK = 0, C_BH = 4, C_PD = 12, C_H1 = 18, C_PAR = 19, C_VOTE = 20, C_FREE = 22, C_QRDY = 23, C_MXC = 25, C_SD = 26, C_GX = 27, C_ML = 28, C_MK = 32, C_P = 44 };

DI u32 kv_mark(int nm, int skip) { return L_CTR + 4 * C_MK + __builtin_amdgcn_readfirstlane(4 * nm + (nm >= skip ? 4 : 0)); }
#define KV_TS
#define KV_TSO
#define KV_TSA
#define KV_TSP
#define KV_TRY(l)                                                                                                        \
  "7: s_getreg_b32 %[h], hwreg(HW_REG_IB_STS)\n s_and_b32 %[h], %[h], 0xC0000F\n s_max_i32 %[h], %[h], %[x]\n s_sub_i32 %[h], %[h], %[x]\n s_cmp_gt_i32 %[h], %[k4]\n s_cbranch_scc1 " l "\n" \
  "s_waitcnt vmcnt(%[x4])\n s_cmp_eq_u32 %[k4], 4\n s_cbranch_scc1 8f\n s_waitcnt vmcnt(%[x])\n"                        \
  "8: v_subrev_u32 %[t], %[k4], %[ml]\n s_mov_b64 exec, 1\n ds_add_u32 %[t], %[one] offset:12\n s_mov_b64 exec, -1\n"   \
  "s_sub_i32 %[k4], %[k4], 4\n" KV_TS "s_branch 7b\n"
template <int X> DI void kv_try(int& k4, u32 ml, u32 one KV_TSA) {
  u32 t, h;
  asm volatile(KV_TRY("9f") "9:" : [t] "=&v"(t), [h] "=&s"(h), [k4] "+s"(k4) KV_TSO : [ml] "v"(ml), [one] "v"(one), [x] "n"(X), [x4] "n"(X + 4) : "memory", "scc");
}
template <int X> DI u32 spin_try(int idx, u32 val, int& k4, u32 ml, u32 one KV_TSA) {
  u32 t, h, c, cap;
  asm volatile("s_mov_b32 %[cap], %[n]\n 1: ds_read_b32 %[t], %[ml] offset:%[o]\n s_waitcnt lgkmcnt(0)\n v_readfirstlane_b32 %[c], %[t]\n s_and_b32 %[h], %[c], 255\n"
               "s_cmp_ge_u32 %[h], %[v]\n s_cbranch_scc1 2f\n" KV_TRY("9f") "9: s_sub_u32 %[cap], %[cap], 1\n s_cbranch_scc0 1b\n 2:"
               : [t] "=&v"(t), [h] "=&s"(h), [c] "=&s"(c), [cap] "=&s"(cap), [k4] "+s"(k4) KV_TSO
               : [ml] "v"(ml), [one] "v"(one), [o] "n"(4 * idx), [v] "n"(val), [n] "n"(LSPIN_CAP), [x] "n"(X), [x4] "n"(X + 4) : "memory", "scc");
  return c;
}
template <int W> DI void kv_load1(const u16* ckv, u32 vl, int lw, u32 g0, u32 g1, u32 m, int n0, int n1, int& nm, int skip) {
#pragma unroll 1
  for (int n = n0; n < n1; ++n) {
#define KV_POST(k) ".if (%[wm] | 4) == " #k "\n s_cmp_gt_i32 %[nm], %[n2]\n s_cbranch_scc1 20f\n s_mov_b64 exec, 1\n ds_add_u32 %[ma], %[one]\n s_mov_b64 exec, -1\n s_add_i32 %[nm], %[nm], 1\n 20:\n .endif\n"
#define KV_W(o, k) "s_waitcnt vmcnt(%[wm])\n" KV_POST(k) "global_load_lds_dwordx4 %[vo], %[p] offset:" #o " nt\n"
#define KV_M "s_add_u32 m0, m0, %[ms]\n"
    asm volatile("s_mov_b32 m0, %[m]\n" KV_W(0, 4) KV_M KV_W(256, 5) KV_M KV_W(512, 6) KV_M KV_W(768, 7)
                 : [nm] "+s"(nm)
                 : [m] "s"(m), [vo] "v"(vl + g0 + ((lw + n) & 3) * 4096), [p] "s"(ckv), [ma] "v"(kv_mark(nm, skip)),
                   [one] "v"(1u), [n2] "s"(__builtin_amdgcn_readfirstlane(n - (W > 4 ? 2 : 1))), [wm] "n"(W - 1), [ms] "n"(PS - 256)
                 : "memory", "scc");
    g0 = g1;
    g1 += 192 * 16384;
    m += n + 1 == skip ? 8192 : 4096;
  }
}
template <int W, int W0> DI void kv_load(const u16* ckv, u32 vl, int lw, u32 g0, u32 g1, u32 m, int n0, int n1, int& nm, int skip) {
  if (W != W0 && skip == 4) kv_load1<W0>(ckv, vl, lw, g0, g1, m, n0, n1, nm, skip); else kv_load1<W>(ckv, vl, lw, g0, g1, m, n0, n1, nm, skip);
}
DI void until(u32 t0, u32 dt) {
  while ((u32)__builtin_amdgcn_s_memrealtime() - t0 < dt) __builtin_amdgcn_s_sleep(2);
}
DI bf16x8 tr_frag(unsigned char* lds, u32 a0, u32 a1) {
  const s16x4 b0 = __builtin_amdgcn_ds_read_tr16_b64_v4i16((LAS s16x4*)(lds + a0));
  const s16x4 b1 = __builtin_amdgcn_ds_read_tr16_b64_v4i16((LAS s16x4*)(lds + a1));
  bf16x8 v;
  v[0] = b0[0]; v[1] = b0[1]; v[2] = b0[2]; v[3] = b0[3]; v[4] = b1[0]; v[5] = b1[1]; v[6] = b1[2]; v[7] = b1[3];
  return v;
}
DI float sum4(f32x4 v) { const f32x2 t = f32x2{v.x, v.y} + f32x2{v.z, v.w}; return t.x + t.y; }

__global__ __launch_bounds__(512, 2) void k_main(const u16* __restrict__ x, const u16* __restrict__ ckv,
                                                 const u16* __restrict__ w_q, const u16* __restrict__ w_uk,
                                                 const u16* __restrict__ w_uv, const u16* __restrict__ w_o,
                                                 const u16* __restrict__ w_gate, const u16* __restrict__ w_up,
                                                 const u16* __restrict__ w_down, const u16* __restrict__ cvec,
                                                 const u16* __restrict__ w_mod, u16* __restrict__ out,
                                                 unsigned char* ws) {
  __shared__ __attribute__((aligned(16))) unsigned char lds[163840];
  const int w = __builtin_amdgcn_readfirstlane(threadIdx.x >> 6), j = blockIdx.x;
  int lane = threadIdx.x & 63;
  const int xc = j & 7, ii = j >> 3;
  STAMP_DECL;
  STAMP(0);
  u32x2 ev;
  u32 xm, vh;
  {
    u64 p, pc, pw;
    u32 a, b, c, d, f, t;
    asm volatile("s_lshr_b32 %[f], %[j], 6\n s_cmp_eq_u32 %[f], 1\n s_cselect_b32 %[a], 0x50, 16\n"
                 "s_load_dwordx2 %[p], %[ka], %[a]\n s_load_dwordx2 %[pw], %[ka], 0x60\n"
                 "s_lshr_b32 %[d], %[j], 3\n s_cmp_lg_u32 %[w], 7\n s_cbranch_scc1 2f\n"
                 "s_and_b32 %[a], %[d], 15\n s_lshl_b32 %[a], %[a], 12\n v_lshl_add_u32 %[t], %[lane], 6, %[a]\n v_min_u32 %[t], %[pfl], %[t]\n"
                 "s_getpc_b64 %[pc]\n s_mov_b32 m0, %[dump]\n s_nop 0\n global_load_lds_dword %[t], %[pc]\n"
                 "2: s_load_dwordx2 %[pc], %[ka], 0x48\n s_and_b32 %[c], %[j], 7\n s_lshl_b32 %[c], %[c], 10\n s_cmp_eq_u32 %[f], 0\n s_cbranch_scc0 3f\n"
                 "s_and_b32 %[a], %[d], 1\n s_lshl1_add_u32 %[a], %[w], %[a]\n s_lshl_b32 %[a], %[a], 18\n s_add_u32 %[c], %[c], %[a]\n"
                 "s_lshr_b32 %[a], %[d], 1\n s_lshl_b32 %[a], %[a], 8\n s_add_u32 %[c], %[c], %[a]\n s_lshl_b32 m0, %[w], 14\n"
                 "v_lshrrev_b32 %[t], 4, %[lane]\n v_mul_u32_u24 %[t], 0x204, %[t]\n v_and_b32 %[vh], 15, %[lane]\n v_xor_b32 %[vh], %[vh], %[t]\n"
                 "v_lshl_add_u32 %[vh], %[vh], 4, %[c]\n v_xor_b32 %[t], 0x8010, %[vh]\n s_waitcnt lgkmcnt(0)\n"
                 "global_load_lds_dwordx4 %[vh], %[p]\n s_add_u32 m0, m0, 0x400\n s_nop 0\n"
                 "global_load_lds_dwordx4 %[t], %[p]\n s_add_u32 m0, m0, 0x400\n v_xor_b32 %[t], 0x10020, %[vh]\n"
                 "global_load_lds_dwordx4 %[t], %[p]\n s_add_u32 m0, m0, 0x400\n v_xor_b32 %[t], 0x18030, %[vh]\n"
                 "global_load_lds_dwordx4 %[t], %[p]\n s_branch 1f\n"
                 "3: s_lshl2_add_u32 %[d], %[d], %[w]\n s_sub_u32 %[d], %[d], 68\n s_lshl_b32 %[d], %[d], 16\n s_add_u32 %[d], %[d], %[c]\n"
                 "s_and_b32 %[a], %[j], 3\n s_lshl2_add_u32 %[b], %[a], %[w]\n s_mul_i32 %[b], %[b], 0x30000\n s_lshl_b32 %[a], %[a], 8\n"
                 "v_lshl_add_u32 %[xm], %[lane], 2, %[a]\n"
                 "s_and_b32 %[a], %[j], 60\n s_lshl_b32 %[a], %[a], 5\n s_add_u32 %[b], %[b], %[a]\n"
                 "s_lshl_b32 %[c], %[w], 12\n s_add_u32 %[c], %[c], %[mq]\n s_lshl_b32 %[a], %[w], 10\n s_add_u32 %[a], %[a], %[mh]\n"
                 "s_cmp_eq_u32 %[f], 1\n s_cselect_b32 %[d], %[b], %[d]\n s_cselect_b32 m0, %[c], %[a]\n s_cselect_b32 %[a], 48, 1\n s_cselect_b32 %[c], 0, 4\n"
                 "v_lshrrev_b32 %[t], 3, %[lane]\n v_mul_u32_u24 %[t], %[a], %[t]\n v_and_b32 %[vh], 7, %[lane]\n v_lshl_add_u32 %[vh], %[t], 3, %[vh]\n"
                 "v_lshl_add_u32 %[vh], %[vh], 4, %[d]\n"
                 "s_cselect_b32 %[b], 6, 1\n s_cselect_b32 %[a], 0x400, 0\n s_lshl_b32 %[b], %[b], 13\n"
                 "s_xor_b32 %[c], %[c], %[w]\n s_bitcmp1_b32 %[c], 2\n v_mov_b32 %[t], 0\n"
                 "s_waitcnt lgkmcnt(0)\n s_cbranch_scc1 1f\n s_cmp_eq_u32 %[f], 1\n s_cbranch_scc0 4f\n"
                 "global_load_dword %[xm], %[xm], %[pc]\n global_load_dwordx2 %[ev], %[t], %[pw] sc1\n"
                 "4: global_load_lds_dwordx4 %[vh], %[p]\n s_add_u32 m0, m0, %[a]\n v_add_u32 %[t], %[b], %[vh]\n"
                 "global_load_lds_dwordx4 %[t], %[p]\n s_add_u32 m0, m0, %[a]\n v_add_u32 %[t], %[b], %[t]\n"
                 "global_load_lds_dwordx4 %[t], %[p]\n s_add_u32 m0, m0, %[a]\n v_add_u32 %[t], %[b], %[t]\n"
                 "global_load_lds_dwordx4 %[t], %[p]\n"
                 "1:\n"
                 : [xm] "=&v"(xm), [ev] "=&v"(ev), [vh] "=&v"(vh), [t] "=&v"(t), [p] "=&s"(p), [pc] "=&s"(pc), [pw] "=&s"(pw), [a] "=&s"(a), [b] "=&s"(b), [c] "=&s"(c),
                   [d] "=&s"(d), [f] "=&s"(f)
                 : [j] "s"(j), [w] "s"(w), [lane] "v"(lane), [ka] "s"(__builtin_amdgcn_kernarg_segment_ptr()), [pfl] "n"(PF_LEN), [dump] "n"(L_DUMP),
                   [mq] "n"(L_QOFF + 8192), [mh] "n"(L_P)
                 : "memory", "scc");
  }
  SB();
  const u32 t0 = (u32)__builtin_amdgcn_s_memrealtime() - 35u;
  const bool isQ = j < 64, isS0 = j >= 64 && j < 128;
  const int hq = xc * 4 + (ii >> 1), dh = ii & 1, sj = j - 64;
  const int lw = w - 4, myh = xc >> 2;
  int col = lane & 15, g4 = lane >> 4, tq = col >> 2, tp = lane & 3;
  const rsrc_t rs = __builtin_amdgcn_make_buffer_rsrc((void*)ws, 0, WS_TOTAL, 0x00020000);
  volatile LAS u32* ctr = (volatile LAS u32*)(lds + L_CTR);
  SB();
  STAMP(25);
  if (w < 2) *(u32*)(lds + (w == 0 ? L_CTR + lane * 4 : L_MX + (lane & 31) * 4)) = w == 0 ? 0u : 0xFF800000u;
  BAR();
  const bool tl2 = isS0;
  const int t3 = xc < 4 ? (ii < 8 ? xc * 8 + ii : -1) : (ii < 6 ? 8 + xc * 6 + ii : -1);
  const int t4 = (ii >= 8 && ii < 10) ? xc * 2 + (ii - 8) : -1;
  const u32 owno = (w >> 1) * 32768 + 8192 + (w & 1) * 12288;
  const int ks = (xc & 3) * 2 + myh, cg = ii & 7;
  const int h = (j >> 2) & 31, c4 = j & 3;
  u32x4 wv[8];
  u32 tlo, thi;
  int gx;
#define RELANE() do { asm volatile("v_mbcnt_lo_u32_b32 %0, -1, 0\n v_mbcnt_hi_u32_b32 %0, -1, %0" : "=v"(lane)); col = lane & 15; g4 = lane >> 4; tq = col >> 2; tp = lane & 3; } while (0)
#define TAG_OF(e0, e1)                                                                                                  \
  do {                                                                                                                  \
    const u64 tag = (((u64)(e1) << 32) | (e0)) + 1ull;                                                                  \
    tlo = __builtin_amdgcn_readfirstlane((u32)tag); thi = __builtin_amdgcn_readfirstlane((u32)(tag >> 32));             \
  } while (0)
  const u32x4 P4 = {POISON, POISON, POISON, POISON};
#define COUNT_VOTES(PRE)                                                                                                \
  do {                                                                                                                  \
    u32x2 a_ = {0u, 0u};                                                                                                \
    if (PRE) a_ = *(const u32x2*)(lds + L_QI + lane * 8);                                                               \
    _Pragma("unroll 1") for (u32 spins = 0; spins < SPIN_CAP; ++spins) {                                                \
      if (((PRE) || spins) && __all(gran_ok(a_.x, a_.y, tlo, thi))) break;                                              \
      if (spins) __builtin_amdgcn_s_sleep(8);                                                                           \
      asm volatile("" ::: "memory");                                                                                    \
      a_ = ld64(rs, WS_VOTE + xc * 2048 + lane * 8);                                                                    \
    }                                                                                                                   \
    const float vt_ = wave_sum((float)(a_.x & 1023u));                                                                  \
    if (lane == 0) ctr[C_PAR] = G_OF(vt_);                                                                              \
    VOTE_STAMP(vt_);                                                                                                    \
    STAMP(8);                                                                                                           \
  } while (0)
#define S_POLL(a_)                                                                                                      \
  _Pragma("unroll 1") for (u32 spins = 0; spins < SPIN_CAP && RARE(__any(((a_.x ^ tlo) | (a_.y ^ thi)) != 0u)); ++spins) { \
    __builtin_amdgcn_s_sleep(8);                                                                                        \
    asm volatile("" ::: "memory");                                                                                      \
    a_ = ld64(rs, WS_SFLAG + xc * 2048 + lane * 8);                                                                     \
  }
#define MB_OFF(i) (WS_MODB + ((lane >> 4) * 2048 + ((i) >> 1) * 512 + 128 * lw + 64 * ((i) & 1) + (lane & 15) * 4) * 4)

  if (!isQ && w >= 4) {
    if (isS0) {
      if (w == 4) {
        const u32x2 e4 = ld64(rs, WS_EPOCH);
        st128(rs, WS_QABS + sj * 1024 + lane * 16, P4);
        st128(rs, lane < 32 ? WS_MODB + sj * 512 + lane * 16 : WS_TOTAL, P4);
        st128(rs, WS_PROBE + il(sj * 1024 + lane * 16, 0), Z4);
        st128(rs, WS_PROBE + il(sj * 1024 + lane * 16, 1), Z4);
        waitvm<0>();
        TAG_OF(e4.x, e4.y);
        if (lane < 8) st64(rs, WS_SFLAG + lane * 2048 + sj * 8, tlo, thi);
        STAMP(27);
      }
      lds_wait(ctr + C_H1, 4u);
    } else {
#pragma unroll
      for (int i = 4; i < 16; ++i) { waitvm<7>(); dma<false>(L_P + w * 1024, vh + (i & 7) * 8192, i < 8 ? w_q : w_uk); }
    }
    const u32 vl = g4 * 1024 + ((col ^ ((g4 << 2) | lw)) << 4);
    int nm = 0, ns = isS0 ? 6 : 7;
    const int skip = isS0 ? 4 : 99;
    kv_load<WIN, WA0>(ckv, vl, lw, (isS0 ? sj : 1792 + j) * 16384, (isS0 ? sj + 192 : sj) * 16384, lw * 1024, 0, 2, nm, skip);
    const rsrc_t rw = __builtin_amdgcn_make_buffer_rsrc((void*)(isS0 ? w_o : w_uv), 0, 4194304, 0x00020000);
    int wo = isS0 ? (ks * 512 + 64 * w + (lane >> 3)) * 1024 + cg * 128 + (lane & 7) * 16 : (c4 * 128 + 32 * lw + g4) * 8192 + h * 256 + col * 16;
    const int wst = isS0 ? 8192 : 32768;
    PIN(wo);
#define WV_LD(i) do { SB(); wv[i] = __builtin_amdgcn_raw_buffer_load_b128(rw, wo, (i) * wst, 2); wv[(i) + 1] = __builtin_amdgcn_raw_buffer_load_b128(rw, wo, ((i) + 1) * wst, 2); SB(); } while (0)
    u32 va[8];
#pragma unroll
    for (int i = 0; i < 8; ++i) { va[i] = lw * PS + tr_addr(lane, i >> 1, 0, i & 1); PIN(va[i]); }
    int eg[8];
#pragma unroll
    for (int u = 0; u < 8; ++u) { eg[u] = WS_PART + il21(((4 * (u >> 1) + 2 * (g4 >> 1) + (u & 1)) * 4 + lw) * 65536 + sj * 256 + col * 16, g4 & 1); PIN(eg[u]); }
    u32 pr_ = L_P + lane * 16;
    PIN(pr_);
    u32 ml = L_CTR + 4 * (C_MK + 7), one = 1u;
    PIN(ml); PIN(one);
    bf16x8 vf[4][4];
#define V_READ(i, slot) do { asm volatile("" ::: "memory"); SB(); _Pragma("unroll") for (int c = 0; c < 4; ++c) vf[i][c] = tr_frag(lds, va[2 * c] + (slot) * 4096, va[2 * c + 1] + (slot) * 4096); SB(); } while (0)
    SB();
    kv_load<WIN, WB0>(ckv, vl, lw, (sj + (isS0 ? 384 : 192)) * 16384, (sj + (isS0 ? 576 : 384)) * 16384, lw * 1024 + 8192, 2, ns, nm, skip);
    lds_wait(ctr + C_MK + 3, 4u);
    V_READ(0, 0); V_READ(1, 1); V_READ(2, 2); V_READ(3, 3);
    lds_add(C_FREE);
#pragma unroll
    for (int i = 0; i < 16; ++i) PIN(vf[i >> 2][i & 3]);
    STAMP(2);
    lds_wait(ctr + C_FREE, 8u);
    STAMP(5);
    kv_load<WIN, WB0>(ckv, vl, lw, (sj + 6 * 192) * 16384, (sj + 7 * 192) * 16384, lw * 1024, ns, ns + 4, nm, skip);
    int k4 = 4;
    STAMP(3);
    f32x16 acc[4];
#pragma unroll
    for (int c = 0; c < 4; ++c)
#pragma unroll
      for (int i = 0; i < 16; ++i) acc[c][i] = 0.f;
    bf16x8 pf[4], pn[4];
    u32 c_;
#define P_SPIN(g, n) c_ = spin_try<(g) ? 8 : 0>(C_P + (g) - C_MK - 7, n, k4, ml, one KV_TSP)
#define TRY() kv_try<8>(k4, ml, one KV_TSP)
#define P_READ(d, i0, i1, T0) _Pragma("unroll") for (int i = (i0); i < (i1); ++i) d[i] = *(volatile LAS bf16x8*)(lds + pr_ + ((T0) + i) * 1024)
#define PV(p, i) do { SB(); _Pragma("unroll") for (int c = 0; c < 4; ++c) acc[c] = __builtin_amdgcn_mfma_f32_32x32x16_bf16(p[i], vf[i][c], acc[c], 0, 0, 0); SB(); } while (0)
#define GX_USE(v) do { gx = __builtin_amdgcn_readfirstlane(v); _Pragma("unroll") for (int u = 0; u < 8; ++u) { eg[u] ^= gx; PIN(eg[u]); } } while (0)
#define MK_WAIT(T) spin_try<8>((T) - 7, 4u, k4, ml, one KV_TSP)
#define PVV(p, i, d, slot)                                                                                               \
  _Pragma("unroll") for (int c = 0; c < 4; ++c) {                                                                        \
    SB();                                                                                                                \
    acc[c] = __builtin_amdgcn_mfma_f32_32x32x16_bf16(p[i], vf[i][c], acc[c], 0, 0, 0);                                   \
    SB();                                                                                                                \
    vf[d][c] = tr_frag(lds, va[2 * c] + (slot) * 4096, va[2 * c + 1] + (slot) * 4096);                                   \
    SB();                                                                                                                \
  }
#define CT(i) (*(volatile LAS u32*)(lds + ml + 4 * ((i) - C_MK - 7)))
    P_SPIN(0, 4u);
    P_READ(pf, 0, 4, 0);
    const u32 k7 = CT(C_MK + 7);
    asm volatile("" ::: "memory");
    STAMP(6);
    PVV(pf, 0, 0, 4); kv_try<0>(k4, ml, one KV_TSP); WV_LD(0);
    PVV(pf, 1, 1, 5); kv_try<2>(k4, ml, one KV_TSP); WV_LD(2);
    PVV(pf, 2, 2, 6); WV_LD(4);
    u32 cv = CT(C_P + 1);
    P_READ(pn, 0, 4, 4);
    PVV(pf, 3, 3, 0); WV_LD(6);
    if (RARE(__builtin_amdgcn_readfirstlane(k7) < 4u)) { MK_WAIT(7); V_READ(0, 4); V_READ(1, 5); V_READ(2, 6); V_READ(3, 0); }
    STAMP(9);
    c_ = __builtin_amdgcn_readfirstlane(cv);
    if (RARE((c_ & 255u) != 4u)) { P_SPIN(1, 4u); P_READ(pn, 0, 4, 4); }
    int slow = -1;
    if (RARE(c_ > 255u)) slow = 4;
    else {
      if (!isS0) PV(pn, 0);
      TRY();
      const u32 k9 = CT(C_MK + 9);
      asm volatile("" ::: "memory");
      PVV(pn, 1, 0, 1); TRY();
      PVV(pn, 2, 1, 2);
      cv = CT(C_P + 2);
      P_READ(pf, 0, 2, 8);
      const u32 k10 = CT(C_MK + 10);
      asm volatile("" ::: "memory");
      PVV(pn, 3, 2, 3);
      if (RARE(__builtin_amdgcn_readfirstlane(k9) < 4u)) { MK_WAIT(9); V_READ(0, 1); V_READ(1, 2); }
      STAMP(10);
      c_ = __builtin_amdgcn_readfirstlane(cv);
      if (RARE((c_ & 255u) != 4u)) { P_SPIN(2, 4u); P_READ(pf, 0, 2, 8); }
      if (RARE(c_ > 255u)) slow = 8;
      else {
        TRY();
        PV(pf, 0);
        const u32 g0 = ctr[C_GX];
        u32 g1 = ctr[C_PAR];
        cv = CT(C_P + 3);
        P_READ(pf, 2, 3, 8);
        PV(pf, 1);
        STAMP(4);
        if (RARE(__builtin_amdgcn_readfirstlane(k10) < 4u)) { waitvm<8>(); TRY(); lds_spin(ctr + C_MK + 10, 4u); V_READ(2, 3); }
        if (RARE(__builtin_amdgcn_readfirstlane(g0) == 0u)) { lds_spin(ctr + C_GX, 1u); g1 = ctr[C_PAR]; }
        GX_USE(g1);
        STAMP(11);
        c_ = __builtin_amdgcn_readfirstlane(cv);
        _Pragma("unroll 1") for (u32 n_ = 0; RARE((c_ & 255u) != 2u) && n_ < LSPIN_CAP; ++n_) { cv = CT(C_P + 3); P_READ(pf, 2, 3, 8); c_ = __builtin_amdgcn_readfirstlane(cv); }
        if (RARE(c_ > 255u)) slow = 10;
        else PV(pf, 2);
      }
    }
    if (RARE(slow >= 0)) {
      MK_WAIT(10);
      float R = ceilf(*(const float*)(lds + L_MX + (lane & 31) * 4));
#pragma unroll 1
      for (int T = slow; T < 11; ++T) {
        if (T == 4 && isS0) continue;
        spin_at(L_CTR + 4 * (C_P + (T < 8 ? 1 : T < 10 ? 2 : 3)), T < 10 ? 4u : 2u);
        const float rf = *(const float*)(lds + L_REF + T * 128 + (lane & 31) * 4);
        u32x4 p = *(const u32x4*)(lds + pr_ + T * 1024);
        const u32 so_ = (T < 7 ? T : T - 7) * 4096;
#pragma unroll
        for (int c = 0; c < 4; ++c) vf[0][c] = tr_frag(lds, va[2 * c] + so_, va[2 * c + 1] + so_);
        const float Rn = fmaxf(R, rf), ps = __builtin_amdgcn_exp2f(rf - Rn);
        *(float*)(lds + L_ALW + lw * 128 + (lane & 31) * 4) = __builtin_amdgcn_exp2f(R - Rn);
        R = Rn;
        WAVE_LDS_SYNC();
#pragma unroll
        for (int i = 0; i < 4; ++i) {
          const f32x4 al = *(const f32x4*)(lds + L_ALW + lw * 128 + (lane >> 5) * 16 + i * 32);
#pragma unroll
          for (int c = 0; c < 4; ++c)
#pragma unroll
            for (int r = 0; r < 4; ++r) acc[c][4 * i + r] *= al[r];
          p[i] = pk2bf(lo16(p[i]) * ps, hi16(p[i]) * ps);
        }
        WAVE_LDS_SYNC();
        pf[0] = __builtin_bit_cast(bf16x8, p);
        PV(pf, 0);
      }
      if (slow != 10) { lds_spin(ctr + C_GX, 1u); GX_USE(ctr[C_PAR]); }
    }
    STAMP(30);
#pragma unroll
    for (int u = 0; u < 8; u += 2) {
      u32 c[8];
#pragma unroll
      for (int k = 0; k < 8; ++k) c[k] = pk2bf(acc[k & 2][2 * u + (k & 1) + (k >> 2) * 2], acc[(k & 2) + 1][2 * u + (k & 1) + (k >> 2) * 2]);
      SB();
#pragma unroll
      for (int k = 0; k < 2; ++k) {
        const auto r0 = __builtin_amdgcn_permlane16_swap(c[4 * k], c[4 * k + 1], false, false);
        const auto r1 = __builtin_amdgcn_permlane16_swap(c[4 * k + 2], c[4 * k + 3], false, false);
        st128(rs, eg[u + k], u32x4{r0[0], r1[0], r0[1], r1[1]});
      }
      SB();
    }
    STAMP(31);
    if (!isS0) {
#pragma unroll
      for (int i = 0; i < 8; ++i) *(u32x4*)(lds + lw * PS + 16384 + off_b(4 * i + g4, col)) = wv[i];
    }
    {
      int fo = lane < 16 ? (WS_LSE + il(sj * 512 + lw * 128 + (lane & 7) * 16, lane >> 3)) ^ gx : WS_TOTAL;
      lds_spin(ctr + C_ML, 1u);
      u32x4 ml4 = *(const u32x4*)(lds + L_WMAX + col * 16);
      PIN(fo); PIN(ml4);
      waitvm<0>();
      __builtin_amdgcn_s_waitcnt(0x0F70);
      st128(rs, fo, ml4);
      lds_add(C_PD);
    }
    STAMP(13);
  } else if (!isQ) {
    if (isS0) {
      u32 ta = L_QOFF + 8192 + (8 * g4 + tq) * 128 + w * 32 + tp * 8;
      u32 sa = L_H1RED + w * 256;
      int so = col < 2 ? WS_SC + (((sj >> 5) * 4 + (sj & 3)) * 512 + ((sj >> 2) & 7) * 64 + 16 * w + 4 * g4 + 2 * col) * 8 : WS_TOTAL;
      PIN(ta); PIN(so); PIN(sa);
      u32 c2, e0, e1;
      asm volatile("s_waitcnt vmcnt(4) ; HEAD_WAIT\n v_mov_b32 %0, %3\n v_mov_b32 %1, %4\n v_mov_b32 %2, %5" : "=&v"(c2), "=&v"(e0), "=&v"(e1) : "v"(xm), "v"(ev.x), "v"(ev.y) : "memory");
      TAG_OF(e0, e1);
      bf16x8 sf[4];
      u32x2 wt[8];
      asm volatile("ds_write_b32 %4, %5\n"
                   "ds_read_b128 %0, %6\n ds_read_b128 %1, %6 offset:64\n ds_read_b128 %2, %6 offset:128\n ds_read_b128 %3, %6 offset:192\n s_waitcnt lgkmcnt(0)"
                   : "=&v"(sf[0]), "=&v"(sf[1]), "=&v"(sf[2]), "=&v"(sf[3])
                   : "v"(sa + lane * 4), "v"(pk2bf(silu_bf(lo16(c2)), silu_bf(hi16(c2)))), "v"(sa + 16 * g4) : "memory");
      waitvm<0>();
      lds_add(C_BH);
      lds_spin(ctr + C_BH, 4u);
      STAMP(1);
      asm volatile("ds_read_b64_tr_b16 %0, %8\n ds_read_b64_tr_b16 %1, %8 offset:512\n ds_read_b64_tr_b16 %2, %8 offset:4096\n ds_read_b64_tr_b16 %3, %8 offset:4608\n"
                   "ds_read_b64_tr_b16 %4, %8 offset:8192\n ds_read_b64_tr_b16 %5, %8 offset:8704\n ds_read_b64_tr_b16 %6, %8 offset:12288\n ds_read_b64_tr_b16 %7, %8 offset:12800\n"
                   "s_waitcnt lgkmcnt(0)"
                   : "=&v"(wt[0]), "=&v"(wt[1]), "=&v"(wt[2]), "=&v"(wt[3]), "=&v"(wt[4]), "=&v"(wt[5]), "=&v"(wt[6]), "=&v"(wt[7]) : "v"(ta) : "memory");
      f32x4 d = {0.f, 0.f, 0.f, 0.f};
#pragma unroll
      for (int k = 0; k < 4; ++k)
        d = __builtin_amdgcn_mfma_f32_16x16x32_bf16(__builtin_bit_cast(bf16x8, u32x4{wt[2 * k].x, wt[2 * k].y, wt[2 * k + 1].x, wt[2 * k + 1].y}), sf[k], d, 0, 0, 0);
      st_gran2(rs, so, (col & 1) ? d[2] : d[0], (col & 1) ? d[3] : d[1], tlo, thi);
      lds_add(C_H1);
      STAMP(2);
    }
    u32 ab = off_b(col, 4 * g4), qv = WS_QABS + myh * 8192 + g4 * 1024 + w * 256 + ((col ^ (g4 << 2)) << 4);
    const int hb = 16 * (w & 1) + col;
    u32 pw = L_P + (g4 >> 1) * 512 + hb * 16 + (g4 & 1) * 8, mw = hb * 4;
    PIN(ab); PIN(qv); PIN(pw); PIN(mw);
    if (w == 2) {
      until(t0, QF_T);
      const u32x2 e2 = ld64(rs, WS_EPOCH);
      u32x2 f2 = ld64(rs, WS_SFLAG + xc * 2048 + lane * 8);
      TAG_OF(e2.x, e2.y);
      S_POLL(f2);
      lds_add(C_SOK);
      STAMP(3);
    }
    bf16x8 af[16], qf[16][2];
    u32 aa[16], qa[32];
#pragma unroll
    for (int i = 0; i < 16; ++i) {
      aa[i] = (ab ^ ((i & 3) << 4)) + (((w + (i >> 2)) & 3) * PS + w * 4096); PIN(aa[i]);
#pragma unroll
      for (int u = 0; u < 2; ++u) { qa[2 * i + u] = (ab ^ ((i & 3) << 4)) + (L_QI + ((w + (i >> 2)) & 3) * 8192 + ((u ^ w) & 1) * 4096); PIN(qa[2 * i + u]); }
    }
    const int sl1 = w == 3 ? 0 : 4 + w, sl2 = 1 + (w >> 1);
#define A_RD(i, o) af[i] = *(const bf16x8*)(lds + aa[i] + (o))
#define A_READ(i0, i1, o) _Pragma("unroll") for (int i = (i0); i < (i1); ++i) A_RD(i, o)
#define A_MOVE(i0, i1, d) _Pragma("unroll") for (int i = (i0); i < (i1); ++i) { aa[i] += (d) * 4096; PIN(aa[i]); }
#define Q_RD(i, u) qf[i][u] = *(const bf16x8*)(lds + qa[2 * (i) + (u)])
#define A_FIRST() do { A_READ(0, 16, 0); lds_add(C_FREE); _Pragma("unroll") for (int i = 0; i < 16; ++i) PIN(af[i]); STAMP(12); } while (0)
    bool have = false;
    _Pragma("unroll 1") while ((u32)__builtin_amdgcn_s_memrealtime() - t0 < E3_T) {
      if (__builtin_amdgcn_readfirstlane(ctr[C_MK + w]) >= 4u) { have = true; break; }
      __builtin_amdgcn_s_sleep(1);
    }
    asm volatile("" ::: "memory");
    if (have) A_FIRST();
    lds_wait(ctr + C_SOK, 1u);
    if (isS0) lds_wait(ctr + C_H1, 4u);
    until(t0, E3_T);
    STAMP(6);
#define Q_DMA() _Pragma("unroll") for (int i = 0; i < 8; ++i) dma_sc1(L_QI + w * 8192 + i * 1024, (qv ^ ((i & 3) << 4)) + (i >> 1) * 16384 + (i & 1) * 4096, ws)
    Q_DMA();
    if (RARE(!have)) { lds_wait(ctr + C_MK + w, 4u); A_FIRST(); }
    A_MOVE(0, 16, sl1 - w);
    u32 cb = L_CTR, cw = L_CTR + 4 * w, mx0 = mw + L_MX, mx1 = (mw ^ 64) + L_MX, pw0 = pw + w * 1024, pw1 = (pw ^ 256) + w * 1024;
    float ninf = -__builtin_inff();
    u32 one = 1u;
    PIN(ninf); PIN(one); PIN(cb); PIN(cw); PIN(mx0); PIN(mx1); PIN(pw0); PIN(pw1);
#define CTS(b, i) (*(volatile LAS u32*)(lds + (b) + 4 * (i)))
    _Pragma("unroll 1") for (u32 spins = 0; spins < SPIN_CAP; ++spins) {
      waitvm<0>();
#pragma unroll
      for (int i = 0; i < 8; ++i) Q_RD(i >> 1, i & 1);
      u32 mxu[2] = {0u, 0u};
#pragma unroll
      for (int i = 0; i < 8; ++i) { const u32x4 t_ = __builtin_bit_cast(u32x4, qf[i >> 1][i & 1]); mxu[i & 1] = max(max(mxu[i & 1], t_.x), t_.y); mxu[i & 1] = max(max(mxu[i & 1], t_.z), t_.w); }
      if (!RARE(__any(max(mxu[0], mxu[1]) == POISON))) break;
      STAMP(29);
      __builtin_amdgcn_s_sleep(2);
      const u64 b0 = __ballot(mxu[w & 1] == POISON), b1 = __ballot(mxu[~w & 1] == POISON);
#pragma unroll 1
      for (int i = 0; i < 8; ++i)
        if (((i < 4 ? b0 : b1) >> (4 * (i & 3))) & 0x000F000F000F000Full) dma_sc1(L_QI + w * 8192 + i * 1024, (qv ^ ((i & 3) << 4)) + (i >> 1) * 16384 + (i & 1) * 4096, ws);
    }
    lds_add(C_QRDY);
    STAMP(7);
    f32x4 sc[2], sn[2];
    sc[0] = sc[1] = sn[0] = sn[1] = f32x4{0.f, 0.f, 0.f, 0.f};
#define QK(d, i0, i1, NU)                                                                                                \
  do {                                                                                                                   \
    SB();                                                                                                                \
    _Pragma("unroll") for (int i = (i0); i < (i1); ++i) _Pragma("unroll") for (int u = 0; u < (NU); ++u)                 \
      d[u] = __builtin_amdgcn_mfma_f32_16x16x32_bf16(af[i], qf[i][u], d[u], 0, 0, 0);                                    \
    SB();                                                                                                                \
  } while (0)
#define MF(d, i, u) do { SB(); d[u] = __builtin_amdgcn_mfma_f32_16x16x32_bf16(af[i], qf[i][u], d[u], 0, 0, 0); SB(); } while (0)
    const bool idle1 = isS0 && w == 0;
    u32 c1;
    {
      const u32 c0 = CTS(cb, C_QRDY);
      QK(sc, 0, 2, 2);
      if (RARE(__builtin_amdgcn_readfirstlane(c0) < 4u)) lds_spin(ctr + C_QRDY, 4u);
      asm volatile("" ::: "memory");
#pragma unroll
      for (int i = 2; i < 4; ++i) { MF(sc, i, 0); Q_RD(2 * i, 0); Q_RD(2 * i, 1); MF(sc, i, 1); Q_RD(2 * i + 1, 0); Q_RD(2 * i + 1, 1); }
#pragma unroll
      for (int i = 4; i < 12; ++i) { MF(sc, i, 0); Q_RD(i + 4, 0); MF(sc, i, 1); Q_RD(i + 4, 1); }
      c1 = CTS(cw, C_MK + 4);
      asm volatile("" ::: "memory");
#pragma unroll
      for (int i = 12; i < 16; ++i) { MF(sc, i, 0); A_RD(2 * i - 24, 0); MF(sc, i, 1); A_RD(2 * i - 23, 0); }
    }
    STAMP(9);
    float mref[2], lsum[2] = {0.f, 0.f};
#define ADDC(i, v) do { SB(); asm volatile("s_mov_b64 exec, 1\n ds_add_u32 %0, %1 offset:%2\n s_mov_b64 exec, -1" ::"v"(cb), "v"(v), "n"(4 * (i)) : "memory"); SB(); } while (0)
#define SM(d, T, NU, G, P0, P1)                                                                                          \
  do {                                                                                                                   \
    SB();                                                                                                                \
    if (RARE(__any(max5(d[0], (NU) > 1 ? max5(d[1], ninf) : ninf) > TAU))) {                                             \
      sig = 257u;                                                                                                        \
      _Pragma("unroll") for (int u = 0; u < (NU); ++u) {                                                                 \
        const float up = ceilf(x32_max(x16_max(max5(d[u], 0.f))));                                                       \
        lsum[u] *= __builtin_amdgcn_exp2f(-up);                                                                          \
        mref[u] += up;                                                                                                   \
        d[u] -= up;                                                                                                      \
        *(float*)(lds + (mw ^ (u * 64)) + L_REF + (T) * 128) = mref[u];                                                  \
      }                                                                                                                  \
    }                                                                                                                    \
    u32x2 pk[2];                                                                                                         \
    _Pragma("unroll") for (int u = 0; u < (NU); ++u) {                                                                   \
      pk[u].x = pk2bf(__builtin_amdgcn_exp2f(d[u].x), __builtin_amdgcn_exp2f(d[u].y));                                   \
      pk[u].y = pk2bf(__builtin_amdgcn_exp2f(d[u].z), __builtin_amdgcn_exp2f(d[u].w));                                   \
      *(u32x2*)(lds + (u ? (P1) : (P0))) = pk[u];                                                                        \
    }                                                                                                                    \
    ADDC(C_P + (G), sig);                                                                                                \
    _Pragma("unroll") for (int u = 0; u < (NU); ++u) { lsum[u] = dot2(pk[u].x, 0x3F803F80u, dot2(pk[u].y, 0x3F803F80u, lsum[u])); PIN(lsum[u]); } \
    SB();                                                                                                                \
  } while (0)
#define REF_WRITE(T, NU) _Pragma("unroll") for (int u = 0; u < (NU); ++u) *(float*)(lds + (mw ^ (u * 64)) + L_REF + (T) * 128) = mref[u]
    u32 sig = one;
    {
      MF(sn, 0, 0); A_RD(8, 0); MF(sn, 0, 1);
      const float m0 = max5(sc[0], ninf), m1 = max5(sc[1], ninf);
      MF(sn, 1, 0);
      asm volatile("ds_max_f32 %0, %2\n ds_max_f32 %1, %3" ::"v"(mx0), "v"(mx1), "v"(m0), "v"(m1) : "memory");
      ADDC(C_MXC, one);
      MF(sn, 1, 1); A_RD(9, 0);
#pragma unroll
      for (int i = 2; i < 4; ++i) { MF(sn, i, 0); A_RD(2 * i + 6, 0); MF(sn, i, 1); A_RD(2 * i + 7, 0); }
      MF(sn, 4, 0);
      const u32 c0 = CTS(cb, C_MXC);
      mref[0] = *(volatile LAS float*)(lds + mx0);
      mref[1] = *(volatile LAS float*)(lds + mx1);
      MF(sn, 4, 1); MF(sn, 5, 0); A_RD(14, 0); MF(sn, 5, 1); A_RD(15, 0); MF(sn, 6, 0); MF(sn, 6, 1);
      if (RARE(__builtin_amdgcn_readfirstlane(c0) < 4u)) {
        lds_spin(ctr + C_MXC, 4u);
        mref[0] = *(volatile LAS float*)(lds + mx0);
        mref[1] = *(volatile LAS float*)(lds + mx1);
      }
      u32x2 pk[2];
#pragma unroll
      for (int u = 0; u < 2; ++u) {
        MF(sn, 7 + 2 * u, 0);
        mref[u] = ceilf(mref[u]);
        float e0 = __builtin_amdgcn_exp2f(sc[u].x - mref[u]); PIN(e0);
        MF(sn, 7 + 2 * u, 1);
        pk[u].x = pk2bf(e0, __builtin_amdgcn_exp2f(sc[u].y - mref[u])); PIN(pk[u].x);
        MF(sn, 8 + 2 * u, 0);
        e0 = __builtin_amdgcn_exp2f(sc[u].z - mref[u]); PIN(e0);
        MF(sn, 8 + 2 * u, 1);
        pk[u].y = pk2bf(e0, __builtin_amdgcn_exp2f(sc[u].w - mref[u]));
        *(u32x2*)(lds + (u ? pw1 : pw0)) = pk[u];
      }
      ADDC(C_P, one);
      MF(sn, 11, 0);
      lsum[0] = dot2(pk[0].x, 0x3F803F80u, dot2(pk[0].y, 0x3F803F80u, 0.f)); PIN(lsum[0]);
      MF(sn, 11, 1);
      lsum[1] = dot2(pk[1].x, 0x3F803F80u, dot2(pk[1].y, 0x3F803F80u, 0.f)); PIN(lsum[1]);
      MF(sn, 12, 0);
      REF_WRITE(4 + w, 2);
      MF(sn, 12, 1);
      QK(sn, 13, 16, 2);
    }
    STAMP(10);
#define QK1(N0, slot)                                                                                                    \
  do {                                                                                                                   \
    _Pragma("unroll") for (int i = 0; i < 16; ++i) { MF(sc, i, 0); if (i + (N0) < 16) A_RD(i + (N0), slot); }            \
  } while (0)
    if (RARE(__builtin_amdgcn_readfirstlane(c1) < 4u && !idle1)) {
      lds_spin(ctr + C_MK + 4 + w, 4u);
      A_READ(0, 16, 0);
      sn[0] = sn[1] = f32x4{0.f, 0.f, 0.f, 0.f};
      QK(sn, 0, 16, 2);
    }
    if (RARE(idle1)) lds_add(C_P + 1);
    else { sn[0] -= mref[0]; sn[1] -= mref[1]; SM(sn, 4 + w, 2, 1, pw0 + 4096, pw1 + 4096); }
    if (RARE(w == 2)) {
      RELANE();
      dma_sc1(L_QI, WS_VOTE + xc * 2048 + lane * 16, ws);
    }
    REF_WRITE(7 + sl2, 1);
    A_MOVE(0, 16, sl2 - sl1);
    pw0 = pw + sl2 * 1024;
    sc[0] = f32x4{-mref[0], -mref[0], -mref[0], -mref[0]};
    PIN(pw0); PIN(sc[0]);
    lds_spin(ctr + C_MK + 7 + sl2, 4u);
    A_READ(0, 6, 0);
    STAMP(4);
    QK1(6, 0);
    SM(sc, 7 + sl2, 1, 2, pw0 + 7168, 0);
    STAMP(11);
    if (!RARE(w >= 2)) {
      REF_WRITE(10, 1);
      sc[0] = f32x4{-mref[0], -mref[0], -mref[0], -mref[0]};
      PIN(sc[0]);
      _Pragma("unroll 1") for (u32 n_ = 0; n_ < LSPIN_CAP; ++n_) {
        const u32 k_ = CTS(cb, C_MK + 10);
        asm volatile("" ::: "memory");
        A_READ(0, 6, 8192);
        SB();
        if (__builtin_amdgcn_readfirstlane(k_) >= 4u) break;
      }
      STAMP(5);
      QK1(6, 8192);
      SM(sc, 10, 1, 3, pw0 + 9216, 0);
    } else if (w == 2) {
      RELANE();
      waitvm<0>();
      COUNT_VOTES(true);
      lds_add(C_GX);
    }
    STAMP(30);
    RELANE();
    {
#pragma unroll
      for (int u = 0; u < 2; ++u) {
        *(float*)(lds + L_ML + (mw ^ (u * 64)) * 4 + w * 4) = mref[u];
        *(float*)(lds + L_ML + 512 + (mw ^ (u * 64)) * 4 + w * 4) = x32_sum(x16_sum(lsum[u]));
      }
      u32 old;
      f32x4 m4, l4;
      asm volatile("s_mov_b64 exec, 1\n ds_add_rtn_u32 %0, %3, %4 offset:%6\n s_mov_b64 exec, -1\n ds_read_b128 %1, %5\n ds_read_b128 %2, %5 offset:512\n s_waitcnt lgkmcnt(0)"
                   : "=&v"(old), "=&v"(m4), "=&v"(l4) : "v"((u32)L_CTR), "v"(1u), "v"(L_ML + (lane & 31) * 16), "n"(4 * C_SD) : "memory");
      if (__builtin_amdgcn_readfirstlane(old) == 3u) {
        const float M = fmaxf(fmaxf(m4.x, m4.y), fmaxf(m4.z, m4.w));
        const float l = (l4.x * __builtin_amdgcn_exp2f(m4.x - M) + l4.y * __builtin_amdgcn_exp2f(m4.y - M)) + (l4.z * __builtin_amdgcn_exp2f(m4.z - M) + l4.w * __builtin_amdgcn_exp2f(m4.w - M));
        *(u32x2*)(lds + L_WMAX + (lane & 1) * 128 + (lane & 30) * 4) = u32x2{fbits(M), fbits(l)};
        lds_add(C_ML);
      }
    }
    lds_wait(ctr + C_GX, 1u);
    gx = __builtin_amdgcn_readfirstlane(ctr[C_PAR]);
    STAMP(13);
  } else {
    const int mu = 2 * j + (w >> 2);
#define Q_LOAD(i0, i1)                                                                                                   \
  _Pragma("unroll") for (int i = (i0); i < (i1); ++i) {                                                                  \
    waitvm<QW - 1>();                                                                                                    \
    dma<false>(w * 16384 + (i & 15) * 1024, (vq ^ ((i & 3) << 4)) + ((i < 8 ? 64 * w + 32 * dh : i < 16 ? 64 * w + 32 - 32 * dh : 256 * dh + 32 * w) + 4 * (i & 7)) * 8192, i < 16 ? w_q : w_uk); \
  }
    const u32 vq = g4 * 8192 + hq * 256 + ((col ^ (g4 << 2)) << 4);
    u32x4 xv;
    u32x2 e2;
    u32 x2_;
    asm volatile("global_load_dwordx4 %0, %3, %5\n global_load_dword %1, %4, %5\n buffer_load_dwordx2 %2, off, %6, 0 sc1"
                 : "=&v"(xv), "=&v"(x2_), "=&v"(e2) : "v"(lane * 16), "v"(128 * w + 4 * (lane & 31)), "s"(x), "s"(rs) : "memory");
    bf16x8 wf[16], wk[8];
    u32 ta0 = w * 16384 + off_b(8 * g4 + tq, tp >> 1) + 8 * (tp & 1), ta1 = w * 16384 + off_b(8 * g4 + 4 + tq, tp >> 1) + 8 * (tp & 1);
    int so = WS_SC + ((lane >> 5) * 2048 + 64 * w + 2 * (lane & 31)) * 8;
    int fo = w == 0 ? WS_SFLAG + xc * 2048 + lane * 8 : WS_TOTAL;
    PIN(ta0); PIN(ta1); PIN(so); PIN(fo);
    Q_LOAD(4, 8 + QW);
    waitvm<QW>();
#pragma unroll
    for (int c = 0; c < 8; ++c) wf[c] = tr_frag(lds, ta0 ^ (c << 5), ta1 ^ (c << 5));
    Q_LOAD(8 + QW, 16);
#pragma unroll
    for (int c = 0; c < 8; ++c) PIN(wf[c]);
    Q_LOAD(16, 24);
    STAMP(12);
    until(t0, E1_T);
    STAMP(23);
    u32x4 g[4];
    asm volatile("buffer_load_dwordx4 %0, %4, %5, 0 offen sc1\n buffer_load_dwordx4 %1, %4, %5, %6 offen sc1\n"
                 "buffer_load_dwordx4 %2, %4, %5, %7 offen sc1\n buffer_load_dwordx4 %3, %4, %5, %8 offen sc1"
                 : "=&v"(g[0]), "=&v"(g[1]), "=&v"(g[2]), "=&v"(g[3]) : "v"(so), "s"(rs), "s"(4096), "s"(8192), "s"(12288) : "memory");
    u32x2 fl;
    asm volatile("buffer_load_dwordx2 %0, %1, %2, 0 offen sc1" : "=&v"(fl) : "v"(fo), "s"(rs) : "memory");
    asm volatile("s_waitcnt vmcnt(13)" : "+v"(xv), "+v"(x2_), "+v"(e2)::"memory");
#pragma unroll
    for (int c = 0; c < 8; ++c) wf[8 + c] = tr_frag(lds, (ta0 ^ (c << 5)) + 8192, (ta1 ^ (c << 5)) + 8192);
    TAG_OF(e2.x, e2.y);
    float ln0, ln1;
    {
      float xe[8] = {lo16(xv.x), hi16(xv.x), lo16(xv.y), hi16(xv.y), lo16(xv.z), hi16(xv.z), lo16(xv.w), hi16(xv.w)};
      float s = 0.f;
#pragma unroll
      for (int k = 0; k < 8; ++k) s += xe[k];
      const float mean = wave_sum(s) * (1.f / 512.f);
      float d2 = 0.f;
#pragma unroll
      for (int k = 0; k < 8; ++k) d2 += (xe[k] - mean) * (xe[k] - mean);
      const float var = rbf(wave_sum(d2) * (1.f / 512.f));
      const float mb = rbf(mean);
      const float sd = rbf(sqrtf(rbf(var + 1e-5f)));
      ln0 = rbf(rbf(lo16(x2_) - mb) / sd);
      ln1 = rbf(rbf(hi16(x2_) - mb) / sd);
    }
    PIN(ln0); PIN(ln1);
    int qpo = (lane & 7) == 0 ? WS_QABS + il(hq * 1024 + dh * 512 + w * 64 + (lane >> 5) * 32 + (lane & 8) * 2, g4 & 1) : WS_TOTAL;
    u32 hw_ = L_H1RED + w * 128 + (lane & 31) * 4, hr_ = L_H1RED + w * 128 + 16 * g4 + 64 * dh, qw_ = L_H1RED + 1024 + w * 272 + lane * 2, qr_ = L_H1RED + 1024 + col * 272 + 64 * g4;
    PIN(qpo); PIN(hw_); PIN(hr_); PIN(qw_); PIN(qr_);
    waitvm<5>();
#pragma unroll
    for (int i = 0; i < 8; ++i) wk[i] = *(const bf16x8*)(lds + w * 16384 + (i >> 2) * 4096 + off_b(col, 4 * g4 + (i & 3)));
#pragma unroll
    for (int c = 0; c < 8; ++c) { PIN(wf[8 + c]); PIN(wk[c]); }
    STAMP(4);
    asm volatile("s_waitcnt vmcnt(0)" : "+v"(g[0]), "+v"(g[1]), "+v"(g[2]), "+v"(g[3]), "+v"(fl)::"memory");
    _Pragma("unroll 1") for (u32 spins = 0; spins < SPIN_CAP; ++spins) {
      bool ok = true;
#pragma unroll
      for (int k = 0; k < 4; ++k) ok = ok & (gran_ok(g[k].x, g[k].y, tlo, thi) & gran_ok(g[k].z, g[k].w, tlo, thi));
      if (!RARE(!__all(ok))) break;
      STAMP(29);
      __builtin_amdgcn_s_sleep(1);
      asm volatile("" ::: "memory");
#pragma unroll
      for (int k = 0; k < 4; ++k) g[k] = ld128(rs, so + k * 4096);
    }
    STAMP(1);
    {
      const float m0 = rbf((gran_val(g[0].x) + gran_val(g[1].x)) + (gran_val(g[2].x) + gran_val(g[3].x)));
      const float m1 = rbf((gran_val(g[0].z) + gran_val(g[1].z)) + (gran_val(g[2].z) + gran_val(g[3].z)));
      const auto s0 = __builtin_amdgcn_permlane32_swap(__float_as_uint(m0), __float_as_uint(m0), false, false);
      const auto s1 = __builtin_amdgcn_permlane32_swap(__float_as_uint(m1), __float_as_uint(m1), false, false);
      *(u32*)(lds + hw_) = pk2bf(rbf(ln0 * rbf(1.f + __uint_as_float(s0[1]))) + __uint_as_float(s0[0]),
                                 rbf(ln1 * rbf(1.f + __uint_as_float(s1[1]))) + __uint_as_float(s1[0]));
    }
    WAVE_LDS_SYNC();
    {
      const bf16x8 h0 = *(const bf16x8*)(lds + hr_), h1 = *(const bf16x8*)(lds + (hr_ ^ 64));
      f32x4 d[8];
#pragma unroll
      for (int c = 0; c < 8; ++c) d[c] = __builtin_amdgcn_mfma_f32_16x16x32_bf16(h0, wf[c], f32x4{0.f, 0.f, 0.f, 0.f}, 0, 0, 0);
#pragma unroll
      for (int c = 0; c < 8; ++c) d[c] = __builtin_amdgcn_mfma_f32_16x16x32_bf16(h1, wf[8 + c], d[c], 0, 0, 0);
#pragma unroll
      for (int k = 0; k < 2; ++k) {
        const float v = (g4 & 2) ? ((g4 & 1) ? d[4 * k + 3][0] : d[4 * k + 2][0]) : ((g4 & 1) ? d[4 * k + 1][0] : d[4 * k][0]);
        const u32 vh = f2bf(v);
        *(u16*)(lds + qw_ + 128 * k) = (u16)vh;
        *(u16*)(lds + qw_ + 128 * k + 8 * 272) = (u16)f2bf(v - bf2f(vh));
      }
    }
    STAMP(2);
    if (w == 0) S_POLL(fl);
    BAR();
    {
      bf16x8 a[4];
#pragma unroll
      for (int s = 0; s < 4; ++s) a[s] = *(const bf16x8*)(lds + qr_ + 16 * s);
      f32x4 d[2] = {f32x4{0.f, 0.f, 0.f, 0.f}, f32x4{0.f, 0.f, 0.f, 0.f}};
#pragma unroll
      for (int s = 0; s < 4; ++s)
#pragma unroll
        for (int c = 0; c < 2; ++c) d[c] = __builtin_amdgcn_mfma_f32_16x16x32_bf16(a[s], wk[4 * c + s], d[c], 0, 0, 0);
      constexpr float SCALE = 0.08838834764831845f * 1.4426950408889634f;
      const float v = SCALE * x16_sum(rs32(sum4(d[0]), sum4(d[1])));
      const float p0 = __uint_as_float(nopois(pk2bf(dppf<0x00>(v), dppf<0x55>(v)))), p1 = __uint_as_float(nopois(pk2bf(dppf<0xAA>(v), dppf<0xFF>(v))));
      st128(rs, qpo, u32x4{__float_as_uint(p0), __float_as_uint(p1), __float_as_uint(dppf<0x104>(p0)), __float_as_uint(dppf<0x104>(p1))});
    }
    STAMP(5);
    SB();
    {
      st128(rs, lane < 50 ? WS_PCTX + (j * 8 + w) * 800 + lane * 16 : WS_TOTAL, P4);
      const int pp = __builtin_amdgcn_readfirstlane(WS_PROBE + il((j * 8 + w) * 128, w & 1));
      u64 ta, tb, tc;
      u32 dm;
      asm volatile("s_load_dword %3, %4, 0x0\n s_waitcnt lgkmcnt(0)\n"
                   "s_memtime %0\n s_waitcnt lgkmcnt(0)\n s_load_dword %3, %4, %5 glc\n s_waitcnt lgkmcnt(0)\n"
                   "s_memtime %1\n s_waitcnt lgkmcnt(0)\n s_load_dword %3, %4, %6 glc\n s_waitcnt lgkmcnt(0)\n"
                   "s_memtime %2\n s_waitcnt vmcnt(0) lgkmcnt(0)"
                   : "=&s"(ta), "=&s"(tb), "=&s"(tc), "=&s"(dm) : "s"(ws), "s"(pp), "s"(pp ^ 8192) : "memory");
      int df = min(max(((int)(u32)(tb - ta) - (int)(u32)(tc - tb)) >> 3, -VCLIP), VCLIP);
      df = ((w ^ myh) & 1) ? -df : df;
      u32 old = 0u;
      if (lane == 0) old = __hip_atomic_fetch_add((LAS u32*)(lds + L_CTR) + C_VOTE, (u32)(65536 + 64 + df), __ATOMIC_RELAXED, __HIP_MEMORY_SCOPE_WORKGROUP);
      old = __builtin_amdgcn_readfirstlane(old) + 65536 + 64 + df;
      if (old >= 524288u && lane < 8) st64(rs, WS_VOTE + lane * 2048 + j * 8, (old & 1023u) | (tlo << 24), (tlo >> 8) | (thi << 24));
      STAMP(6);
    }
    {
      const int r0 = 128 * (mu & 3) + 32 * (w & 3) + 4 * (lane >> 3);
      const u32x2 cvb = *(const u32x2*)(cvec + r0);
      u32x4 wsc[4];
#pragma unroll
      for (int i = 0; i < 4; ++i) wsc[i] = ldnt(w_mod + (size_t)(r0 + i) * 3072 + 1024 + (mu >> 2) * 64 + (lane & 7) * 8);
      float a[8];
#pragma unroll
      for (int k = 0; k < 8; ++k) a[k] = 0.f;
#pragma unroll
      for (int i = 0; i < 4; ++i) fma8b(a, f2bf(silu_bf((i & 1) ? hi16(i < 2 ? cvb.x : cvb.y) : lo16(i < 2 ? cvb.x : cvb.y))), wsc[i]);
      float r[4];
#pragma unroll
      for (int k = 0; k < 4; ++k) r[k] = rs32(a[k], a[k + 4]);
      const float o0 = b3_sum(rs16(r[0], r[2])), o1 = b3_sum(rs16(r[1], r[3]));
      float* red = (float*)(lds + L_H1RED + 8192);
      if ((lane & 8) == 0) *(f32x2*)(red + w * 64 + (lane & 7) * 8 + (lane >> 5) * 4 + ((lane >> 4) & 1) * 2) = f32x2{o0, o1};
      BAR();
      if ((w & 3) == 3 && lane < 16) {
        red += (w >> 2) * 256 + lane * 4;
        const f32x4 sm = (*(const f32x4*)(red) + *(const f32x4*)(red + 64)) + (*(const f32x4*)(red + 128) + *(const f32x4*)(red + 192));
        st128(rs, WS_MODB + ((mu & 3) * 2048 + (mu >> 2) * 64 + lane * 4) * 4, u32x4{fbits(sm.x), fbits(sm.y), fbits(sm.z), fbits(sm.w)});
      }
    }
    gx = 0;
  }

  RELANE();
  const int r16 = lane >> 2, ch = lane & 3;
  if (ii >= 16) {
    int mo = lane < 24 ? (WS_LSE + il((24 * w + lane) * 512 + c4 * 128 + (h >> 1) * 8, h & 1)) ^ gx : WS_TOTAL;
    PIN(mo);
    lds_spin(ctr + C_PD, 4u);
    STAMP(23);
    u32x2 mt = ld64(rs, mo);
    SB();
    const u32 pb = (w >> 1) * PS + (w & 1) * 6144;
    u32 po[6], pa[16], ea[8];
    {
      u32 t_ = g4 * 256 + ((col ^ (g4 << 2)) << 4) + ((WS_PART + il21(((h >> 1) * 4 + c4) * 65536, h & 1)) ^ gx);
      PIN(t_);
#pragma unroll
      for (int k = 0; k < 6; ++k) { po[k] = (t_ ^ ((k & 3) << 4)) + il((6 * w + k) * 1024, 0); PIN(po[k]); }
    }
    bf16x8 wk[4];
    {
      u32 wa = tq * PS + 16384 + off_b(g4, 2 * w + (tp >> 1)) + 8 * (tp & 1);
      PIN(wa);
#pragma unroll
      for (int s = 0; s < 4; ++s) wk[s] = tr_frag(lds, (wa ^ (s << 4)) + s * 1024, (wa ^ (s << 4)) + s * 1024 + 4096);
    }
    pa[0] = pb + off_b(8 * (g4 == 3 ? 0 : g4) + tq, tp >> 1) + 8 * (tp & 1);
    PIN(pa[0]);
#pragma unroll
    for (int i = 1; i < 16; ++i) { pa[i] = pa[0] ^ (i << 4); PIN(pa[i]); }
    u32 bo = L_QOFF + col * 272 + g4 * 16, mlo = L_QOFF + 4352 + (col & 7) * 8, ro = L_QOFF + (w + (col & 8)) * 272 + (col & 7) * 32 + g4 * 8;
    int so1 = (WS_PCTX + h * 2048 + c4 * 512 + (w >> 2) * 256 + (w & 1) * 128 + (w & 2) * 16 + (g4 >> 1) * 64 + (g4 & 1) * 8 + (col < 2 ? col * 16 : WS_TOTAL)) ^ gx;
    u32 eo = L_QOFF + 4416 + w * 128 + lane * 2, mw_ = L_QOFF + 4352 + w * 8;
#pragma unroll
    for (int i = 0; i < 8; ++i) { ea[i] = L_QOFF + 4416 + w * 128 + ((col & 7) == i ? 16 * g4 : 64); PIN(ea[i]); }
    float pad = lane < 24 ? 0.f : -__builtin_inff();
    PIN(bo); PIN(mlo); PIN(ro); PIN(so1); PIN(eo); PIN(mw_); PIN(pad);
    SB();
    u32 done = 0u;
    _Pragma("unroll 1") for (u32 spins = 0; spins < SPIN_CAP; ++spins) {
      const u32 ok = ~(u32)__ballot(max(mt.x, mt.y) == POISON);
      u32 nw = ok & (ok >> 1);
      nw = nw & (nw >> 2) & 0x111111u & ~done;
      done |= nw;
#define T1F(k, b) "s_bitcmp0_b32 %[nw], " #b "\n s_cbranch_scc1 1f\n global_load_lds_dwordx4 %[p" #k "], %[ws] sc1\n 1: s_add_u32 m0, m0, 0x400\n"
      asm volatile("s_mov_b32 m0, %[m]\n" T1F(0, 0) T1F(1, 4) T1F(2, 8) T1F(3, 12) T1F(4, 16) T1F(5, 20)
                   ::[m] "s"(pb), [nw] "s"(nw), [ws] "s"(ws), [p0] "v"(po[0]), [p1] "v"(po[1]), [p2] "v"(po[2]), [p3] "v"(po[3]), [p4] "v"(po[4]), [p5] "v"(po[5]) : "memory", "scc");
      if (done == 0x111111u) break;
      __builtin_amdgcn_s_sleep(1);
      if (max(mt.x, mt.y) == POISON) mt = ld64(rs, mo);
      PIN(mt);
    }
    STAMP(16);
    {
      const float m = __uint_as_float(mt.x) + pad;
      float M = m;
#define MX(C) "s_nop 1\n v_max_f32_dpp %0, %0, %0 " C " row_mask:0xf bank_mask:0xf\n"
      asm(MX("quad_perm:[1,0,3,2]") MX("quad_perm:[2,3,0,1]") MX("row_ror:8") MX("row_ror:4") "s_nop 1" : "+v"(M));
      M = fmaxf(__uint_as_float(__builtin_amdgcn_readlane(__float_as_uint(M), 0)), __uint_as_float(__builtin_amdgcn_readlane(__float_as_uint(M), 16)));
      const float e = __builtin_amdgcn_exp2f(m - M);
      *(u16*)(lds + eo) = (u16)f2bf(e);
      WAVE_LDS_SYNC();
      bf16x8 wf[8], pt[8];
#pragma unroll
      for (int i = 0; i < 8; ++i) wf[i] = *(const bf16x8*)(lds + ea[i]);
      SB();
      const float L = x16_sum(row16_sum(e * __uint_as_float(mt.y)));
      if (lane == 0) *(f32x2*)(lds + mw_) = f32x2{M, L};
      SB();
      waitvm<0>();
      STAMP(17);
#pragma unroll
      for (int i = 0; i < 8; ++i) pt[i] = tr_frag(lds, pa[2 * i], pa[2 * i + 1] + 1024);
      SB();
      f32x4 d = {0.f, 0.f, 0.f, 0.f};
#pragma unroll
      for (int i = 0; i < 8; ++i) d = __builtin_amdgcn_mfma_f32_16x16x32_bf16(pt[i], wf[i], d, 0, 0, 0);
      const u32 h0 = pk2bf(d[0], d[1]), h1 = pk2bf(d[2], d[3]);
      const u32 l0 = pk2bf(d[0] - lo16(h0), d[1] - hi16(h0)), l1 = pk2bf(d[2] - lo16(h1), d[3] - hi16(h1));
      *(u32x2*)(lds + ro) = (col & 8) ? u32x2{l0, l1} : u32x2{h0, h1};
    }
    BAR();
    STAMP(14);
    const f32x2 ml = *(const f32x2*)(lds + mlo);
    bf16x8 lf[4];
#pragma unroll
    for (int s = 0; s < 4; ++s) lf[s] = *(const bf16x8*)(lds + bo + 64 * s);
    SB();
    f32x4 d = {0.f, 0.f, 0.f, 0.f};
#pragma unroll
    for (int s = 0; s < 4; ++s) d = __builtin_amdgcn_mfma_f32_16x16x32_bf16(wk[s], lf[s], d, 0, 0, 0);
    SB();
    float Mg = fmaxf(ml.x, dppf<0xB1>(ml.x));
    Mg = fmaxf(Mg, dppf<0x4E>(Mg)); Mg = fmaxf(Mg, dppf<0x141>(Mg));
    const float e = __builtin_amdgcn_exp2f(ml.x - Mg);
    float Lg = quad_sum(e * ml.y);
    Lg += dppf<0x141>(Lg);
    const float sc = e * __builtin_amdgcn_rcpf(Lg);
    {
      float d0 = d[0] * sc, d1 = d[1] * sc, d2 = d[2] * sc, d3 = d[3] * sc;
      row16_sum4(d0, d1, d2, d3);
      d = f32x4{d0, d1, d2, d3};
    }
    const u32 h0 = pk2bf(d[0], d[1]), h1 = pk2bf(d[2], d[3]);
    const u32 l0 = pk2bf(d[0] - lo16(h0), d[1] - hi16(h0)), l1 = pk2bf(d[2] - lo16(h1), d[3] - hi16(h1));
    st64(rs, so1, nopois(col ? l0 : h0), nopois(col ? l1 : h1));
    STAMP(15);
    STAMP_FLUSH();
    return;
  }
  if (tl2) {
    BAR0();
    if (w < 4) {
#pragma unroll
      for (int i = 0; i < 8; ++i) dma<true>(owno + i * 1024, (ks * 512 + 64 * w + 8 * i + (lane >> 3)) * 1024 + cg * 128 + (lane & 7) * 16, w_o);
      waitvm<0>();
    } else {
#pragma unroll
      for (int i = 0; i < 8; ++i) *(u32x4*)(lds + owno + i * 1024 + lane * 16) = wv[i];
      WAVE_LDS_SYNC();
    }
    STAMP(26);
    bf16x8 wf[8];
#pragma unroll
    for (int i = 0; i < 8; ++i) {
      const u32 a = owno + (32 * (i >> 2) + 8 * g4 + tq) * 128 + (i & 3) * 32 + tp * 8;
      wf[i] = tr_frag(lds, a, a + 512);
    }
#pragma unroll
    for (int i = 0; i < 8; ++i) PIN(wf[i]);
    STAMP(23);
    int off[1] = {(WS_PCTX + (ks * 4 + (w >> 1)) * 2048 + tq * 512 + (w & 1) * 256 + g4 * 64 + tp * 16) ^ gx};
    u32 rw = L_RED + lane * 32 + w * 4;
    PIN(rw);
    int so2 = tp < 2 ? WS_PATTN + il((ks * 512 + cg * 64 + (lane & 60)) * 4, tp) : WS_TOTAL;
    PIN(so2);
    u32x4 g[1];
    gather128<1, 4>(rs, off, g);
    STAMP(24);
    const bf16x8 a0 = __builtin_bit_cast(bf16x8, (col & 2) ? Z4 : g[0]), a1 = __builtin_bit_cast(bf16x8, (col & 2) ? g[0] : Z4);
    f32x4 d[4];
#pragma unroll
    for (int c = 0; c < 4; ++c) d[c] = __builtin_amdgcn_mfma_f32_16x16x32_bf16(a0, wf[c], f32x4{0.f, 0.f, 0.f, 0.f}, 0, 0, 0);
#pragma unroll
    for (int c = 0; c < 4; ++c) d[c] = __builtin_amdgcn_mfma_f32_16x16x32_bf16(a1, wf[4 + c], d[c], 0, 0, 0);
    *(float*)(lds + rw) = rs16(rs32(sum4(d[0]), sum4(d[2])), rs32(sum4(d[1]), sum4(d[3])));
    BAR();
    if (w == 0) {
      const float v = sum4(*(const f32x4*)(lds + L_RED + lane * 32) + *(const f32x4*)(lds + L_RED + lane * 32 + 16));
      const float vn = __uint_as_float(fbits(v));
      st128(rs, so2, u32x4{__float_as_uint(dppf<0x00>(vn)), __float_as_uint(dppf<0x55>(vn)), __float_as_uint(dppf<0xAA>(vn)), __float_as_uint(dppf<0xFF>(vn))});
    }
    STAMP(17);
  } else if (t3 >= 0) {
    STAMP(20);
    if (w < 4) {
      const int fr = ((128 * w + r16) * DF + t3 * 32) * 2, ca_ = ch ^ ((r16 >> 2) & 2) ^ (t3 & 1);
      const u16* wa = (t3 & 1) ? w_up : w_gate;
      const u16* wb = (t3 & 1) ? w_gate : w_up;
      const u32 d = w * 32768 + 8192;
#pragma unroll
      for (int i = 0; i < 8; ++i) { waitvm<WING - 1>(); dma<false>(d + (i >> 2) * 12288 + (t3 & 1) * 4096 + (i & 3) * 1024, fr + ca_ * 16 + 32 * i * DF, wa); }
#pragma unroll
      for (int i = 0; i < 8; ++i) { waitvm<WING - 1>(); dma<false>(d + (i >> 2) * 12288 + 4096 - (t3 & 1) * 4096 + (i & 3) * 1024, fr + (ca_ ^ 1) * 16 + 32 * i * DF, wb); }
    } else {
#pragma unroll
      for (int i = 0; i < 8; ++i) {
        waitvm<WING - 1>();
        dma<true>(lw * 32768 + 16384 + (i >> 2) * 12288 + (i & 3) * 1024, (32 * t3 + 8 * (i & 3) + (lane >> 3)) * 1024 + 256 * lw + 128 * (i >> 2) + (lane & 7) * 16, w_down);
      }
      const u32 x2 = *(const u32*)(x + 128 * lw + 2 * lane);
      u32x4 m6[6];
      int mo[6];
#pragma unroll
      for (int i = 0; i < 6; ++i) mo[i] = MB_OFF(i);
      gather128<6, 8>(rs, mo, m6);
#pragma unroll
      for (int i = 0; i < 6; ++i) {
        const f32x4 f = __builtin_bit_cast(f32x4, m6[i]);
        const float v = rbf(rs16(rs32(f.x, f.z), rs32(f.y, f.w)));
        *(u16*)(lds + L_Z + (i < 2 ? 5 : i < 4 ? 3 : 2) * 1088 + (128 * lw + 64 * (i & 1) + 4 * col + g4) * 2) = (u16)f2bf(i < 4 ? v : 1.f + v);
      }
      *(u32*)(lds + L_Z + 6 * 1088 + lw * 256 + lane * 4) = x2;
      if (w == 4 && lane < 32) *(u32*)(lds + L_ZERO + lane * 4) = lane < 16 ? 0u : POISON;
      if (w == 6) COUNT_VOTES(false);
    }
    waitvm<0>();
    STAMP(26);
    BAR();
    gx = __builtin_amdgcn_readfirstlane(ctr[C_PAR]);
    u32 zw = L_Z + (64 * w + 4 * col + g4) * 2;
    u32 hr[4];
#pragma unroll
    for (int i = 0; i < 4; ++i) { hr[i] = (col == i ? L_HH : L_ZERO) + 16 * g4; PIN(hr[i]); }
    int off[2];
#pragma unroll
    for (int i = 0; i < 2; ++i) { off[i] = (WS_PATTN + il(((4 * i + g4) * 512 + 64 * w + col * 4) * 4, myh)) ^ gx; PIN(off[i]); }
    int so = col < 4 ? (WS_PDOWN + il((t3 * 256 + 64 * (w & 3) + 16 * col + 4 * g4) * 4, w >> 2)) ^ gx : WS_TOTAL;
    u32 zr = L_Z + tp * 1088 + 16 * g4, yr = L_Z + 4 * 1088 + lane * 16, hw = L_HH + (w & 3) * 16 + (g4 & 1) * 8 + tp * 2, cr = L_CF + (w & 3) * 16 + tp * 4;
    PIN(cr); PIN(so); PIN(zw); PIN(zr); PIN(yr); PIN(hw);
    float ga = bf2f(*(const u16*)(lds + zw + 5 * 1088)), cb1 = bf2f(*(const u16*)(lds + zw + 2 * 1088)), xe = bf2f(*(const u16*)(lds + zw + 6 * 1088));
    bf16x8 wf[16], wd[4];
    if (w < 4) {
#pragma unroll
      for (int k = 0; k < 16; ++k) {
        const u32 a = (k >> 2) * 32768 + 8192 + ((k >> 1) & 1) * 12288 + (tp >> 1) * 4096 + ((k & 1) * 32 + 8 * g4 + tq) * 64 + (w ^ ((g4 & 1) * 2) ^ (tp >> 1)) * 16 + (tp & 1) * 8;
        wf[k] = tr_frag(lds, a, a + 256);
      }
#pragma unroll
      for (int k = 0; k < 16; ++k) PIN(wf[k]);
    }
#pragma unroll
    for (int i = 0; i < 4; ++i) {
      const u32 a = owno + 8192 + (8 * g4 + tq) * 128 + i * 32 + tp * 8;
      wd[i] = tr_frag(lds, a, a + 512);
    }
#pragma unroll
    for (int i = 0; i < 4; ++i) PIN(wd[i]);
    PIN(ga); PIN(cb1); PIN(xe);
    STAMP(23);
    until(t0, E6_T);
    u32x4 pa2[2];
    gather128<2, 4>(rs, off, pa2);
    STAMP(18);
    {
      const f32x4 s4 = __builtin_bit_cast(f32x4, pa2[0]) + __builtin_bit_cast(f32x4, pa2[1]);
      const u32 yb = f2bf(xe + rbf(ga * rbf(rs16(rs32(s4.x, s4.z), rs32(s4.y, s4.w)))));
      const float z = bf2f(yb) * cb1;
      const u32 zh = f2bf(z);
      *(u16*)(lds + zw + 4 * 1088) = (u16)yb;
      *(u16*)(lds + zw) = (u16)zh;
      *(u16*)(lds + zw + 1088) = (u16)f2bf(z - bf2f(zh));
    }
    BAR();
    if (w < 4) {
      bf16x8 zf[16];
#pragma unroll
      for (int k = 0; k < 16; ++k) zf[k] = *(const bf16x8*)(lds + zr + 64 * k);
      __builtin_amdgcn_sched_barrier(0);
      f32x4 d = {0.f, 0.f, 0.f, 0.f};
#pragma unroll
      for (int k = 0; k < 16; ++k) d = __builtin_amdgcn_mfma_f32_16x16x32_bf16(wf[k], zf[k], d, 0, 0, 0);
      PIN(d);
      u32 cfb;
      do { cfb = *(volatile LAS u32*)(lds + cr); } while (__any(cfb == POISON));
      d *= __uint_as_float(cfb);
      const float a0 = (tp & 1) ? d[1] : d[0], b0 = (tp & 1) ? d[0] : d[1], a1 = (tp & 1) ? d[3] : d[2], b1 = (tp & 1) ? d[2] : d[3];
      const float e0 = a0 + dppf<0xB1>(b0), e1 = a1 + dppf<0xB1>(b1);
      const float v = rbf(((tp & 2) ? e1 : e0) + dppf<0x4E>((tp & 2) ? e0 : e1));
      const auto sw = __builtin_amdgcn_permlane32_swap(__float_as_uint(v), __float_as_uint(v), false, false);
      const float g = __uint_as_float(sw[0]), u = __uint_as_float(sw[1]);
      const float e2 = __builtin_amdgcn_exp2f(g * (-2.3022081981f - 0.1029432396f * g * g));
      *(u16*)(lds + hw) = (u16)f2bf(rbf(g * __builtin_amdgcn_rcpf(e2 + 1.f)) * u);
    } else {
      const u32x4 yv = *(const u32x4*)(lds + yr);
      const float a1 = dot2(yv.x, 0x3F803F80u, dot2(yv.y, 0x3F803F80u, 0.f)) + dot2(yv.z, 0x3F803F80u, dot2(yv.w, 0x3F803F80u, 0.f));
      const float a2 = dot2(yv.x, yv.x, dot2(yv.y, yv.y, 0.f)) + dot2(yv.z, yv.z, dot2(yv.w, yv.w, 0.f));
      const float sr = x16_sum(row16_sum(rs32(a1, a2))) * (1.f / 512.f);
      const float s1 = __uint_as_float(__builtin_amdgcn_readlane(__float_as_uint(sr), 0)), s2 = __uint_as_float(__builtin_amdgcn_readlane(__float_as_uint(sr), 32));
      const float var = rbf(fmaxf(s2 - s1 * s1, 0.f));
      const float mb = rbf(s1);
      const float sd = rbf(__builtin_amdgcn_sqrtf(rbf(var + 1e-5f)));
      float rsd = __builtin_amdgcn_rcpf(sd);
      rsd = rsd * (2.f - sd * rsd);
      *(u32*)(lds + cr) = fbits(tp == 3 ? 1.f : rsd * (tp == 2 ? -mb : 1.f));
    }
    BAR();
    {
      bf16x8 hf[4];
#pragma unroll
      for (int i = 0; i < 4; ++i) hf[i] = *(const bf16x8*)(lds + hr[i]);
      __builtin_amdgcn_sched_barrier(0);
      f32x4 d = {0.f, 0.f, 0.f, 0.f};
#pragma unroll
      for (int i = 0; i < 4; ++i) d = __builtin_amdgcn_mfma_f32_16x16x32_bf16(wd[i], hf[i], d, 0, 0, 0);
      st128(rs, so, u32x4{fbits(d[0]), fbits(d[1]), fbits(d[2]), fbits(d[3])});
    }
    STAMP(19);
  }
  if (t4 >= 0 && lw == 0) {
#define RED8(f) b3_sum(rs16(rs32(f.x, f.z), rs32(f.y, f.w)))
    const int oc = t4 * 32 + (lane & 7) * 4 + g4;
    const u32 xb = x[oc];
    float x1, gb;
    {
      int off[2];
      off[0] = lane < 32 ? WS_MODB + ((lane >> 3) * 2048 + t4 * 32 + (lane & 7) * 4) * 4 : WS_TOTAL;
      off[1] = lane < 32 ? WS_MODB + ((lane >> 3) * 2048 + 1536 + t4 * 32 + (lane & 7) * 4) * 4 : WS_TOTAL;
      u32x4 mg[2];
      gather128<2>(rs, off, mg);
      const f32x4 fa = __builtin_bit_cast(f32x4, mg[0]), fb = __builtin_bit_cast(f32x4, mg[1]);
      const float ga = rbf(RED8(fa));
      gb = rbf(RED8(fb));
      int of1[1] = {(WS_PATTN + il(((lane >> 3) * 512 + t4 * 32 + (lane & 7) * 4) * 4, myh)) ^ gx};
      u32x4 g[1];
      gather128<1, 4>(rs, of1, g);
      const f32x4 gf = __builtin_bit_cast(f32x4, g[0]);
      x1 = rbf(bf2f(xb) + rbf(ga * rbf(RED8(gf))));
    }
    STAMP(20);
    if (t4 == 0 && lane == 0) st64(rs, WS_EPOCH, tlo, thi);
    int off[7];
#pragma unroll
    for (int i = 0; i < 7; ++i) { off[i] = (WS_PDOWN + il(((7 * (lane >> 3) + i) * 256 + (t4 & 7) * 32 + (lane & 7) * 4) * 4, myh)) ^ gx; PIN(off[i]); }
    const rsrc_t ro = __builtin_amdgcn_make_buffer_rsrc((void*)out, 0, 1024, 0x00020000);
    int oo = (lane & 8) ? 1024 : oc * 2;
    PIN(x1); PIN(gb); PIN(oo);
    u32x4 g[7];
    __builtin_amdgcn_s_sleep(T4_DELAY);
#define F4(i) __builtin_bit_cast(f32x4, g[i])
    float s;
    _Pragma("unroll 1") for (u32 spins = 0; spins < SPIN_CAP; ++spins) {
      asm volatile("" ::: "memory");
#pragma unroll
      for (int i = 0; i < 7; ++i) g[i] = ld128(rs, off[i]);
      SB();
      const f32x4 s4 = ((F4(0) + F4(1)) + (F4(2) + F4(3))) + ((F4(4) + F4(5)) + F4(6));
      s = RED8(s4);
      if (!__any(s != s)) break;
      bool bad = false;
#pragma unroll
      for (int i = 0; i < 7; ++i) bad = bad | pois4(g[i]);
      if (!__any(bad)) break;
    }
    STAMP(21);
    __builtin_amdgcn_raw_buffer_store_b16((u16)f2bf(x1 + rbf(gb * rbf(s))), ro, oo, 0, 0);
    STAMP(22);
  }
  STAMP_FLUSH();
}

extern "C" void kernel_launch(void* const* d_in, const int* in_sizes, int n_in, void* d_out, int out_size,
                              void* d_ws, size_t ws_size, hipStream_t stream) {
  k_main<<<NWG, 512, 0, stream>>>((const u16*)d_in[0], (const u16*)d_in[1], (const u16*)d_in[2], (const u16*)d_in[3],
                                  (const u16*)d_in[4], (const u16*)d_in[5], (const u16*)d_in[7], (const u16*)d_in[8],
                                  (const u16*)d_in[9], (const u16*)d_in[10], (const u16*)d_in[11], (u16*)d_out,
                                  (unsigned char*)d_ws);
}
```

```cpp
#include <hip/hip_runtime.h>
#include <hip/hip_bf16.h>
#include <stdint.h>

typedef unsigned short u16;
typedef unsigned int u32;
typedef unsigned long long u64;
typedef float f32x2 __attribute__((ext_vector_type(2)));
typedef float f32x4 __attribute__((ext_vector_type(4)));
typedef float f32x16 __attribute__((ext_vector_type(16)));
typedef u32 u32x2 __attribute__((ext_vector_type(2)));
typedef u32 u32x4 __attribute__((ext_vector_type(4)));
typedef short bf16x8 __attribute__((ext_vector_type(8)));
typedef short s16x4 __attribute__((ext_vector_type(4)));
#define LAS __attribute__((address_space(3)))
#define DI __device__ __forceinline__

constexpr int DF = 1792;
constexpr int NWG = 256;
constexpr int AUX_SC1 = 16;

constexpr int WS_EPOCH = 0;
constexpr int WS_SFLAG = 4096;
constexpr int WS_QABS = 32768;
constexpr int WS_PCTX = WS_QABS + 65536;
constexpr int WS_PATTN = WS_PCTX + 65536;
constexpr int WS_PDOWN = WS_PATTN + 32768;
constexpr int WS_LSE = WS_PDOWN + 114688;
constexpr int WS_MODB = WS_LSE + 196608;
constexpr int WS_SC = WS_MODB + 32768;
constexpr int WS_PROBE = WS_SC + 32768;
constexpr int WS_VOTE = WS_PROBE + 262144;
constexpr int WS_PART = 2097152;
constexpr int PART_BYTES = 32 * 8 * 256 * 128;
constexpr int WS_TOTAL = WS_PART + PART_BYTES;

constexpr int il(int o, int hf) { return (o >> 13) * 16384 + hf * 8192 + (o & 8191); }
constexpr int il21(int o, int hf) { return il(o, hf ^ ((o >> 20) & 1) ^ 1); }
constexpr u32 SPIN_CAP = 1u << 16;
#define WIN 8
#define WA0 5
#define WB0 6
#define QW 4
#define WING 2
#define QF_T 300
#define E1_T 250
#define SCP_T 215
#define E3_T 470
#define E6_T 1200
#define T4_DELAY 12
#define TAU 32.f
#define VCLIP 40
#define PF_LEN 33600
#define G_OF(v) ((v) > 32768.f ? 8192u : 0u)
#define VOTE_STAMP(v)
#define STAMP_DECL
#define STAMP(i)
#define STAMP_FLUSH()

DI float bf2f(u32 b) { return __uint_as_float(b << 16); }
DI float rbf(float f) { return (float)(__bf16)f; }
DI u32 f2bf(float f) { return (u32)__builtin_bit_cast(u16, (__bf16)f); }
typedef __bf16 bf16x2_t __attribute__((ext_vector_type(2)));
DI u32 pk2bf(float a, float b) { return __builtin_bit_cast(u32, __builtin_convertvector(f32x2{a, b}, bf16x2_t)); }
DI float lo16(u32 v) { return __uint_as_float(v << 16); }
DI float hi16(u32 v) { return __uint_as_float(v & 0xFFFF0000u); }
DI u32x4 ldnt(const u16* p) { return __builtin_nontemporal_load((const u32x4*)p); }

DI float silu_bf(float a) { return rbf(a * __builtin_amdgcn_rcpf(1.f + __expf(-a))); }

template <int CTRL> DI float dppf(float v) {
  return __int_as_float(__builtin_amdgcn_update_dpp(0, __float_as_int(v), CTRL, 0xF, 0xF, true));
}
DI float quad_sum(float v) { v += dppf<0xB1>(v); v += dppf<0x4E>(v); return v; }
DI float b23_sum(float v) { v += dppf<0x128>(v); v += dppf<0x124>(v); return v; }
DI float b3_sum(float v) { return v + dppf<0x128>(v); }
DI float x16_sum(float v) {
  const auto r = __builtin_amdgcn_permlane16_swap(__float_as_uint(v), __float_as_uint(v), false, false);
  return __uint_as_float(r[0]) + __uint_as_float(r[1]);
}
DI float x32_sum(float v) {
  const auto r = __builtin_amdgcn_permlane32_swap(__float_as_uint(v), __float_as_uint(v), false, false);
  return __uint_as_float(r[0]) + __uint_as_float(r[1]);
}
DI float rs32(float a, float b) {
  const auto r = __builtin_amdgcn_permlane32_swap(__float_as_uint(a), __float_as_uint(b), false, false);
  return __uint_as_float(r[0]) + __uint_as_float(r[1]);
}
DI float rs16(float a, float b) {
  const auto r = __builtin_amdgcn_permlane16_swap(__float_as_uint(a), __float_as_uint(b), false, false);
  return __uint_as_float(r[0]) + __uint_as_float(r[1]);
}
DI float row16_sum(float v) { return b23_sum(quad_sum(v)); }
DI float wave_sum(float v) { return x32_sum(x16_sum(row16_sum(v))); }
DI void row16_sum4(float& a, float& b, float& c, float& d) {
#define R4(C) "v_add_f32_dpp %0, %0, %0 " C " row_mask:0xf bank_mask:0xf\n v_add_f32_dpp %1, %1, %1 " C " row_mask:0xf bank_mask:0xf\n" \
              "v_add_f32_dpp %2, %2, %2 " C " row_mask:0xf bank_mask:0xf\n v_add_f32_dpp %3, %3, %3 " C " row_mask:0xf bank_mask:0xf\n"
  asm("s_nop 1\n" R4("quad_perm:[1,0,3,2]") R4("quad_perm:[2,3,0,1]") R4("row_ror:8") R4("row_ror:4") : "+v"(a), "+v"(b), "+v"(c), "+v"(d));
#undef R4
}
DI float row16_max(float v) {
  v = fmaxf(v, dppf<0xB1>(v)); v = fmaxf(v, dppf<0x4E>(v));
  v = fmaxf(v, dppf<0x128>(v)); v = fmaxf(v, dppf<0x124>(v));
  return v;
}
DI float x16_max(float v) {
  const auto r = __builtin_amdgcn_permlane16_swap(__float_as_uint(v), __float_as_uint(v), false, false);
  return fmaxf(__uint_as_float(r[0]), __uint_as_float(r[1]));
}
DI float x32_max(float v) {
  const auto r = __builtin_amdgcn_permlane32_swap(__float_as_uint(v), __float_as_uint(v), false, false);
  return fmaxf(__uint_as_float(r[0]), __uint_as_float(r[1]));
}
DI float wave_max(float v) { return x32_max(x16_max(row16_max(v))); }

DI float dot2(u32 a, u32 b, float c) {
  return __builtin_amdgcn_fdot2_f32_bf16(__builtin_bit_cast(bf16x2_t, a), __builtin_bit_cast(bf16x2_t, b), c, false);
}
DI void fma8b(float* acc, u32 sbits, u32x4 w) {
  const u32 sh = sbits << 16;
  acc[0] = dot2(w.x, sbits, acc[0]); acc[1] = dot2(w.x, sh, acc[1]);
  acc[2] = dot2(w.y, sbits, acc[2]); acc[3] = dot2(w.y, sh, acc[3]);
  acc[4] = dot2(w.z, sbits, acc[4]); acc[5] = dot2(w.z, sh, acc[5]);
  acc[6] = dot2(w.w, sbits, acc[6]); acc[7] = dot2(w.w, sh, acc[7]);
}

DI unsigned fsw(unsigned row) { return ((row & 3) << 2) | ((row >> 2) & 3); }
DI unsigned off_b(unsigned row, unsigned ch) { return 256u * row + 16u * (ch ^ fsw(row)); }
DI unsigned tr_addr(unsigned lane, unsigned c, unsigned ks, unsigned t) {
  const unsigned h = lane >> 5, blk = (lane >> 4) & 1, q = (lane & 15) >> 2, p = lane & 3;
  return off_b(16 * ks + 8 * h + 4 * t + q, 4 * c + 2 * blk + (p >> 1)) + 8 * (p & 1);
}

typedef __amdgpu_buffer_rsrc_t rsrc_t;
DI u32x4 ld128(rsrc_t rs, int off) { return __builtin_amdgcn_raw_buffer_load_b128(rs, off, 0, AUX_SC1); }
DI u32x2 ld64(rsrc_t rs, int off) { return __builtin_amdgcn_raw_buffer_load_b64(rs, off, 0, AUX_SC1); }
DI void st64(rsrc_t rs, int off, u32 lo, u32 hi) { u32x2 v; v.x = lo; v.y = hi; __builtin_amdgcn_raw_buffer_store_b64(v, rs, off, 0, AUX_SC1); }
DI void st128(rsrc_t rs, int off, u32x4 v) { __builtin_amdgcn_raw_buffer_store_b128(v, rs, off, 0, AUX_SC1); }
#define BAR() do { asm volatile("s_waitcnt lgkmcnt(0)" ::: "memory"); __builtin_amdgcn_s_barrier(); asm volatile("" ::: "memory"); } while (0)
#define BAR0() do { __builtin_amdgcn_sched_barrier(0); asm volatile("" ::: "memory"); __builtin_amdgcn_s_barrier(); asm volatile("" ::: "memory"); __builtin_amdgcn_sched_barrier(0); } while (0)
#define PIN(x) asm volatile("" : "+v"(x))
constexpr u32 POISON = 0xFFFFFFFFu;
DI bool pois4(u32x4 v) { return max(max(v.x, v.y), max(v.z, v.w)) == POISON; }
DI u32 nopois(u32 v) { return min(v, 0xFFFFFFFEu); }
DI u32 fbits(float v) { return nopois(__float_as_uint(v)); }

DI u32x2 mk_gran(float v, u32 tlo, u32 thi) {
  u32 b = __float_as_uint(v);
  if ((b & 0x7F800000u) != 0x7F800000u) b += 0x80u;
  u32x2 g; g.x = (b >> 8) | (tlo << 24); g.y = (tlo >> 8) | (thi << 24);
  return g;
}
DI void st_gran2(rsrc_t rs, int off, float v0, float v1, u32 tlo, u32 thi) {
  const u32x2 a = mk_gran(v0, tlo, thi), b = mk_gran(v1, tlo, thi);
  u32x4 v; v.x = a.x; v.y = a.y; v.z = b.x; v.w = b.y;
  st128(rs, off, v);
}
DI bool gran_ok(u32 lo, u32 hi, u32 tlo, u32 thi) { return (((lo >> 24) ^ (tlo & 0xFFu)) | (hi ^ ((tlo >> 8) | (thi << 24)))) == 0u; }
DI float gran_val(u32 lo) { return __uint_as_float(lo << 8); }

template <int N, int SLP = 1> DI void gather128(rsrc_t rs, const int (&off)[N], u32x4 (&v)[N]) {
#pragma unroll
  for (int i = 0; i < N; ++i) v[i] = ld128(rs, off[i]);
  _Pragma("unroll 1") for (u32 spins = 0; spins < SPIN_CAP; ++spins) {
    bool need[N], bad = false;
#pragma unroll
    for (int i = 0; i < N; ++i) { need[i] = pois4(v[i]); bad = bad || need[i]; }
    if (!__any(bad)) break;
    __builtin_amdgcn_s_sleep(SLP);
    asm volatile("" ::: "memory");
    __builtin_amdgcn_sched_barrier(0);
#pragma unroll
    for (int i = 0; i < N; ++i)
      if (need[i]) v[i] = ld128(rs, off[i]);
  }
}
template <int N> DI void waitvm() { asm volatile("s_waitcnt vmcnt(%0)" ::"n"(N) : "memory"); }
constexpr u32 LSPIN_CAP = 1u << 22;
DI void lds_wait(volatile LAS u32* p, u32 val) {
  _Pragma("unroll 1") for (u32 n = 0; *p < val && n < LSPIN_CAP; ++n) __builtin_amdgcn_s_sleep(1);
  asm volatile("" ::: "memory");
}
#define WAVE_LDS_SYNC() do { __builtin_amdgcn_fence(__ATOMIC_RELEASE, "wavefront"); __builtin_amdgcn_wave_barrier(); __builtin_amdgcn_fence(__ATOMIC_ACQUIRE, "wavefront"); } while (0)
#define SB() __builtin_amdgcn_sched_barrier(0)
#define RARE(c) __builtin_expect(!!(c), 0)

constexpr int PS = 28672;
constexpr int L_QI = 4 * PS;
constexpr int L_P = L_QI + 32768;
constexpr int L_REF = L_P + 11264;
constexpr int L_MX = L_REF + 1408;
constexpr int L_ALW = L_MX + 128;
constexpr int L_DUMP = L_ALW + 512;
constexpr int L_QOFF = 131072;
constexpr int L_H1RED = L_QOFF;
constexpr int L_RED = L_QOFF + 16384;
constexpr int L_Z = L_RED, L_HH = L_RED + 7 * 1088, L_ZERO = L_HH + 64, L_CF = L_ZERO + 64;
constexpr int L_MISC = L_QOFF + 30720;
constexpr int L_CTR = L_MISC;
constexpr int L_WMAX = L_MISC + 256;
constexpr int L_ML = L_MISC + 512;

#define Z4 (u32x4{0u, 0u, 0u, 0u})
DI float max5(f32x4 v, float e) { return fmaxf(fmaxf(fmaxf(fmaxf(v.x, v.y), v.z), v.w), e); }

template <bool NT> DI void dma(u32 m, u32 voff, const void* sb) {
  if (NT) asm volatile("s_mov_b32 m0, %0\n s_nop 0\n global_load_lds_dwordx4 %1, %2 nt" ::"s"(m), "v"(voff), "s"(sb) : "memory");
  else asm volatile("s_mov_b32 m0, %0\n s_nop 0\n global_load_lds_dwordx4 %1, %2" ::"s"(m), "v"(voff), "s"(sb) : "memory");
}
DI void dma_sc1(u32 m, u32 voff, const void* sb) {
  asm volatile("s_mov_b32 m0, %0\n s_nop 0\n global_load_lds_dwordx4 %1, %2 sc1" ::"s"(m), "v"(voff), "s"(sb) : "memory");
}
DI void lds_add(int idx) {
  SB();
  asm volatile("s_mov_b64 exec, 1\n ds_add_u32 %0, %1 offset:%2\n s_mov_b64 exec, -1" ::"v"((u32)L_CTR), "v"(1u), "n"(idx * 4) : "memory");
  SB();
}
DI u32 spin_at(u32 a, u32 val) {
  u32 t, c, m, cap;
  asm volatile("s_mov_b32 %3, %6\n 1: ds_read_b32 %0, %4\n s_waitcnt lgkmcnt(0)\n v_readfirstlane_b32 %1, %0\n s_and_b32 %2, %1, 255\n s_cmp_ge_u32 %2, %5\n s_cbranch_scc1 2f\n"
               "s_sub_u32 %3, %3, 1\n s_cbranch_scc0 1b\n 2:"
               : "=&v"(t), "=&s"(c), "=&s"(m), "=&s"(cap) : "v"(a), "s"(val), "n"(LSPIN_CAP) : "memory", "scc");
  return c;
}
DI void lds_spin(volatile LAS u32* p, u32 val) { spin_at((u32)(uintptr_t)p, val); }
enum { C_SOK = 0, C_BH = 4, C_PD = 12, C_H1 = 18, C_PAR = 19, C_VOTE = 20, C_FREE = 22, C_QRDY = 23, C_MXC = 25, C_SD = 26, C_GX = 27, C_ML = 28, C_MK = 32, C_P = 44 };

DI u32 kv_mark(int nm, int skip) { return L_CTR + 4 * C_MK + __builtin_amdgcn_readfirstlane(4 * nm + (nm >= skip ? 4 : 0)); }
#define KV_TS
#define KV_TSO
#define KV_TSA
#define KV_TSP
#define KV_TRY(l)                                                                                                        \
  "7: s_getreg_b32 %[h], hwreg(HW_REG_IB_STS)\n s_and_b32 %[h], %[h], 0xC0000F\n s_max_i32 %[h], %[h], %[x]\n s_sub_i32 %[h], %[h], %[x]\n s_cmp_gt_i32 %[h], %[k4]\n s_cbranch_scc1 " l "\n" \
  "s_waitcnt vmcnt(%[x4])\n s_cmp_eq_u32 %[k4], 4\n s_cbranch_scc1 8f\n s_waitcnt vmcnt(%[x])\n"                        \
  "8: v_subrev_u32 %[t], %[k4], %[ml]\n s_mov_b64 exec, 1\n ds_add_u32 %[t], %[one] offset:12\n s_mov_b64 exec, -1\n"   \
  "s_sub_i32 %[k4], %[k4], 4\n" KV_TS "s_branch 7b\n"
template <int X> DI void kv_try(int& k4, u32 ml, u32 one KV_TSA) {
  u32 t, h;
  asm volatile(KV_TRY("9f") "9:" : [t] "=&v"(t), [h] "=&s"(h), [k4] "+s"(k4) KV_TSO : [ml] "v"(ml), [one] "v"(one), [x] "n"(X), [x4] "n"(X + 4) : "memory", "scc");
}
template <int X> DI u32 spin_try(int idx, u32 val, int& k4, u32 ml, u32 one KV_TSA) {
  u32 t, h, c, cap;
  asm volatile("s_mov_b32 %[cap], %[n]\n 1: ds_read_b32 %[t], %[ml] offset:%[o]\n s_waitcnt lgkmcnt(0)\n v_readfirstlane_b32 %[c], %[t]\n s_and_b32 %[h], %[c], 255\n"
               "s_cmp_ge_u32 %[h], %[v]\n s_cbranch_scc1 2f\n" KV_TRY("9f") "9: s_sub_u32 %[cap], %[cap], 1\n s_cbranch_scc0 1b\n 2:"
               : [t] "=&v"(t), [h] "=&s"(h), [c] "=&s"(c), [cap] "=&s"(cap), [k4] "+s"(k4) KV_TSO
               : [ml] "v"(ml), [one] "v"(one), [o] "n"(4 * idx), [v] "n"(val), [n] "n"(LSPIN_CAP), [x] "n"(X), [x4] "n"(X + 4) : "memory", "scc");
  return c;
}
template <int W> DI void kv_load1(const u16* ckv, u32 vl, int lw, u32 g0, u32 g1, u32 m, int n0, int n1, int& nm, int skip) {
#pragma unroll 1
  for (int n = n0; n < n1; ++n) {
#define KV_POST(k) ".if (%[wm] | 4) == " #k "\n s_cmp_gt_i32 %[nm], %[n2]\n s_cbranch_scc1 20f\n s_mov_b64 exec, 1\n ds_add_u32 %[ma], %[one]\n s_mov_b64 exec, -1\n s_add_i32 %[nm], %[nm], 1\n 20:\n .endif\n"
#define KV_W(o, k) "s_waitcnt vmcnt(%[wm])\n" KV_POST(k) "global_load_lds_dwordx4 %[vo], %[p] offset:" #o " nt\n"
#define KV_M "s_add_u32 m0, m0, %[ms]\n"
    asm volatile("s_mov_b32 m0, %[m]\n" KV_W(0, 4) KV_M KV_W(256, 5) KV_M KV_W(512, 6) KV_M KV_W(768, 7)
                 : [nm] "+s"(nm)
                 : [m] "s"(m), [vo] "v"(vl + g0 + ((lw + n) & 3) * 4096), [p] "s"(ckv), [ma] "v"(kv_mark(nm, skip)),
                   [one] "v"(1u), [n2] "s"(__builtin_amdgcn_readfirstlane(n - (W > 4 ? 2 : 1))), [wm] "n"(W - 1), [ms] "n"(PS - 256)
                 : "memory", "scc");
    g0 = g1;
    g1 += 192 * 16384;
    m += n + 1 == skip ? 8192 : 4096;
  }
}
template <int W, int W0> DI void kv_load(const u16* ckv, u32 vl, int lw, u32 g0, u32 g1, u32 m, int n0, int n1, int& nm, int skip) {
  if (W != W0 && skip == 4) kv_load1<W0>(ckv, vl, lw, g0, g1, m, n0, n1, nm, skip); else kv_load1<W>(ckv, vl, lw, g0, g1, m, n0, n1, nm, skip);
}
DI void until(u32 t0, u32 dt) {
  while ((u32)__builtin_amdgcn_s_memrealtime() - t0 < dt) __builtin_amdgcn_s_sleep(2);
}
DI bf16x8 tr_frag(unsigned char* lds, u32 a0, u32 a1) {
  const s16x4 b0 = __builtin_amdgcn_ds_read_tr16_b64_v4i16((LAS s16x4*)(lds + a0));
  const s16x4 b1 = __builtin_amdgcn_ds_read_tr16_b64_v4i16((LAS s16x4*)(lds + a1));
  bf16x8 v;
  v[0] = b0[0]; v[1] = b0[1]; v[2] = b0[2]; v[3] = b0[3]; v[4] = b1[0]; v[5] = b1[1]; v[6] = b1[2]; v[7] = b1[3];
  return v;
}
DI float sum4(f32x4 v) { const f32x2 t = f32x2{v.x, v.y} + f32x2{v.z, v.w}; return t.x + t.y; }

DI void ln_stats(u32x4 yv, float& mb, float& rsd) {
  const float a1 = dot2(yv.x, 0x3F803F80u, dot2(yv.y, 0x3F803F80u, 0.f)) + dot2(yv.z, 0x3F803F80u, dot2(yv.w, 0x3F803F80u, 0.f));
  const float a2 = dot2(yv.x, yv.x, dot2(yv.y, yv.y, 0.f)) + dot2(yv.z, yv.z, dot2(yv.w, yv.w, 0.f));
  const float sr = x16_sum(row16_sum(rs32(a1, a2))) * (1.f / 512.f);
  const float s1 = __uint_as_float(__builtin_amdgcn_readlane(__float_as_uint(sr), 0)), s2 = __uint_as_float(__builtin_amdgcn_readlane(__float_as_uint(sr), 32));
  const float var = rbf(fmaxf(s2 - s1 * s1, 0.f));
  mb = rbf(s1);
  const float sd = rbf(__builtin_amdgcn_sqrtf(rbf(var + 1e-5f)));
  rsd = __builtin_amdgcn_rcpf(sd);
  rsd = rsd * (2.f - sd * rsd);
}

__global__ __launch_bounds__(512, 2) void k_main(const u16* __restrict__ x, const u16* __restrict__ ckv,
                                                 const u16* __restrict__ w_q, const u16* __restrict__ w_uk,
                                                 const u16* __restrict__ w_uv, const u16* __restrict__ w_o,
                                                 const u16* __restrict__ w_gate, const u16* __restrict__ w_up,
                                                 const u16* __restrict__ w_down, const u16* __restrict__ cvec,
                                                 const u16* __restrict__ w_mod, u16* __restrict__ out,
                                                 unsigned char* ws) {
  __shared__ __attribute__((aligned(16))) unsigned char lds[163840];
  const int w = __builtin_amdgcn_readfirstlane(threadIdx.x >> 6), j = blockIdx.x;
  int lane = threadIdx.x & 63;
  const int xc = j & 7, ii = j >> 3;
  STAMP_DECL;
  STAMP(0);
  u32x2 ev;
  u32 xm, vh;
  {
    u64 p, pc, pw;
    u32 a, b, c, d, f, t;
    asm volatile("s_lshr_b32 %[f], %[j], 6\n s_cmp_eq_u32 %[f], 1\n s_cselect_b32 %[a], 0x50, 16\n"
                 "s_load_dwordx2 %[p], %[ka], %[a]\n s_load_dwordx2 %[pw], %[ka], 0x60\n"
                 "s_lshr_b32 %[d], %[j], 3\n s_cmp_lg_u32 %[w], 7\n s_cbranch_scc1 2f\n"
                 "s_and_b32 %[a], %[d], 15\n s_lshl_b32 %[a], %[a], 12\n v_lshl_add_u32 %[t], %[lane], 6, %[a]\n v_min_u32 %[t], %[pfl], %[t]\n"
                 "s_getpc_b64 %[pc]\n s_mov_b32 m0, %[dump]\n s_nop 0\n global_load_lds_dword %[t], %[pc]\n"
                 "2: s_load_dwordx2 %[pc], %[ka], 0x48\n s_and_b32 %[c], %[j], 7\n s_lshl_b32 %[c], %[c], 10\n s_cmp_eq_u32 %[f], 0\n s_cbranch_scc0 3f\n"
                 "s_and_b32 %[a], %[d], 1\n s_lshl1_add_u32 %[a], %[w], %[a]\n s_lshl_b32 %[a], %[a], 18\n s_add_u32 %[c], %[c], %[a]\n"
                 "s_lshr_b32 %[a], %[d], 1\n s_lshl_b32 %[a], %[a], 8\n s_add_u32 %[c], %[c], %[a]\n s_lshl_b32 m0, %[w], 14\n"
                 "v_lshrrev_b32 %[t], 4, %[lane]\n v_mul_u32_u24 %[t], 0x204, %[t]\n v_and_b32 %[vh], 15, %[lane]\n v_xor_b32 %[vh], %[vh], %[t]\n"
                 "v_lshl_add_u32 %[vh], %[vh], 4, %[c]\n v_xor_b32 %[t], 0x8010, %[vh]\n s_waitcnt lgkmcnt(0)\n"
                 "global_load_lds_dwordx4 %[vh], %[p]\n s_add_u32 m0, m0, 0x400\n s_nop 0\n"
                 "global_load_lds_dwordx4 %[t], %[p]\n s_add_u32 m0, m0, 0x400\n v_xor_b32 %[t], 0x10020, %[vh]\n"
                 "global_load_lds_dwordx4 %[t], %[p]\n s_add_u32 m0, m0, 0x400\n v_xor_b32 %[t], 0x18030, %[vh]\n"
                 "global_load_lds_dwordx4 %[t], %[p]\n s_branch 1f\n"
                 "3: s_lshl2_add_u32 %[d], %[d], %[w]\n s_sub_u32 %[d], %[d], 68\n s_lshl_b32 %[d], %[d], 16\n s_add_u32 %[d], %[d], %[c]\n"
                 "s_and_b32 %[a], %[j], 3\n s_lshl2_add_u32 %[b], %[a], %[w]\n s_mul_i32 %[b], %[b], 0x30000\n s_lshl_b32 %[a], %[a], 8\n"
                 "v_lshl_add_u32 %[xm], %[lane], 2, %[a]\n"
                 "s_and_b32 %[a], %[j], 60\n s_lshl_b32 %[a], %[a], 5\n s_add_u32 %[b], %[b], %[a]\n"
                 "s_lshl_b32 %[c], %[w], 12\n s_add_u32 %[c], %[c], %[mq]\n s_lshl_b32 %[a], %[w], 10\n s_add_u32 %[a], %[a], %[mh]\n"
                 "s_cmp_eq_u32 %[f], 1\n s_cselect_b32 %[d], %[b], %[d]\n s_cselect_b32 m0, %[c], %[a]\n s_cselect_b32 %[a], 48, 1\n s_cselect_b32 %[c], 0, 4\n"
                 "v_lshrrev_b32 %[t], 3, %[lane]\n v_mul_u32_u24 %[t], %[a], %[t]\n v_and_b32 %[vh], 7, %[lane]\n v_lshl_add_u32 %[vh], %[t], 3, %[vh]\n"
                 "v_lshl_add_u32 %[vh], %[vh], 4, %[d]\n"
                 "s_cselect_b32 %[b], 6, 1\n s_cselect_b32 %[a], 0x400, 0\n s_lshl_b32 %[b], %[b], 13\n"
                 "s_xor_b32 %[c], %[c], %[w]\n s_bitcmp1_b32 %[c], 2\n v_mov_b32 %[t], 0\n"
                 "s_waitcnt lgkmcnt(0)\n s_cbranch_scc1 1f\n s_cmp_eq_u32 %[f], 1\n s_cbranch_scc0 4f\n"
                 "global_load_dword %[xm], %[xm], %[pc]\n global_load_dwordx2 %[ev], %[t], %[pw] sc1\n"
                 "4: global_load_lds_dwordx4 %[vh], %[p]\n s_add_u32 m0, m0, %[a]\n v_add_u32 %[t], %[b], %[vh]\n"
                 "global_load_lds_dwordx4 %[t], %[p]\n s_add_u32 m0, m0, %[a]\n v_add_u32 %[t], %[b], %[t]\n"
                 "global_load_lds_dwordx4 %[t], %[p]\n s_add_u32 m0, m0, %[a]\n v_add_u32 %[t], %[b], %[t]\n"
                 "global_load_lds_dwordx4 %[t], %[p]\n"
                 "1:\n"
                 : [xm] "=&v"(xm), [ev] "=&v"(ev), [vh] "=&v"(vh), [t] "=&v"(t), [p] "=&s"(p), [pc] "=&s"(pc), [pw] "=&s"(pw), [a] "=&s"(a), [b] "=&s"(b), [c] "=&s"(c),
                   [d] "=&s"(d), [f] "=&s"(f)
                 : [j] "s"(j), [w] "s"(w), [lane] "v"(lane), [ka] "s"(__builtin_amdgcn_kernarg_segment_ptr()), [pfl] "n"(PF_LEN), [dump] "n"(L_DUMP),
                   [mq] "n"(L_QOFF + 8192), [mh] "n"(L_P)
                 : "memory", "scc");
  }
  SB();
  const u32 t0 = (u32)__builtin_amdgcn_s_memrealtime() - 35u;
  const bool isQ = j < 64, isS0 = j >= 64 && j < 128;
  const int hq = xc * 4 + (ii >> 1), dh = ii & 1, sj = j - 64;
  const int lw = w - 4, myh = xc >> 2;
  int col = lane & 15, g4 = lane >> 4, tq = col >> 2, tp = lane & 3;
  const rsrc_t rs = __builtin_amdgcn_make_buffer_rsrc((void*)ws, 0, WS_TOTAL, 0x00020000);
  volatile LAS u32* ctr = (volatile LAS u32*)(lds + L_CTR);
  SB();
  STAMP(25);
  if (w < 2) *(u32*)(lds + (w == 0 ? L_CTR + lane * 4 : L_MX + (lane & 31) * 4)) = w == 0 ? 0u : 0xFF800000u;
  BAR();
  const bool tl2 = isS0;
  const int t3 = xc < 4 ? (ii < 8 ? xc * 8 + ii : -1) : (ii < 6 ? 8 + xc * 6 + ii : -1);
  const int t4 = (ii >= 8 && ii < 10) ? xc * 2 + (ii - 8) : -1;
  const u32 owno = (w >> 1) * 32768 + 8192 + (w & 1) * 12288;
  const int ks = (xc & 3) * 2 + myh, cg = ii & 7;
  const int h = (j >> 2) & 31, c4 = j & 3;
  u32x4 wv[8];
  u32 tlo, thi;
  int gx;
#define RELANE() do { asm volatile("v_mbcnt_lo_u32_b32 %0, -1, 0\n v_mbcnt_hi_u32_b32 %0, -1, %0" : "=v"(lane)); col = lane & 15; g4 = lane >> 4; tq = col >> 2; tp = lane & 3; } while (0)
#define TAG_OF(e0, e1)                                                                                                  \
  do {                                                                                                                  \
    const u64 tag = (((u64)(e1) << 32) | (e0)) + 1ull;                                                                  \
    tlo = __builtin_amdgcn_readfirstlane((u32)tag); thi = __builtin_amdgcn_readfirstlane((u32)(tag >> 32));             \
  } while (0)
  const u32x4 P4 = {POISON, POISON, POISON, POISON};
#define COUNT_VOTES(PRE)                                                                                                \
  do {                                                                                                                  \
    u32x2 a_ = {0u, 0u};                                                                                                \
    if (PRE) a_ = *(const u32x2*)(lds + L_QI + lane * 8);                                                               \
    _Pragma("unroll 1") for (u32 spins = 0; spins < SPIN_CAP; ++spins) {                                                \
      if (((PRE) || spins) && __all(gran_ok(a_.x, a_.y, tlo, thi))) break;                                              \
      if (spins) __builtin_amdgcn_s_sleep(8);                                                                           \
      asm volatile("" ::: "memory");                                                                                    \
      a_ = ld64(rs, WS_VOTE + xc * 2048 + lane * 8);                                                                    \
    }                                                                                                                   \
    const float vt_ = wave_sum((float)(a_.x & 1023u));                                                                  \
    if (lane == 0) ctr[C_PAR] = G_OF(vt_);                                                                              \
    VOTE_STAMP(vt_);                                                                                                    \
    STAMP(8);                                                                                                           \
  } while (0)
#define S_POLL(a_)                                                                                                      \
  _Pragma("unroll 1") for (u32 spins = 0; spins < SPIN_CAP && RARE(__any(((a_.x ^ tlo) | (a_.y ^ thi)) != 0u)); ++spins) { \
    __builtin_amdgcn_s_sleep(8);                                                                                        \
    asm volatile("" ::: "memory");                                                                                      \
    a_ = ld64(rs, WS_SFLAG + xc * 2048 + lane * 8);                                                                     \
  }
#define MB_OFF(i) (WS_MODB + ((lane >> 4) * 2048 + ((i) >> 1) * 512 + 128 * lw + 64 * ((i) & 1) + (lane & 15) * 4) * 4)

  if (!isQ && w >= 4) {
    if (isS0) {
      if (w == 4) {
        const u32x2 e4 = ld64(rs, WS_EPOCH);
        st128(rs, WS_QABS + sj * 1024 + lane * 16, P4);
        st128(rs, lane < 32 ? WS_MODB + sj * 512 + lane * 16 : WS_TOTAL, P4);
        st128(rs, WS_PROBE + il(sj * 1024 + lane * 16, 0), Z4);
        st128(rs, WS_PROBE + il(sj * 1024 + lane * 16, 1), Z4);
        waitvm<0>();
        TAG_OF(e4.x, e4.y);
        if (lane < 8) st64(rs, WS_SFLAG + lane * 2048 + sj * 8, tlo, thi);
        STAMP(27);
      }
      lds_wait(ctr + C_H1, 4u);
    } else {
#pragma unroll
      for (int i = 4; i < 16; ++i) { waitvm<7>(); dma<false>(L_P + w * 1024, vh + (i & 7) * 8192, i < 8 ? w_q : w_uk); }
    }
    const u32 vl = g4 * 1024 + ((col ^ ((g4 << 2) | lw)) << 4);
    int nm = 0, ns = isS0 ? 6 : 7;
    const int skip = isS0 ? 4 : 99;
    kv_load<WIN, WA0>(ckv, vl, lw, (isS0 ? sj : 1792 + j) * 16384, (isS0 ? sj + 192 : sj) * 16384, lw * 1024, 0, 2, nm, skip);
    const rsrc_t rw = __builtin_amdgcn_make_buffer_rsrc((void*)(isS0 ? w_o : w_uv), 0, 4194304, 0x00020000);
    int wo = isS0 ? (ks * 512 + 64 * w + (lane >> 3)) * 1024 + cg * 128 + (lane & 7) * 16 : (c4 * 128 + 32 * lw + g4) * 8192 + h * 256 + col * 16;
    const int wst = isS0 ? 8192 : 32768;
    PIN(wo);
#define WV_LD(i) do { SB(); wv[i] = __builtin_amdgcn_raw_buffer_load_b128(rw, wo, (i) * wst, 2); wv[(i) + 1] = __builtin_amdgcn_raw_buffer_load_b128(rw, wo, ((i) + 1) * wst, 2); SB(); } while (0)
    u32 va[8];
#pragma unroll
    for (int i = 0; i < 8; ++i) { va[i] = lw * PS + tr_addr(lane, i >> 1, 0, i & 1); PIN(va[i]); }
    int eg[8];
#pragma unroll
    for (int u = 0; u < 8; ++u) { eg[u] = WS_PART + il21(((4 * (u >> 1) + 2 * (g4 >> 1) + (u & 1)) * 4 + lw) * 65536 + sj * 256 + col * 16, g4 & 1); PIN(eg[u]); }
    u32 pr_ = L_P + lane * 16;
    PIN(pr_);
    u32 ml = L_CTR + 4 * (C_MK + 7), one = 1u;
    PIN(ml); PIN(one);
    bf16x8 vf[4][4];
#define V_READ(i, slot) do { asm volatile("" ::: "memory"); SB(); _Pragma("unroll") for (int c = 0; c < 4; ++c) vf[i][c] = tr_frag(lds, va[2 * c] + (slot) * 4096, va[2 * c + 1] + (slot) * 4096); SB(); } while (0)
    SB();
    kv_load<WIN, WB0>(ckv, vl, lw, (sj + (isS0 ? 384 : 192)) * 16384, (sj + (isS0 ? 576 : 384)) * 16384, lw * 1024 + 8192, 2, ns, nm, skip);
    lds_wait(ctr + C_MK + 3, 4u);
    V_READ(0, 0); V_READ(1, 1); V_READ(2, 2); V_READ(3, 3);
    lds_add(C_FREE);
#pragma unroll
    for (int i = 0; i < 16; ++i) PIN(vf[i >> 2][i & 3]);
    STAMP(2);
    lds_wait(ctr + C_FREE, 8u);
    STAMP(5);
    kv_load<WIN, WB0>(ckv, vl, lw, (sj + 6 * 192) * 16384, (sj + 7 * 192) * 16384, lw * 1024, ns, ns + 4, nm, skip);
    int k4 = 4;
    STAMP(3);
    f32x16 acc[4];
#pragma unroll
    for (int c = 0; c < 4; ++c)
#pragma unroll
      for (int i = 0; i < 16; ++i) acc[c][i] = 0.f;
    bf16x8 pf[4], pn[4];
    u32 c_;
#define P_SPIN(g, n) c_ = spin_try<(g) ? 8 : 0>(C_P + (g) - C_MK - 7, n, k4, ml, one KV_TSP)
#define TRY() kv_try<8>(k4, ml, one KV_TSP)
#define P_READ(d, i0, i1, T0) _Pragma("unroll") for (int i = (i0); i < (i1); ++i) d[i] = *(volatile LAS bf16x8*)(lds + pr_ + ((T0) + i) * 1024)
#define PV(p, i) do { SB(); _Pragma("unroll") for (int c = 0; c < 4; ++c) acc[c] = __builtin_amdgcn_mfma_f32_32x32x16_bf16(p[i], vf[i][c], acc[c], 0, 0, 0); SB(); } while (0)
#define GX_USE(v) do { gx = __builtin_amdgcn_readfirstlane(v); _Pragma("unroll") for (int u = 0; u < 8; ++u) { eg[u] ^= gx; PIN(eg[u]); } } while (0)
#define MK_WAIT(T) spin_try<8>((T) - 7, 4u, k4, ml, one KV_TSP)
#define PVV(p, i, d, slot)                                                                                               \
  _Pragma("unroll") for (int c = 0; c < 4; ++c) {                                                                        \
    SB();                                                                                                                \
    acc[c] = __builtin_amdgcn_mfma_f32_32x32x16_bf16(p[i], vf[i][c], acc[c], 0, 0, 0);                                   \
    SB();                                                                                                                \
    vf[d][c] = tr_frag(lds, va[2 * c] + (slot) * 4096, va[2 * c + 1] + (slot) * 4096);                                   \
    SB();                                                                                                                \
  }
#define CT(i) (*(volatile LAS u32*)(lds + ml + 4 * ((i) - C_MK - 7)))
    P_SPIN(0, 4u);
    P_READ(pf, 0, 4, 0);
    const u32 k7 = CT(C_MK + 7);
    asm volatile("" ::: "memory");
    STAMP(6);
    PVV(pf, 0, 0, 4); kv_try<0>(k4, ml, one KV_TSP); WV_LD(0);
    PVV(pf, 1, 1, 5); kv_try<2>(k4, ml, one KV_TSP); WV_LD(2);
    PVV(pf, 2, 2, 6); WV_LD(4);
    u32 cv = CT(C_P + 1);
    P_READ(pn, 0, 4, 4);
    PVV(pf, 3, 3, 0); WV_LD(6);
    if (RARE(__builtin_amdgcn_readfirstlane(k7) < 4u)) { MK_WAIT(7); V_READ(0, 4); V_READ(1, 5); V_READ(2, 6); V_READ(3, 0); }
    STAMP(9);
    c_ = __builtin_amdgcn_readfirstlane(cv);
    if (RARE((c_ & 255u) != 4u)) { P_SPIN(1, 4u); P_READ(pn, 0, 4, 4); }
    int slow = -1;
    if (RARE(c_ > 255u)) slow = 4;
    else {
      if (!isS0) PV(pn, 0);
      TRY();
      const u32 k9 = CT(C_MK + 9);
      asm volatile("" ::: "memory");
      PVV(pn, 1, 0, 1); TRY();
      PVV(pn, 2, 1, 2);
      cv = CT(C_P + 2);
      P_READ(pf, 0, 2, 8);
      const u32 k10 = CT(C_MK + 10);
      asm volatile("" ::: "memory");
      PVV(pn, 3, 2, 3);
      if (RARE(__builtin_amdgcn_readfirstlane(k9) < 4u)) { MK_WAIT(9); V_READ(0, 1); V_READ(1, 2); }
      STAMP(10);
      c_ = __builtin_amdgcn_readfirstlane(cv);
      if (RARE((c_ & 255u) != 4u)) { P_SPIN(2, 4u); P_READ(pf, 0, 2, 8); }
      if (RARE(c_ > 255u)) slow = 8;
      else {
        TRY();
        PV(pf, 0);
        const u32 g0 = ctr[C_GX];
        u32 g1 = ctr[C_PAR];
        cv = CT(C_P + 3);
        P_READ(pf, 2, 3, 8);
        PV(pf, 1);
        STAMP(4);
        if (RARE(__builtin_amdgcn_readfirstlane(k10) < 4u)) { waitvm<8>(); TRY(); lds_spin(ctr + C_MK + 10, 4u); V_READ(2, 3); }
        if (RARE(__builtin_amdgcn_readfirstlane(g0) == 0u)) { lds_spin(ctr + C_GX, 1u); g1 = ctr[C_PAR]; }
        GX_USE(g1);
        STAMP(11);
        c_ = __builtin_amdgcn_readfirstlane(cv);
        _Pragma("unroll 1") for (u32 n_ = 0; RARE((c_ & 255u) != 2u) && n_ < LSPIN_CAP; ++n_) { cv = CT(C_P + 3); P_READ(pf, 2, 3, 8); c_ = __builtin_amdgcn_readfirstlane(cv); }
        if (RARE(c_ > 255u)) slow = 10;
        else PV(pf, 2);
      }
    }
    if (RARE(slow >= 0)) {
      MK_WAIT(10);
      float R = ceilf(*(const float*)(lds + L_MX + (lane & 31) * 4));
#pragma unroll 1
      for (int T = slow; T < 11; ++T) {
        if (T == 4 && isS0) continue;
        spin_at(L_CTR + 4 * (C_P + (T < 8 ? 1 : T < 10 ? 2 : 3)), T < 10 ? 4u : 2u);
        const float rf = *(const float*)(lds + L_REF + T * 128 + (lane & 31) * 4);
        u32x4 p = *(const u32x4*)(lds + pr_ + T * 1024);
        const u32 so_ = (T < 7 ? T : T - 7) * 4096;
#pragma unroll
        for (int c = 0; c < 4; ++c) vf[0][c] = tr_frag(lds, va[2 * c] + so_, va[2 * c + 1] + so_);
        const float Rn = fmaxf(R, rf), ps = __builtin_amdgcn_exp2f(rf - Rn);
        *(float*)(lds + L_ALW + lw * 128 + (lane & 31) * 4) = __builtin_amdgcn_exp2f(R - Rn);
        R = Rn;
        WAVE_LDS_SYNC();
#pragma unroll
        for (int i = 0; i < 4; ++i) {
          const f32x4 al = *(const f32x4*)(lds + L_ALW + lw * 128 + (lane >> 5) * 16 + i * 32);
#pragma unroll
          for (int c = 0; c < 4; ++c)
#pragma unroll
            for (int r = 0; r < 4; ++r) acc[c][4 * i + r] *= al[r];
          p[i] = pk2bf(lo16(p[i]) * ps, hi16(p[i]) * ps);
        }
        WAVE_LDS_SYNC();
        pf[0] = __builtin_bit_cast(bf16x8, p);
        PV(pf, 0);
      }
      if (slow != 10) { lds_spin(ctr + C_GX, 1u); GX_USE(ctr[C_PAR]); }
    }
    STAMP(30);
#pragma unroll
    for (int u = 0; u < 8; u += 2) {
      u32 c[8];
#pragma unroll
      for (int k = 0; k < 8; ++k) c[k] = pk2bf(acc[k & 2][2 * u + (k & 1) + (k >> 2) * 2], acc[(k & 2) + 1][2 * u + (k & 1) + (k >> 2) * 2]);
      SB();
#pragma unroll
      for (int k = 0; k < 2; ++k) {
        const auto r0 = __builtin_amdgcn_permlane16_swap(c[4 * k], c[4 * k + 1], false, false);
        const auto r1 = __builtin_amdgcn_permlane16_swap(c[4 * k + 2], c[4 * k + 3], false, false);
        st128(rs, eg[u + k], u32x4{r0[0], r1[0], r0[1], r1[1]});
      }
      SB();
    }
    STAMP(31);
    if (!isS0) {
#pragma unroll
      for (int i = 0; i < 8; ++i) *(u32x4*)(lds + lw * PS + 16384 + off_b(4 * i + g4, col)) = wv[i];
    }
    {
      int fo = lane < 16 ? (WS_LSE + il(sj * 512 + lw * 128 + (lane & 7) * 16, lane >> 3)) ^ gx : WS_TOTAL;
      lds_spin(ctr + C_ML, 1u);
      u32x4 ml4 = *(const u32x4*)(lds + L_WMAX + col * 16);
      PIN(fo); PIN(ml4);
      waitvm<0>();
      __builtin_amdgcn_s_waitcnt(0x0F70);
      st128(rs, fo, ml4);
      lds_add(C_PD);
    }
    STAMP(13);
  } else if (!isQ) {
    if (isS0) {
      u32 ta = L_QOFF + 8192 + (8 * g4 + tq) * 128 + w * 32 + tp * 8;
      u32 sa = L_H1RED + w * 256;
      int so = col < 2 ? WS_SC + (((sj >> 5) * 4 + (sj & 3)) * 512 + ((sj >> 2) & 7) * 64 + 16 * w + 4 * g4 + 2 * col) * 8 : WS_TOTAL;
      PIN(ta); PIN(so); PIN(sa);
      u32 c2, e0, e1;
      asm volatile("s_waitcnt vmcnt(4) ; HEAD_WAIT\n v_mov_b32 %0, %3\n v_mov_b32 %1, %4\n v_mov_b32 %2, %5" : "=&v"(c2), "=&v"(e0), "=&v"(e1) : "v"(xm), "v"(ev.x), "v"(ev.y) : "memory");
      TAG_OF(e0, e1);
      bf16x8 sf[4];
      u32x2 wt[8];
      asm volatile("ds_write_b32 %4, %5\n"
                   "ds_read_b128 %0, %6\n ds_read_b128 %1, %6 offset:64\n ds_read_b128 %2, %6 offset:128\n ds_read_b128 %3, %6 offset:192\n s_waitcnt lgkmcnt(0)"
                   : "=&v"(sf[0]), "=&v"(sf[1]), "=&v"(sf[2]), "=&v"(sf[3])
                   : "v"(sa + lane * 4), "v"(pk2bf(silu_bf(lo16(c2)), silu_bf(hi16(c2)))), "v"(sa + 16 * g4) : "memory");
      waitvm<0>();
      lds_add(C_BH);
      lds_spin(ctr + C_BH, 4u);
      STAMP(1);
      asm volatile("ds_read_b64_tr_b16 %0, %8\n ds_read_b64_tr_b16 %1, %8 offset:512\n ds_read_b64_tr_b16 %2, %8 offset:4096\n ds_read_b64_tr_b16 %3, %8 offset:4608\n"
                   "ds_read_b64_tr_b16 %4, %8 offset:8192\n ds_read_b64_tr_b16 %5, %8 offset:8704\n ds_read_b64_tr_b16 %6, %8 offset:12288\n ds_read_b64_tr_b16 %7, %8 offset:12800\n"
                   "s_waitcnt lgkmcnt(0)"
                   : "=&v"(wt[0]), "=&v"(wt[1]), "=&v"(wt[2]), "=&v"(wt[3]), "=&v"(wt[4]), "=&v"(wt[5]), "=&v"(wt[6]), "=&v"(wt[7]) : "v"(ta) : "memory");
      f32x4 d = {0.f, 0.f, 0.f, 0.f};
#pragma unroll
      for (int k = 0; k < 4; ++k)
        d = __builtin_amdgcn_mfma_f32_16x16x32_bf16(__builtin_bit_cast(bf16x8, u32x4{wt[2 * k].x, wt[2 * k].y, wt[2 * k + 1].x, wt[2 * k + 1].y}), sf[k], d, 0, 0, 0);
      st_gran2(rs, so, (col & 1) ? d[2] : d[0], (col & 1) ? d[3] : d[1], tlo, thi);
      lds_add(C_H1);
      STAMP(2);
      until(t0, SCP_T);
      dma_sc1(L_P + w * 1024, WS_SC + ((ii & 7) * 4 + w) * 1024 + lane * 16, ws);
    }
    u32 ab = off_b(col, 4 * g4), qv = WS_QABS + myh * 8192 + g4 * 1024 + w * 256 + ((col ^ (g4 << 2)) << 4);
    const int hb = 16 * (w & 1) + col;
    u32 pw = L_P + (g4 >> 1) * 512 + hb * 16 + (g4 & 1) * 8, mw = hb * 4;
    PIN(ab); PIN(qv); PIN(pw); PIN(mw);
    if (w == 2) {
      until(t0, QF_T);
      const u32x2 e2 = ld64(rs, WS_EPOCH);
      u32x2 f2 = ld64(rs, WS_SFLAG + xc * 2048 + lane * 8);
      TAG_OF(e2.x, e2.y);
      S_POLL(f2);
      lds_add(C_SOK);
      STAMP(3);
    }
    bf16x8 af[16], qf[16][2];
    u32 aa[16], qa[32];
#pragma unroll
    for (int i = 0; i < 16; ++i) {
      aa[i] = (ab ^ ((i & 3) << 4)) + (((w + (i >> 2)) & 3) * PS + w * 4096); PIN(aa[i]);
#pragma unroll
      for (int u = 0; u < 2; ++u) { qa[2 * i + u] = (ab ^ ((i & 3) << 4)) + (L_QI + ((w + (i >> 2)) & 3) * 8192 + ((u ^ w) & 1) * 4096); PIN(qa[2 * i + u]); }
    }
    const int sl1 = w == 3 ? 0 : 4 + w, sl2 = 1 + (w >> 1);
#define A_RD(i, o) af[i] = *(const bf16x8*)(lds + aa[i] + (o))
#define A_READ(i0, i1, o) _Pragma("unroll") for (int i = (i0); i < (i1); ++i) A_RD(i, o)
#define A_MOVE(i0, i1, d) _Pragma("unroll") for (int i = (i0); i < (i1); ++i) { aa[i] += (d) * 4096; PIN(aa[i]); }
#define Q_RD(i, u) qf[i][u] = *(const bf16x8*)(lds + qa[2 * (i) + (u)])
#define A_FIRST() do { A_READ(0, 16, 0); lds_add(C_FREE); _Pragma("unroll") for (int i = 0; i < 16; ++i) PIN(af[i]); STAMP(12); } while (0)
    bool have = false;
    _Pragma("unroll 1") while ((u32)__builtin_amdgcn_s_memrealtime() - t0 < E3_T) {
      if (__builtin_amdgcn_readfirstlane(ctr[C_MK + w]) >= 4u) { have = true; break; }
      __builtin_amdgcn_s_sleep(1);
    }
    asm volatile("" ::: "memory");
    if (have) A_FIRST();
    lds_wait(ctr + C_SOK, 1u);
    if (isS0) lds_wait(ctr + C_H1, 4u);
    until(t0, E3_T);
    STAMP(6);
#define Q_DMA() _Pragma("unroll") for (int i = 0; i < 8; ++i) dma_sc1(L_QI + w * 8192 + i * 1024, (qv ^ ((i & 3) << 4)) + (i >> 1) * 16384 + (i & 1) * 4096, ws)
    Q_DMA();
    if (RARE(!have)) { lds_wait(ctr + C_MK + w, 4u); A_FIRST(); }
    A_MOVE(0, 16, sl1 - w);
    u32 cb = L_CTR, cw = L_CTR + 4 * w, mx0 = mw + L_MX, mx1 = (mw ^ 64) + L_MX, pw0 = pw + w * 1024, pw1 = (pw ^ 256) + w * 1024;
    float ninf = -__builtin_inff();
    u32 one = 1u;
    PIN(ninf); PIN(one); PIN(cb); PIN(cw); PIN(mx0); PIN(mx1); PIN(pw0); PIN(pw1);
#define CTS(b, i) (*(volatile LAS u32*)(lds + (b) + 4 * (i)))
    _Pragma("unroll 1") for (u32 spins = 0; spins < SPIN_CAP; ++spins) {
      waitvm<0>();
#pragma unroll
      for (int i = 0; i < 8; ++i) Q_RD(i >> 1, i & 1);
      u32 mxu[2] = {0u, 0u};
#pragma unroll
      for (int i = 0; i < 8; ++i) { const u32x4 t_ = __builtin_bit_cast(u32x4, qf[i >> 1][i & 1]); mxu[i & 1] = max(max(mxu[i & 1], t_.x), t_.y); mxu[i & 1] = max(max(mxu[i & 1], t_.z), t_.w); }
      if (!RARE(__any(max(mxu[0], mxu[1]) == POISON))) break;
      STAMP(29);
      __builtin_amdgcn_s_sleep(2);
      const u64 b0 = __ballot(mxu[w & 1] == POISON), b1 = __ballot(mxu[~w & 1] == POISON);
#pragma unroll 1
      for (int i = 0; i < 8; ++i)
        if (((i < 4 ? b0 : b1) >> (4 * (i & 3))) & 0x000F000F000F000Full) dma_sc1(L_QI + w * 8192 + i * 1024, (qv ^ ((i & 3) << 4)) + (i >> 1) * 16384 + (i & 1) * 4096, ws);
    }
    lds_add(C_QRDY);
    STAMP(7);
    f32x4 sc[2], sn[2];
    sc[0] = sc[1] = sn[0] = sn[1] = f32x4{0.f, 0.f, 0.f, 0.f};
#define QK(d, i0, i1, NU)                                                                                                \
  do {                                                                                                                   \
    SB();                                                                                                                \
    _Pragma("unroll") for (int i = (i0); i < (i1); ++i) _Pragma("unroll") for (int u = 0; u < (NU); ++u)                 \
      d[u] = __builtin_amdgcn_mfma_f32_16x16x32_bf16(af[i], qf[i][u], d[u], 0, 0, 0);                                    \
    SB();                                                                                                                \
  } while (0)
#define MF(d, i, u) do { SB(); d[u] = __builtin_amdgcn_mfma_f32_16x16x32_bf16(af[i], qf[i][u], d[u], 0, 0, 0); SB(); } while (0)
    const bool idle1 = isS0 && w == 0;
    u32 c1;
    {
      const u32 c0 = CTS(cb, C_QRDY);
      QK(sc, 0, 2, 2);
      if (RARE(__builtin_amdgcn_readfirstlane(c0) < 4u)) lds_spin(ctr + C_QRDY, 4u);
      asm volatile("" ::: "memory");
#pragma unroll
      for (int i = 2; i < 4; ++i) { MF(sc, i, 0); Q_RD(2 * i, 0); Q_RD(2 * i, 1); MF(sc, i, 1); Q_RD(2 * i + 1, 0); Q_RD(2 * i + 1, 1); }
#pragma unroll
      for (int i = 4; i < 12; ++i) { MF(sc, i, 0); Q_RD(i + 4, 0); MF(sc, i, 1); Q_RD(i + 4, 1); }
      c1 = CTS(cw, C_MK + 4);
      asm volatile("" ::: "memory");
#pragma unroll
      for (int i = 12; i < 16; ++i) { MF(sc, i, 0); A_RD(2 * i - 24, 0); MF(sc, i, 1); A_RD(2 * i - 23, 0); }
    }
    STAMP(9);
    float mref[2], lsum[2] = {0.f, 0.f};
#define ADDC(i, v) do { SB(); asm volatile("s_mov_b64 exec, 1\n ds_add_u32 %0, %1 offset:%2\n s_mov_b64 exec, -1" ::"v"(cb), "v"(v), "n"(4 * (i)) : "memory"); SB(); } while (0)
#define SM(d, T, NU, G, P0, P1)                                                                                          \
  do {                                                                                                                   \
    SB();                                                                                                                \
    if (RARE(__any(max5(d[0], (NU) > 1 ? max5(d[1], ninf) : ninf) > TAU))) {                                             \
      sig = 257u;                                                                                                        \
      _Pragma("unroll") for (int u = 0; u < (NU); ++u) {                                                                 \
        const float up = ceilf(x32_max(x16_max(max5(d[u], 0.f))));                                                       \
        lsum[u] *= __builtin_amdgcn_exp2f(-up);                                                                          \
        mref[u] += up;                                                                                                   \
        d[u] -= up;                                                                                                      \
        *(float*)(lds + (mw ^ (u * 64)) + L_REF + (T) * 128) = mref[u];                                                  \
      }                                                                                                                  \
    }                                                                                                                    \
    u32x2 pk[2];                                                                                                         \
    _Pragma("unroll") for (int u = 0; u < (NU); ++u) {                                                                   \
      pk[u].x = pk2bf(__builtin_amdgcn_exp2f(d[u].x), __builtin_amdgcn_exp2f(d[u].y));                                   \
      pk[u].y = pk2bf(__builtin_amdgcn_exp2f(d[u].z), __builtin_amdgcn_exp2f(d[u].w));                                   \
      *(u32x2*)(lds + (u ? (P1) : (P0))) = pk[u];                                                                        \
    }                                                                                                                    \
    ADDC(C_P + (G), sig);                                                                                                \
    _Pragma("unroll") for (int u = 0; u < (NU); ++u) { lsum[u] = dot2(pk[u].x, 0x3F803F80u, dot2(pk[u].y, 0x3F803F80u, lsum[u])); PIN(lsum[u]); } \
    SB();                                                                                                                \
  } while (0)
#define REF_WRITE(T, NU) _Pragma("unroll") for (int u = 0; u < (NU); ++u) *(float*)(lds + (mw ^ (u * 64)) + L_REF + (T) * 128) = mref[u]
    u32 sig = one;
    {
      MF(sn, 0, 0); A_RD(8, 0); MF(sn, 0, 1);
      const float m0 = max5(sc[0], ninf), m1 = max5(sc[1], ninf);
      MF(sn, 1, 0);
      asm volatile("ds_max_f32 %0, %2\n ds_max_f32 %1, %3" ::"v"(mx0), "v"(mx1), "v"(m0), "v"(m1) : "memory");
      ADDC(C_MXC, one);
      MF(sn, 1, 1); A_RD(9, 0);
#pragma unroll
      for (int i = 2; i < 4; ++i) { MF(sn, i, 0); A_RD(2 * i + 6, 0); MF(sn, i, 1); A_RD(2 * i + 7, 0); }
      MF(sn, 4, 0);
      const u32 c0 = CTS(cb, C_MXC);
      mref[0] = *(volatile LAS float*)(lds + mx0);
      mref[1] = *(volatile LAS float*)(lds + mx1);
      MF(sn, 4, 1); MF(sn, 5, 0); A_RD(14, 0); MF(sn, 5, 1); A_RD(15, 0); MF(sn, 6, 0); MF(sn, 6, 1);
      if (RARE(__builtin_amdgcn_readfirstlane(c0) < 4u)) {
        lds_spin(ctr + C_MXC, 4u);
        mref[0] = *(volatile LAS float*)(lds + mx0);
        mref[1] = *(volatile LAS float*)(lds + mx1);
      }
      u32x2 pk[2];
#pragma unroll
      for (int u = 0; u < 2; ++u) {
        MF(sn, 7 + 2 * u, 0);
        mref[u] = ceilf(mref[u]);
        float e0 = __builtin_amdgcn_exp2f(sc[u].x - mref[u]); PIN(e0);
        MF(sn, 7 + 2 * u, 1);
        pk[u].x = pk2bf(e0, __builtin_amdgcn_exp2f(sc[u].y - mref[u])); PIN(pk[u].x);
        MF(sn, 8 + 2 * u, 0);
        e0 = __builtin_amdgcn_exp2f(sc[u].z - mref[u]); PIN(e0);
        MF(sn, 8 + 2 * u, 1);
        pk[u].y = pk2bf(e0, __builtin_amdgcn_exp2f(sc[u].w - mref[u]));
        *(u32x2*)(lds + (u ? pw1 : pw0)) = pk[u];
      }
      ADDC(C_P, one);
      MF(sn, 11, 0);
      lsum[0] = dot2(pk[0].x, 0x3F803F80u, dot2(pk[0].y, 0x3F803F80u, 0.f)); PIN(lsum[0]);
      MF(sn, 11, 1);
      lsum[1] = dot2(pk[1].x, 0x3F803F80u, dot2(pk[1].y, 0x3F803F80u, 0.f)); PIN(lsum[1]);
      MF(sn, 12, 0);
      REF_WRITE(4 + w, 2);
      MF(sn, 12, 1);
      QK(sn, 13, 16, 2);
    }
    STAMP(10);
#define QK1(N0, slot)                                                                                                    \
  do {                                                                                                                   \
    _Pragma("unroll") for (int i = 0; i < 16; ++i) { MF(sc, i, 0); if (i + (N0) < 16) A_RD(i + (N0), slot); }            \
  } while (0)
    if (RARE(__builtin_amdgcn_readfirstlane(c1) < 4u && !idle1)) {
      lds_spin(ctr + C_MK + 4 + w, 4u);
      A_READ(0, 16, 0);
      sn[0] = sn[1] = f32x4{0.f, 0.f, 0.f, 0.f};
      QK(sn, 0, 16, 2);
    }
    if (RARE(idle1)) lds_add(C_P + 1);
    else { sn[0] -= mref[0]; sn[1] -= mref[1]; SM(sn, 4 + w, 2, 1, pw0 + 4096, pw1 + 4096); }
    if (RARE(w == 2)) {
      RELANE();
      dma_sc1(L_QI, WS_VOTE + xc * 2048 + lane * 16, ws);
    }
    REF_WRITE(7 + sl2, 1);
    A_MOVE(0, 16, sl2 - sl1);
    pw0 = pw + sl2 * 1024;
    sc[0] = f32x4{-mref[0], -mref[0], -mref[0], -mref[0]};
    PIN(pw0); PIN(sc[0]);
    lds_spin(ctr + C_MK + 7 + sl2, 4u);
    A_READ(0, 6, 0);
    STAMP(4);
    QK1(6, 0);
    SM(sc, 7 + sl2, 1, 2, pw0 + 7168, 0);
    STAMP(11);
    if (!RARE(w >= 2)) {
      REF_WRITE(10, 1);
      sc[0] = f32x4{-mref[0], -mref[0], -mref[0], -mref[0]};
      PIN(sc[0]);
      _Pragma("unroll 1") for (u32 n_ = 0; n_ < LSPIN_CAP; ++n_) {
        const u32 k_ = CTS(cb, C_MK + 10);
        asm volatile("" ::: "memory");
        A_READ(0, 6, 8192);
        SB();
        if (__builtin_amdgcn_readfirstlane(k_) >= 4u) break;
      }
      STAMP(5);
      QK1(6, 8192);
      SM(sc, 10, 1, 3, pw0 + 9216, 0);
    } else if (w == 2) {
      RELANE();
      waitvm<0>();
      COUNT_VOTES(true);
      lds_add(C_GX);
    }
    STAMP(30);
    RELANE();
    {
#pragma unroll
      for (int u = 0; u < 2; ++u) {
        *(float*)(lds + L_ML + (mw ^ (u * 64)) * 4 + w * 4) = mref[u];
        *(float*)(lds + L_ML + 512 + (mw ^ (u * 64)) * 4 + w * 4) = x32_sum(x16_sum(lsum[u]));
      }
      u32 old;
      f32x4 m4, l4;
      asm volatile("s_mov_b64 exec, 1\n ds_add_rtn_u32 %0, %3, %4 offset:%6\n s_mov_b64 exec, -1\n ds_read_b128 %1, %5\n ds_read_b128 %2, %5 offset:512\n s_waitcnt lgkmcnt(0)"
                   : "=&v"(old), "=&v"(m4), "=&v"(l4) : "v"((u32)L_CTR), "v"(1u), "v"(L_ML + (lane & 31) * 16), "n"(4 * C_SD) : "memory");
      if (__builtin_amdgcn_readfirstlane(old) == 3u) {
        const float M = fmaxf(fmaxf(m4.x, m4.y), fmaxf(m4.z, m4.w));
        const float l = (l4.x * __builtin_amdgcn_exp2f(m4.x - M) + l4.y * __builtin_amdgcn_exp2f(m4.y - M)) + (l4.z * __builtin_amdgcn_exp2f(m4.z - M) + l4.w * __builtin_amdgcn_exp2f(m4.w - M));
        *(u32x2*)(lds + L_WMAX + (lane & 1) * 128 + (lane & 30) * 4) = u32x2{fbits(M), fbits(l)};
        lds_add(C_ML);
      }
    }
    lds_wait(ctr + C_GX, 1u);
    gx = __builtin_amdgcn_readfirstlane(ctr[C_PAR]);
    STAMP(13);
  } else {
    const int mu = 2 * j + (w >> 2);
#define Q_LOAD(i0, i1)                                                                                                   \
  _Pragma("unroll") for (int i = (i0); i < (i1); ++i) {                                                                  \
    waitvm<QW - 1>();                                                                                                    \
    dma<false>(w * 16384 + (i & 15) * 1024, (vq ^ ((i & 3) << 4)) + ((i < 8 ? 64 * w + 32 * dh : i < 16 ? 64 * w + 32 - 32 * dh : 256 * dh + 32 * w) + 4 * (i & 7)) * 8192, i < 16 ? w_q : w_uk); \
  }
    const u32 vq = g4 * 8192 + hq * 256 + ((col ^ (g4 << 2)) << 4);
    u32x4 xv;
    u32x2 e2;
    u32 x2_;
    asm volatile("global_load_dwordx4 %0, %3, %5\n global_load_dword %1, %4, %5\n buffer_load_dwordx2 %2, off, %6, 0 sc1"
                 : "=&v"(xv), "=&v"(x2_), "=&v"(e2) : "v"(lane * 16), "v"(128 * w + 4 * (lane & 31)), "s"(x), "s"(rs) : "memory");
    bf16x8 wf[16], wk[8];
    u32 ta0 = w * 16384 + off_b(8 * g4 + tq, tp >> 1) + 8 * (tp & 1), ta1 = w * 16384 + off_b(8 * g4 + 4 + tq, tp >> 1) + 8 * (tp & 1);
    int so = WS_SC + ((lane >> 5) * 2048 + 64 * w + 2 * (lane & 31)) * 8;
    int fo = w == 0 ? WS_SFLAG + xc * 2048 + lane * 8 : WS_TOTAL;
    PIN(ta0); PIN(ta1); PIN(so); PIN(fo);
    int qpo = WS_QABS + il(hq * 1024 + dh * 512 + w * 64, 0) + (lane & 32) + (lane & 8) * 2 + (g4 & 1) * 8192 + ((lane & 7) ? WS_TOTAL : 0);
    u32 hw_ = L_H1RED + w * 128 + (lane & 31) * 4, hr_ = L_H1RED + w * 128 + 16 * g4 + 64 * dh, qw_ = L_H1RED + 1024 + w * 272 + lane * 2, qr_ = L_H1RED + 1024 + col * 272 + 64 * g4;
    PIN(qpo); PIN(hw_); PIN(hr_); PIN(qw_); PIN(qr_);
    u32 ka = w * 16384 + off_b(col, 4 * g4), hr2 = hr_ ^ 64;
    PIN(ka); PIN(hr2);
    Q_LOAD(4, 8);
    asm volatile("s_waitcnt vmcnt(4)" : "+v"(xv), "+v"(x2_), "+v"(e2)::"memory");
    TAG_OF(e2.x, e2.y);
    float ln0, ln1;
    {
      float mb, rsd;
      ln_stats(xv, mb, rsd);
      ln0 = rbf(rbf(lo16(x2_) - mb) * rsd);
      ln1 = rbf(rbf(hi16(x2_) - mb) * rsd);
    }
    PIN(ln0); PIN(ln1);
    u32 t0_ = tlo << 24, t1_ = (tlo >> 8) | (thi << 24), f0_ = w ? 0u : tlo, f1_ = w ? 0u : thi;
    PIN(t0_); PIN(t1_); PIN(f0_); PIN(f1_);
    Q_LOAD(8, 8 + QW);
    waitvm<QW>();
#pragma unroll
    for (int c = 0; c < 8; ++c) wf[c] = tr_frag(lds, ta0 ^ (c << 5), ta1 ^ (c << 5));
    Q_LOAD(8 + QW, 16);
#pragma unroll
    for (int c = 0; c < 8; ++c) PIN(wf[c]);
    Q_LOAD(16, 16 + QW);
    waitvm<QW>();
#pragma unroll
    for (int c = 0; c < 8; ++c) wf[8 + c] = tr_frag(lds, (ta0 ^ (c << 5)) + 8192, (ta1 ^ (c << 5)) + 8192);
    Q_LOAD(16 + QW, 24);
    STAMP(12);
    until(t0, E1_T);
    STAMP(23);
    u32x4 g[4];
    u32x2 fl;
#define E1_LOAD(C)                                                                                                       \
  asm volatile("buffer_load_dwordx4 %0, %5, %7, 0 offen sc1\n buffer_load_dwordx4 %1, %5, %7, %8 offen sc1\n"             \
               "buffer_load_dwordx4 %2, %5, %7, %9 offen sc1\n buffer_load_dwordx4 %3, %5, %7, %10 offen sc1\n buffer_load_dwordx2 %4, %6, %7, 0 offen sc1" \
               : C(g[0]), C(g[1]), C(g[2]), C(g[3]), C(fl) : "v"(so), "v"(fo), "s"(rs), "s"(4096), "s"(8192), "s"(12288) : "memory")
    E1_LOAD("=&v");
    waitvm<5>();
#pragma unroll
    for (int i = 0; i < 8; ++i) wk[i] = *(const bf16x8*)(lds + (ka ^ ((i & 3) << 4)) + (i >> 2) * 4096);
#pragma unroll
    for (int c = 0; c < 8; ++c) { PIN(wf[8 + c]); PIN(wk[c]); }
    STAMP(4);
    _Pragma("unroll 1") for (u32 spins = 0; spins < SPIN_CAP; ++spins) {
      asm volatile("s_waitcnt vmcnt(0)" : "+v"(g[0]), "+v"(g[1]), "+v"(g[2]), "+v"(g[3]), "+v"(fl)::"memory");
      u32 bl = g[0].x ^ t0_, bh = (fl.x ^ f0_) | (fl.y ^ f1_);
#pragma unroll
      for (int k = 0; k < 8; ++k) {
        if (k) bl = __builtin_amdgcn_bitop3_b32(bl, g[k >> 1][2 * (k & 1)], t0_, 0xF6);
        bh = __builtin_amdgcn_bitop3_b32(bh, g[k >> 1][2 * (k & 1) + 1], t1_, 0xF6);
      }
      if (!RARE(__any(((bl >> 24) | bh) != 0u))) break;
      STAMP(29);
      __builtin_amdgcn_s_sleep(1);
      E1_LOAD("+v");
    }
    STAMP(1);
    {
      const float m0 = rbf((gran_val(g[0].x) + gran_val(g[1].x)) + (gran_val(g[2].x) + gran_val(g[3].x)));
      const float m1 = rbf((gran_val(g[0].z) + gran_val(g[1].z)) + (gran_val(g[2].z) + gran_val(g[3].z)));
      const auto s0 = __builtin_amdgcn_permlane32_swap(__float_as_uint(m0), __float_as_uint(m0), false, false);
      const auto s1 = __builtin_amdgcn_permlane32_swap(__float_as_uint(m1), __float_as_uint(m1), false, false);
      *(u32*)(lds + hw_) = pk2bf(rbf(ln0 * rbf(1.f + __uint_as_float(s0[1]))) + __uint_as_float(s0[0]),
                                 rbf(ln1 * rbf(1.f + __uint_as_float(s1[1]))) + __uint_as_float(s1[0]));
    }
    WAVE_LDS_SYNC();
    {
      const bf16x8 h0 = *(const bf16x8*)(lds + hr_), h1 = *(const bf16x8*)(lds + hr2);
      f32x4 d[8];
      SB();
#pragma unroll
      for (int c = 0; c < 8; ++c) { d[c] = __builtin_amdgcn_mfma_f32_16x16x32_bf16(h0, wf[c], f32x4{0.f, 0.f, 0.f, 0.f}, 0, 0, 0); SB(); }
#pragma unroll
      for (int c = 0; c < 8; ++c) { d[c] = __builtin_amdgcn_mfma_f32_16x16x32_bf16(h1, wf[8 + c], d[c], 0, 0, 0); SB(); }
#pragma unroll
      for (int k = 0; k < 2; ++k) {
        const float v = (g4 & 2) ? ((g4 & 1) ? d[4 * k + 3][0] : d[4 * k + 2][0]) : ((g4 & 1) ? d[4 * k + 1][0] : d[4 * k][0]);
        const u32 vh = f2bf(v);
        *(u16*)(lds + qw_ + 128 * k) = (u16)vh;
        *(u16*)(lds + qw_ + 128 * k + 8 * 272) = (u16)f2bf(v - bf2f(vh));
      }
    }
    STAMP(2);
    BAR();
    {
      bf16x8 a[4];
#pragma unroll
      for (int s = 0; s < 4; ++s) a[s] = *(const bf16x8*)(lds + qr_ + 16 * s);
      SB();
      f32x4 d[2] = {f32x4{0.f, 0.f, 0.f, 0.f}, f32x4{0.f, 0.f, 0.f, 0.f}};
#pragma unroll
      for (int s = 0; s < 4; ++s)
#pragma unroll
        for (int c = 0; c < 2; ++c) d[c] = __builtin_amdgcn_mfma_f32_16x16x32_bf16(a[s], wk[4 * c + s], d[c], 0, 0, 0);
      constexpr float SCALE = 0.08838834764831845f * 1.4426950408889634f;
      const float v = SCALE * x16_sum(rs32(sum4(d[0]), sum4(d[1])));
      const float p0 = __uint_as_float(nopois(pk2bf(dppf<0x00>(v), dppf<0x55>(v)))), p1 = __uint_as_float(nopois(pk2bf(dppf<0xAA>(v), dppf<0xFF>(v))));
      st128(rs, qpo, u32x4{__float_as_uint(p0), __float_as_uint(p1), __float_as_uint(dppf<0x104>(p0)), __float_as_uint(dppf<0x104>(p1))});
    }
    STAMP(5);
    SB();
    {
      st128(rs, lane < 50 ? WS_PCTX + (j * 8 + w) * 800 + lane * 16 : WS_TOTAL, P4);
      const int pp = __builtin_amdgcn_readfirstlane(WS_PROBE + il((j * 8 + w) * 128, w & 1));
      u64 ta, tb, tc;
      u32 dm;
      asm volatile("s_load_dword %3, %4, 0x0\n s_waitcnt lgkmcnt(0)\n"
                   "s_memtime %0\n s_waitcnt lgkmcnt(0)\n s_load_dword %3, %4, %5 glc\n s_waitcnt lgkmcnt(0)\n"
                   "s_memtime %1\n s_waitcnt lgkmcnt(0)\n s_load_dword %3, %4, %6 glc\n s_waitcnt lgkmcnt(0)\n"
                   "s_memtime %2\n s_waitcnt vmcnt(0) lgkmcnt(0)"
                   : "=&s"(ta), "=&s"(tb), "=&s"(tc), "=&s"(dm) : "s"(ws), "s"(pp), "s"(pp ^ 8192) : "memory");
      int df = min(max(((int)(u32)(tb - ta) - (int)(u32)(tc - tb)) >> 3, -VCLIP), VCLIP);
      df = ((w ^ myh) & 1) ? -df : df;
      u32 old = 0u;
      if (lane == 0) old = __hip_atomic_fetch_add((LAS u32*)(lds + L_CTR) + C_VOTE, (u32)(65536 + 64 + df), __ATOMIC_RELAXED, __HIP_MEMORY_SCOPE_WORKGROUP);
      old = __builtin_amdgcn_readfirstlane(old) + 65536 + 64 + df;
      if (old >= 524288u && lane < 8) st64(rs, WS_VOTE + lane * 2048 + j * 8, (old & 1023u) | (tlo << 24), (tlo >> 8) | (thi << 24));
      STAMP(6);
    }
    {
      const int r0 = 128 * (mu & 3) + 32 * (w & 3) + 4 * (lane >> 3);
      const u32x2 cvb = *(const u32x2*)(cvec + r0);
      u32x4 wsc[4];
#pragma unroll
      for (int i = 0; i < 4; ++i) wsc[i] = ldnt(w_mod + (size_t)(r0 + i) * 3072 + 1024 + (mu >> 2) * 64 + (lane & 7) * 8);
      float a[8];
#pragma unroll
      for (int k = 0; k < 8; ++k) a[k] = 0.f;
#pragma unroll
      for (int i = 0; i < 4; ++i) fma8b(a, f2bf(silu_bf((i & 1) ? hi16(i < 2 ? cvb.x : cvb.y) : lo16(i < 2 ? cvb.x : cvb.y))), wsc[i]);
      float r[4];
#pragma unroll
      for (int k = 0; k < 4; ++k) r[k] = rs32(a[k], a[k + 4]);
      const float o0 = b3_sum(rs16(r[0], r[2])), o1 = b3_sum(rs16(r[1], r[3]));
      float* red = (float*)(lds + L_H1RED + 8192);
      if ((lane & 8) == 0) *(f32x2*)(red + w * 64 + (lane & 7) * 8 + (lane >> 5) * 4 + ((lane >> 4) & 1) * 2) = f32x2{o0, o1};
      BAR();
      if ((w & 3) == 3 && lane < 16) {
        red += (w >> 2) * 256 + lane * 4;
        const f32x4 sm = (*(const f32x4*)(red) + *(const f32x4*)(red + 64)) + (*(const f32x4*)(red + 128) + *(const f32x4*)(red + 192));
        st128(rs, WS_MODB + ((mu & 3) * 2048 + (mu >> 2) * 64 + lane * 4) * 4, u32x4{fbits(sm.x), fbits(sm.y), fbits(sm.z), fbits(sm.w)});
      }
    }
    gx = 0;
  }

  RELANE();
  const int r16 = lane >> 2, ch = lane & 3;
  if (ii >= 16) {
    int mo = lane < 24 ? (WS_LSE + il((24 * w + lane) * 512 + c4 * 128 + (h >> 1) * 8, h & 1)) ^ gx : WS_TOTAL;
    PIN(mo);
    lds_spin(ctr + C_PD, 4u);
    STAMP(23);
    u32x2 mt = ld64(rs, mo);
    SB();
    const u32 pb = (w >> 1) * PS + (w & 1) * 6144;
    u32 po[6], pa[16], ea[8];
    {
      u32 t_ = g4 * 256 + ((col ^ (g4 << 2)) << 4) + ((WS_PART + il21(((h >> 1) * 4 + c4) * 65536, h & 1)) ^ gx);
      PIN(t_);
#pragma unroll
      for (int k = 0; k < 6; ++k) { po[k] = (t_ ^ ((k & 3) << 4)) + il((6 * w + k) * 1024, 0); PIN(po[k]); }
    }
    bf16x8 wk[4];
    {
      u32 wa = tq * PS + 16384 + off_b(g4, 2 * w + (tp >> 1)) + 8 * (tp & 1);
      PIN(wa);
#pragma unroll
      for (int s = 0; s < 4; ++s) wk[s] = tr_frag(lds, (wa ^ (s << 4)) + s * 1024, (wa ^ (s << 4)) + s * 1024 + 4096);
    }
    pa[0] = pb + off_b(8 * (g4 == 3 ? 0 : g4) + tq, tp >> 1) + 8 * (tp & 1);
    PIN(pa[0]);
#pragma unroll
    for (int i = 1; i < 16; ++i) { pa[i] = pa[0] ^ (i << 4); PIN(pa[i]); }
    u32 bo = L_QOFF + col * 272 + g4 * 16, mlo = L_QOFF + 4352 + (col & 7) * 8, ro = L_QOFF + (w + (col & 8)) * 272 + (col & 7) * 32 + g4 * 8;
    int so1 = (WS_PCTX + h * 2048 + c4 * 512 + (w >> 2) * 256 + (w & 1) * 128 + (w & 2) * 16 + (g4 >> 1) * 64 + (g4 & 1) * 8 + (col < 2 ? col * 16 : WS_TOTAL)) ^ gx;
    u32 eo = L_QOFF + 4416 + w * 128 + lane * 2, mw_ = L_QOFF + 4352 + w * 8;
#pragma unroll
    for (int i = 0; i < 8; ++i) { ea[i] = L_QOFF + 4416 + w * 128 + ((col & 7) == i ? 16 * g4 : 64); PIN(ea[i]); }
    float pad = lane < 24 ? 0.f : -__builtin_inff();
    PIN(bo); PIN(mlo); PIN(ro); PIN(so1); PIN(eo); PIN(mw_); PIN(pad);
    SB();
    u32 done = 0u;
    _Pragma("unroll 1") for (u32 spins = 0; spins < SPIN_CAP; ++spins) {
      const u32 ok = ~(u32)__ballot(max(mt.x, mt.y) == POISON);
      u32 nw = ok & (ok >> 1);
      nw = nw & (nw >> 2) & 0x111111u & ~done;
      done |= nw;
#define T1F(k, b) "s_bitcmp0_b32 %[nw], " #b "\n s_cbranch_scc1 1f\n global_load_lds_dwordx4 %[p" #k "], %[ws] sc1\n 1: s_add_u32 m0, m0, 0x400\n"
      asm volatile("s_mov_b32 m0, %[m]\n" T1F(0, 0) T1F(1, 4) T1F(2, 8) T1F(3, 12) T1F(4, 16) T1F(5, 20)
                   ::[m] "s"(pb), [nw] "s"(nw), [ws] "s"(ws), [p0] "v"(po[0]), [p1] "v"(po[1]), [p2] "v"(po[2]), [p3] "v"(po[3]), [p4] "v"(po[4]), [p5] "v"(po[5]) : "memory", "scc");
      if (done == 0x111111u) break;
      __builtin_amdgcn_s_sleep(1);
      if (max(mt.x, mt.y) == POISON) mt = ld64(rs, mo);
      PIN(mt);
    }
    STAMP(16);
    {
      const float m = __uint_as_float(mt.x) + pad;
      float M = m;
#define MX(C) "s_nop 1\n v_max_f32_dpp %0, %0, %0 " C " row_mask:0xf bank_mask:0xf\n"
      asm(MX("quad_perm:[1,0,3,2]") MX("quad_perm:[2,3,0,1]") MX("row_ror:8") MX("row_ror:4") "s_nop 1" : "+v"(M));
      M = fmaxf(__uint_as_float(__builtin_amdgcn_readlane(__float_as_uint(M), 0)), __uint_as_float(__builtin_amdgcn_readlane(__float_as_uint(M), 16)));
      const float e = __builtin_amdgcn_exp2f(m - M);
      *(u16*)(lds + eo) = (u16)f2bf(e);
      WAVE_LDS_SYNC();
      bf16x8 wf[8], pt[8];
#pragma unroll
      for (int i = 0; i < 8; ++i) wf[i] = *(const bf16x8*)(lds + ea[i]);
      SB();
      const float L = x16_sum(row16_sum(e * __uint_as_float(mt.y)));
      if (lane == 0) *(f32x2*)(lds + mw_) = f32x2{M, L};
      SB();
      waitvm<0>();
      STAMP(17);
#pragma unroll
      for (int i = 0; i < 8; ++i) pt[i] = tr_frag(lds, pa[2 * i], pa[2 * i + 1] + 1024);
      SB();
      f32x4 d = {0.f, 0.f, 0.f, 0.f};
#pragma unroll
      for (int i = 0; i < 8; ++i) d = __builtin_amdgcn_mfma_f32_16x16x32_bf16(pt[i], wf[i], d, 0, 0, 0);
      const u32 h0 = pk2bf(d[0], d[1]), h1 = pk2bf(d[2], d[3]);
      const u32 l0 = pk2bf(d[0] - lo16(h0), d[1] - hi16(h0)), l1 = pk2bf(d[2] - lo16(h1), d[3] - hi16(h1));
      *(u32x2*)(lds + ro) = (col & 8) ? u32x2{l0, l1} : u32x2{h0, h1};
    }
    BAR();
    STAMP(14);
    const f32x2 ml = *(const f32x2*)(lds + mlo);
    bf16x8 lf[4];
#pragma unroll
    for (int s = 0; s < 4; ++s) lf[s] = *(const bf16x8*)(lds + bo + 64 * s);
    SB();
    f32x4 d = {0.f, 0.f, 0.f, 0.f};
#pragma unroll
    for (int s = 0; s < 4; ++s) d = __builtin_amdgcn_mfma_f32_16x16x32_bf16(wk[s], lf[s], d, 0, 0, 0);
    SB();
    float Mg = fmaxf(ml.x, dppf<0xB1>(ml.x));
    Mg = fmaxf(Mg, dppf<0x4E>(Mg)); Mg = fmaxf(Mg, dppf<0x141>(Mg));
    const float e = __builtin_amdgcn_exp2f(ml.x - Mg);
    float Lg = quad_sum(e * ml.y);
    Lg += dppf<0x141>(Lg);
    const float sc = e * __builtin_amdgcn_rcpf(Lg);
    {
      float d0 = d[0] * sc, d1 = d[1] * sc, d2 = d[2] * sc, d3 = d[3] * sc;
      row16_sum4(d0, d1, d2, d3);
      d = f32x4{d0, d1, d2, d3};
    }
    const u32 h0 = pk2bf(d[0], d[1]), h1 = pk2bf(d[2], d[3]);
    const u32 l0 = pk2bf(d[0] - lo16(h0), d[1] - hi16(h0)), l1 = pk2bf(d[2] - lo16(h1), d[3] - hi16(h1));
    st64(rs, so1, nopois(col ? l0 : h0), nopois(col ? l1 : h1));
    STAMP(15);
    STAMP_FLUSH();
    return;
  }
  if (tl2) {
    BAR0();
    if (w < 4) {
#pragma unroll
      for (int i = 0; i < 8; ++i) dma<true>(owno + i * 1024, (ks * 512 + 64 * w + 8 * i + (lane >> 3)) * 1024 + cg * 128 + (lane & 7) * 16, w_o);
      waitvm<0>();
    } else {
#pragma unroll
      for (int i = 0; i < 8; ++i) *(u32x4*)(lds + owno + i * 1024 + lane * 16) = wv[i];
      WAVE_LDS_SYNC();
    }
    STAMP(26);
    bf16x8 wf[8];
#pragma unroll
    for (int i = 0; i < 8; ++i) {
      const u32 a = owno + (32 * (i >> 2) + 8 * g4 + tq) * 128 + (i & 3) * 32 + tp * 8;
      wf[i] = tr_frag(lds, a, a + 512);
    }
#pragma unroll
    for (int i = 0; i < 8; ++i) PIN(wf[i]);
    STAMP(23);
    int off[1] = {(WS_PCTX + (ks * 4 + (w >> 1)) * 2048 + tq * 512 + (w & 1) * 256 + g4 * 64 + tp * 16) ^ gx};
    u32 rw = L_RED + lane * 32 + w * 4;
    PIN(rw);
    int so2 = tp < 2 ? WS_PATTN + il((ks * 512 + cg * 64 + (lane & 60)) * 4, tp) : WS_TOTAL;
    PIN(so2);
    u32x4 g[1];
    gather128<1, 4>(rs, off, g);
    STAMP(24);
    const bf16x8 a0 = __builtin_bit_cast(bf16x8, (col & 2) ? Z4 : g[0]), a1 = __builtin_bit_cast(bf16x8, (col & 2) ? g[0] : Z4);
    f32x4 d[4];
#pragma unroll
    for (int c = 0; c < 4; ++c) d[c] = __builtin_amdgcn_mfma_f32_16x16x32_bf16(a0, wf[c], f32x4{0.f, 0.f, 0.f, 0.f}, 0, 0, 0);
#pragma unroll
    for (int c = 0; c < 4; ++c) d[c] = __builtin_amdgcn_mfma_f32_16x16x32_bf16(a1, wf[4 + c], d[c], 0, 0, 0);
    *(float*)(lds + rw) = rs16(rs32(sum4(d[0]), sum4(d[2])), rs32(sum4(d[1]), sum4(d[3])));
    BAR();
    if (w == 0) {
      const float v = sum4(*(const f32x4*)(lds + L_RED + lane * 32) + *(const f32x4*)(lds + L_RED + lane * 32 + 16));
      const float vn = __uint_as_float(fbits(v));
      st128(rs, so2, u32x4{__float_as_uint(dppf<0x00>(vn)), __float_as_uint(dppf<0x55>(vn)), __float_as_uint(dppf<0xAA>(vn)), __float_as_uint(dppf<0xFF>(vn))});
    }
    STAMP(17);
  } else if (t3 >= 0) {
    STAMP(20);
    if (w < 4) {
      const int fr = ((128 * w + r16) * DF + t3 * 32) * 2, ca_ = ch ^ ((r16 >> 2) & 2) ^ (t3 & 1);
      const u16* wa = (t3 & 1) ? w_up : w_gate;
      const u16* wb = (t3 & 1) ? w_gate : w_up;
      const u32 d = w * 32768 + 8192;
#pragma unroll
      for (int i = 0; i < 8; ++i) { waitvm<WING - 1>(); dma<false>(d + (i >> 2) * 12288 + (t3 & 1) * 4096 + (i & 3) * 1024, fr + ca_ * 16 + 32 * i * DF, wa); }
#pragma unroll
      for (int i = 0; i < 8; ++i) { waitvm<WING - 1>(); dma<false>(d + (i >> 2) * 12288 + 4096 - (t3 & 1) * 4096 + (i & 3) * 1024, fr + (ca_ ^ 1) * 16 + 32 * i * DF, wb); }
    } else {
#pragma unroll
      for (int i = 0; i < 8; ++i) {
        waitvm<WING - 1>();
        dma<true>(lw * 32768 + 16384 + (i >> 2) * 12288 + (i & 3) * 1024, (32 * t3 + 8 * (i & 3) + (lane >> 3)) * 1024 + 256 * lw + 128 * (i >> 2) + (lane & 7) * 16, w_down);
      }
      const u32 x2 = *(const u32*)(x + 128 * lw + 2 * lane);
      u32x4 m6[6];
      int mo[6];
#pragma unroll
      for (int i = 0; i < 6; ++i) mo[i] = MB_OFF(i);
      gather128<6, 8>(rs, mo, m6);
#pragma unroll
      for (int i = 0; i < 6; ++i) {
        const f32x4 f = __builtin_bit_cast(f32x4, m6[i]);
        const float v = rbf(rs16(rs32(f.x, f.z), rs32(f.y, f.w)));
        *(u16*)(lds + L_Z + (i < 2 ? 5 : i < 4 ? 3 : 2) * 1088 + (128 * lw + 64 * (i & 1) + 4 * col + g4) * 2) = (u16)f2bf(i < 4 ? v : 1.f + v);
      }
      *(u32*)(lds + L_Z + 6 * 1088 + lw * 256 + lane * 4) = x2;
      if (w == 4 && lane < 32) *(u32*)(lds + L_ZERO + lane * 4) = lane < 16 ? 0u : POISON;
      if (w == 6) COUNT_VOTES(false);
    }
    waitvm<0>();
    STAMP(26);
    BAR();
    gx = __builtin_amdgcn_readfirstlane(ctr[C_PAR]);
    u32 zw = L_Z + (64 * w + 4 * col + g4) * 2;
    u32 hr[4];
#pragma unroll
    for (int i = 0; i < 4; ++i) { hr[i] = (col == i ? L_HH : L_ZERO) + 16 * g4; PIN(hr[i]); }
    int off[2];
#pragma unroll
    for (int i = 0; i < 2; ++i) { off[i] = (WS_PATTN + il(((4 * i + g4) * 512 + 64 * w + col * 4) * 4, myh)) ^ gx; PIN(off[i]); }
    int so = col < 4 ? (WS_PDOWN + il((t3 * 256 + 64 * (w & 3) + 16 * col + 4 * g4) * 4, w >> 2)) ^ gx : WS_TOTAL;
    u32 zr = L_Z + tp * 1088 + 16 * g4, yr = L_Z + 4 * 1088 + lane * 16, hw = L_HH + (w & 3) * 16 + (g4 & 1) * 8 + tp * 2, cr = L_CF + (w & 3) * 16 + tp * 4;
    PIN(cr); PIN(so); PIN(zw); PIN(zr); PIN(yr); PIN(hw);
    float ga = bf2f(*(const u16*)(lds + zw + 5 * 1088)), cb1 = bf2f(*(const u16*)(lds + zw + 2 * 1088)), xe = bf2f(*(const u16*)(lds + zw + 6 * 1088));
    bf16x8 wf[16], wd[4];
    if (w < 4) {
#pragma unroll
      for (int k = 0; k < 16; ++k) {
        const u32 a = (k >> 2) * 32768 + 8192 + ((k >> 1) & 1) * 12288 + (tp >> 1) * 4096 + ((k & 1) * 32 + 8 * g4 + tq) * 64 + (w ^ ((g4 & 1) * 2) ^ (tp >> 1)) * 16 + (tp & 1) * 8;
        wf[k] = tr_frag(lds, a, a + 256);
      }
#pragma unroll
      for (int k = 0; k < 16; ++k) PIN(wf[k]);
    }
#pragma unroll
    for (int i = 0; i < 4; ++i) {
      const u32 a = owno + 8192 + (8 * g4 + tq) * 128 + i * 32 + tp * 8;
      wd[i] = tr_frag(lds, a, a + 512);
    }
#pragma unroll
    for (int i = 0; i < 4; ++i) PIN(wd[i]);
    PIN(ga); PIN(cb1); PIN(xe);
    STAMP(23);
    until(t0, E6_T);
    u32x4 pa2[2];
    gather128<2, 4>(rs, off, pa2);
    STAMP(18);
    {
      const f32x4 s4 = __builtin_bit_cast(f32x4, pa2[0]) + __builtin_bit_cast(f32x4, pa2[1]);
      const u32 yb = f2bf(xe + rbf(ga * rbf(rs16(rs32(s4.x, s4.z), rs32(s4.y, s4.w)))));
      const float z = bf2f(yb) * cb1;
      const u32 zh = f2bf(z);
      *(u16*)(lds + zw + 4 * 1088) = (u16)yb;
      *(u16*)(lds + zw) = (u16)zh;
      *(u16*)(lds + zw + 1088) = (u16)f2bf(z - bf2f(zh));
    }
    BAR();
    if (w < 4) {
      bf16x8 zf[16];
#pragma unroll
      for (int k = 0; k < 16; ++k) zf[k] = *(const bf16x8*)(lds + zr + 64 * k);
      __builtin_amdgcn_sched_barrier(0);
      f32x4 d = {0.f, 0.f, 0.f, 0.f};
#pragma unroll
      for (int k = 0; k < 16; ++k) d = __builtin_amdgcn_mfma_f32_16x16x32_bf16(wf[k], zf[k], d, 0, 0, 0);
      PIN(d);
      u32 cfb;
      do { cfb = *(volatile LAS u32*)(lds + cr); } while (__any(cfb == POISON));
      d *= __uint_as_float(cfb);
      const float a0 = (tp & 1) ? d[1] : d[0], b0 = (tp & 1) ? d[0] : d[1], a1 = (tp & 1) ? d[3] : d[2], b1 = (tp & 1) ? d[2] : d[3];
      const float e0 = a0 + dppf<0xB1>(b0), e1 = a1 + dppf<0xB1>(b1);
      const float v = rbf(((tp & 2) ? e1 : e0) + dppf<0x4E>((tp & 2) ? e0 : e1));
      const auto sw = __builtin_amdgcn_permlane32_swap(__float_as_uint(v), __float_as_uint(v), false, false);
      const float g = __uint_as_float(sw[0]), u = __uint_as_float(sw[1]);
      const float e2 = __builtin_amdgcn_exp2f(g * (-2.3022081981f - 0.1029432396f * g * g));
      *(u16*)(lds + hw) = (u16)f2bf(rbf(g * __builtin_amdgcn_rcpf(e2 + 1.f)) * u);
    } else {
      float mb, rsd;
      ln_stats(*(const u32x4*)(lds + yr), mb, rsd);
      *(u32*)(lds + cr) = fbits(tp == 3 ? 1.f : rsd * (tp == 2 ? -mb : 1.f));
    }
    BAR();
    {
      bf16x8 hf[4];
#pragma unroll
      for (int i = 0; i < 4; ++i) hf[i] = *(const bf16x8*)(lds + hr[i]);
      __builtin_amdgcn_sched_barrier(0);
      f32x4 d = {0.f, 0.f, 0.f, 0.f};
#pragma unroll
      for (int i = 0; i < 4; ++i) d = __builtin_amdgcn_mfma_f32_16x16x32_bf16(wd[i], hf[i], d, 0, 0, 0);
      st128(rs, so, u32x4{fbits(d[0]), fbits(d[1]), fbits(d[2]), fbits(d[3])});
    }
    STAMP(19);
  }
  if (t4 >= 0 && lw == 0) {
#define RED8(f) b3_sum(rs16(rs32(f.x, f.z), rs32(f.y, f.w)))
    const int oc = t4 * 32 + (lane & 7) * 4 + g4;
    const u32 xb = x[oc];
    float x1, gb;
    {
      int off[2];
      off[0] = lane < 32 ? WS_MODB + ((lane >> 3) * 2048 + t4 * 32 + (lane & 7) * 4) * 4 : WS_TOTAL;
      off[1] = lane < 32 ? WS_MODB + ((lane >> 3) * 2048 + 1536 + t4 * 32 + (lane & 7) * 4) * 4 : WS_TOTAL;
      u32x4 mg[2];
      gather128<2>(rs, off, mg);
      const f32x4 fa = __builtin_bit_cast(f32x4, mg[0]), fb = __builtin_bit_cast(f32x4, mg[1]);
      const float ga = rbf(RED8(fa));
      gb = rbf(RED8(fb));
      int of1[1] = {(WS_PATTN + il(((lane >> 3) * 512 + t4 * 32 + (lane & 7) * 4) * 4, myh)) ^ gx};
      u32x4 g[1];
      gather128<1, 4>(rs, of1, g);
      const f32x4 gf = __builtin_bit_cast(f32x4, g[0]);
      x1 = rbf(bf2f(xb) + rbf(ga * rbf(RED8(gf))));
    }
    STAMP(20);
    if (t4 == 0 && lane == 0) st64(rs, WS_EPOCH, tlo, thi);
    int off[7];
#pragma unroll
    for (int i = 0; i < 7; ++i) { off[i] = (WS_PDOWN + il(((7 * (lane >> 3) + i) * 256 + (t4 & 7) * 32 + (lane & 7) * 4) * 4, myh)) ^ gx; PIN(off[i]); }
    const rsrc_t ro = __builtin_amdgcn_make_buffer_rsrc((void*)out, 0, 1024, 0x00020000);
    int oo = (lane & 8) ? 1024 : oc * 2;
    PIN(x1); PIN(gb); PIN(oo);
    u32x4 g[7];
    __builtin_amdgcn_s_sleep(T4_DELAY);
#define F4(i) __builtin_bit_cast(f32x4, g[i])
    float s;
    _Pragma("unroll 1") for (u32 spins = 0; spins < SPIN_CAP; ++spins) {
      asm volatile("" ::: "memory");
#pragma unroll
      for (int i = 0; i < 7; ++i) g[i] = ld128(rs, off[i]);
      SB();
      const f32x4 s4 = ((F4(0) + F4(1)) + (F4(2) + F4(3))) + ((F4(4) + F4(5)) + F4(6));
      s = RED8(s4);
      if (!__any(s != s)) break;
      bool bad = false;
#pragma unroll
      for (int i = 0; i < 7; ++i) bad = bad | pois4(g[i]);
      if (!__any(bad)) break;
    }
    STAMP(21);
    __builtin_amdgcn_raw_buffer_store_b16((u16)f2bf(x1 + rbf(gb * rbf(s))), ro, oo, 0, 0);
    STAMP(22);
  }
  STAMP_FLUSH();
}

extern "C" void kernel_launch(void* const* d_in, const int* in_sizes, int n_in, void* d_out, int out_size,
                              void* d_ws, size_t ws_size, hipStream_t stream) {
  k_main<<<NWG, 512, 0, stream>>>((const u16*)d_in[0], (const u16*)d_in[1], (const u16*)d_in[2], (const u16*)d_in[3],
                                  (const u16*)d_in[4], (const u16*)d_in[5], (const u16*)d_in[7], (const u16*)d_in[8],
                                  (const u16*)d_in[9], (const u16*)d_in[10], (const u16*)d_in[11], (u16*)d_out,
                                  (unsigned char*)d_ws);
}
```

```cpp
#include <hip/hip_runtime.h>
#include <hip/hip_bf16.h>
#include <stdint.h>

typedef unsigned short u16;
typedef unsigned int u32;
typedef unsigned long long u64;
typedef float f32x2 __attribute__((ext_vector_type(2)));
typedef float f32x4 __attribute__((ext_vector_type(4)));
typedef float f32x16 __attribute__((ext_vector_type(16)));
typedef u32 u32x2 __attribute__((ext_vector_type(2)));
typedef u32 u32x4 __attribute__((ext_vector_type(4)));
typedef short bf16x8 __attribute__((ext_vector_type(8)));
typedef short s16x4 __attribute__((ext_vector_type(4)));
#define LAS __attribute__((address_space(3)))
#define DI __device__ __forceinline__

constexpr int DF = 1792;
constexpr int NWG = 256;
constexpr int AUX_SC1 = 16;

constexpr int WS_EPOCH = 0;
constexpr int WS_SFLAG = 4096;
constexpr int WS_QABS = 32768;
constexpr int WS_PCTX = WS_QABS + 65536;
constexpr int WS_PATTN = WS_PCTX + 65536;
constexpr int WS_PDOWN = WS_PATTN + 32768;
constexpr int WS_LSE = WS_PDOWN + 114688;
constexpr int WS_MODB = WS_LSE + 196608;
constexpr int WS_SC = WS_MODB + 32768;
constexpr int WS_PROBE = WS_SC + 32768;
constexpr int WS_VOTE = WS_PROBE + 262144;
constexpr int WS_PART = 2097152;
constexpr int PART_BYTES = 32 * 8 * 256 * 128;
constexpr int WS_TOTAL = WS_PART + PART_BYTES;

constexpr int il(int o, int hf) { return (o >> 13) * 16384 + hf * 8192 + (o & 8191); }
constexpr int il21(int o, int hf) { return il(o, hf ^ ((o >> 20) & 1) ^ 1); }
constexpr u32 SPIN_CAP = 1u << 16;
#define WIN 7
#define WA0 5
#define QW 4
#define WING 3
#define QF_T 300
#define E1_T 250
#define SCP_T 215
#define E3_T 470
#define E6_T 1200
#define T4_DELAY 12
#define TAU 32.f
#define VCLIP 40
#define PF_LEN 33000
#define G_OF(v) ((v) > 32768.f ? 8192u : 0u)
#define VOTE_STAMP(v)
#define STAMP_DECL
#define STAMP(i)
#define STAMP_FLUSH()

DI float bf2f(u32 b) { return __uint_as_float(b << 16); }
DI float rbf(float f) { return (float)(__bf16)f; }
DI u32 f2bf(float f) { return (u32)__builtin_bit_cast(u16, (__bf16)f); }
typedef __bf16 bf16x2_t __attribute__((ext_vector_type(2)));
DI u32 pk2bf(float a, float b) { return __builtin_bit_cast(u32, __builtin_convertvector(f32x2{a, b}, bf16x2_t)); }
DI float lo16(u32 v) { return __uint_as_float(v << 16); }
DI float hi16(u32 v) { return __uint_as_float(v & 0xFFFF0000u); }
DI u32x4 ldnt(const u16* p) { return __builtin_nontemporal_load((const u32x4*)p); }

DI float silu_bf(float a) { return rbf(a * __builtin_amdgcn_rcpf(1.f + __expf(-a))); }

template <int CTRL> DI float dppf(float v) {
  return __int_as_float(__builtin_amdgcn_update_dpp(0, __float_as_int(v), CTRL, 0xF, 0xF, true));
}
DI float quad_sum(float v) { v += dppf<0xB1>(v); v += dppf<0x4E>(v); return v; }
DI float b23_sum(float v) { v += dppf<0x128>(v); v += dppf<0x124>(v); return v; }
DI float b3_sum(float v) { return v + dppf<0x128>(v); }
DI float x16_sum(float v) {
  const auto r = __builtin_amdgcn_permlane16_swap(__float_as_uint(v), __float_as_uint(v), false, false);
  return __uint_as_float(r[0]) + __uint_as_float(r[1]);
}
DI float x32_sum(float v) {
  const auto r = __builtin_amdgcn_permlane32_swap(__float_as_uint(v), __float_as_uint(v), false, false);
  return __uint_as_float(r[0]) + __uint_as_float(r[1]);
}
DI float rs32(float a, float b) {
  const auto r = __builtin_amdgcn_permlane32_swap(__float_as_uint(a), __float_as_uint(b), false, false);
  return __uint_as_float(r[0]) + __uint_as_float(r[1]);
}
DI float rs16(float a, float b) {
  const auto r = __builtin_amdgcn_permlane16_swap(__float_as_uint(a), __float_as_uint(b), false, false);
  return __uint_as_float(r[0]) + __uint_as_float(r[1]);
}
DI float row16_sum(float v) { return b23_sum(quad_sum(v)); }
DI float wave_sum(float v) { return x32_sum(x16_sum(row16_sum(v))); }
DI void row16_sum4(float& a, float& b, float& c, float& d) {
#define R4(C) "v_add_f32_dpp %0, %0, %0 " C " row_mask:0xf bank_mask:0xf\n v_add_f32_dpp %1, %1, %1 " C " row_mask:0xf bank_mask:0xf\n" \
              "v_add_f32_dpp %2, %2, %2 " C " row_mask:0xf bank_mask:0xf\n v_add_f32_dpp %3, %3, %3 " C " row_mask:0xf bank_mask:0xf\n"
  asm("s_nop 1\n" R4("quad_perm:[1,0,3,2]") R4("quad_perm:[2,3,0,1]") R4("row_ror:8") R4("row_ror:4") : "+v"(a), "+v"(b), "+v"(c), "+v"(d));
#undef R4
}
DI float row16_max(float v) {
  v = fmaxf(v, dppf<0xB1>(v)); v = fmaxf(v, dppf<0x4E>(v));
  v = fmaxf(v, dppf<0x128>(v)); v = fmaxf(v, dppf<0x124>(v));
  return v;
}
DI float x16_max(float v) {
  const auto r = __builtin_amdgcn_permlane16_swap(__float_as_uint(v), __float_as_uint(v), false, false);
  return fmaxf(__uint_as_float(r[0]), __uint_as_float(r[1]));
}
DI float x32_max(float v) {
  const auto r = __builtin_amdgcn_permlane32_swap(__float_as_uint(v), __float_as_uint(v), false, false);
  return fmaxf(__uint_as_float(r[0]), __uint_as_float(r[1]));
}
DI float wave_max(float v) { return x32_max(x16_max(row16_max(v))); }

DI float dot2(u32 a, u32 b, float c) {
  return __builtin_amdgcn_fdot2_f32_bf16(__builtin_bit_cast(bf16x2_t, a), __builtin_bit_cast(bf16x2_t, b), c, false);
}
DI void fma8b(float* acc, u32 sbits, u32x4 w) {
  const u32 sh = sbits << 16;
  acc[0] = dot2(w.x, sbits, acc[0]); acc[1] = dot2(w.x, sh, acc[1]);
  acc[2] = dot2(w.y, sbits, acc[2]); acc[3] = dot2(w.y, sh, acc[3]);
  acc[4] = dot2(w.z, sbits, acc[4]); acc[5] = dot2(w.z, sh, acc[5]);
  acc[6] = dot2(w.w, sbits, acc[6]); acc[7] = dot2(w.w, sh, acc[7]);
}

DI unsigned fsw(unsigned row) { return ((row & 3) << 2) | ((row >> 2) & 3); }
DI unsigned off_b(unsigned row, unsigned ch) { return 256u * row + 16u * (ch ^ fsw(row)); }
DI unsigned tr_addr(unsigned lane, unsigned c, unsigned ks, unsigned t) {
  const unsigned h = lane >> 5, blk = (lane >> 4) & 1, q = (lane & 15) >> 2, p = lane & 3;
  return off_b(16 * ks + 8 * h + 4 * t + q, 4 * c + 2 * blk + (p >> 1)) + 8 * (p & 1);
}

typedef __amdgpu_buffer_rsrc_t rsrc_t;
DI u32x4 ld128(rsrc_t rs, int off) { return __builtin_amdgcn_raw_buffer_load_b128(rs, off, 0, AUX_SC1); }
DI u32x2 ld64(rsrc_t rs, int off) { return __builtin_amdgcn_raw_buffer_load_b64(rs, off, 0, AUX_SC1); }
DI void st64(rsrc_t rs, int off, u32 lo, u32 hi) { u32x2 v; v.x = lo; v.y = hi; __builtin_amdgcn_raw_buffer_store_b64(v, rs, off, 0, AUX_SC1); }
DI void st128(rsrc_t rs, int off, u32x4 v) { __builtin_amdgcn_raw_buffer_store_b128(v, rs, off, 0, AUX_SC1); }
#define BAR() do { asm volatile("s_waitcnt lgkmcnt(0)" ::: "memory"); __builtin_amdgcn_s_barrier(); asm volatile("" ::: "memory"); } while (0)
#define BAR0() do { __builtin_amdgcn_sched_barrier(0); asm volatile("" ::: "memory"); __builtin_amdgcn_s_barrier(); asm volatile("" ::: "memory"); __builtin_amdgcn_sched_barrier(0); } while (0)
#define PIN(x) asm volatile("" : "+v"(x))
constexpr u32 POISON = 0xFFFFFFFFu;
DI bool pois4(u32x4 v) { return max(max(v.x, v.y), max(v.z, v.w)) == POISON; }
DI u32 nopois(u32 v) { return min(v, 0xFFFFFFFEu); }
DI u32 fbits(float v) { return nopois(__float_as_uint(v)); }

DI u32x2 mk_gran(float v, u32 tlo, u32 thi) {
  u32 b = __float_as_uint(v);
  if ((b & 0x7F800000u) != 0x7F800000u) b += 0x80u;
  u32x2 g; g.x = (b >> 8) | (tlo << 24); g.y = (tlo >> 8) | (thi << 24);
  return g;
}
DI void st_gran2(rsrc_t rs, int off, float v0, float v1, u32 tlo, u32 thi) {
  const u32x2 a = mk_gran(v0, tlo, thi), b = mk_gran(v1, tlo, thi);
  u32x4 v; v.x = a.x; v.y = a.y; v.z = b.x; v.w = b.y;
  st128(rs, off, v);
}
DI bool gran_ok(u32 lo, u32 hi, u32 tlo, u32 thi) { return (((lo >> 24) ^ (tlo & 0xFFu)) | (hi ^ ((tlo >> 8) | (thi << 24)))) == 0u; }
DI float gran_val(u32 lo) { return __uint_as_float(lo << 8); }

template <int N, int SLP = 1> DI void gather128(rsrc_t rs, const int (&off)[N], u32x4 (&v)[N]) {
#pragma unroll
  for (int i = 0; i < N; ++i) v[i] = ld128(rs, off[i]);
  _Pragma("unroll 1") for (u32 spins = 0; spins < SPIN_CAP; ++spins) {
    bool need[N], bad = false;
#pragma unroll
    for (int i = 0; i < N; ++i) { need[i] = pois4(v[i]); bad = bad || need[i]; }
    if (!__any(bad)) break;
    __builtin_amdgcn_s_sleep(SLP);
    asm volatile("" ::: "memory");
    __builtin_amdgcn_sched_barrier(0);
#pragma unroll
    for (int i = 0; i < N; ++i)
      if (need[i]) v[i] = ld128(rs, off[i]);
  }
}
template <int N> DI void waitvm() { asm volatile("s_waitcnt vmcnt(%0)" ::"n"(N) : "memory"); }
constexpr u32 LSPIN_CAP = 1u << 22;
DI void lds_wait(volatile LAS u32* p, u32 val) {
  _Pragma("unroll 1") for (u32 n = 0; *p < val && n < LSPIN_CAP; ++n) __builtin_amdgcn_s_sleep(1);
  asm volatile("" ::: "memory");
}
#define WAVE_LDS_SYNC() do { __builtin_amdgcn_fence(__ATOMIC_RELEASE, "wavefront"); __builtin_amdgcn_wave_barrier(); __builtin_amdgcn_fence(__ATOMIC_ACQUIRE, "wavefront"); } while (0)
#define SB() __builtin_amdgcn_sched_barrier(0)
#define RARE(c) __builtin_expect(!!(c), 0)

constexpr int PS = 28672;
constexpr int L_QI = 4 * PS;
constexpr int L_P = L_QI + 32768;
constexpr int L_REF = L_P + 11264;
constexpr int L_MX = L_REF + 1408;
constexpr int L_ALW = L_MX + 128;
constexpr int L_DUMP = L_ALW + 512;
constexpr int L_QOFF = 131072;
constexpr int L_H1RED = L_QOFF;
constexpr int L_RED = L_QOFF + 16384;
constexpr int L_Z = L_RED, L_HH = L_RED + 7 * 1088, L_ZERO = L_HH + 64, L_CF = L_ZERO + 64;
constexpr int L_MISC = L_QOFF + 30720;
constexpr int L_CTR = L_MISC;
constexpr int L_WMAX = L_MISC + 256;
constexpr int L_ML = L_MISC + 512;

#define Z4 (u32x4{0u, 0u, 0u, 0u})
DI float max5(f32x4 v, float e) { return fmaxf(fmaxf(fmaxf(fmaxf(v.x, v.y), v.z), v.w), e); }

template <bool NT> DI void dma(u32 m, u32 voff, const void* sb) {
  if (NT) asm volatile("s_mov_b32 m0, %0\n s_nop 0\n global_load_lds_dwordx4 %1, %2 nt" ::"s"(m), "v"(voff), "s"(sb) : "memory");
  else asm volatile("s_mov_b32 m0, %0\n s_nop 0\n global_load_lds_dwordx4 %1, %2" ::"s"(m), "v"(voff), "s"(sb) : "memory");
}
DI void dma_sc1(u32 m, u32 voff, const void* sb) {
  asm volatile("s_mov_b32 m0, %0\n s_nop 0\n global_load_lds_dwordx4 %1, %2 sc1" ::"s"(m), "v"(voff), "s"(sb) : "memory");
}
DI void lds_add(int idx) {
  SB();
  asm volatile("s_mov_b64 exec, 1\n ds_add_u32 %0, %1 offset:%2\n s_mov_b64 exec, -1" ::"v"((u32)L_CTR), "v"(1u), "n"(idx * 4) : "memory");
  SB();
}
DI u32 spin_at(u32 a, u32 val) {
  u32 t, c, m, cap;
  asm volatile("s_mov_b32 %3, %6\n 1: ds_read_b32 %0, %4\n s_waitcnt lgkmcnt(0)\n v_readfirstlane_b32 %1, %0\n s_and_b32 %2, %1, 255\n s_cmp_ge_u32 %2, %5\n s_cbranch_scc1 2f\n"
               "s_sub_u32 %3, %3, 1\n s_cbranch_scc0 1b\n 2:"
               : "=&v"(t), "=&s"(c), "=&s"(m), "=&s"(cap) : "v"(a), "s"(val), "n"(LSPIN_CAP) : "memory", "scc");
  return c;
}
DI void lds_spin(volatile LAS u32* p, u32 val) { spin_at((u32)(uintptr_t)p, val); }
enum { C_SOK = 0, C_BH = 4, C_PD = 12, C_H1 = 18, C_PAR = 19, C_VOTE = 20, C_FREE = 22, C_QRDY = 23, C_MXC = 25, C_SD = 26, C_GX = 27, C_ML = 28, C_MK = 32, C_P = 44 };

DI u32 kv_mark(int nm, int skip) { return L_CTR + 4 * C_MK + __builtin_amdgcn_readfirstlane(4 * nm + (nm >= skip ? 4 : 0)); }
#define KV_TS
#define KV_TSO
#define KV_TSA
#define KV_TSP
#define KV_TRY(l)                                                                                                        \
  "7: s_getreg_b32 %[h], hwreg(HW_REG_IB_STS)\n s_and_b32 %[h], %[h], 0xC0000F\n s_max_i32 %[h], %[h], %[x]\n s_sub_i32 %[h], %[h], %[x]\n s_cmp_gt_i32 %[h], %[k4]\n s_cbranch_scc1 " l "\n" \
  "s_waitcnt vmcnt(%[x4])\n s_cmp_eq_u32 %[k4], 4\n s_cbranch_scc1 8f\n s_waitcnt vmcnt(%[x])\n"                        \
  "8: v_subrev_u32 %[t], %[k4], %[ml]\n s_mov_b64 exec, 1\n ds_add_u32 %[t], %[one] offset:12\n s_mov_b64 exec, -1\n"   \
  "s_sub_i32 %[k4], %[k4], 4\n" KV_TS "s_branch 7b\n"
template <int X> DI void kv_try(int& k4, u32 ml, u32 one KV_TSA) {
  u32 t, h;
  asm volatile(KV_TRY("9f") "9:" : [t] "=&v"(t), [h] "=&s"(h), [k4] "+s"(k4) KV_TSO : [ml] "v"(ml), [one] "v"(one), [x] "n"(X), [x4] "n"(X + 4) : "memory", "scc");
}
template <int X> DI u32 spin_try(int idx, u32 val, int& k4, u32 ml, u32 one KV_TSA) {
  u32 t, h, c, cap;
  asm volatile("s_mov_b32 %[cap], %[n]\n 1: ds_read_b32 %[t], %[ml] offset:%[o]\n s_waitcnt lgkmcnt(0)\n v_readfirstlane_b32 %[c], %[t]\n s_and_b32 %[h], %[c], 255\n"
               "s_cmp_ge_u32 %[h], %[v]\n s_cbranch_scc1 2f\n" KV_TRY("9f") "9: s_sub_u32 %[cap], %[cap], 1\n s_cbranch_scc0 1b\n 2:"
               : [t] "=&v"(t), [h] "=&s"(h), [c] "=&s"(c), [cap] "=&s"(cap), [k4] "+s"(k4) KV_TSO
               : [ml] "v"(ml), [one] "v"(one), [o] "n"(4 * idx), [v] "n"(val), [n] "n"(LSPIN_CAP), [x] "n"(X), [x4] "n"(X + 4) : "memory", "scc");
  return c;
}
template <int W> DI void kv_load(const u16* ckv, u32 vl, int lw, u32 g0, u32 g1, u32 m, int n0, int n1, int& nm, int skip) {
#pragma unroll 1
  for (int n = n0; n < n1; ++n) {
#define KV_POST(k) ".if (%[wm] | 4) == " #k "\n s_cmp_gt_i32 %[nm], %[n2]\n s_cbranch_scc1 20f\n s_mov_b64 exec, 1\n ds_add_u32 %[ma], %[one]\n s_mov_b64 exec, -1\n s_add_i32 %[nm], %[nm], 1\n 20:\n .endif\n"
#define KV_W(o, k) "s_waitcnt vmcnt(%[wm])\n" KV_POST(k) "global_load_lds_dwordx4 %[vo], %[p] offset:" #o " nt\n"
#define KV_M "s_add_u32 m0, m0, %[ms]\n"
    asm volatile("s_mov_b32 m0, %[m]\n" KV_W(0, 4) KV_M KV_W(256, 5) KV_M KV_W(512, 6) KV_M KV_W(768, 7)
                 : [nm] "+s"(nm)
                 : [m] "s"(m), [vo] "v"(vl + g0 + ((lw + n) & 3) * 4096), [p] "s"(ckv), [ma] "v"(kv_mark(nm, skip)),
                   [one] "v"(1u), [n2] "s"(__builtin_amdgcn_readfirstlane(n - (W > 4 ? 2 : 1))), [wm] "n"(W - 1), [ms] "n"(PS - 256)
                 : "memory", "scc");
    g0 = g1;
    g1 += 192 * 16384;
    m += n + 1 == skip ? 8192 : 4096;
  }
}
DI void until(u32 t0, u32 dt) {
  while ((u32)__builtin_amdgcn_s_memrealtime() - t0 < dt) __builtin_amdgcn_s_sleep(2);
}
DI bf16x8 tr_frag(unsigned char* lds, u32 a0, u32 a1) {
  const s16x4 b0 = __builtin_amdgcn_ds_read_tr16_b64_v4i16((LAS s16x4*)(lds + a0));
  const s16x4 b1 = __builtin_amdgcn_ds_read_tr16_b64_v4i16((LAS s16x4*)(lds + a1));
  bf16x8 v;
  v[0] = b0[0]; v[1] = b0[1]; v[2] = b0[2]; v[3] = b0[3]; v[4] = b1[0]; v[5] = b1[1]; v[6] = b1[2]; v[7] = b1[3];
  return v;
}
DI float sum4(f32x4 v) { const f32x2 t = f32x2{v.x, v.y} + f32x2{v.z, v.w}; return t.x + t.y; }

DI void ln_stats(u32x4 yv, float& mb, float& rsd) {
  const float a1 = dot2(yv.x, 0x3F803F80u, dot2(yv.y, 0x3F803F80u, 0.f)) + dot2(yv.z, 0x3F803F80u, dot2(yv.w, 0x3F803F80u, 0.f));
  const float a2 = dot2(yv.x, yv.x, dot2(yv.y, yv.y, 0.f)) + dot2(yv.z, yv.z, dot2(yv.w, yv.w, 0.f));
  const float sr = x16_sum(row16_sum(rs32(a1, a2))) * (1.f / 512.f);
  const float s1 = __uint_as_float(__builtin_amdgcn_readlane(__float_as_uint(sr), 0)), s2 = __uint_as_float(__builtin_amdgcn_readlane(__float_as_uint(sr), 32));
  const float var = rbf(fmaxf(s2 - s1 * s1, 0.f));
  mb = rbf(s1);
  const float sd = rbf(__builtin_amdgcn_sqrtf(rbf(var + 1e-5f)));
  rsd = __builtin_amdgcn_rcpf(sd);
  rsd = rsd * (2.f - sd * rsd);
}

__global__ __launch_bounds__(512, 2) void k_main(const u16* __restrict__ x, const u16* __restrict__ ckv,
                                                 const u16* __restrict__ w_q, const u16* __restrict__ w_uk,
                                                 const u16* __restrict__ w_uv, const u16* __restrict__ w_o,
                                                 const u16* __restrict__ w_gate, const u16* __restrict__ w_up,
                                                 const u16* __restrict__ w_down, const u16* __restrict__ cvec,
                                                 const u16* __restrict__ w_mod, u16* __restrict__ out,
                                                 unsigned char* ws) {
  __shared__ __attribute__((aligned(16))) unsigned char lds[163840];
  const int w = __builtin_amdgcn_readfirstlane(threadIdx.x >> 6), j = blockIdx.x;
  int lane = threadIdx.x & 63;
  const int xc = j & 7, ii = j >> 3;
  STAMP_DECL;
  STAMP(0);
  u32x2 ev;
  u32 xm, vh;
  {
    u64 p, pc, pw;
    u32 a, b, c, d, f, t;
    asm volatile("s_lshr_b32 %[f], %[j], 6\n s_cmp_eq_u32 %[f], 1\n s_cselect_b32 %[a], 0x50, 16\n"
                 "s_load_dwordx2 %[p], %[ka], %[a]\n s_load_dwordx2 %[pw], %[ka], 0x60\n"
                 "s_lshr_b32 %[d], %[j], 3\n s_cmp_lg_u32 %[w], 7\n s_cbranch_scc1 2f\n"
                 "s_and_b32 %[a], %[d], 15\n s_lshl_b32 %[a], %[a], 12\n v_lshl_add_u32 %[t], %[lane], 6, %[a]\n v_min_u32 %[t], %[pfl], %[t]\n"
                 "s_getpc_b64 %[pc]\n s_mov_b32 m0, %[dump]\n s_nop 0\n global_load_lds_dword %[t], %[pc]\n"
                 "2: s_load_dwordx2 %[pc], %[ka], 0x48\n s_and_b32 %[c], %[j], 7\n s_lshl_b32 %[c], %[c], 10\n s_cmp_eq_u32 %[f], 0\n s_cbranch_scc0 3f\n"
                 "s_and_b32 %[a], %[d], 1\n s_lshl1_add_u32 %[a], %[w], %[a]\n s_lshl_b32 %[a], %[a], 18\n s_add_u32 %[c], %[c], %[a]\n"
                 "s_lshr_b32 %[a], %[d], 1\n s_lshl_b32 %[a], %[a], 8\n s_add_u32 %[c], %[c], %[a]\n s_lshl_b32 m0, %[w], 14\n"
                 "v_lshrrev_b32 %[t], 4, %[lane]\n v_mul_u32_u24 %[t], 0x204, %[t]\n v_and_b32 %[vh], 15, %[lane]\n v_xor_b32 %[vh], %[vh], %[t]\n"
                 "v_lshl_add_u32 %[vh], %[vh], 4, %[c]\n v_xor_b32 %[t], 0x8010, %[vh]\n s_waitcnt lgkmcnt(0)\n"
                 "global_load_lds_dwordx4 %[vh], %[p]\n s_add_u32 m0, m0, 0x400\n s_nop 0\n"
                 "global_load_lds_dwordx4 %[t], %[p]\n s_add_u32 m0, m0, 0x400\n v_xor_b32 %[t], 0x10020, %[vh]\n"
                 "global_load_lds_dwordx4 %[t], %[p]\n s_add_u32 m0, m0, 0x400\n v_xor_b32 %[t], 0x18030, %[vh]\n"
                 "global_load_lds_dwordx4 %[t], %[p]\n s_branch 1f\n"
                 "3: s_lshl2_add_u32 %[d], %[d], %[w]\n s_sub_u32 %[d], %[d], 68\n s_lshl_b32 %[d], %[d], 16\n s_add_u32 %[d], %[d], %[c]\n"
                 "s_and_b32 %[a], %[j], 3\n s_lshl2_add_u32 %[b], %[a], %[w]\n s_mul_i32 %[b], %[b], 0x30000\n s_lshl_b32 %[a], %[a], 8\n"
                 "v_lshl_add_u32 %[xm], %[lane], 2, %[a]\n"
                 "s_and_b32 %[a], %[j], 60\n s_lshl_b32 %[a], %[a], 5\n s_add_u32 %[b], %[b], %[a]\n"
                 "s_lshl_b32 %[c], %[w], 12\n s_add_u32 %[c], %[c], %[mq]\n s_lshl_b32 %[a], %[w], 10\n s_add_u32 %[a], %[a], %[mh]\n"
                 "s_cmp_eq_u32 %[f], 1\n s_cselect_b32 %[d], %[b], %[d]\n s_cselect_b32 m0, %[c], %[a]\n s_cselect_b32 %[a], 48, 1\n s_cselect_b32 %[c], 0, 4\n"
                 "v_lshrrev_b32 %[t], 3, %[lane]\n v_mul_u32_u24 %[t], %[a], %[t]\n v_and_b32 %[vh], 7, %[lane]\n v_lshl_add_u32 %[vh], %[t], 3, %[vh]\n"
                 "v_lshl_add_u32 %[vh], %[vh], 4, %[d]\n"
                 "s_cselect_b32 %[b], 6, 1\n s_cselect_b32 %[a], 0x400, 0\n s_lshl_b32 %[b], %[b], 13\n"
                 "s_xor_b32 %[c], %[c], %[w]\n s_bitcmp1_b32 %[c], 2\n v_mov_b32 %[t], 0\n"
                 "s_waitcnt lgkmcnt(0)\n s_cbranch_scc1 1f\n s_cmp_eq_u32 %[f], 1\n s_cbranch_scc0 4f\n"
                 "global_load_dword %[xm], %[xm], %[pc]\n global_load_dwordx2 %[ev], %[t], %[pw] sc1\n"
                 "4: global_load_lds_dwordx4 %[vh], %[p]\n s_add_u32 m0, m0, %[a]\n v_add_u32 %[t], %[b], %[vh]\n"
                 "global_load_lds_dwordx4 %[t], %[p]\n s_add_u32 m0, m0, %[a]\n v_add_u32 %[t], %[b], %[t]\n"
                 "global_load_lds_dwordx4 %[t], %[p]\n s_add_u32 m0, m0, %[a]\n v_add_u32 %[t], %[b], %[t]\n"
                 "global_load_lds_dwordx4 %[t], %[p]\n"
                 "1:\n"
                 : [xm] "=&v"(xm), [ev] "=&v"(ev), [vh] "=&v"(vh), [t] "=&v"(t), [p] "=&s"(p), [pc] "=&s"(pc), [pw] "=&s"(pw), [a] "=&s"(a), [b] "=&s"(b), [c] "=&s"(c),
                   [d] "=&s"(d), [f] "=&s"(f)
                 : [j] "s"(j), [w] "s"(w), [lane] "v"(lane), [ka] "s"(__builtin_amdgcn_kernarg_segment_ptr()), [pfl] "n"(PF_LEN), [dump] "n"(L_DUMP),
                   [mq] "n"(L_QOFF + 8192), [mh] "n"(L_P)
                 : "memory", "scc");
  }
  SB();
  const u32 t0 = (u32)__builtin_amdgcn_s_memrealtime() - 35u;
  const bool isQ = j < 64, isS0 = j >= 64 && j < 128;
  const int hq = xc * 4 + (ii >> 1), dh = ii & 1, sj = j - 64;
  const int lw = w - 4, myh = xc >> 2;
  int col = lane & 15, g4 = lane >> 4, tq = col >> 2, tp = lane & 3;
  const rsrc_t rs = __builtin_amdgcn_make_buffer_rsrc((void*)ws, 0, WS_TOTAL, 0x00020000);
  volatile LAS u32* ctr = (volatile LAS u32*)(lds + L_CTR);
  SB();
  STAMP(25);
  if (w < 2) *(u32*)(lds + (w == 0 ? L_CTR + lane * 4 : L_MX + (lane & 31) * 4)) = w == 0 ? 0u : 0xFF800000u;
  BAR();
  const bool tl2 = isS0;
  const int t3 = xc < 4 ? (ii < 8 ? xc * 8 + ii : -1) : (ii < 6 ? 8 + xc * 6 + ii : -1);
  const int t4 = (ii >= 8 && ii < 10) ? xc * 2 + (ii - 8) : -1;
  const u32 owno = (w >> 1) * 32768 + 8192 + (w & 1) * 12288;
  const int ks = (xc & 3) * 2 + myh, cg = ii & 7;
  const int h = (j >> 2) & 31, c4 = j & 3;
  u32x4 wv[8];
  u32 tlo, thi;
  int gx;
#define RELANE() do { asm volatile("v_mbcnt_lo_u32_b32 %0, -1, 0\n v_mbcnt_hi_u32_b32 %0, -1, %0" : "=v"(lane)); col = lane & 15; g4 = lane >> 4; tq = col >> 2; tp = lane & 3; } while (0)
#define TAG_OF(e0, e1)                                                                                                  \
  do {                                                                                                                  \
    const u64 tag = (((u64)(e1) << 32) | (e0)) + 1ull;                                                                  \
    tlo = __builtin_amdgcn_readfirstlane((u32)tag); thi = __builtin_amdgcn_readfirstlane((u32)(tag >> 32));             \
  } while (0)
  const u32x4 P4 = {POISON, POISON, POISON, POISON};
#define COUNT_VOTES(PRE)                                                                                                \
  do {                                                                                                                  \
    u32x2 a_ = {0u, 0u};                                                                                                \
    if (PRE) a_ = *(const u32x2*)(lds + L_QI + lane * 8);                                                               \
    _Pragma("unroll 1") for (u32 spins = 0; spins < SPIN_CAP; ++spins) {                                                \
      if (((PRE) || spins) && __all(gran_ok(a_.x, a_.y, tlo, thi))) break;                                              \
      if (spins) __builtin_amdgcn_s_sleep(8);                                                                           \
      asm volatile("" ::: "memory");                                                                                    \
      a_ = ld64(rs, WS_VOTE + xc * 2048 + lane * 8);                                                                    \
    }                                                                                                                   \
    const float vt_ = wave_sum((float)(a_.x & 1023u));                                                                  \
    if (lane == 0) ctr[C_PAR] = G_OF(vt_);                                                                              \
    VOTE_STAMP(vt_);                                                                                                    \
    STAMP(8);                                                                                                           \
  } while (0)
#define S_POLL(a_)                                                                                                      \
  _Pragma("unroll 1") for (u32 spins = 0; spins < SPIN_CAP && RARE(__any(((a_.x ^ tlo) | (a_.y ^ thi)) != 0u)); ++spins) { \
    __builtin_amdgcn_s_sleep(8);                                                                                        \
    asm volatile("" ::: "memory");                                                                                      \
    a_ = ld64(rs, WS_SFLAG + xc * 2048 + lane * 8);                                                                     \
  }
#define MB_OFF(i) (WS_MODB + ((lane >> 4) * 2048 + ((i) >> 1) * 512 + 128 * lw + 64 * ((i) & 1) + (lane & 15) * 4) * 4)

  if (!isQ && w >= 4) {
    if (isS0) {
      if (w == 4) {
        const u32x2 e4 = ld64(rs, WS_EPOCH);
        st128(rs, WS_QABS + sj * 1024 + lane * 16, P4);
        st128(rs, lane < 32 ? WS_MODB + sj * 512 + lane * 16 : WS_TOTAL, P4);
        st128(rs, WS_PROBE + il(sj * 1024 + lane * 16, 0), Z4);
        st128(rs, WS_PROBE + il(sj * 1024 + lane * 16, 1), Z4);
        waitvm<0>();
        TAG_OF(e4.x, e4.y);
        if (lane < 8) st64(rs, WS_SFLAG + lane * 2048 + sj * 8, tlo, thi);
        STAMP(27);
      }
      lds_wait(ctr + C_H1, 4u);
    } else {
#pragma unroll
      for (int i = 4; i < 16; ++i) { waitvm<7>(); dma<false>(L_P + w * 1024, vh + (i & 7) * 8192, i < 8 ? w_q : w_uk); }
    }
    const u32 vl = g4 * 1024 + ((col ^ ((g4 << 2) | lw)) << 4);
    int nm = 0, ns = isS0 ? 6 : 7;
    const int skip = isS0 ? 4 : 99;
    if (isS0) kv_load<WA0>(ckv, vl, lw, sj * 16384, (sj + 192) * 16384, lw * 1024, 0, 2, nm, skip);
    else kv_load<WIN>(ckv, vl, lw, (1792 + j) * 16384, sj * 16384, lw * 1024, 0, 2, nm, skip);
    const rsrc_t rw = __builtin_amdgcn_make_buffer_rsrc((void*)(isS0 ? w_o : w_uv), 0, 4194304, 0x00020000);
    int wo = isS0 ? (ks * 512 + 64 * w + (lane >> 3)) * 1024 + cg * 128 + (lane & 7) * 16 : (c4 * 128 + 32 * lw + g4) * 8192 + h * 256 + col * 16;
    const int wst = isS0 ? 8192 : 32768;
    PIN(wo);
#define WV_LD(i) do { SB(); wv[i] = __builtin_amdgcn_raw_buffer_load_b128(rw, wo, (i) * wst, 2); wv[(i) + 1] = __builtin_amdgcn_raw_buffer_load_b128(rw, wo, ((i) + 1) * wst, 2); SB(); } while (0)
    u32 va[8];
#pragma unroll
    for (int i = 0; i < 8; ++i) { va[i] = lw * PS + tr_addr(lane, i >> 1, 0, i & 1); PIN(va[i]); }
    int eg[8];
#pragma unroll
    for (int u = 0; u < 8; ++u) { eg[u] = WS_PART + il21(((4 * (u >> 1) + 2 * (g4 >> 1) + (u & 1)) * 4 + lw) * 65536 + sj * 256 + col * 16, g4 & 1); PIN(eg[u]); }
    u32 pr_ = L_P + lane * 16;
    PIN(pr_);
    u32 ml = L_CTR + 4 * (C_MK + 7), one = 1u;
    PIN(ml); PIN(one);
    bf16x8 vf[4][4];
#define V_READ(i, slot) do { asm volatile("" ::: "memory"); SB(); _Pragma("unroll") for (int c = 0; c < 4; ++c) vf[i][c] = tr_frag(lds, va[2 * c] + (slot) * 4096, va[2 * c + 1] + (slot) * 4096); SB(); } while (0)
    SB();
    kv_load<WIN>(ckv, vl, lw, (sj + (isS0 ? 384 : 192)) * 16384, (sj + (isS0 ? 576 : 384)) * 16384, lw * 1024 + 8192, 2, ns, nm, skip);
    lds_wait(ctr + C_MK + 3, 4u);
    V_READ(0, 0); V_READ(1, 1); V_READ(2, 2); V_READ(3, 3);
    lds_add(C_FREE);
#pragma unroll
    for (int i = 0; i < 16; ++i) PIN(vf[i >> 2][i & 3]);
    STAMP(2);
    lds_wait(ctr + C_FREE, 8u);
    STAMP(5);
    kv_load<WIN>(ckv, vl, lw, (sj + 6 * 192) * 16384, (sj + 7 * 192) * 16384, lw * 1024, ns, ns + 4, nm, skip);
    int k4 = 4;
    STAMP(3);
    f32x16 acc[4];
#pragma unroll
    for (int c = 0; c < 4; ++c)
#pragma unroll
      for (int i = 0; i < 16; ++i) acc[c][i] = 0.f;
    bf16x8 pf[4], pn[4];
    u32 c_;
#define P_SPIN(g, n) c_ = spin_try<(g) ? 8 : 0>(C_P + (g) - C_MK - 7, n, k4, ml, one KV_TSP)
#define TRY() kv_try<8>(k4, ml, one KV_TSP)
#define P_READ(d, i0, i1, T0) _Pragma("unroll") for (int i = (i0); i < (i1); ++i) d[i] = *(volatile LAS bf16x8*)(lds + pr_ + ((T0) + i) * 1024)
#define PV(p, i) do { SB(); _Pragma("unroll") for (int c = 0; c < 4; ++c) acc[c] = __builtin_amdgcn_mfma_f32_32x32x16_bf16(p[i], vf[i][c], acc[c], 0, 0, 0); SB(); } while (0)
#define GX_USE(v) do { gx = __builtin_amdgcn_readfirstlane(v); _Pragma("unroll") for (int u = 0; u < 8; ++u) { eg[u] ^= gx; PIN(eg[u]); } } while (0)
#define MK_WAIT(T) spin_try<8>((T) - 7, 4u, k4, ml, one KV_TSP)
#define PVV(p, i, d, slot)                                                                                               \
  _Pragma("unroll") for (int c = 0; c < 4; ++c) {                                                                        \
    SB();                                                                                                                \
    acc[c] = __builtin_amdgcn_mfma_f32_32x32x16_bf16(p[i], vf[i][c], acc[c], 0, 0, 0);                                   \
    SB();                                                                                                                \
    vf[d][c] = tr_frag(lds, va[2 * c] + (slot) * 4096, va[2 * c + 1] + (slot) * 4096);                                   \
    SB();                                                                                                                \
  }
#define CT(i) (*(volatile LAS u32*)(lds + ml + 4 * ((i) - C_MK - 7)))
    P_SPIN(0, 4u);
    P_READ(pf, 0, 4, 0);
    const u32 k7 = CT(C_MK + 7);
    asm volatile("" ::: "memory");
    STAMP(6);
    PVV(pf, 0, 0, 4); kv_try<0>(k4, ml, one KV_TSP); WV_LD(0);
    PVV(pf, 1, 1, 5); kv_try<2>(k4, ml, one KV_TSP); WV_LD(2);
    PVV(pf, 2, 2, 6); WV_LD(4);
    u32 cv = CT(C_P + 1);
    P_READ(pn, 0, 4, 4);
    PVV(pf, 3, 3, 0); WV_LD(6);
    if (RARE(__builtin_amdgcn_readfirstlane(k7) < 4u)) { MK_WAIT(7); V_READ(0, 4); V_READ(1, 5); V_READ(2, 6); V_READ(3, 0); }
    STAMP(9);
    c_ = __builtin_amdgcn_readfirstlane(cv);
    if (RARE((c_ & 255u) != 4u)) { P_SPIN(1, 4u); P_READ(pn, 0, 4, 4); }
    int slow = -1;
    if (RARE(c_ > 255u)) slow = 4;
    else {
      if (!isS0) PV(pn, 0);
      TRY();
      const u32 k9 = CT(C_MK + 9);
      asm volatile("" ::: "memory");
      PVV(pn, 1, 0, 1); TRY();
      PVV(pn, 2, 1, 2);
      cv = CT(C_P + 2);
      P_READ(pf, 0, 2, 8);
      const u32 k10 = CT(C_MK + 10);
      asm volatile("" ::: "memory");
      PVV(pn, 3, 2, 3);
      if (RARE(__builtin_amdgcn_readfirstlane(k9) < 4u)) { MK_WAIT(9); V_READ(0, 1); V_READ(1, 2); }
      STAMP(10);
      c_ = __builtin_amdgcn_readfirstlane(cv);
      if (RARE((c_ & 255u) != 4u)) { P_SPIN(2, 4u); P_READ(pf, 0, 2, 8); }
      if (RARE(c_ > 255u)) slow = 8;
      else {
        TRY();
        PV(pf, 0);
        const u32 g0 = ctr[C_GX];
        u32 g1 = ctr[C_PAR];
        cv = CT(C_P + 3);
        P_READ(pf, 2, 3, 8);
        PV(pf, 1);
        STAMP(4);
        if (RARE(__builtin_amdgcn_readfirstlane(k10) < 4u)) { waitvm<8>(); TRY(); lds_spin(ctr + C_MK + 10, 4u); V_READ(2, 3); }
        if (RARE(__builtin_amdgcn_readfirstlane(g0) == 0u)) { lds_spin(ctr + C_GX, 1u); g1 = ctr[C_PAR]; }
        GX_USE(g1);
        STAMP(11);
        c_ = __builtin_amdgcn_readfirstlane(cv);
        _Pragma("unroll 1") for (u32 n_ = 0; RARE((c_ & 255u) != 2u) && n_ < LSPIN_CAP; ++n_) { cv = CT(C_P + 3); P_READ(pf, 2, 3, 8); c_ = __builtin_amdgcn_readfirstlane(cv); }
        if (RARE(c_ > 255u)) slow = 10;
        else PV(pf, 2);
      }
    }
    if (RARE(slow >= 0)) {
      MK_WAIT(10);
      float R = ceilf(*(const float*)(lds + L_MX + (lane & 31) * 4));
#pragma unroll 1
      for (int T = slow; T < 11; ++T) {
        if (T == 4 && isS0) continue;
        spin_at(L_CTR + 4 * (C_P + (T < 8 ? 1 : T < 10 ? 2 : 3)), T < 10 ? 4u : 2u);
        const float rf = *(const float*)(lds + L_REF + T * 128 + (lane & 31) * 4);
        u32x4 p = *(const u32x4*)(lds + pr_ + T * 1024);
        const u32 so_ = (T < 7 ? T : T - 7) * 4096;
#pragma unroll
        for (int c = 0; c < 4; ++c) vf[0][c] = tr_frag(lds, va[2 * c] + so_, va[2 * c + 1] + so_);
        const float Rn = fmaxf(R, rf), ps = __builtin_amdgcn_exp2f(rf - Rn);
        *(float*)(lds + L_ALW + lw * 128 + (lane & 31) * 4) = __builtin_amdgcn_exp2f(R - Rn);
        R = Rn;
        WAVE_LDS_SYNC();
#pragma unroll
        for (int i = 0; i < 4; ++i) {
          const f32x4 al = *(const f32x4*)(lds + L_ALW + lw * 128 + (lane >> 5) * 16 + i * 32);
#pragma unroll
          for (int c = 0; c < 4; ++c)
#pragma unroll
            for (int r = 0; r < 4; ++r) acc[c][4 * i + r] *= al[r];
          p[i] = pk2bf(lo16(p[i]) * ps, hi16(p[i]) * ps);
        }
        WAVE_LDS_SYNC();
        pf[0] = __builtin_bit_cast(bf16x8, p);
        PV(pf, 0);
      }
      if (slow != 10) { lds_spin(ctr + C_GX, 1u); GX_USE(ctr[C_PAR]); }
    }
    STAMP(30);
#pragma unroll
    for (int u = 0; u < 8; u += 2) {
      u32 c[8];
#pragma unroll
      for (int k = 0; k < 8; ++k) c[k] = pk2bf(acc[k & 2][2 * u + (k & 1) + (k >> 2) * 2], acc[(k & 2) + 1][2 * u + (k & 1) + (k >> 2) * 2]);
      SB();
#pragma unroll
      for (int k = 0; k < 2; ++k) {
        const auto r0 = __builtin_amdgcn_permlane16_swap(c[4 * k], c[4 * k + 1], false, false);
        const auto r1 = __builtin_amdgcn_permlane16_swap(c[4 * k + 2], c[4 * k + 3], false, false);
        st128(rs, eg[u + k], u32x4{r0[0], r1[0], r0[1], r1[1]});
      }
      SB();
    }
    STAMP(31);
    if (!isS0) {
#pragma unroll
      for (int i = 0; i < 8; ++i) *(u32x4*)(lds + lw * PS + 16384 + off_b(4 * i + g4, col)) = wv[i];
    }
    {
      int fo = lane < 16 ? (WS_LSE + il(sj * 512 + lw * 128 + (lane & 7) * 16, lane >> 3)) ^ gx : WS_TOTAL;
      lds_spin(ctr + C_ML, 1u);
      u32x4 ml4 = *(const u32x4*)(lds + L_WMAX + col * 16);
      PIN(fo); PIN(ml4);
      waitvm<0>();
      __builtin_amdgcn_s_waitcnt(0x0F70);
      st128(rs, fo, ml4);
      lds_add(C_PD);
    }
    STAMP(13);
  } else if (!isQ) {
    if (isS0) {
      u32 ta = L_QOFF + 8192 + (8 * g4 + tq) * 128 + w * 32 + tp * 8;
      u32 sa = L_H1RED + w * 256;
      int so = col < 2 ? WS_SC + (((sj >> 5) * 4 + (sj & 3)) * 512 + ((sj >> 2) & 7) * 64 + 16 * w + 4 * g4 + 2 * col) * 8 : WS_TOTAL;
      PIN(ta); PIN(so); PIN(sa);
      u32 c2, e0, e1;
      asm volatile("s_waitcnt vmcnt(4) ; HEAD_WAIT\n v_mov_b32 %0, %3\n v_mov_b32 %1, %4\n v_mov_b32 %2, %5" : "=&v"(c2), "=&v"(e0), "=&v"(e1) : "v"(xm), "v"(ev.x), "v"(ev.y) : "memory");
      TAG_OF(e0, e1);
      bf16x8 sf[4];
      u32x2 wt[8];
      asm volatile("ds_write_b32 %4, %5\n"
                   "ds_read_b128 %0, %6\n ds_read_b128 %1, %6 offset:64\n ds_read_b128 %2, %6 offset:128\n ds_read_b128 %3, %6 offset:192\n s_waitcnt lgkmcnt(0)"
                   : "=&v"(sf[0]), "=&v"(sf[1]), "=&v"(sf[2]), "=&v"(sf[3])
                   : "v"(sa + lane * 4), "v"(pk2bf(silu_bf(lo16(c2)), silu_bf(hi16(c2)))), "v"(sa + 16 * g4) : "memory");
      waitvm<0>();
      lds_add(C_BH);
      lds_spin(ctr + C_BH, 4u);
      STAMP(1);
      asm volatile("ds_read_b64_tr_b16 %0, %8\n ds_read_b64_tr_b16 %1, %8 offset:512\n ds_read_b64_tr_b16 %2, %8 offset:4096\n ds_read_b64_tr_b16 %3, %8 offset:4608\n"
                   "ds_read_b64_tr_b16 %4, %8 offset:8192\n ds_read_b64_tr_b16 %5, %8 offset:8704\n ds_read_b64_tr_b16 %6, %8 offset:12288\n ds_read_b64_tr_b16 %7, %8 offset:12800\n"
                   "s_waitcnt lgkmcnt(0)"
                   : "=&v"(wt[0]), "=&v"(wt[1]), "=&v"(wt[2]), "=&v"(wt[3]), "=&v"(wt[4]), "=&v"(wt[5]), "=&v"(wt[6]), "=&v"(wt[7]) : "v"(ta) : "memory");
      f32x4 d = {0.f, 0.f, 0.f, 0.f};
#pragma unroll
      for (int k = 0; k < 4; ++k)
        d = __builtin_amdgcn_mfma_f32_16x16x32_bf16(__builtin_bit_cast(bf16x8, u32x4{wt[2 * k].x, wt[2 * k].y, wt[2 * k + 1].x, wt[2 * k + 1].y}), sf[k], d, 0, 0, 0);
      st_gran2(rs, so, (col & 1) ? d[2] : d[0], (col & 1) ? d[3] : d[1], tlo, thi);
      lds_add(C_H1);
      STAMP(2);
      until(t0, SCP_T);
      dma_sc1(L_P + w * 1024, WS_SC + ((ii & 7) * 4 + w) * 1024 + lane * 16, ws);
    }
    u32 ab = off_b(col, 4 * g4), qv = WS_QABS + myh * 8192 + g4 * 1024 + w * 256 + ((col ^ (g4 << 2)) << 4);
    const int hb = 16 * (w & 1) + col;
    u32 pw = L_P + (g4 >> 1) * 512 + hb * 16 + (g4 & 1) * 8, mw = hb * 4;
    PIN(ab); PIN(qv); PIN(pw); PIN(mw);
    if (w == 2) {
      until(t0, QF_T);
      const u32x2 e2 = ld64(rs, WS_EPOCH);
      u32x2 f2 = ld64(rs, WS_SFLAG + xc * 2048 + lane * 8);
      TAG_OF(e2.x, e2.y);
      S_POLL(f2);
      lds_add(C_SOK);
      STAMP(3);
    }
    bf16x8 af[16], qf[16][2];
    u32 aa[16], qa[32];
#pragma unroll
    for (int i = 0; i < 16; ++i) {
      aa[i] = (ab ^ ((i & 3) << 4)) + (((w + (i >> 2)) & 3) * PS + w * 4096); PIN(aa[i]);
#pragma unroll
      for (int u = 0; u < 2; ++u) { qa[2 * i + u] = (ab ^ ((i & 3) << 4)) + (L_QI + ((w + (i >> 2)) & 3) * 8192 + ((u ^ w) & 1) * 4096); PIN(qa[2 * i + u]); }
    }
    const int sl1 = w == 3 ? 0 : 4 + w, sl2 = 1 + (w >> 1);
#define A_RD(i, o) af[i] = *(const bf16x8*)(lds + aa[i] + (o))
#define A_READ(i0, i1, o) _Pragma("unroll") for (int i = (i0); i < (i1); ++i) A_RD(i, o)
#define A_MOVE(i0, i1, d) _Pragma("unroll") for (int i = (i0); i < (i1); ++i) { aa[i] += (d) * 4096; PIN(aa[i]); }
#define Q_RD(i, u) qf[i][u] = *(const bf16x8*)(lds + qa[2 * (i) + (u)])
#define A_FIRST() do { A_READ(0, 16, 0); lds_add(C_FREE); _Pragma("unroll") for (int i = 0; i < 16; ++i) PIN(af[i]); STAMP(12); } while (0)
    u32 qd[8];
#pragma unroll
    for (int i = 0; i < 8; ++i) { qd[i] = (qv ^ ((i & 3) << 4)) + (i >> 1) * 16384 + (i & 1) * 4096; PIN(qd[i]); }
    bool have = false;
    _Pragma("unroll 1") while ((u32)__builtin_amdgcn_s_memrealtime() - t0 < E3_T) {
      if (__builtin_amdgcn_readfirstlane(ctr[C_MK + w]) >= 4u) { have = true; break; }
      __builtin_amdgcn_s_sleep(1);
    }
    asm volatile("" ::: "memory");
    if (have) A_FIRST();
    lds_wait(ctr + C_SOK, 1u);
    if (isS0) lds_wait(ctr + C_H1, 4u);
    until(t0, E3_T - 10);
    _Pragma("unroll 1") while ((u32)__builtin_amdgcn_s_memrealtime() - t0 < E3_T) {}
    STAMP(6);
#define QD(i) "global_load_lds_dwordx4 %[q" #i "], %[ws] sc1\n s_add_u32 m0, m0, 0x400\n s_nop 0\n"
    asm volatile("s_mov_b32 m0, %[m]\n s_nop 0\n" QD(0) QD(1) QD(2) QD(3) QD(4) QD(5) QD(6) QD(7)
                 ::[m] "s"(L_QI + w * 8192), [ws] "s"(ws), [q0] "v"(qd[0]), [q1] "v"(qd[1]), [q2] "v"(qd[2]), [q3] "v"(qd[3]), [q4] "v"(qd[4]), [q5] "v"(qd[5]), [q6] "v"(qd[6]), [q7] "v"(qd[7]) : "memory", "scc");
    if (RARE(!have)) { lds_wait(ctr + C_MK + w, 4u); A_FIRST(); }
    A_MOVE(0, 16, sl1 - w);
    u32 cb = L_CTR, cw = L_CTR + 4 * w, mx0 = mw + L_MX, mx1 = (mw ^ 64) + L_MX, pw0 = pw + w * 1024, pw1 = (pw ^ 256) + w * 1024;
    float ninf = -__builtin_inff();
    u32 one = 1u;
    PIN(ninf); PIN(one); PIN(cb); PIN(cw); PIN(mx0); PIN(mx1); PIN(pw0); PIN(pw1);
#define CTS(b, i) (*(volatile LAS u32*)(lds + (b) + 4 * (i)))
    _Pragma("unroll 1") for (u32 spins = 0; spins < SPIN_CAP; ++spins) {
      waitvm<0>();
#pragma unroll
      for (int i = 0; i < 8; ++i) Q_RD(i >> 1, i & 1);
      u32 mxu[2] = {0u, 0u};
#pragma unroll
      for (int i = 0; i < 8; ++i) { const u32x4 t_ = __builtin_bit_cast(u32x4, qf[i >> 1][i & 1]); mxu[i & 1] = max(max(mxu[i & 1], t_.x), t_.y); mxu[i & 1] = max(max(mxu[i & 1], t_.z), t_.w); }
      if (!RARE(__any(max(mxu[0], mxu[1]) == POISON))) break;
      STAMP(29);
      __builtin_amdgcn_s_sleep(2);
      const u64 b0 = __ballot(mxu[w & 1] == POISON), b1 = __ballot(mxu[~w & 1] == POISON);
#pragma unroll 1
      for (int i = 0; i < 8; ++i)
        if (((i < 4 ? b0 : b1) >> (4 * (i & 3))) & 0x000F000F000F000Full) dma_sc1(L_QI + w * 8192 + i * 1024, (qv ^ ((i & 3) << 4)) + (i >> 1) * 16384 + (i & 1) * 4096, ws);
    }
    lds_add(C_QRDY);
    STAMP(7);
    f32x4 sc[2], sn[2];
    sc[0] = sc[1] = sn[0] = sn[1] = f32x4{0.f, 0.f, 0.f, 0.f};
#define QK(d, i0, i1, NU)                                                                                                \
  do {                                                                                                                   \
    SB();                                                                                                                \
    _Pragma("unroll") for (int i = (i0); i < (i1); ++i) _Pragma("unroll") for (int u = 0; u < (NU); ++u)                 \
      d[u] = __builtin_amdgcn_mfma_f32_16x16x32_bf16(af[i], qf[i][u], d[u], 0, 0, 0);                                    \
    SB();                                                                                                                \
  } while (0)
#define MF(d, i, u) do { SB(); d[u] = __builtin_amdgcn_mfma_f32_16x16x32_bf16(af[i], qf[i][u], d[u], 0, 0, 0); SB(); } while (0)
    const bool idle1 = isS0 && w == 0;
    u32 c1;
    {
      const u32 c0 = CTS(cb, C_QRDY);
      QK(sc, 0, 2, 2);
      if (RARE(__builtin_amdgcn_readfirstlane(c0) < 4u)) lds_spin(ctr + C_QRDY, 4u);
      asm volatile("" ::: "memory");
#pragma unroll
      for (int i = 2; i < 4; ++i) { MF(sc, i, 0); Q_RD(2 * i, 0); Q_RD(2 * i, 1); MF(sc, i, 1); Q_RD(2 * i + 1, 0); Q_RD(2 * i + 1, 1); }
#pragma unroll
      for (int i = 4; i < 12; ++i) { MF(sc, i, 0); Q_RD(i + 4, 0); MF(sc, i, 1); Q_RD(i + 4, 1); }
      c1 = CTS(cw, C_MK + 4);
      asm volatile("" ::: "memory");
#pragma unroll
      for (int i = 12; i < 16; ++i) { MF(sc, i, 0); A_RD(2 * i - 24, 0); MF(sc, i, 1); A_RD(2 * i - 23, 0); }
    }
    STAMP(9);
    float mref[2], lsum[2] = {0.f, 0.f};
#define ADDC(i, v) do { SB(); asm volatile("s_mov_b64 exec, 1\n ds_add_u32 %0, %1 offset:%2\n s_mov_b64 exec, -1" ::"v"(cb), "v"(v), "n"(4 * (i)) : "memory"); SB(); } while (0)
#define SM(d, T, NU, G, P0, P1)                                                                                          \
  do {                                                                                                                   \
    SB();                                                                                                                \
    if (RARE(__any(max5(d[0], (NU) > 1 ? max5(d[1], ninf) : ninf) > TAU))) {                                             \
      sig = 257u;                                                                                                        \
      _Pragma("unroll") for (int u = 0; u < (NU); ++u) {                                                                 \
        const float up = ceilf(x32_max(x16_max(max5(d[u], 0.f))));                                                       \
        lsum[u] *= __builtin_amdgcn_exp2f(-up);                                                                          \
        mref[u] += up;                                                                                                   \
        d[u] -= up;                                                                                                      \
        *(float*)(lds + (mw ^ (u * 64)) + L_REF + (T) * 128) = mref[u];                                                  \
      }                                                                                                                  \
    }                                                                                                                    \
    u32x2 pk[2];                                                                                                         \
    _Pragma("unroll") for (int u = 0; u < (NU); ++u) {                                                                   \
      pk[u].x = pk2bf(__builtin_amdgcn_exp2f(d[u].x), __builtin_amdgcn_exp2f(d[u].y));                                   \
      pk[u].y = pk2bf(__builtin_amdgcn_exp2f(d[u].z), __builtin_amdgcn_exp2f(d[u].w));                                   \
      *(u32x2*)(lds + (u ? (P1) : (P0))) = pk[u];                                                                        \
    }                                                                                                                    \
    ADDC(C_P + (G), sig);                                                                                                \
    _Pragma("unroll") for (int u = 0; u < (NU); ++u) { lsum[u] = dot2(pk[u].x, 0x3F803F80u, dot2(pk[u].y, 0x3F803F80u, lsum[u])); PIN(lsum[u]); } \
    SB();                                                                                                                \
  } while (0)
#define REF_WRITE(T, NU) _Pragma("unroll") for (int u = 0; u < (NU); ++u) *(float*)(lds + (mw ^ (u * 64)) + L_REF + (T) * 128) = mref[u]
    u32 sig = one;
    {
      MF(sn, 0, 0); A_RD(8, 0); MF(sn, 0, 1);
      const float m0 = max5(sc[0], ninf), m1 = max5(sc[1], ninf);
      MF(sn, 1, 0);
      asm volatile("ds_max_f32 %0, %2\n ds_max_f32 %1, %3" ::"v"(mx0), "v"(mx1), "v"(m0), "v"(m1) : "memory");
      ADDC(C_MXC, one);
      MF(sn, 1, 1); A_RD(9, 0);
#pragma unroll
      for (int i = 2; i < 4; ++i) { MF(sn, i, 0); A_RD(2 * i + 6, 0); MF(sn, i, 1); A_RD(2 * i + 7, 0); }
      MF(sn, 4, 0);
      const u32 c0 = CTS(cb, C_MXC);
      mref[0] = *(volatile LAS float*)(lds + mx0);
      mref[1] = *(volatile LAS float*)(lds + mx1);
      MF(sn, 4, 1); MF(sn, 5, 0); A_RD(14, 0); MF(sn, 5, 1); A_RD(15, 0); MF(sn, 6, 0); MF(sn, 6, 1);
      if (RARE(__builtin_amdgcn_readfirstlane(c0) < 4u)) {
        lds_spin(ctr + C_MXC, 4u);
        mref[0] = *(volatile LAS float*)(lds + mx0);
        mref[1] = *(volatile LAS float*)(lds + mx1);
      }
      u32x2 pk[2];
#pragma unroll
      for (int u = 0; u < 2; ++u) {
        MF(sn, 7 + 2 * u, 0);
        mref[u] = ceilf(mref[u]);
        float e0 = __builtin_amdgcn_exp2f(sc[u].x - mref[u]); PIN(e0);
        MF(sn, 7 + 2 * u, 1);
        pk[u].x = pk2bf(e0, __builtin_amdgcn_exp2f(sc[u].y - mref[u])); PIN(pk[u].x);
        MF(sn, 8 + 2 * u, 0);
        e0 = __builtin_amdgcn_exp2f(sc[u].z - mref[u]); PIN(e0);
        MF(sn, 8 + 2 * u, 1);
        pk[u].y = pk2bf(e0, __builtin_amdgcn_exp2f(sc[u].w - mref[u]));
        *(u32x2*)(lds + (u ? pw1 : pw0)) = pk[u];
      }
      ADDC(C_P, one);
      MF(sn, 11, 0);
      lsum[0] = dot2(pk[0].x, 0x3F803F80u, dot2(pk[0].y, 0x3F803F80u, 0.f)); PIN(lsum[0]);
      MF(sn, 11, 1);
      lsum[1] = dot2(pk[1].x, 0x3F803F80u, dot2(pk[1].y, 0x3F803F80u, 0.f)); PIN(lsum[1]);
      MF(sn, 12, 0);
      REF_WRITE(4 + w, 2);
      MF(sn, 12, 1);
      QK(sn, 13, 16, 2);
    }
    STAMP(10);
#define QK1(N0, slot)                                                                                                    \
  do {                                                                                                                   \
    _Pragma("unroll") for (int i = 0; i < 16; ++i) { MF(sc, i, 0); if (i + (N0) < 16) A_RD(i + (N0), slot); }            \
  } while (0)
    if (RARE(__builtin_amdgcn_readfirstlane(c1) < 4u && !idle1)) {
      lds_spin(ctr + C_MK + 4 + w, 4u);
      A_READ(0, 16, 0);
      sn[0] = sn[1] = f32x4{0.f, 0.f, 0.f, 0.f};
      QK(sn, 0, 16, 2);
    }
    if (RARE(idle1)) lds_add(C_P + 1);
    else { sn[0] -= mref[0]; sn[1] -= mref[1]; SM(sn, 4 + w, 2, 1, pw0 + 4096, pw1 + 4096); }
    if (RARE(w == 2)) {
      RELANE();
      dma_sc1(L_QI, WS_VOTE + xc * 2048 + lane * 16, ws);
    }
    REF_WRITE(7 + sl2, 1);
    A_MOVE(0, 16, sl2 - sl1);
    pw0 = pw + sl2 * 1024;
    sc[0] = f32x4{-mref[0], -mref[0], -mref[0], -mref[0]};
    PIN(pw0); PIN(sc[0]);
    lds_spin(ctr + C_MK + 7 + sl2, 4u);
    A_READ(0, 6, 0);
    STAMP(4);
    QK1(6, 0);
    SM(sc, 7 + sl2, 1, 2, pw0 + 7168, 0);
    STAMP(11);
    if (!RARE(w >= 2)) {
      REF_WRITE(10, 1);
      sc[0] = f32x4{-mref[0], -mref[0], -mref[0], -mref[0]};
      PIN(sc[0]);
      _Pragma("unroll 1") for (u32 n_ = 0; n_ < LSPIN_CAP; ++n_) {
        const u32 k_ = CTS(cb, C_MK + 10);
        asm volatile("" ::: "memory");
        A_READ(0, 6, 8192);
        SB();
        if (__builtin_amdgcn_readfirstlane(k_) >= 4u) break;
      }
      STAMP(5);
      QK1(6, 8192);
      SM(sc, 10, 1, 3, pw0 + 9216, 0);
    } else if (w == 2) {
      RELANE();
      waitvm<0>();
      COUNT_VOTES(true);
      lds_add(C_GX);
    }
    STAMP(30);
    RELANE();
    {
#pragma unroll
      for (int u = 0; u < 2; ++u) {
        *(float*)(lds + L_ML + (mw ^ (u * 64)) * 4 + w * 4) = mref[u];
        *(float*)(lds + L_ML + 512 + (mw ^ (u * 64)) * 4 + w * 4) = x32_sum(x16_sum(lsum[u]));
      }
      u32 old;
      f32x4 m4, l4;
      asm volatile("s_mov_b64 exec, 1\n ds_add_rtn_u32 %0, %3, %4 offset:%6\n s_mov_b64 exec, -1\n ds_read_b128 %1, %5\n ds_read_b128 %2, %5 offset:512\n s_waitcnt lgkmcnt(0)"
                   : "=&v"(old), "=&v"(m4), "=&v"(l4) : "v"((u32)L_CTR), "v"(1u), "v"(L_ML + (lane & 31) * 16), "n"(4 * C_SD) : "memory");
      if (__builtin_amdgcn_readfirstlane(old) == 3u) {
        const float M = fmaxf(fmaxf(m4.x, m4.y), fmaxf(m4.z, m4.w));
        const float l = (l4.x * __builtin_amdgcn_exp2f(m4.x - M) + l4.y * __builtin_amdgcn_exp2f(m4.y - M)) + (l4.z * __builtin_amdgcn_exp2f(m4.z - M) + l4.w * __builtin_amdgcn_exp2f(m4.w - M));
        *(u32x2*)(lds + L_WMAX + (lane & 1) * 128 + (lane & 30) * 4) = u32x2{fbits(M), fbits(l)};
        lds_add(C_ML);
      }
    }
    lds_wait(ctr + C_GX, 1u);
    gx = __builtin_amdgcn_readfirstlane(ctr[C_PAR]);
    STAMP(13);
  } else {
    const int mu = 2 * j + (w >> 2);
#define Q_LOAD(i0, i1)                                                                                                   \
  _Pragma("unroll") for (int i = (i0); i < (i1); ++i) {                                                                  \
    waitvm<QW - 1>();                                                                                                    \
    dma<false>(w * 16384 + (i & 15) * 1024, (vq ^ ((i & 3) << 4)) + ((i < 8 ? 64 * w + 32 * dh : i < 16 ? 64 * w + 32 - 32 * dh : 256 * dh + 32 * w) + 4 * (i & 7)) * 8192, i < 16 ? w_q : w_uk); \
  }
    const u32 vq = g4 * 8192 + hq * 256 + ((col ^ (g4 << 2)) << 4);
    u32x4 xv;
    u32x2 e2;
    u32 x2_;
    asm volatile("global_load_dwordx4 %0, %3, %5\n global_load_dword %1, %4, %5\n buffer_load_dwordx2 %2, off, %6, 0 sc1"
                 : "=&v"(xv), "=&v"(x2_), "=&v"(e2) : "v"(lane * 16), "v"(128 * w + 4 * (lane & 31)), "s"(x), "s"(rs) : "memory");
    bf16x8 wf[16], wk[8];
    u32 ta0 = w * 16384 + off_b(8 * g4 + tq, tp >> 1) + 8 * (tp & 1), ta1 = w * 16384 + off_b(8 * g4 + 4 + tq, tp >> 1) + 8 * (tp & 1);
    int so = WS_SC + ((lane >> 5) * 2048 + 64 * w + 2 * (lane & 31)) * 8;
    int fo = w == 0 ? WS_SFLAG + xc * 2048 + lane * 8 : WS_TOTAL;
    PIN(ta0); PIN(ta1); PIN(so); PIN(fo);
    int qpo = WS_QABS + il(hq * 1024 + dh * 512 + w * 64, 0) + (lane & 32) + (lane & 8) * 2 + (g4 & 1) * 8192 + ((lane & 7) ? WS_TOTAL : 0);
    u32 hw_ = L_H1RED + w * 128 + (lane & 31) * 4, hr_ = L_H1RED + w * 128 + 16 * g4 + 64 * dh, qw_ = L_H1RED + 1024 + w * 272 + lane * 2, qr_ = L_H1RED + 1024 + col * 272 + 64 * g4;
    PIN(qpo); PIN(hw_); PIN(hr_); PIN(qw_); PIN(qr_);
    u32 ka = w * 16384 + off_b(col, 4 * g4), hr2 = hr_ ^ 64;
    PIN(ka); PIN(hr2);
    Q_LOAD(4, 8);
    asm volatile("s_waitcnt vmcnt(4)" : "+v"(xv), "+v"(x2_), "+v"(e2)::"memory");
    TAG_OF(e2.x, e2.y);
    float ln0, ln1;
    {
      float mb, rsd;
      ln_stats(xv, mb, rsd);
      ln0 = rbf(rbf(lo16(x2_) - mb) * rsd);
      ln1 = rbf(rbf(hi16(x2_) - mb) * rsd);
    }
    PIN(ln0); PIN(ln1);
    u32 t0_ = tlo << 24, t1_ = (tlo >> 8) | (thi << 24), f0_ = w ? 0u : tlo, f1_ = w ? 0u : thi;
    PIN(t0_); PIN(t1_); PIN(f0_); PIN(f1_);
    Q_LOAD(8, 8 + QW);
    waitvm<QW>();
#pragma unroll
    for (int c = 0; c < 8; ++c) wf[c] = tr_frag(lds, ta0 ^ (c << 5), ta1 ^ (c << 5));
    Q_LOAD(8 + QW, 16);
#pragma unroll
    for (int c = 0; c < 8; ++c) PIN(wf[c]);
    Q_LOAD(16, 16 + QW);
    waitvm<QW>();
#pragma unroll
    for (int c = 0; c < 8; ++c) wf[8 + c] = tr_frag(lds, (ta0 ^ (c << 5)) + 8192, (ta1 ^ (c << 5)) + 8192);
    Q_LOAD(16 + QW, 24);
    STAMP(12);
    until(t0, E1_T);
    STAMP(23);
    u32x4 g[4];
    u32x2 fl;
#define E1_LOAD(C)                                                                                                       \
  asm volatile("buffer_load_dwordx4 %0, %5, %7, 0 offen sc1\n buffer_load_dwordx4 %1, %5, %7, %8 offen sc1\n"             \
               "buffer_load_dwordx4 %2, %5, %7, %9 offen sc1\n buffer_load_dwordx4 %3, %5, %7, %10 offen sc1\n buffer_load_dwordx2 %4, %6, %7, 0 offen sc1" \
               : C(g[0]), C(g[1]), C(g[2]), C(g[3]), C(fl) : "v"(so), "v"(fo), "s"(rs), "s"(4096), "s"(8192), "s"(12288) : "memory")
    E1_LOAD("=&v");
    waitvm<5>();
#pragma unroll
    for (int i = 0; i < 8; ++i) wk[i] = *(const bf16x8*)(lds + (ka ^ ((i & 3) << 4)) + (i >> 2) * 4096);
#pragma unroll
    for (int c = 0; c < 8; ++c) { PIN(wf[8 + c]); PIN(wk[c]); }
    STAMP(4);
    _Pragma("unroll 1") for (u32 spins = 0; spins < SPIN_CAP; ++spins) {
      asm volatile("s_waitcnt vmcnt(0)" : "+v"(g[0]), "+v"(g[1]), "+v"(g[2]), "+v"(g[3]), "+v"(fl)::"memory");
      u32 bl = g[0].x ^ t0_, bh = (fl.x ^ f0_) | (fl.y ^ f1_);
#pragma unroll
      for (int k = 0; k < 8; ++k) {
        if (k) bl = __builtin_amdgcn_bitop3_b32(bl, g[k >> 1][2 * (k & 1)], t0_, 0xF6);
        bh = __builtin_amdgcn_bitop3_b32(bh, g[k >> 1][2 * (k & 1) + 1], t1_, 0xF6);
      }
      if (!RARE(__any(((bl >> 24) | bh) != 0u))) break;
      STAMP(29);
      __builtin_amdgcn_s_sleep(1);
      E1_LOAD("+v");
    }
    STAMP(1);
    {
      const float m0 = rbf((gran_val(g[0].x) + gran_val(g[1].x)) + (gran_val(g[2].x) + gran_val(g[3].x)));
      const float m1 = rbf((gran_val(g[0].z) + gran_val(g[1].z)) + (gran_val(g[2].z) + gran_val(g[3].z)));
      const auto s0 = __builtin_amdgcn_permlane32_swap(__float_as_uint(m0), __float_as_uint(m0), false, false);
      const auto s1 = __builtin_amdgcn_permlane32_swap(__float_as_uint(m1), __float_as_uint(m1), false, false);
      *(u32*)(lds + hw_) = pk2bf(rbf(ln0 * rbf(1.f + __uint_as_float(s0[1]))) + __uint_as_float(s0[0]),
                                 rbf(ln1 * rbf(1.f + __uint_as_float(s1[1]))) + __uint_as_float(s1[0]));
    }
    WAVE_LDS_SYNC();
    {
      const bf16x8 h0 = *(const bf16x8*)(lds + hr_), h1 = *(const bf16x8*)(lds + hr2);
      f32x4 d[8];
      SB();
#pragma unroll
      for (int c = 0; c < 8; ++c) { d[c] = __builtin_amdgcn_mfma_f32_16x16x32_bf16(h0, wf[c], f32x4{0.f, 0.f, 0.f, 0.f}, 0, 0, 0); SB(); }
#pragma unroll
      for (int c = 0; c < 8; ++c) { d[c] = __builtin_amdgcn_mfma_f32_16x16x32_bf16(h1, wf[8 + c], d[c], 0, 0, 0); SB(); }
#pragma unroll
      for (int k = 0; k < 2; ++k) {
        const float v = (g4 & 2) ? ((g4 & 1) ? d[4 * k + 3][0] : d[4 * k + 2][0]) : ((g4 & 1) ? d[4 * k + 1][0] : d[4 * k][0]);
        const u32 vh = f2bf(v);
        *(u16*)(lds + qw_ + 128 * k) = (u16)vh;
        *(u16*)(lds + qw_ + 128 * k + 8 * 272) = (u16)f2bf(v - bf2f(vh));
      }
    }
    STAMP(2);
    BAR();
    {
      bf16x8 a[4];
#pragma unroll
      for (int s = 0; s < 4; ++s) a[s] = *(const bf16x8*)(lds + qr_ + 16 * s);
      SB();
      f32x4 d[2] = {f32x4{0.f, 0.f, 0.f, 0.f}, f32x4{0.f, 0.f, 0.f, 0.f}};
#pragma unroll
      for (int s = 0; s < 4; ++s)
#pragma unroll
        for (int c = 0; c < 2; ++c) d[c] = __builtin_amdgcn_mfma_f32_16x16x32_bf16(a[s], wk[4 * c + s], d[c], 0, 0, 0);
      constexpr float SCALE = 0.08838834764831845f * 1.4426950408889634f;
      const float v = SCALE * x16_sum(rs32(sum4(d[0]), sum4(d[1])));
      const float p0 = __uint_as_float(nopois(pk2bf(dppf<0x00>(v), dppf<0x55>(v)))), p1 = __uint_as_float(nopois(pk2bf(dppf<0xAA>(v), dppf<0xFF>(v))));
      st128(rs, qpo, u32x4{__float_as_uint(p0), __float_as_uint(p1), __float_as_uint(dppf<0x104>(p0)), __float_as_uint(dppf<0x104>(p1))});
    }
    STAMP(5);
    SB();
    {
      st128(rs, lane < 50 ? WS_PCTX + (j * 8 + w) * 800 + lane * 16 : WS_TOTAL, P4);
      const int pp = __builtin_amdgcn_readfirstlane(WS_PROBE + il((j * 8 + w) * 128, w & 1));
      u64 ta, tb, tc;
      u32 dm;
      asm volatile("s_load_dword %3, %4, 0x0\n s_waitcnt lgkmcnt(0)\n"
                   "s_memtime %0\n s_waitcnt lgkmcnt(0)\n s_load_dword %3, %4, %5 glc\n s_waitcnt lgkmcnt(0)\n"
                   "s_memtime %1\n s_waitcnt lgkmcnt(0)\n s_load_dword %3, %4, %6 glc\n s_waitcnt lgkmcnt(0)\n"
                   "s_memtime %2\n s_waitcnt vmcnt(0) lgkmcnt(0)"
                   : "=&s"(ta), "=&s"(tb), "=&s"(tc), "=&s"(dm) : "s"(ws), "s"(pp), "s"(pp ^ 8192) : "memory");
      int df = min(max(((int)(u32)(tb - ta) - (int)(u32)(tc - tb)) >> 3, -VCLIP), VCLIP);
      df = ((w ^ myh) & 1) ? -df : df;
      u32 old = 0u;
      if (lane == 0) old = __hip_atomic_fetch_add((LAS u32*)(lds + L_CTR) + C_VOTE, (u32)(65536 + 64 + df), __ATOMIC_RELAXED, __HIP_MEMORY_SCOPE_WORKGROUP);
      old = __builtin_amdgcn_readfirstlane(old) + 65536 + 64 + df;
      if (old >= 524288u && lane < 8) st64(rs, WS_VOTE + lane * 2048 + j * 8, (old & 1023u) | (tlo << 24), (tlo >> 8) | (thi << 24));
      STAMP(6);
    }
    {
      const int r0 = 128 * (mu & 3) + 32 * (w & 3) + 4 * (lane >> 3);
      const u32x2 cvb = *(const u32x2*)(cvec + r0);
      u32x4 wsc[4];
#pragma unroll
      for (int i = 0; i < 4; ++i) wsc[i] = ldnt(w_mod + (size_t)(r0 + i) * 3072 + 1024 + (mu >> 2) * 64 + (lane & 7) * 8);
      float a[8];
#pragma unroll
      for (int k = 0; k < 8; ++k) a[k] = 0.f;
#pragma unroll
      for (int i = 0; i < 4; ++i) fma8b(a, f2bf(silu_bf((i & 1) ? hi16(i < 2 ? cvb.x : cvb.y) : lo16(i < 2 ? cvb.x : cvb.y))), wsc[i]);
      float r[4];
#pragma unroll
      for (int k = 0; k < 4; ++k) r[k] = rs32(a[k], a[k + 4]);
      const float o0 = b3_sum(rs16(r[0], r[2])), o1 = b3_sum(rs16(r[1], r[3]));
      float* red = (float*)(lds + L_H1RED + 8192);
      if ((lane & 8) == 0) *(f32x2*)(red + w * 64 + (lane & 7) * 8 + (lane >> 5) * 4 + ((lane >> 4) & 1) * 2) = f32x2{o0, o1};
      BAR();
      if ((w & 3) == 3 && lane < 16) {
        red += (w >> 2) * 256 + lane * 4;
        const f32x4 sm = (*(const f32x4*)(red) + *(const f32x4*)(red + 64)) + (*(const f32x4*)(red + 128) + *(const f32x4*)(red + 192));
        st128(rs, WS_MODB + ((mu & 3) * 2048 + (mu >> 2) * 64 + lane * 4) * 4, u32x4{fbits(sm.x), fbits(sm.y), fbits(sm.z), fbits(sm.w)});
      }
    }
    gx = 0;
  }

  RELANE();
  const int r16 = lane >> 2, ch = lane & 3;
  if (ii >= 16) {
    int mo = lane < 24 ? (WS_LSE + il((24 * w + lane) * 512 + c4 * 128 + (h >> 1) * 8, h & 1)) ^ gx : WS_TOTAL;
    PIN(mo);
    lds_spin(ctr + C_PD, 4u);
    STAMP(23);
    u32x2 mt = ld64(rs, mo);
    SB();
    const u32 pb = (w >> 1) * PS + (w & 1) * 6144;
    u32 po[6], pa[16], ea[8];
    {
      u32 t_ = g4 * 256 + ((col ^ (g4 << 2)) << 4) + ((WS_PART + il21(((h >> 1) * 4 + c4) * 65536, h & 1)) ^ gx);
      PIN(t_);
#pragma unroll
      for (int k = 0; k < 6; ++k) { po[k] = (t_ ^ ((k & 3) << 4)) + il((6 * w + k) * 1024, 0); PIN(po[k]); }
    }
    bf16x8 wk[4];
    {
      u32 wa = tq * PS + 16384 + off_b(g4, 2 * w + (tp >> 1)) + 8 * (tp & 1);
      PIN(wa);
#pragma unroll
      for (int s = 0; s < 4; ++s) wk[s] = tr_frag(lds, (wa ^ (s << 4)) + s * 1024, (wa ^ (s << 4)) + s * 1024 + 4096);
    }
    pa[0] = pb + off_b(8 * (g4 == 3 ? 0 : g4) + tq, tp >> 1) + 8 * (tp & 1);
    PIN(pa[0]);
#pragma unroll
    for (int i = 1; i < 16; ++i) { pa[i] = pa[0] ^ (i << 4); PIN(pa[i]); }
    u32 bo = L_QOFF + col * 272 + g4 * 16, mlo = L_QOFF + 4352 + (col & 7) * 8, ro = L_QOFF + (w + (col & 8)) * 272 + (col & 7) * 32 + g4 * 8;
    int so1 = (WS_PCTX + h * 2048 + c4 * 512 + (w >> 2) * 256 + (w & 1) * 128 + (w & 2) * 16 + (g4 >> 1) * 64 + (g4 & 1) * 8 + (col < 2 ? col * 16 : WS_TOTAL)) ^ gx;
    u32 eo = L_QOFF + 4416 + w * 128 + lane * 2, mw_ = L_QOFF + 4352 + w * 8;
#pragma unroll
    for (int i = 0; i < 8; ++i) { ea[i] = L_QOFF + 4416 + w * 128 + ((col & 7) == i ? 16 * g4 : 64); PIN(ea[i]); }
    float pad = lane < 24 ? 0.f : -__builtin_inff();
    PIN(bo); PIN(mlo); PIN(ro); PIN(so1); PIN(eo); PIN(mw_); PIN(pad);
    SB();
    u32 done = 0u;
    _Pragma("unroll 1") for (u32 spins = 0; spins < SPIN_CAP; ++spins) {
      const u32 ok = ~(u32)__ballot(max(mt.x, mt.y) == POISON);
      u32 nw = ok & (ok >> 1);
      nw = nw & (nw >> 2) & 0x111111u & ~done;
      done |= nw;
#define T1F(k, b) "s_bitcmp0_b32 %[nw], " #b "\n s_cbranch_scc1 1f\n global_load_lds_dwordx4 %[p" #k "], %[ws] sc1\n 1: s_add_u32 m0, m0, 0x400\n"
      asm volatile("s_mov_b32 m0, %[m]\n" T1F(0, 0) T1F(1, 4) T1F(2, 8) T1F(3, 12) T1F(4, 16) T1F(5, 20)
                   ::[m] "s"(pb), [nw] "s"(nw), [ws] "s"(ws), [p0] "v"(po[0]), [p1] "v"(po[1]), [p2] "v"(po[2]), [p3] "v"(po[3]), [p4] "v"(po[4]), [p5] "v"(po[5]) : "memory", "scc");
      if (done == 0x111111u) break;
      __builtin_amdgcn_s_sleep(1);
      if (max(mt.x, mt.y) == POISON) mt = ld64(rs, mo);
      PIN(mt);
    }
    STAMP(16);
    {
      const float m = __uint_as_float(mt.x) + pad;
      float M = m;
#define MX(C) "s_nop 1\n v_max_f32_dpp %0, %0, %0 " C " row_mask:0xf bank_mask:0xf\n"
      asm(MX("quad_perm:[1,0,3,2]") MX("quad_perm:[2,3,0,1]") MX("row_ror:8") MX("row_ror:4") "s_nop 1" : "+v"(M));
      M = fmaxf(__uint_as_float(__builtin_amdgcn_readlane(__float_as_uint(M), 0)), __uint_as_float(__builtin_amdgcn_readlane(__float_as_uint(M), 16)));
      const float e = __builtin_amdgcn_exp2f(m - M);
      *(u16*)(lds + eo) = (u16)f2bf(e);
      WAVE_LDS_SYNC();
      bf16x8 wf[8], pt[8];
#pragma unroll
      for (int i = 0; i < 8; ++i) wf[i] = *(const bf16x8*)(lds + ea[i]);
      SB();
      const float L = x16_sum(row16_sum(e * __uint_as_float(mt.y)));
      if (lane == 0) *(f32x2*)(lds + mw_) = f32x2{M, L};
      SB();
      waitvm<0>();
      STAMP(17);
#pragma unroll
      for (int i = 0; i < 8; ++i) pt[i] = tr_frag(lds, pa[2 * i], pa[2 * i + 1] + 1024);
      SB();
      f32x4 d = {0.f, 0.f, 0.f, 0.f};
#pragma unroll
      for (int i = 0; i < 8; ++i) d = __builtin_amdgcn_mfma_f32_16x16x32_bf16(pt[i], wf[i], d, 0, 0, 0);
      const u32 h0 = pk2bf(d[0], d[1]), h1 = pk2bf(d[2], d[3]);
      const u32 l0 = pk2bf(d[0] - lo16(h0), d[1] - hi16(h0)), l1 = pk2bf(d[2] - lo16(h1), d[3] - hi16(h1));
      *(u32x2*)(lds + ro) = (col & 8) ? u32x2{l0, l1} : u32x2{h0, h1};
    }
    BAR();
    STAMP(14);
    const f32x2 ml = *(const f32x2*)(lds + mlo);
    bf16x8 lf[4];
#pragma unroll
    for (int s = 0; s < 4; ++s) lf[s] = *(const bf16x8*)(lds + bo + 64 * s);
    SB();
    f32x4 d = {0.f, 0.f, 0.f, 0.f};
#pragma unroll
    for (int s = 0; s < 4; ++s) d = __builtin_amdgcn_mfma_f32_16x16x32_bf16(wk[s], lf[s], d, 0, 0, 0);
    SB();
    float Mg = fmaxf(ml.x, dppf<0xB1>(ml.x));
    Mg = fmaxf(Mg, dppf<0x4E>(Mg)); Mg = fmaxf(Mg, dppf<0x141>(Mg));
    const float e = __builtin_amdgcn_exp2f(ml.x - Mg);
    float Lg = quad_sum(e * ml.y);
    Lg += dppf<0x141>(Lg);
    const float sc = e * __builtin_amdgcn_rcpf(Lg);
    {
      float d0 = d[0] * sc, d1 = d[1] * sc, d2 = d[2] * sc, d3 = d[3] * sc;
      row16_sum4(d0, d1, d2, d3);
      d = f32x4{d0, d1, d2, d3};
    }
    const u32 h0 = pk2bf(d[0], d[1]), h1 = pk2bf(d[2], d[3]);
    const u32 l0 = pk2bf(d[0] - lo16(h0), d[1] - hi16(h0)), l1 = pk2bf(d[2] - lo16(h1), d[3] - hi16(h1));
    st64(rs, so1, nopois(col ? l0 : h0), nopois(col ? l1 : h1));
    STAMP(15);
    STAMP_FLUSH();
    return;
  }
  if (tl2) {
    BAR0();
    if (w < 4) {
#pragma unroll
      for (int i = 0; i < 8; ++i) dma<true>(owno + i * 1024, (ks * 512 + 64 * w + 8 * i + (lane >> 3)) * 1024 + cg * 128 + (lane & 7) * 16, w_o);
      waitvm<0>();
    } else {
#pragma unroll
      for (int i = 0; i < 8; ++i) *(u32x4*)(lds + owno + i * 1024 + lane * 16) = wv[i];
      WAVE_LDS_SYNC();
    }
    STAMP(26);
    bf16x8 wf[8];
#pragma unroll
    for (int i = 0; i < 8; ++i) {
      const u32 a = owno + (32 * (i >> 2) + 8 * g4 + tq) * 128 + (i & 3) * 32 + tp * 8;
      wf[i] = tr_frag(lds, a, a + 512);
    }
#pragma unroll
    for (int i = 0; i < 8; ++i) PIN(wf[i]);
    STAMP(23);
    int off[1] = {(WS_PCTX + (ks * 4 + (w >> 1)) * 2048 + tq * 512 + (w & 1) * 256 + g4 * 64 + tp * 16) ^ gx};
    u32 rw = L_RED + lane * 32 + w * 4;
    PIN(rw);
    int so2 = tp < 2 ? WS_PATTN + il((ks * 512 + cg * 64 + (lane & 60)) * 4, tp) : WS_TOTAL;
    PIN(so2);
    u32x4 g[1];
    gather128<1, 4>(rs, off, g);
    STAMP(24);
    const bf16x8 a0 = __builtin_bit_cast(bf16x8, (col & 2) ? Z4 : g[0]), a1 = __builtin_bit_cast(bf16x8, (col & 2) ? g[0] : Z4);
    f32x4 d[4];
#pragma unroll
    for (int c = 0; c < 4; ++c) d[c] = __builtin_amdgcn_mfma_f32_16x16x32_bf16(a0, wf[c], f32x4{0.f, 0.f, 0.f, 0.f}, 0, 0, 0);
#pragma unroll
    for (int c = 0; c < 4; ++c) d[c] = __builtin_amdgcn_mfma_f32_16x16x32_bf16(a1, wf[4 + c], d[c], 0, 0, 0);
    *(float*)(lds + rw) = rs16(rs32(sum4(d[0]), sum4(d[2])), rs32(sum4(d[1]), sum4(d[3])));
    BAR();
    if (w == 0) {
      const float v = sum4(*(const f32x4*)(lds + L_RED + lane * 32) + *(const f32x4*)(lds + L_RED + lane * 32 + 16));
      const float vn = __uint_as_float(fbits(v));
      st128(rs, so2, u32x4{__float_as_uint(dppf<0x00>(vn)), __float_as_uint(dppf<0x55>(vn)), __float_as_uint(dppf<0xAA>(vn)), __float_as_uint(dppf<0xFF>(vn))});
    }
    STAMP(17);
  } else if (t3 >= 0) {
    STAMP(20);
    if (w < 4) {
      const int fr = ((128 * w + r16) * DF + t3 * 32) * 2, ca_ = ch ^ ((r16 >> 2) & 2) ^ (t3 & 1);
      const u16* wa = (t3 & 1) ? w_up : w_gate;
      const u16* wb = (t3 & 1) ? w_gate : w_up;
      const u32 d = w * 32768 + 8192;
#pragma unroll
      for (int i = 0; i < 8; ++i) { waitvm<WING - 1>(); dma<false>(d + (i >> 2) * 12288 + (t3 & 1) * 4096 + (i & 3) * 1024, fr + ca_ * 16 + 32 * i * DF, wa); }
#pragma unroll
      for (int i = 0; i < 8; ++i) { waitvm<WING - 1>(); dma<false>(d + (i >> 2) * 12288 + 4096 - (t3 & 1) * 4096 + (i & 3) * 1024, fr + (ca_ ^ 1) * 16 + 32 * i * DF, wb); }
    } else {
#pragma unroll
      for (int i = 0; i < 8; ++i) {
        waitvm<WING - 1>();
        dma<true>(lw * 32768 + 16384 + (i >> 2) * 12288 + (i & 3) * 1024, (32 * t3 + 8 * (i & 3) + (lane >> 3)) * 1024 + 256 * lw + 128 * (i >> 2) + (lane & 7) * 16, w_down);
      }
      const u32 x2 = *(const u32*)(x + 128 * lw + 2 * lane);
      u32x4 m6[6];
      int mo[6];
#pragma unroll
      for (int i = 0; i < 6; ++i) mo[i] = MB_OFF(i);
      gather128<6, 8>(rs, mo, m6);
#pragma unroll
      for (int i = 0; i < 6; ++i) {
        const f32x4 f = __builtin_bit_cast(f32x4, m6[i]);
        const float v = rbf(rs16(rs32(f.x, f.z), rs32(f.y, f.w)));
        *(u16*)(lds + L_Z + (i < 2 ? 5 : i < 4 ? 3 : 2) * 1088 + (128 * lw + 64 * (i & 1) + 4 * col + g4) * 2) = (u16)f2bf(i < 4 ? v : 1.f + v);
      }
      *(u32*)(lds + L_Z + 6 * 1088 + lw * 256 + lane * 4) = x2;
      if (w == 4 && lane < 32) *(u32*)(lds + L_ZERO + lane * 4) = lane < 16 ? 0u : POISON;
      if (w == 6) COUNT_VOTES(false);
    }
    waitvm<0>();
    STAMP(26);
    BAR();
    gx = __builtin_amdgcn_readfirstlane(ctr[C_PAR]);
    u32 zw = L_Z + (64 * w + 4 * col + g4) * 2;
    u32 hr[4];
#pragma unroll
    for (int i = 0; i < 4; ++i) { hr[i] = (col == i ? L_HH : L_ZERO) + 16 * g4; PIN(hr[i]); }
    int off[2];
#pragma unroll
    for (int i = 0; i < 2; ++i) { off[i] = (WS_PATTN + il(((4 * i + g4) * 512 + 64 * w + col * 4) * 4, myh)) ^ gx; PIN(off[i]); }
    int so = col < 4 ? (WS_PDOWN + il((t3 * 256 + 64 * (w & 3) + 16 * col + 4 * g4) * 4, w >> 2)) ^ gx : WS_TOTAL;
    u32 zr = L_Z + tp * 1088 + 16 * g4, yr = L_Z + 4 * 1088 + lane * 16, hw = L_HH + (w & 3) * 16 + (g4 & 1) * 8 + tp * 2, cr = L_CF + (w & 3) * 16 + tp * 4;
    PIN(cr); PIN(so); PIN(zw); PIN(zr); PIN(yr); PIN(hw);
    float ga = bf2f(*(const u16*)(lds + zw + 5 * 1088)), cb1 = bf2f(*(const u16*)(lds + zw + 2 * 1088)), xe = bf2f(*(const u16*)(lds + zw + 6 * 1088));
    bf16x8 wf[16], wd[4];
    if (w < 4) {
#pragma unroll
      for (int k = 0; k < 16; ++k) {
        const u32 a = (k >> 2) * 32768 + 8192 + ((k >> 1) & 1) * 12288 + (tp >> 1) * 4096 + ((k & 1) * 32 + 8 * g4 + tq) * 64 + (w ^ ((g4 & 1) * 2) ^ (tp >> 1)) * 16 + (tp & 1) * 8;
        wf[k] = tr_frag(lds, a, a + 256);
      }
#pragma unroll
      for (int k = 0; k < 16; ++k) PIN(wf[k]);
    }
#pragma unroll
    for (int i = 0; i < 4; ++i) {
      const u32 a = owno + 8192 + (8 * g4 + tq) * 128 + i * 32 + tp * 8;
      wd[i] = tr_frag(lds, a, a + 512);
    }
#pragma unroll
    for (int i = 0; i < 4; ++i) PIN(wd[i]);
    PIN(ga); PIN(cb1); PIN(xe);
    STAMP(23);
    until(t0, E6_T);
    u32x4 pa2[2];
    gather128<2, 4>(rs, off, pa2);
    STAMP(18);
    {
      const f32x4 s4 = __builtin_bit_cast(f32x4, pa2[0]) + __builtin_bit_cast(f32x4, pa2[1]);
      const u32 yb = f2bf(xe + rbf(ga * rbf(rs16(rs32(s4.x, s4.z), rs32(s4.y, s4.w)))));
      const float z = bf2f(yb) * cb1;
      const u32 zh = f2bf(z);
      *(u16*)(lds + zw + 4 * 1088) = (u16)yb;
      *(u16*)(lds + zw) = (u16)zh;
      *(u16*)(lds + zw + 1088) = (u16)f2bf(z - bf2f(zh));
    }
    BAR();
    if (w < 4) {
      bf16x8 zf[16];
#pragma unroll
      for (int k = 0; k < 16; ++k) zf[k] = *(const bf16x8*)(lds + zr + 64 * k);
      __builtin_amdgcn_sched_barrier(0);
      f32x4 d = {0.f, 0.f, 0.f, 0.f};
#pragma unroll
      for (int k = 0; k < 16; ++k) d = __builtin_amdgcn_mfma_f32_16x16x32_bf16(wf[k], zf[k], d, 0, 0, 0);
      PIN(d);
      u32 cfb;
      do { cfb = *(volatile LAS u32*)(lds + cr); } while (__any(cfb == POISON));
      d *= __uint_as_float(cfb);
      const float a0 = (tp & 1) ? d[1] : d[0], b0 = (tp & 1) ? d[0] : d[1], a1 = (tp & 1) ? d[3] : d[2], b1 = (tp & 1) ? d[2] : d[3];
      const float e0 = a0 + dppf<0xB1>(b0), e1 = a1 + dppf<0xB1>(b1);
      const float v = rbf(((tp & 2) ? e1 : e0) + dppf<0x4E>((tp & 2) ? e0 : e1));
      const auto sw = __builtin_amdgcn_permlane32_swap(__float_as_uint(v), __float_as_uint(v), false, false);
      const float g = __uint_as_float(sw[0]), u = __uint_as_float(sw[1]);
      const float e2 = __builtin_amdgcn_exp2f(g * (-2.3022081981f - 0.1029432396f * g * g));
      *(u16*)(lds + hw) = (u16)f2bf(rbf(g * __builtin_amdgcn_rcpf(e2 + 1.f)) * u);
    } else {
      float mb, rsd;
      ln_stats(*(const u32x4*)(lds + yr), mb, rsd);
      *(u32*)(lds + cr) = fbits(tp == 3 ? 1.f : rsd * (tp == 2 ? -mb : 1.f));
    }
    BAR();
    {
      bf16x8 hf[4];
#pragma unroll
      for (int i = 0; i < 4; ++i) hf[i] = *(const bf16x8*)(lds + hr[i]);
      __builtin_amdgcn_sched_barrier(0);
      f32x4 d = {0.f, 0.f, 0.f, 0.f};
#pragma unroll
      for (int i = 0; i < 4; ++i) d = __builtin_amdgcn_mfma_f32_16x16x32_bf16(wd[i], hf[i], d, 0, 0, 0);
      st128(rs, so, u32x4{fbits(d[0]), fbits(d[1]), fbits(d[2]), fbits(d[3])});
    }
    STAMP(19);
  }
  if (t4 >= 0 && lw == 0) {
#define RED8(f) b3_sum(rs16(rs32(f.x, f.z), rs32(f.y, f.w)))
    const int oc = t4 * 32 + (lane & 7) * 4 + g4;
    const u32 xb = x[oc];
    float x1, gb;
    {
      int off[2];
      off[0] = lane < 32 ? WS_MODB + ((lane >> 3) * 2048 + t4 * 32 + (lane & 7) * 4) * 4 : WS_TOTAL;
      off[1] = lane < 32 ? WS_MODB + ((lane >> 3) * 2048 + 1536 + t4 * 32 + (lane & 7) * 4) * 4 : WS_TOTAL;
      u32x4 mg[2];
      gather128<2>(rs, off, mg);
      const f32x4 fa = __builtin_bit_cast(f32x4, mg[0]), fb = __builtin_bit_cast(f32x4, mg[1]);
      const float ga = rbf(RED8(fa));
      gb = rbf(RED8(fb));
      int of1[1] = {(WS_PATTN + il(((lane >> 3) * 512 + t4 * 32 + (lane & 7) * 4) * 4, myh)) ^ gx};
      u32x4 g[1];
      gather128<1, 4>(rs, of1, g);
      const f32x4 gf = __builtin_bit_cast(f32x4, g[0]);
      x1 = rbf(bf2f(xb) + rbf(ga * rbf(RED8(gf))));
    }
    STAMP(20);
    if (t4 == 0 && lane == 0) st64(rs, WS_EPOCH, tlo, thi);
    int off[7];
#pragma unroll
    for (int i = 0; i < 7; ++i) { off[i] = (WS_PDOWN + il(((7 * (lane >> 3) + i) * 256 + (t4 & 7) * 32 + (lane & 7) * 4) * 4, myh)) ^ gx; PIN(off[i]); }
    const rsrc_t ro = __builtin_amdgcn_make_buffer_rsrc((void*)out, 0, 1024, 0x00020000);
    int oo = (lane & 8) ? 1024 : oc * 2;
    PIN(x1); PIN(gb); PIN(oo);
    u32x4 g[7];
    __builtin_amdgcn_s_sleep(T4_DELAY);
#define F4(i) __builtin_bit_cast(f32x4, g[i])
    float s;
    _Pragma("unroll 1") for (u32 spins = 0; spins < SPIN_CAP; ++spins) {
      asm volatile("" ::: "memory");
#pragma unroll
      for (int i = 0; i < 7; ++i) g[i] = ld128(rs, off[i]);
      SB();
      const f32x4 s4 = ((F4(0) + F4(1)) + (F4(2) + F4(3))) + ((F4(4) + F4(5)) + F4(6));
      s = RED8(s4);
      if (!__any(s != s)) break;
      bool bad = false;
#pragma unroll
      for (int i = 0; i < 7; ++i) bad = bad | pois4(g[i]);
      if (!__any(bad)) break;
    }
    STAMP(21);
    __builtin_amdgcn_raw_buffer_store_b16((u16)f2bf(x1 + rbf(gb * rbf(s))), ro, oo, 0, 0);
    STAMP(22);
  }
  STAMP_FLUSH();
}

extern "C" void kernel_launch(void* const* d_in, const int* in_sizes, int n_in, void* d_out, int out_size,
                              void* d_ws, size_t ws_size, hipStream_t stream) {
  k_main<<<NWG, 512, 0, stream>>>((const u16*)d_in[0], (const u16*)d_in[1], (const u16*)d_in[2], (const u16*)d_in[3],
                                  (const u16*)d_in[4], (const u16*)d_in[5], (const u16*)d_in[7], (const u16*)d_in[8],
                                  (const u16*)d_in[9], (const u16*)d_in[10], (const u16*)d_in[11], (u16*)d_out,
                                  (unsigned char*)d_ws);
}
```

```cpp
#include <hip/hip_runtime.h>
#include <hip/hip_bf16.h>
#include <stdint.h>

typedef unsigned short u16;
typedef unsigned int u32;
typedef unsigned long long u64;
typedef float f32x2 __attribute__((ext_vector_type(2)));
typedef float f32x4 __attribute__((ext_vector_type(4)));
typedef float f32x16 __attribute__((ext_vector_type(16)));
typedef u32 u32x2 __attribute__((ext_vector_type(2)));
typedef u32 u32x4 __attribute__((ext_vector_type(4)));
typedef short bf16x8 __attribute__((ext_vector_type(8)));
typedef short s16x4 __attribute__((ext_vector_type(4)));
#define LAS __attribute__((address_space(3)))
#define DI __device__ __forceinline__

constexpr int DF = 1792;
constexpr int NWG = 256;
constexpr int AUX_SC1 = 16;

constexpr int WS_EPOCH = 0;
constexpr int WS_SFLAG = 4096;
constexpr int WS_QABS = 32768;
constexpr int WS_PCTX = WS_QABS + 65536;
constexpr int WS_PATTN = WS_PCTX + 65536;
constexpr int WS_PDOWN = WS_PATTN + 32768;
constexpr int WS_LSE = WS_PDOWN + 114688;
constexpr int WS_MODB = WS_LSE + 196608;
constexpr int WS_SC = WS_MODB + 32768;
constexpr int WS_PROBE = WS_SC + 32768;
constexpr int WS_VOTE = WS_PROBE + 262144;
constexpr int WS_PART = 2097152;
constexpr int PART_BYTES = 32 * 8 * 256 * 128;
constexpr int WS_TOTAL = WS_PART + PART_BYTES;

constexpr int il(int o, int hf) { return (o >> 13) * 16384 + hf * 8192 + (o & 8191); }
constexpr int il21(int o, int hf) { return il(o, hf ^ ((o >> 20) & 1) ^ 1); }
constexpr u32 SPIN_CAP = 1u << 16;
#define WIN 7
#define WA0 5
#define QW 4
#define WING 3
#define QF_T 300
#define E1_T 250
#define SCP_T 215
#define E3_T 470
#define E6_T 1200
#define T4_DELAY 30
#define TAU 32.f
#define VCLIP 40
#define PF_LEN 33100
#define G_OF(v) ((v) > 32768.f ? 8192u : 0u)
#define VOTE_STAMP(v)
#define STAMP_DECL
#define STAMP(i)
#define STAMP_FLUSH()

DI float bf2f(u32 b) { return __uint_as_float(b << 16); }
DI float rbf(float f) { return (float)(__bf16)f; }
DI u32 f2bf(float f) { return (u32)__builtin_bit_cast(u16, (__bf16)f); }
typedef __bf16 bf16x2_t __attribute__((ext_vector_type(2)));
DI u32 pk2bf(float a, float b) { return __builtin_bit_cast(u32, __builtin_convertvector(f32x2{a, b}, bf16x2_t)); }
DI float lo16(u32 v) { return __uint_as_float(v << 16); }
DI float hi16(u32 v) { return __uint_as_float(v & 0xFFFF0000u); }
DI u32x4 ldnt(const u16* p) { return __builtin_nontemporal_load((const u32x4*)p); }

DI float silu_bf(float a) { return rbf(a * __builtin_amdgcn_rcpf(1.f + __expf(-a))); }

template <int CTRL> DI float dppf(float v) {
  return __int_as_float(__builtin_amdgcn_update_dpp(0, __float_as_int(v), CTRL, 0xF, 0xF, true));
}
DI float quad_sum(float v) { v += dppf<0xB1>(v); v += dppf<0x4E>(v); return v; }
DI float b23_sum(float v) { v += dppf<0x128>(v); v += dppf<0x124>(v); return v; }
DI float b3_sum(float v) { return v + dppf<0x128>(v); }
DI float x16_sum(float v) {
  const auto r = __builtin_amdgcn_permlane16_swap(__float_as_uint(v), __float_as_uint(v), false, false);
  return __uint_as_float(r[0]) + __uint_as_float(r[1]);
}
DI float x32_sum(float v) {
  const auto r = __builtin_amdgcn_permlane32_swap(__float_as_uint(v), __float_as_uint(v), false, false);
  return __uint_as_float(r[0]) + __uint_as_float(r[1]);
}
DI float rs32(float a, float b) {
  const auto r = __builtin_amdgcn_permlane32_swap(__float_as_uint(a), __float_as_uint(b), false, false);
  return __uint_as_float(r[0]) + __uint_as_float(r[1]);
}
DI float rs16(float a, float b) {
  const auto r = __builtin_amdgcn_permlane16_swap(__float_as_uint(a), __float_as_uint(b), false, false);
  return __uint_as_float(r[0]) + __uint_as_float(r[1]);
}
DI float row16_sum(float v) { return b23_sum(quad_sum(v)); }
DI float wave_sum(float v) { return x32_sum(x16_sum(row16_sum(v))); }
DI void row16_sum4(float& a, float& b, float& c, float& d) {
#define R4(C) "v_add_f32_dpp %0, %0, %0 " C " row_mask:0xf bank_mask:0xf\n v_add_f32_dpp %1, %1, %1 " C " row_mask:0xf bank_mask:0xf\n" \
              "v_add_f32_dpp %2, %2, %2 " C " row_mask:0xf bank_mask:0xf\n v_add_f32_dpp %3, %3, %3 " C " row_mask:0xf bank_mask:0xf\n"
  asm("s_nop 1\n" R4("quad_perm:[1,0,3,2]") R4("quad_perm:[2,3,0,1]") R4("row_ror:8") R4("row_ror:4") : "+v"(a), "+v"(b), "+v"(c), "+v"(d));
#undef R4
}
DI float row16_max(float v) {
  v = fmaxf(v, dppf<0xB1>(v)); v = fmaxf(v, dppf<0x4E>(v));
  v = fmaxf(v, dppf<0x128>(v)); v = fmaxf(v, dppf<0x124>(v));
  return v;
}
DI float x16_max(float v) {
  const auto r = __builtin_amdgcn_permlane16_swap(__float_as_uint(v), __float_as_uint(v), false, false);
  return fmaxf(__uint_as_float(r[0]), __uint_as_float(r[1]));
}
DI float x32_max(float v) {
  const auto r = __builtin_amdgcn_permlane32_swap(__float_as_uint(v), __float_as_uint(v), false, false);
  return fmaxf(__uint_as_float(r[0]), __uint_as_float(r[1]));
}
DI float wave_max(float v) { return x32_max(x16_max(row16_max(v))); }

DI float dot2(u32 a, u32 b, float c) {
  return __builtin_amdgcn_fdot2_f32_bf16(__builtin_bit_cast(bf16x2_t, a), __builtin_bit_cast(bf16x2_t, b), c, false);
}
DI void fma8b(float* acc, u32 sbits, u32x4 w) {
  const u32 sh = sbits << 16;
  acc[0] = dot2(w.x, sbits, acc[0]); acc[1] = dot2(w.x, sh, acc[1]);
  acc[2] = dot2(w.y, sbits, acc[2]); acc[3] = dot2(w.y, sh, acc[3]);
  acc[4] = dot2(w.z, sbits, acc[4]); acc[5] = dot2(w.z, sh, acc[5]);
  acc[6] = dot2(w.w, sbits, acc[6]); acc[7] = dot2(w.w, sh, acc[7]);
}

DI unsigned fsw(unsigned row) { return ((row & 3) << 2) | ((row >> 2) & 3); }
DI unsigned off_b(unsigned row, unsigned ch) { return 256u * row + 16u * (ch ^ fsw(row)); }
DI unsigned tr_addr(unsigned lane, unsigned c, unsigned ks, unsigned t) {
  const unsigned h = lane >> 5, blk = (lane >> 4) & 1, q = (lane & 15) >> 2, p = lane & 3;
  return off_b(16 * ks + 8 * h + 4 * t + q, 4 * c + 2 * blk + (p >> 1)) + 8 * (p & 1);
}

typedef __amdgpu_buffer_rsrc_t rsrc_t;
DI u32x4 ld128(rsrc_t rs, int off) { return __builtin_amdgcn_raw_buffer_load_b128(rs, off, 0, AUX_SC1); }
DI u32x2 ld64(rsrc_t rs, int off) { return __builtin_amdgcn_raw_buffer_load_b64(rs, off, 0, AUX_SC1); }
DI void st64(rsrc_t rs, int off, u32 lo, u32 hi) { u32x2 v; v.x = lo; v.y = hi; __builtin_amdgcn_raw_buffer_store_b64(v, rs, off, 0, AUX_SC1); }
DI void st128(rsrc_t rs, int off, u32x4 v) { __builtin_amdgcn_raw_buffer_store_b128(v, rs, off, 0, AUX_SC1); }
#define BAR() do { asm volatile("s_waitcnt lgkmcnt(0)" ::: "memory"); __builtin_amdgcn_s_barrier(); asm volatile("" ::: "memory"); } while (0)
#define BAR0() do { __builtin_amdgcn_sched_barrier(0); asm volatile("" ::: "memory"); __builtin_amdgcn_s_barrier(); asm volatile("" ::: "memory"); __builtin_amdgcn_sched_barrier(0); } while (0)
#define PIN(x) asm volatile("" : "+v"(x))
constexpr u32 POISON = 0xFFFFFFFFu;
DI bool pois4(u32x4 v) { return max(max(v.x, v.y), max(v.z, v.w)) == POISON; }
DI u32 nopois(u32 v) { return min(v, 0xFFFFFFFEu); }
DI u32 fbits(float v) { return nopois(__float_as_uint(v)); }

DI u32x2 mk_gran(float v, u32 tlo, u32 thi) {
  u32 b = __float_as_uint(v);
  if ((b & 0x7F800000u) != 0x7F800000u) b += 0x80u;
  u32x2 g; g.x = (b >> 8) | (tlo << 24); g.y = (tlo >> 8) | (thi << 24);
  return g;
}
DI void st_gran2(rsrc_t rs, int off, float v0, float v1, u32 tlo, u32 thi) {
  const u32x2 a = mk_gran(v0, tlo, thi), b = mk_gran(v1, tlo, thi);
  u32x4 v; v.x = a.x; v.y = a.y; v.z = b.x; v.w = b.y;
  st128(rs, off, v);
}
DI bool gran_ok(u32 lo, u32 hi, u32 tlo, u32 thi) { return (((lo >> 24) ^ (tlo & 0xFFu)) | (hi ^ ((tlo >> 8) | (thi << 24)))) == 0u; }
DI float gran_val(u32 lo) { return __uint_as_float(lo << 8); }

template <int N, int SLP = 1> DI void gather128(rsrc_t rs, const int (&off)[N], u32x4 (&v)[N]) {
#pragma unroll
  for (int i = 0; i < N; ++i) v[i] = ld128(rs, off[i]);
  _Pragma("unroll 1") for (u32 spins = 0; spins < SPIN_CAP; ++spins) {
    bool need[N], bad = false;
#pragma unroll
    for (int i = 0; i < N; ++i) { need[i] = pois4(v[i]); bad = bad || need[i]; }
    if (!__any(bad)) break;
    __builtin_amdgcn_s_sleep(SLP);
    asm volatile("" ::: "memory");
    __builtin_amdgcn_sched_barrier(0);
#pragma unroll
    for (int i = 0; i < N; ++i)
      if (need[i]) v[i] = ld128(rs, off[i]);
  }
}
template <int N> DI void waitvm() { asm volatile("s_waitcnt vmcnt(%0)" ::"n"(N) : "memory"); }
constexpr u32 LSPIN_CAP = 1u << 22;
DI void lds_wait(volatile LAS u32* p, u32 val) {
  _Pragma("unroll 1") for (u32 n = 0; *p < val && n < LSPIN_CAP; ++n) __builtin_amdgcn_s_sleep(1);
  asm volatile("" ::: "memory");
}
#define WAVE_LDS_SYNC() do { __builtin_amdgcn_fence(__ATOMIC_RELEASE, "wavefront"); __builtin_amdgcn_wave_barrier(); __builtin_amdgcn_fence(__ATOMIC_ACQUIRE, "wavefront"); } while (0)
#define SB() __builtin_amdgcn_sched_barrier(0)
#define RARE(c) __builtin_expect(!!(c), 0)

constexpr int PS = 28672;
constexpr int L_QI = 4 * PS;
constexpr int L_P = L_QI + 32768;
constexpr int L_REF = L_P + 11264;
constexpr int L_MX = L_REF + 1408;
constexpr int L_ALW = L_MX + 128;
constexpr int L_DUMP = L_ALW + 512;
constexpr int L_QOFF = 131072;
constexpr int L_H1RED = L_QOFF;
constexpr int L_RED = L_QOFF + 16384;
constexpr int L_Z = L_RED, L_HH = L_RED + 7 * 1088, L_ZERO = L_HH + 64, L_CF = L_ZERO + 64;
constexpr int L_MISC = L_QOFF + 30720;
constexpr int L_CTR = L_MISC;
constexpr int L_WMAX = L_MISC + 256;
constexpr int L_ML = L_MISC + 512;

#define Z4 (u32x4{0u, 0u, 0u, 0u})
DI float max5(f32x4 v, float e) { return fmaxf(fmaxf(fmaxf(fmaxf(v.x, v.y), v.z), v.w), e); }

template <bool NT> DI void dma(u32 m, u32 voff, const void* sb) {
  if (NT) asm volatile("s_mov_b32 m0, %0\n s_nop 0\n global_load_lds_dwordx4 %1, %2 nt" ::"s"(m), "v"(voff), "s"(sb) : "memory");
  else asm volatile("s_mov_b32 m0, %0\n s_nop 0\n global_load_lds_dwordx4 %1, %2" ::"s"(m), "v"(voff), "s"(sb) : "memory");
}
DI void dma_sc1(u32 m, u32 voff, const void* sb) {
  asm volatile("s_mov_b32 m0, %0\n s_nop 0\n global_load_lds_dwordx4 %1, %2 sc1" ::"s"(m), "v"(voff), "s"(sb) : "memory");
}
DI void lds_add(int idx) {
  SB();
  asm volatile("s_mov_b64 exec, 1\n ds_add_u32 %0, %1 offset:%2\n s_mov_b64 exec, -1" ::"v"((u32)L_CTR), "v"(1u), "n"(idx * 4) : "memory");
  SB();
}
DI u32 spin_at(u32 a, u32 val) {
  u32 t, c, m, cap;
  asm volatile("s_mov_b32 %3, %6\n 1: ds_read_b32 %0, %4\n s_waitcnt lgkmcnt(0)\n v_readfirstlane_b32 %1, %0\n s_and_b32 %2, %1, 255\n s_cmp_ge_u32 %2, %5\n s_cbranch_scc1 2f\n"
               "s_sub_u32 %3, %3, 1\n s_cbranch_scc0 1b\n 2:"
               : "=&v"(t), "=&s"(c), "=&s"(m), "=&s"(cap) : "v"(a), "s"(val), "n"(LSPIN_CAP) : "memory", "scc");
  return c;
}
DI void lds_spin(volatile LAS u32* p, u32 val) { spin_at((u32)(uintptr_t)p, val); }
enum { C_SOK = 0, C_BH = 4, C_PD = 12, C_H1 = 18, C_PAR = 19, C_VOTE = 20, C_FREE = 22, C_QRDY = 23, C_MXC = 25, C_SD = 26, C_GX = 27, C_ML = 28, C_MK = 32, C_P = 44 };

DI u32 kv_mark(int nm, int skip) { return L_CTR + 4 * C_MK + __builtin_amdgcn_readfirstlane(4 * nm + (nm >= skip ? 4 : 0)); }
#define KV_TS
#define KV_TSO
#define KV_TSA
#define KV_TSP
#define KV_TRY(l)                                                                                                        \
  "7: s_getreg_b32 %[h], hwreg(HW_REG_IB_STS)\n s_and_b32 %[h], %[h], 0xC0000F\n s_max_i32 %[h], %[h], %[x]\n s_sub_i32 %[h], %[h], %[x]\n s_cmp_gt_i32 %[h], %[k4]\n s_cbranch_scc1 " l "\n" \
  "s_waitcnt vmcnt(%[x4])\n s_cmp_eq_u32 %[k4], 4\n s_cbranch_scc1 8f\n s_waitcnt vmcnt(%[x])\n"                        \
  "8: v_subrev_u32 %[t], %[k4], %[ml]\n s_mov_b64 exec, 1\n ds_add_u32 %[t], %[one] offset:12\n s_mov_b64 exec, -1\n"   \
  "s_sub_i32 %[k4], %[k4], 4\n" KV_TS "s_branch 7b\n"
template <int X> DI void kv_try(int& k4, u32 ml, u32 one KV_TSA) {
  u32 t, h;
  asm volatile(KV_TRY("9f") "9:" : [t] "=&v"(t), [h] "=&s"(h), [k4] "+s"(k4) KV_TSO : [ml] "v"(ml), [one] "v"(one), [x] "n"(X), [x4] "n"(X + 4) : "memory", "scc");
}
template <int X> DI u32 spin_try(int idx, u32 val, int& k4, u32 ml, u32 one KV_TSA) {
  u32 t, h, c, cap;
  asm volatile("s_mov_b32 %[cap], %[n]\n 1: ds_read_b32 %[t], %[ml] offset:%[o]\n s_waitcnt lgkmcnt(0)\n v_readfirstlane_b32 %[c], %[t]\n s_and_b32 %[h], %[c], 255\n"
               "s_cmp_ge_u32 %[h], %[v]\n s_cbranch_scc1 2f\n" KV_TRY("9f") "9: s_sub_u32 %[cap], %[cap], 1\n s_cbranch_scc0 1b\n 2:"
               : [t] "=&v"(t), [h] "=&s"(h), [c] "=&s"(c), [cap] "=&s"(cap), [k4] "+s"(k4) KV_TSO
               : [ml] "v"(ml), [one] "v"(one), [o] "n"(4 * idx), [v] "n"(val), [n] "n"(LSPIN_CAP), [x] "n"(X), [x4] "n"(X + 4) : "memory", "scc");
  return c;
}
template <int W> DI void kv_load(const u16* ckv, u32 vl, int lw, u32 g0, u32 g1, u32 m, int n0, int n1, int& nm, int skip) {
#pragma unroll 1
  for (int n = n0; n < n1; ++n) {
#define KV_POST(k) ".if (%[wm] | 4) == " #k "\n s_cmp_gt_i32 %[nm], %[n2]\n s_cbranch_scc1 20f\n s_mov_b64 exec, 1\n ds_add_u32 %[ma], %[one]\n s_mov_b64 exec, -1\n s_add_i32 %[nm], %[nm], 1\n 20:\n .endif\n"
#define KV_W(o, k) "s_waitcnt vmcnt(%[wm])\n" KV_POST(k) "global_load_lds_dwordx4 %[vo], %[p] offset:" #o " nt\n"
#define KV_M "s_add_u32 m0, m0, %[ms]\n"
    asm volatile("s_mov_b32 m0, %[m]\n" KV_W(0, 4) KV_M KV_W(256, 5) KV_M KV_W(512, 6) KV_M KV_W(768, 7)
                 : [nm] "+s"(nm)
                 : [m] "s"(m), [vo] "v"(vl + g0 + ((lw + n) & 3) * 4096), [p] "s"(ckv), [ma] "v"(kv_mark(nm, skip)),
                   [one] "v"(1u), [n2] "s"(__builtin_amdgcn_readfirstlane(n - (W > 4 ? 2 : 1))), [wm] "n"(W - 1), [ms] "n"(PS - 256)
                 : "memory", "scc");
    g0 = g1;
    g1 += 192 * 16384;
    m += n + 1 == skip ? 8192 : 4096;
  }
}
DI void until(u32 t0, u32 dt) {
  while ((u32)__builtin_amdgcn_s_memrealtime() - t0 < dt) __builtin_amdgcn_s_sleep(2);
}
DI bf16x8 tr_frag(unsigned char* lds, u32 a0, u32 a1) {
  const s16x4 b0 = __builtin_amdgcn_ds_read_tr16_b64_v4i16((LAS s16x4*)(lds + a0));
  const s16x4 b1 = __builtin_amdgcn_ds_read_tr16_b64_v4i16((LAS s16x4*)(lds + a1));
  bf16x8 v;
  v[0] = b0[0]; v[1] = b0[1]; v[2] = b0[2]; v[3] = b0[3]; v[4] = b1[0]; v[5] = b1[1]; v[6] = b1[2]; v[7] = b1[3];
  return v;
}
DI float sum4(f32x4 v) { const f32x2 t = f32x2{v.x, v.y} + f32x2{v.z, v.w}; return t.x + t.y; }

DI void ln_stats(u32x4 yv, float& mb, float& rsd) {
  const float a1 = dot2(yv.x, 0x3F803F80u, dot2(yv.y, 0x3F803F80u, 0.f)) + dot2(yv.z, 0x3F803F80u, dot2(yv.w, 0x3F803F80u, 0.f));
  const float a2 = dot2(yv.x, yv.x, dot2(yv.y, yv.y, 0.f)) + dot2(yv.z, yv.z, dot2(yv.w, yv.w, 0.f));
  const float sr = x16_sum(row16_sum(rs32(a1, a2))) * (1.f / 512.f);
  const float s1 = __uint_as_float(__builtin_amdgcn_readlane(__float_as_uint(sr), 0)), s2 = __uint_as_float(__builtin_amdgcn_readlane(__float_as_uint(sr), 32));
  const float var = rbf(fmaxf(s2 - s1 * s1, 0.f));
  mb = rbf(s1);
  const float sd = rbf(__builtin_amdgcn_sqrtf(rbf(var + 1e-5f)));
  rsd = __builtin_amdgcn_rcpf(sd);
  rsd = rsd * (2.f - sd * rsd);
}

__global__ __launch_bounds__(512, 2) void k_main(const u16* __restrict__ x, const u16* __restrict__ ckv,
                                                 const u16* __restrict__ w_q, const u16* __restrict__ w_uk,
                                                 const u16* __restrict__ w_uv, const u16* __restrict__ w_o,
                                                 const u16* __restrict__ w_gate, const u16* __restrict__ w_up,
                                                 const u16* __restrict__ w_down, const u16* __restrict__ cvec,
                                                 const u16* __restrict__ w_mod, u16* __restrict__ out,
                                                 unsigned char* ws) {
  __shared__ __attribute__((aligned(16))) unsigned char lds[163840];
  const int w = __builtin_amdgcn_readfirstlane(threadIdx.x >> 6), j = blockIdx.x;
  int lane = threadIdx.x & 63;
  const int xc = j & 7, ii = j >> 3;
  STAMP_DECL;
  STAMP(0);
  u32x2 ev;
  u32 xm, vh;
  {
    u64 p, pc, pw;
    u32 a, b, c, d, f, t;
    asm volatile("s_lshr_b32 %[f], %[j], 6\n s_cmp_eq_u32 %[f], 1\n s_cselect_b32 %[a], 0x50, 16\n"
                 "s_load_dwordx2 %[p], %[ka], %[a]\n s_load_dwordx2 %[pw], %[ka], 0x60\n"
                 "s_lshr_b32 %[d], %[j], 3\n s_cmp_lg_u32 %[w], 7\n s_cbranch_scc1 2f\n"
                 "s_and_b32 %[a], %[d], 15\n s_lshl_b32 %[a], %[a], 12\n v_lshl_add_u32 %[t], %[lane], 6, %[a]\n v_min_u32 %[t], %[pfl], %[t]\n"
                 "s_getpc_b64 %[pc]\n s_mov_b32 m0, %[dump]\n s_nop 0\n global_load_lds_dword %[t], %[pc]\n"
                 "2: s_load_dwordx2 %[pc], %[ka], 0x48\n s_and_b32 %[c], %[j], 7\n s_lshl_b32 %[c], %[c], 10\n s_cmp_eq_u32 %[f], 0\n s_cbranch_scc0 3f\n"
                 "s_and_b32 %[a], %[d], 1\n s_lshl1_add_u32 %[a], %[w], %[a]\n s_lshl_b32 %[a], %[a], 18\n s_add_u32 %[c], %[c], %[a]\n"
                 "s_lshr_b32 %[a], %[d], 1\n s_lshl_b32 %[a], %[a], 8\n s_add_u32 %[c], %[c], %[a]\n s_lshl_b32 m0, %[w], 14\n"
                 "v_lshrrev_b32 %[t], 4, %[lane]\n v_mul_u32_u24 %[t], 0x204, %[t]\n v_and_b32 %[vh], 15, %[lane]\n v_xor_b32 %[vh], %[vh], %[t]\n"
                 "v_lshl_add_u32 %[vh], %[vh], 4, %[c]\n v_xor_b32 %[t], 0x8010, %[vh]\n s_waitcnt lgkmcnt(0)\n"
                 "global_load_lds_dwordx4 %[vh], %[p]\n s_add_u32 m0, m0, 0x400\n s_nop 0\n"
                 "global_load_lds_dwordx4 %[t], %[p]\n s_add_u32 m0, m0, 0x400\n v_xor_b32 %[t], 0x10020, %[vh]\n"
                 "global_load_lds_dwordx4 %[t], %[p]\n s_add_u32 m0, m0, 0x400\n v_xor_b32 %[t], 0x18030, %[vh]\n"
                 "global_load_lds_dwordx4 %[t], %[p]\n s_branch 1f\n"
                 "3: s_lshl2_add_u32 %[d], %[d], %[w]\n s_sub_u32 %[d], %[d], 68\n s_lshl_b32 %[d], %[d], 16\n s_add_u32 %[d], %[d], %[c]\n"
                 "s_and_b32 %[a], %[j], 3\n s_lshl2_add_u32 %[b], %[a], %[w]\n s_mul_i32 %[b], %[b], 0x30000\n s_lshl_b32 %[a], %[a], 8\n"
                 "v_lshl_add_u32 %[xm], %[lane], 2, %[a]\n"
                 "s_and_b32 %[a], %[j], 60\n s_lshl_b32 %[a], %[a], 5\n s_add_u32 %[b], %[b], %[a]\n"
                 "s_lshl_b32 %[c], %[w], 12\n s_add_u32 %[c], %[c], %[mq]\n s_lshl_b32 %[a], %[w], 10\n s_add_u32 %[a], %[a], %[mh]\n"
                 "s_cmp_eq_u32 %[f], 1\n s_cselect_b32 %[d], %[b], %[d]\n s_cselect_b32 m0, %[c], %[a]\n s_cselect_b32 %[a], 48, 1\n s_cselect_b32 %[c], 0, 4\n"
                 "v_lshrrev_b32 %[t], 3, %[lane]\n v_mul_u32_u24 %[t], %[a], %[t]\n v_and_b32 %[vh], 7, %[lane]\n v_lshl_add_u32 %[vh], %[t], 3, %[vh]\n"
                 "v_lshl_add_u32 %[vh], %[vh], 4, %[d]\n"
                 "s_cselect_b32 %[b], 6, 1\n s_cselect_b32 %[a], 0x400, 0\n s_lshl_b32 %[b], %[b], 13\n"
                 "s_xor_b32 %[c], %[c], %[w]\n s_bitcmp1_b32 %[c], 2\n v_mov_b32 %[t], 0\n"
                 "s_waitcnt lgkmcnt(0)\n s_cbranch_scc1 1f\n s_cmp_eq_u32 %[f], 1\n s_cbranch_scc0 4f\n"
                 "global_load_dword %[xm], %[xm], %[pc]\n global_load_dwordx2 %[ev], %[t], %[pw] sc1\n"
                 "4: global_load_lds_dwordx4 %[vh], %[p]\n s_add_u32 m0, m0, %[a]\n v_add_u32 %[t], %[b], %[vh]\n"
                 "global_load_lds_dwordx4 %[t], %[p]\n s_add_u32 m0, m0, %[a]\n v_add_u32 %[t], %[b], %[t]\n"
                 "global_load_lds_dwordx4 %[t], %[p]\n s_add_u32 m0, m0, %[a]\n v_add_u32 %[t], %[b], %[t]\n"
                 "global_load_lds_dwordx4 %[t], %[p]\n"
                 "1:\n"
                 : [xm] "=&v"(xm), [ev] "=&v"(ev), [vh] "=&v"(vh), [t] "=&v"(t), [p] "=&s"(p), [pc] "=&s"(pc), [pw] "=&s"(pw), [a] "=&s"(a), [b] "=&s"(b), [c] "=&s"(c),
                   [d] "=&s"(d), [f] "=&s"(f)
                 : [j] "s"(j), [w] "s"(w), [lane] "v"(lane), [ka] "s"(__builtin_amdgcn_kernarg_segment_ptr()), [pfl] "n"(PF_LEN), [dump] "n"(L_DUMP),
                   [mq] "n"(L_QOFF + 8192), [mh] "n"(L_P)
                 : "memory", "scc");
  }
  SB();
  const u32 t0 = (u32)__builtin_amdgcn_s_memrealtime() - 35u;
  const bool isQ = j < 64, isS0 = j >= 64 && j < 128;
  const int hq = xc * 4 + (ii >> 1), dh = ii & 1, sj = j - 64;
  const int lw = w - 4, myh = xc >> 2;
  int col = lane & 15, g4 = lane >> 4, tq = col >> 2, tp = lane & 3;
  const rsrc_t rs = __builtin_amdgcn_make_buffer_rsrc((void*)ws, 0, WS_TOTAL, 0x00020000);
  volatile LAS u32* ctr = (volatile LAS u32*)(lds + L_CTR);
  SB();
  STAMP(25);
  if (w < 2) *(u32*)(lds + (w == 0 ? L_CTR + lane * 4 : L_MX + (lane & 31) * 4)) = w == 0 ? 0u : 0xFF800000u;
  BAR();
  const bool tl2 = isS0;
  const int t3 = xc < 4 ? (ii < 8 ? xc * 8 + ii : -1) : (ii < 6 ? 8 + xc * 6 + ii : -1);
  const int t4 = (ii >= 8 && ii < 10) ? xc * 2 + (ii - 8) : -1;
  const u32 owno = (w >> 1) * 32768 + 8192 + (w & 1) * 12288;
  const int ks = (xc & 3) * 2 + myh, cg = ii & 7;
  const int h = (j >> 2) & 31, c4 = j & 3;
  u32x4 wv[8];
  u32 tlo, thi;
  int gx;
#define RELANE() do { asm volatile("v_mbcnt_lo_u32_b32 %0, -1, 0\n v_mbcnt_hi_u32_b32 %0, -1, %0" : "=v"(lane)); col = lane & 15; g4 = lane >> 4; tq = col >> 2; tp = lane & 3; } while (0)
#define TAG_OF(e0, e1)                                                                                                  \
  do {                                                                                                                  \
    const u64 tag = (((u64)(e1) << 32) | (e0)) + 1ull;                                                                  \
    tlo = __builtin_amdgcn_readfirstlane((u32)tag); thi = __builtin_amdgcn_readfirstlane((u32)(tag >> 32));             \
  } while (0)
  const u32x4 P4 = {POISON, POISON, POISON, POISON};
#define COUNT_VOTES(PRE)                                                                                                \
  do {                                                                                                                  \
    u32x2 a_ = {0u, 0u};                                                                                                \
    if (PRE) a_ = *(const u32x2*)(lds + L_QI + lane * 8);                                                               \
    _Pragma("unroll 1") for (u32 spins = 0; spins < SPIN_CAP; ++spins) {                                                \
      if (((PRE) || spins) && __all(gran_ok(a_.x, a_.y, tlo, thi))) break;                                              \
      if (spins) __builtin_amdgcn_s_sleep(8);                                                                           \
      asm volatile("" ::: "memory");                                                                                    \
      a_ = ld64(rs, WS_VOTE + xc * 2048 + lane * 8);                                                                    \
    }                                                                                                                   \
    const float vt_ = wave_sum((float)(a_.x & 1023u));                                                                  \
    if (lane == 0) ctr[C_PAR] = G_OF(vt_);                                                                              \
    VOTE_STAMP(vt_);                                                                                                    \
    STAMP(8);                                                                                                           \
  } while (0)
#define S_POLL(a_)                                                                                                      \
  _Pragma("unroll 1") for (u32 spins = 0; spins < SPIN_CAP && RARE(__any(((a_.x ^ tlo) | (a_.y ^ thi)) != 0u)); ++spins) { \
    __builtin_amdgcn_s_sleep(8);                                                                                        \
    asm volatile("" ::: "memory");                                                                                      \
    a_ = ld64(rs, WS_SFLAG + xc * 2048 + lane * 8);                                                                     \
  }
#define MB_OFF(i) (WS_MODB + ((lane >> 4) * 2048 + ((i) >> 1) * 512 + 128 * lw + 64 * ((i) & 1) + (lane & 15) * 4) * 4)

  if (!isQ && w >= 4) {
    if (isS0) {
      if (w == 4) {
        const u32x2 e4 = ld64(rs, WS_EPOCH);
        st128(rs, WS_QABS + sj * 1024 + lane * 16, P4);
        st128(rs, lane < 32 ? WS_MODB + sj * 512 + lane * 16 : WS_TOTAL, P4);
        st128(rs, WS_PROBE + il(sj * 1024 + lane * 16, 0), Z4);
        st128(rs, WS_PROBE + il(sj * 1024 + lane * 16, 1), Z4);
        waitvm<0>();
        TAG_OF(e4.x, e4.y);
        if (lane < 8) st64(rs, WS_SFLAG + lane * 2048 + sj * 8, tlo, thi);
        STAMP(27);
      }
      lds_wait(ctr + C_H1, 4u);
    } else {
#pragma unroll
      for (int i = 4; i < 16; ++i) { waitvm<7>(); dma<false>(L_P + w * 1024, vh + (i & 7) * 8192, i < 8 ? w_q : w_uk); }
    }
    const u32 vl = g4 * 1024 + ((col ^ ((g4 << 2) | lw)) << 4);
    int nm = 0, ns = isS0 ? 6 : 7;
    const int skip = isS0 ? 4 : 99;
    if (isS0) kv_load<WA0>(ckv, vl, lw, sj * 16384, (sj + 192) * 16384, lw * 1024, 0, 2, nm, skip);
    else kv_load<WIN>(ckv, vl, lw, (1792 + j) * 16384, sj * 16384, lw * 1024, 0, 2, nm, skip);
    const rsrc_t rw = __builtin_amdgcn_make_buffer_rsrc((void*)(isS0 ? w_o : w_uv), 0, 4194304, 0x00020000);
    int wo = isS0 ? (ks * 512 + 64 * w + (lane >> 3)) * 1024 + cg * 128 + (lane & 7) * 16 : (c4 * 128 + 32 * lw + g4) * 8192 + h * 256 + col * 16;
    const int wst = isS0 ? 8192 : 32768;
    PIN(wo);
#define WV_LD(i) do { SB(); wv[i] = __builtin_amdgcn_raw_buffer_load_b128(rw, wo, (i) * wst, 2); wv[(i) + 1] = __builtin_amdgcn_raw_buffer_load_b128(rw, wo, ((i) + 1) * wst, 2); SB(); } while (0)
    u32 va[8];
#pragma unroll
    for (int i = 0; i < 8; ++i) { va[i] = lw * PS + tr_addr(lane, i >> 1, 0, i & 1); PIN(va[i]); }
    int eg[8];
#pragma unroll
    for (int u = 0; u < 8; ++u) { eg[u] = WS_PART + il21(((4 * (u >> 1) + 2 * (g4 >> 1) + (u & 1)) * 4 + lw) * 65536 + sj * 256 + col * 16, g4 & 1); PIN(eg[u]); }
    u32 pr_ = L_P + lane * 16;
    PIN(pr_);
    u32 ml = L_CTR + 4 * (C_MK + 7), one = 1u;
    PIN(ml); PIN(one);
    bf16x8 vf[4][4];
#define V_READ(i, slot) do { asm volatile("" ::: "memory"); SB(); _Pragma("unroll") for (int c = 0; c < 4; ++c) vf[i][c] = tr_frag(lds, va[2 * c] + (slot) * 4096, va[2 * c + 1] + (slot) * 4096); SB(); } while (0)
    SB();
    kv_load<WIN>(ckv, vl, lw, (sj + (isS0 ? 384 : 192)) * 16384, (sj + (isS0 ? 576 : 384)) * 16384, lw * 1024 + 8192, 2, ns, nm, skip);
    lds_wait(ctr + C_MK + 3, 4u);
    V_READ(0, 0); V_READ(1, 1); V_READ(2, 2); V_READ(3, 3);
    lds_add(C_FREE);
#pragma unroll
    for (int i = 0; i < 16; ++i) PIN(vf[i >> 2][i & 3]);
    STAMP(2);
    lds_wait(ctr + C_FREE, 8u);
    STAMP(5);
    kv_load<WIN>(ckv, vl, lw, (sj + 6 * 192) * 16384, (sj + 7 * 192) * 16384, lw * 1024, ns, ns + 4, nm, skip);
    int k4 = 4;
    STAMP(3);
    f32x16 acc[4];
#pragma unroll
    for (int c = 0; c < 4; ++c)
#pragma unroll
      for (int i = 0; i < 16; ++i) acc[c][i] = 0.f;
    bf16x8 pf[4], pn[4];
    u32 c_;
#define P_SPIN(g, n) c_ = spin_try<(g) ? 8 : 0>(C_P + (g) - C_MK - 7, n, k4, ml, one KV_TSP)
#define TRY() kv_try<8>(k4, ml, one KV_TSP)
#define P_READ(d, i0, i1, T0) _Pragma("unroll") for (int i = (i0); i < (i1); ++i) d[i] = *(volatile LAS bf16x8*)(lds + pr_ + ((T0) + i) * 1024)
#define PV(p, i) do { SB(); _Pragma("unroll") for (int c = 0; c < 4; ++c) acc[c] = __builtin_amdgcn_mfma_f32_32x32x16_bf16(p[i], vf[i][c], acc[c], 0, 0, 0); SB(); } while (0)
#define GX_USE(v) do { gx = __builtin_amdgcn_readfirstlane(v); _Pragma("unroll") for (int u = 0; u < 8; ++u) { eg[u] ^= gx; PIN(eg[u]); } } while (0)
#define MK_WAIT(T) spin_try<8>((T) - 7, 4u, k4, ml, one KV_TSP)
#define PVV(p, i, d, slot)                                                                                               \
  _Pragma("unroll") for (int c = 0; c < 4; ++c) {                                                                        \
    SB();                                                                                                                \
    acc[c] = __builtin_amdgcn_mfma_f32_32x32x16_bf16(p[i], vf[i][c], acc[c], 0, 0, 0);                                   \
    SB();                                                                                                                \
    vf[d][c] = tr_frag(lds, va[2 * c] + (slot) * 4096, va[2 * c + 1] + (slot) * 4096);                                   \
    SB();                                                                                                                \
  }
#define CT(i) (*(volatile LAS u32*)(lds + ml + 4 * ((i) - C_MK - 7)))
    P_SPIN(0, 4u);
    P_READ(pf, 0, 4, 0);
    const u32 k7 = CT(C_MK + 7);
    asm volatile("" ::: "memory");
    STAMP(6);
    PVV(pf, 0, 0, 4); kv_try<0>(k4, ml, one KV_TSP); WV_LD(0);
    PVV(pf, 1, 1, 5); kv_try<2>(k4, ml, one KV_TSP); WV_LD(2);
    PVV(pf, 2, 2, 6); WV_LD(4);
    u32 cv = CT(C_P + 1);
    P_READ(pn, 0, 4, 4);
    PVV(pf, 3, 3, 0); WV_LD(6);
    if (RARE(__builtin_amdgcn_readfirstlane(k7) < 4u)) { MK_WAIT(7); V_READ(0, 4); V_READ(1, 5); V_READ(2, 6); V_READ(3, 0); }
    STAMP(9);
    c_ = __builtin_amdgcn_readfirstlane(cv);
    if (RARE((c_ & 255u) != 4u)) { P_SPIN(1, 4u); P_READ(pn, 0, 4, 4); }
    int slow = -1;
    if (RARE(c_ > 255u)) slow = 4;
    else {
      if (!isS0) PV(pn, 0);
      TRY();
      const u32 k9 = CT(C_MK + 9);
      asm volatile("" ::: "memory");
      PVV(pn, 1, 0, 1); TRY();
      PVV(pn, 2, 1, 2);
      cv = CT(C_P + 2);
      P_READ(pf, 0, 2, 8);
      const u32 k10 = CT(C_MK + 10);
      asm volatile("" ::: "memory");
      PVV(pn, 3, 2, 3);
      if (RARE(__builtin_amdgcn_readfirstlane(k9) < 4u)) { MK_WAIT(9); V_READ(0, 1); V_READ(1, 2); }
      STAMP(10);
      c_ = __builtin_amdgcn_readfirstlane(cv);
      if (RARE((c_ & 255u) != 4u)) { P_SPIN(2, 4u); P_READ(pf, 0, 2, 8); }
      if (RARE(c_ > 255u)) slow = 8;
      else {
        TRY();
        PV(pf, 0);
        const u32 g0 = ctr[C_GX];
        u32 g1 = ctr[C_PAR];
        cv = CT(C_P + 3);
        P_READ(pf, 2, 3, 8);
        PV(pf, 1);
        STAMP(4);
        if (RARE(__builtin_amdgcn_readfirstlane(k10) < 4u)) { waitvm<8>(); TRY(); lds_spin(ctr + C_MK + 10, 4u); V_READ(2, 3); }
        if (RARE(__builtin_amdgcn_readfirstlane(g0) == 0u)) { lds_spin(ctr + C_GX, 1u); g1 = ctr[C_PAR]; }
        GX_USE(g1);
        STAMP(11);
        c_ = __builtin_amdgcn_readfirstlane(cv);
        _Pragma("unroll 1") for (u32 n_ = 0; RARE((c_ & 255u) != 2u) && n_ < LSPIN_CAP; ++n_) { cv = CT(C_P + 3); P_READ(pf, 2, 3, 8); c_ = __builtin_amdgcn_readfirstlane(cv); }
        if (RARE(c_ > 255u)) slow = 10;
        else PV(pf, 2);
      }
    }
    if (RARE(slow >= 0)) {
      MK_WAIT(10);
      float R = ceilf(*(const float*)(lds + L_MX + (lane & 31) * 4));
#pragma unroll 1
      for (int T = slow; T < 11; ++T) {
        if (T == 4 && isS0) continue;
        spin_at(L_CTR + 4 * (C_P + (T < 8 ? 1 : T < 10 ? 2 : 3)), T < 10 ? 4u : 2u);
        const float rf = *(const float*)(lds + L_REF + T * 128 + (lane & 31) * 4);
        u32x4 p = *(const u32x4*)(lds + pr_ + T * 1024);
        const u32 so_ = (T < 7 ? T : T - 7) * 4096;
#pragma unroll
        for (int c = 0; c < 4; ++c) vf[0][c] = tr_frag(lds, va[2 * c] + so_, va[2 * c + 1] + so_);
        const float Rn = fmaxf(R, rf), ps = __builtin_amdgcn_exp2f(rf - Rn);
        *(float*)(lds + L_ALW + lw * 128 + (lane & 31) * 4) = __builtin_amdgcn_exp2f(R - Rn);
        R = Rn;
        WAVE_LDS_SYNC();
#pragma unroll
        for (int i = 0; i < 4; ++i) {
          const f32x4 al = *(const f32x4*)(lds + L_ALW + lw * 128 + (lane >> 5) * 16 + i * 32);
#pragma unroll
          for (int c = 0; c < 4; ++c)
#pragma unroll
            for (int r = 0; r < 4; ++r) acc[c][4 * i + r] *= al[r];
          p[i] = pk2bf(lo16(p[i]) * ps, hi16(p[i]) * ps);
        }
        WAVE_LDS_SYNC();
        pf[0] = __builtin_bit_cast(bf16x8, p);
        PV(pf, 0);
      }
      if (slow != 10) { lds_spin(ctr + C_GX, 1u); GX_USE(ctr[C_PAR]); }
    }
    STAMP(30);
#pragma unroll
    for (int u = 0; u < 8; u += 2) {
      u32 c[8];
#pragma unroll
      for (int k = 0; k < 8; ++k) c[k] = pk2bf(acc[k & 2][2 * u + (k & 1) + (k >> 2) * 2], acc[(k & 2) + 1][2 * u + (k & 1) + (k >> 2) * 2]);
      SB();
#pragma unroll
      for (int k = 0; k < 2; ++k) {
        const auto r0 = __builtin_amdgcn_permlane16_swap(c[4 * k], c[4 * k + 1], false, false);
        const auto r1 = __builtin_amdgcn_permlane16_swap(c[4 * k + 2], c[4 * k + 3], false, false);
        st128(rs, eg[u + k], u32x4{r0[0], r1[0], r0[1], r1[1]});
      }
      SB();
    }
    STAMP(31);
    if (!isS0) {
#pragma unroll
      for (int i = 0; i < 8; ++i) *(u32x4*)(lds + lw * PS + 16384 + off_b(4 * i + g4, col)) = wv[i];
    }
    {
      int fo = lane < 16 ? (WS_LSE + il(sj * 512 + lw * 128 + (lane & 7) * 16, lane >> 3)) ^ gx : WS_TOTAL;
      lds_spin(ctr + C_ML, 1u);
      u32x4 ml4 = *(const u32x4*)(lds + L_WMAX + col * 16);
      PIN(fo); PIN(ml4);
      waitvm<0>();
      __builtin_amdgcn_s_waitcnt(0x0F70);
      st128(rs, fo, ml4);
      lds_add(C_PD);
    }
    STAMP(13);
  } else if (!isQ) {
    if (isS0) {
      u32 ta = L_QOFF + 8192 + (8 * g4 + tq) * 128 + w * 32 + tp * 8;
      u32 sa = L_H1RED + w * 256;
      int so = col < 2 ? WS_SC + (((sj >> 5) * 4 + (sj & 3)) * 512 + ((sj >> 2) & 7) * 64 + 16 * w + 4 * g4 + 2 * col) * 8 : WS_TOTAL;
      PIN(ta); PIN(so); PIN(sa);
      u32 c2, e0, e1;
      asm volatile("s_waitcnt vmcnt(4) ; HEAD_WAIT\n v_mov_b32 %0, %3\n v_mov_b32 %1, %4\n v_mov_b32 %2, %5" : "=&v"(c2), "=&v"(e0), "=&v"(e1) : "v"(xm), "v"(ev.x), "v"(ev.y) : "memory");
      TAG_OF(e0, e1);
      bf16x8 sf[4];
      u32x2 wt[8];
      asm volatile("ds_write_b32 %4, %5\n"
                   "ds_read_b128 %0, %6\n ds_read_b128 %1, %6 offset:64\n ds_read_b128 %2, %6 offset:128\n ds_read_b128 %3, %6 offset:192\n s_waitcnt lgkmcnt(0)"
                   : "=&v"(sf[0]), "=&v"(sf[1]), "=&v"(sf[2]), "=&v"(sf[3])
                   : "v"(sa + lane * 4), "v"(pk2bf(silu_bf(lo16(c2)), silu_bf(hi16(c2)))), "v"(sa + 16 * g4) : "memory");
      waitvm<0>();
      lds_add(C_BH);
      lds_spin(ctr + C_BH, 4u);
      STAMP(1);
      asm volatile("ds_read_b64_tr_b16 %0, %8\n ds_read_b64_tr_b16 %1, %8 offset:512\n ds_read_b64_tr_b16 %2, %8 offset:4096\n ds_read_b64_tr_b16 %3, %8 offset:4608\n"
                   "ds_read_b64_tr_b16 %4, %8 offset:8192\n ds_read_b64_tr_b16 %5, %8 offset:8704\n ds_read_b64_tr_b16 %6, %8 offset:12288\n ds_read_b64_tr_b16 %7, %8 offset:12800\n"
                   "s_waitcnt lgkmcnt(0)"
                   : "=&v"(wt[0]), "=&v"(wt[1]), "=&v"(wt[2]), "=&v"(wt[3]), "=&v"(wt[4]), "=&v"(wt[5]), "=&v"(wt[6]), "=&v"(wt[7]) : "v"(ta) : "memory");
      f32x4 d = {0.f, 0.f, 0.f, 0.f};
#pragma unroll
      for (int k = 0; k < 4; ++k)
        d = __builtin_amdgcn_mfma_f32_16x16x32_bf16(__builtin_bit_cast(bf16x8, u32x4{wt[2 * k].x, wt[2 * k].y, wt[2 * k + 1].x, wt[2 * k + 1].y}), sf[k], d, 0, 0, 0);
      st_gran2(rs, so, (col & 1) ? d[2] : d[0], (col & 1) ? d[3] : d[1], tlo, thi);
      lds_add(C_H1);
      STAMP(2);
      until(t0, SCP_T);
      dma_sc1(L_P + w * 1024, WS_SC + ((ii & 7) * 4 + w) * 1024 + lane * 16, ws);
    }
    u32 ab = off_b(col, 4 * g4), qv = WS_QABS + myh * 8192 + g4 * 1024 + w * 256 + ((col ^ (g4 << 2)) << 4);
    const int hb = 16 * (w & 1) + col;
    u32 pw = L_P + (g4 >> 1) * 512 + hb * 16 + (g4 & 1) * 8, mw = hb * 4;
    PIN(ab); PIN(qv); PIN(pw); PIN(mw);
    if (w == 2) {
      until(t0, QF_T);
      const u32x2 e2 = ld64(rs, WS_EPOCH);
      u32x2 f2 = ld64(rs, WS_SFLAG + xc * 2048 + lane * 8);
      TAG_OF(e2.x, e2.y);
      S_POLL(f2);
      lds_add(C_SOK);
      STAMP(3);
    }
    bf16x8 af[16], qf[16][2];
    u32 aa[16], qa[32];
#pragma unroll
    for (int i = 0; i < 16; ++i) {
      aa[i] = (ab ^ ((i & 3) << 4)) + (((w + (i >> 2)) & 3) * PS + w * 4096); PIN(aa[i]);
#pragma unroll
      for (int u = 0; u < 2; ++u) { qa[2 * i + u] = (ab ^ ((i & 3) << 4)) + (L_QI + ((w + (i >> 2)) & 3) * 8192 + ((u ^ w) & 1) * 4096); PIN(qa[2 * i + u]); }
    }
    const int sl1 = w == 3 ? 0 : 4 + w, sl2 = 1 + (w >> 1);
#define A_RD(i, o) af[i] = *(const bf16x8*)(lds + aa[i] + (o))
#define A_READ(i0, i1, o) _Pragma("unroll") for (int i = (i0); i < (i1); ++i) A_RD(i, o)
#define A_MOVE(i0, i1, d) _Pragma("unroll") for (int i = (i0); i < (i1); ++i) { aa[i] += (d) * 4096; PIN(aa[i]); }
#define Q_RD(i, u) qf[i][u] = *(const bf16x8*)(lds + qa[2 * (i) + (u)])
#define A_FIRST() do { A_READ(0, 16, 0); lds_add(C_FREE); _Pragma("unroll") for (int i = 0; i < 16; ++i) PIN(af[i]); STAMP(12); } while (0)
    u32 qd[8];
#pragma unroll
    for (int i = 0; i < 8; ++i) { qd[i] = (qv ^ ((i & 3) << 4)) + (i >> 1) * 16384 + (i & 1) * 4096; PIN(qd[i]); }
    bool have = false;
    _Pragma("unroll 1") while ((u32)__builtin_amdgcn_s_memrealtime() - t0 < E3_T) {
      if (__builtin_amdgcn_readfirstlane(ctr[C_MK + w]) >= 4u) { have = true; break; }
      __builtin_amdgcn_s_sleep(1);
    }
    asm volatile("" ::: "memory");
    if (have) A_FIRST();
    lds_wait(ctr + C_SOK, 1u);
    if (isS0) lds_wait(ctr + C_H1, 4u);
    until(t0, E3_T - 10);
    _Pragma("unroll 1") while ((u32)__builtin_amdgcn_s_memrealtime() - t0 < E3_T) {}
    STAMP(6);
#define QD(i) "global_load_lds_dwordx4 %[q" #i "], %[ws] sc1\n s_add_u32 m0, m0, 0x400\n s_nop 0\n"
    asm volatile("s_mov_b32 m0, %[m]\n s_nop 0\n" QD(0) QD(1) QD(2) QD(3) QD(4) QD(5) QD(6) QD(7)
                 ::[m] "s"(L_QI + w * 8192), [ws] "s"(ws), [q0] "v"(qd[0]), [q1] "v"(qd[1]), [q2] "v"(qd[2]), [q3] "v"(qd[3]), [q4] "v"(qd[4]), [q5] "v"(qd[5]), [q6] "v"(qd[6]), [q7] "v"(qd[7]) : "memory", "scc");
    if (RARE(!have)) { lds_wait(ctr + C_MK + w, 4u); A_FIRST(); }
    A_MOVE(0, 16, sl1 - w);
    u32 cb = L_CTR, cw = L_CTR + 4 * w, mx0 = mw + L_MX, mx1 = (mw ^ 64) + L_MX, pw0 = pw + w * 1024, pw1 = (pw ^ 256) + w * 1024;
    float ninf = -__builtin_inff();
    u32 one = 1u;
    PIN(ninf); PIN(one); PIN(cb); PIN(cw); PIN(mx0); PIN(mx1); PIN(pw0); PIN(pw1);
#define CTS(b, i) (*(volatile LAS u32*)(lds + (b) + 4 * (i)))
    _Pragma("unroll 1") for (u32 spins = 0; spins < SPIN_CAP; ++spins) {
      waitvm<0>();
#pragma unroll
      for (int i = 0; i < 8; ++i) Q_RD(i >> 1, i & 1);
      u32 mxu[2] = {0u, 0u};
#pragma unroll
      for (int i = 0; i < 8; ++i) { const u32x4 t_ = __builtin_bit_cast(u32x4, qf[i >> 1][i & 1]); mxu[i & 1] = max(max(mxu[i & 1], t_.x), t_.y); mxu[i & 1] = max(max(mxu[i & 1], t_.z), t_.w); }
      if (!RARE(__any(max(mxu[0], mxu[1]) == POISON))) break;
      STAMP(29);
      __builtin_amdgcn_s_sleep(2);
      const u64 b0 = __ballot(mxu[w & 1] == POISON), b1 = __ballot(mxu[~w & 1] == POISON);
#pragma unroll 1
      for (int i = 0; i < 8; ++i)
        if (((i < 4 ? b0 : b1) >> (4 * (i & 3))) & 0x000F000F000F000Full) dma_sc1(L_QI + w * 8192 + i * 1024, (qv ^ ((i & 3) << 4)) + (i >> 1) * 16384 + (i & 1) * 4096, ws);
    }
    lds_add(C_QRDY);
    STAMP(7);
    f32x4 sc[2], sn[2];
    sc[0] = sc[1] = sn[0] = sn[1] = f32x4{0.f, 0.f, 0.f, 0.f};
#define QK(d, i0, i1, NU)                                                                                                \
  do {                                                                                                                   \
    SB();                                                                                                                \
    _Pragma("unroll") for (int i = (i0); i < (i1); ++i) _Pragma("unroll") for (int u = 0; u < (NU); ++u)                 \
      d[u] = __builtin_amdgcn_mfma_f32_16x16x32_bf16(af[i], qf[i][u], d[u], 0, 0, 0);                                    \
    SB();                                                                                                                \
  } while (0)
#define MF(d, i, u) do { SB(); d[u] = __builtin_amdgcn_mfma_f32_16x16x32_bf16(af[i], qf[i][u], d[u], 0, 0, 0); SB(); } while (0)
    const bool idle1 = isS0 && w == 0;
    u32 c1;
    {
      const u32 c0 = CTS(cb, C_QRDY);
      QK(sc, 0, 2, 2);
      if (RARE(__builtin_amdgcn_readfirstlane(c0) < 4u)) lds_spin(ctr + C_QRDY, 4u);
      asm volatile("" ::: "memory");
#pragma unroll
      for (int i = 2; i < 4; ++i) { MF(sc, i, 0); Q_RD(2 * i, 0); Q_RD(2 * i, 1); MF(sc, i, 1); Q_RD(2 * i + 1, 0); Q_RD(2 * i + 1, 1); }
#pragma unroll
      for (int i = 4; i < 12; ++i) { MF(sc, i, 0); Q_RD(i + 4, 0); MF(sc, i, 1); Q_RD(i + 4, 1); }
      c1 = CTS(cw, C_MK + 4);
      asm volatile("" ::: "memory");
#pragma unroll
      for (int i = 12; i < 16; ++i) { MF(sc, i, 0); A_RD(2 * i - 24, 0); MF(sc, i, 1); A_RD(2 * i - 23, 0); }
    }
    STAMP(9);
    float mref[2], lsum[2] = {0.f, 0.f};
#define ADDC(i, v) do { SB(); asm volatile("s_mov_b64 exec, 1\n ds_add_u32 %0, %1 offset:%2\n s_mov_b64 exec, -1" ::"v"(cb), "v"(v), "n"(4 * (i)) : "memory"); SB(); } while (0)
#define SM(d, T, NU, G, P0, P1)                                                                                          \
  do {                                                                                                                   \
    SB();                                                                                                                \
    if (RARE(__any(max5(d[0], (NU) > 1 ? max5(d[1], ninf) : ninf) > TAU))) {                                             \
      sig = 257u;                                                                                                        \
      _Pragma("unroll") for (int u = 0; u < (NU); ++u) {                                                                 \
        const float up = ceilf(x32_max(x16_max(max5(d[u], 0.f))));                                                       \
        lsum[u] *= __builtin_amdgcn_exp2f(-up);                                                                          \
        mref[u] += up;                                                                                                   \
        d[u] -= up;                                                                                                      \
        *(float*)(lds + (mw ^ (u * 64)) + L_REF + (T) * 128) = mref[u];                                                  \
      }                                                                                                                  \
    }                                                                                                                    \
    u32x2 pk[2];                                                                                                         \
    _Pragma("unroll") for (int u = 0; u < (NU); ++u) {                                                                   \
      pk[u].x = pk2bf(__builtin_amdgcn_exp2f(d[u].x), __builtin_amdgcn_exp2f(d[u].y));                                   \
      pk[u].y = pk2bf(__builtin_amdgcn_exp2f(d[u].z), __builtin_amdgcn_exp2f(d[u].w));                                   \
      *(u32x2*)(lds + (u ? (P1) : (P0))) = pk[u];                                                                        \
    }                                                                                                                    \
    ADDC(C_P + (G), sig);                                                                                                \
    _Pragma("unroll") for (int u = 0; u < (NU); ++u) { lsum[u] = dot2(pk[u].x, 0x3F803F80u, dot2(pk[u].y, 0x3F803F80u, lsum[u])); PIN(lsum[u]); } \
    SB();                                                                                                                \
  } while (0)
#define REF_WRITE(T, NU) _Pragma("unroll") for (int u = 0; u < (NU); ++u) *(float*)(lds + (mw ^ (u * 64)) + L_REF + (T) * 128) = mref[u]
    u32 sig = one;
    {
      MF(sn, 0, 0); A_RD(8, 0); MF(sn, 0, 1);
      const float m0 = max5(sc[0], ninf), m1 = max5(sc[1], ninf);
      MF(sn, 1, 0);
      asm volatile("ds_max_f32 %0, %2\n ds_max_f32 %1, %3" ::"v"(mx0), "v"(mx1), "v"(m0), "v"(m1) : "memory");
      ADDC(C_MXC, one);
      MF(sn, 1, 1); A_RD(9, 0);
#pragma unroll
      for (int i = 2; i < 4; ++i) { MF(sn, i, 0); A_RD(2 * i + 6, 0); MF(sn, i, 1); A_RD(2 * i + 7, 0); }
      MF(sn, 4, 0);
      const u32 c0 = CTS(cb, C_MXC);
      mref[0] = *(volatile LAS float*)(lds + mx0);
      mref[1] = *(volatile LAS float*)(lds + mx1);
      MF(sn, 4, 1); MF(sn, 5, 0); A_RD(14, 0); MF(sn, 5, 1); A_RD(15, 0); MF(sn, 6, 0); MF(sn, 6, 1);
      if (RARE(__builtin_amdgcn_readfirstlane(c0) < 4u)) {
        lds_spin(ctr + C_MXC, 4u);
        mref[0] = *(volatile LAS float*)(lds + mx0);
        mref[1] = *(volatile LAS float*)(lds + mx1);
      }
      u32x2 pk[2];
#pragma unroll
      for (int u = 0; u < 2; ++u) {
        MF(sn, 7 + 2 * u, 0);
        mref[u] = ceilf(mref[u]);
        float e0 = __builtin_amdgcn_exp2f(sc[u].x - mref[u]); PIN(e0);
        MF(sn, 7 + 2 * u, 1);
        pk[u].x = pk2bf(e0, __builtin_amdgcn_exp2f(sc[u].y - mref[u])); PIN(pk[u].x);
        MF(sn, 8 + 2 * u, 0);
        e0 = __builtin_amdgcn_exp2f(sc[u].z - mref[u]); PIN(e0);
        MF(sn, 8 + 2 * u, 1);
        pk[u].y = pk2bf(e0, __builtin_amdgcn_exp2f(sc[u].w - mref[u]));
        *(u32x2*)(lds + (u ? pw1 : pw0)) = pk[u];
      }
      ADDC(C_P, one);
      MF(sn, 11, 0);
      lsum[0] = dot2(pk[0].x, 0x3F803F80u, dot2(pk[0].y, 0x3F803F80u, 0.f)); PIN(lsum[0]);
      MF(sn, 11, 1);
      lsum[1] = dot2(pk[1].x, 0x3F803F80u, dot2(pk[1].y, 0x3F803F80u, 0.f)); PIN(lsum[1]);
      MF(sn, 12, 0);
      REF_WRITE(4 + w, 2);
      MF(sn, 12, 1);
      QK(sn, 13, 16, 2);
    }
    STAMP(10);
#define QK1(N0, slot)                                                                                                    \
  do {                                                                                                                   \
    _Pragma("unroll") for (int i = 0; i < 16; ++i) { MF(sc, i, 0); if (i + (N0) < 16) A_RD(i + (N0), slot); }            \
  } while (0)
    if (RARE(__builtin_amdgcn_readfirstlane(c1) < 4u && !idle1)) {
      lds_spin(ctr + C_MK + 4 + w, 4u);
      A_READ(0, 16, 0);
      sn[0] = sn[1] = f32x4{0.f, 0.f, 0.f, 0.f};
      QK(sn, 0, 16, 2);
    }
    if (RARE(idle1)) lds_add(C_P + 1);
    else { sn[0] -= mref[0]; sn[1] -= mref[1]; SM(sn, 4 + w, 2, 1, pw0 + 4096, pw1 + 4096); }
    if (RARE(w == 2)) {
      RELANE();
      dma_sc1(L_QI, WS_VOTE + xc * 2048 + lane * 16, ws);
    }
    REF_WRITE(7 + sl2, 1);
    A_MOVE(0, 16, sl2 - sl1);
    pw0 = pw + sl2 * 1024;
    sc[0] = f32x4{-mref[0], -mref[0], -mref[0], -mref[0]};
    PIN(pw0); PIN(sc[0]);
    lds_spin(ctr + C_MK + 7 + sl2, 4u);
    A_READ(0, 6, 0);
    STAMP(4);
    QK1(6, 0);
    SM(sc, 7 + sl2, 1, 2, pw0 + 7168, 0);
    STAMP(11);
    if (!RARE(w >= 2)) {
      REF_WRITE(10, 1);
      sc[0] = f32x4{-mref[0], -mref[0], -mref[0], -mref[0]};
      PIN(sc[0]);
      _Pragma("unroll 1") for (u32 n_ = 0; n_ < LSPIN_CAP; ++n_) {
        const u32 k_ = CTS(cb, C_MK + 10);
        asm volatile("" ::: "memory");
        A_READ(0, 6, 8192);
        SB();
        if (__builtin_amdgcn_readfirstlane(k_) >= 4u) break;
      }
      STAMP(5);
      QK1(6, 8192);
      SM(sc, 10, 1, 3, pw0 + 9216, 0);
    } else if (w == 2) {
      RELANE();
      waitvm<0>();
      COUNT_VOTES(true);
      lds_add(C_GX);
    }
    STAMP(30);
    RELANE();
    {
#pragma unroll
      for (int u = 0; u < 2; ++u) {
        *(float*)(lds + L_ML + (mw ^ (u * 64)) * 4 + w * 4) = mref[u];
        *(float*)(lds + L_ML + 512 + (mw ^ (u * 64)) * 4 + w * 4) = x32_sum(x16_sum(lsum[u]));
      }
      u32 old;
      f32x4 m4, l4;
      asm volatile("s_mov_b64 exec, 1\n ds_add_rtn_u32 %0, %3, %4 offset:%6\n s_mov_b64 exec, -1\n ds_read_b128 %1, %5\n ds_read_b128 %2, %5 offset:512\n s_waitcnt lgkmcnt(0)"
                   : "=&v"(old), "=&v"(m4), "=&v"(l4) : "v"((u32)L_CTR), "v"(1u), "v"(L_ML + (lane & 31) * 16), "n"(4 * C_SD) : "memory");
      if (__builtin_amdgcn_readfirstlane(old) == 3u) {
        const float M = fmaxf(fmaxf(m4.x, m4.y), fmaxf(m4.z, m4.w));
        const float l = (l4.x * __builtin_amdgcn_exp2f(m4.x - M) + l4.y * __builtin_amdgcn_exp2f(m4.y - M)) + (l4.z * __builtin_amdgcn_exp2f(m4.z - M) + l4.w * __builtin_amdgcn_exp2f(m4.w - M));
        *(u32x2*)(lds + L_WMAX + (lane & 1) * 128 + (lane & 30) * 4) = u32x2{fbits(M), fbits(l)};
        lds_add(C_ML);
      }
    }
    lds_wait(ctr + C_GX, 1u);
    gx = __builtin_amdgcn_readfirstlane(ctr[C_PAR]);
    STAMP(13);
  } else {
    const int mu = 2 * j + (w >> 2);
#define Q_LOAD(i0, i1)                                                                                                   \
  _Pragma("unroll") for (int i = (i0); i < (i1); ++i) {                                                                  \
    waitvm<QW - 1>();                                                                                                    \
    dma<false>(w * 16384 + (i & 15) * 1024, (vq ^ ((i & 3) << 4)) + ((i < 8 ? 64 * w + 32 * dh : i < 16 ? 64 * w + 32 - 32 * dh : 256 * dh + 32 * w) + 4 * (i & 7)) * 8192, i < 16 ? w_q : w_uk); \
  }
    const u32 vq = g4 * 8192 + hq * 256 + ((col ^ (g4 << 2)) << 4);
    u32x4 xv;
    u32x2 e2;
    u32 x2_;
    asm volatile("global_load_dwordx4 %0, %3, %5\n global_load_dword %1, %4, %5\n buffer_load_dwordx2 %2, off, %6, 0 sc1"
                 : "=&v"(xv), "=&v"(x2_), "=&v"(e2) : "v"(lane * 16), "v"(128 * w + 4 * (lane & 31)), "s"(x), "s"(rs) : "memory");
    bf16x8 wf[16], wk[8];
    u32 ta0 = w * 16384 + off_b(8 * g4 + tq, tp >> 1) + 8 * (tp & 1), ta1 = w * 16384 + off_b(8 * g4 + 4 + tq, tp >> 1) + 8 * (tp & 1);
    int so = WS_SC + ((lane >> 5) * 2048 + 64 * w + 2 * (lane & 31)) * 8;
    int fo = w == 0 ? WS_SFLAG + xc * 2048 + lane * 8 : WS_TOTAL;
    PIN(ta0); PIN(ta1); PIN(so); PIN(fo);
    int qpo = WS_QABS + il(hq * 1024 + dh * 512 + w * 64, 0) + (lane & 32) + (lane & 8) * 2 + (g4 & 1) * 8192 + ((lane & 7) ? WS_TOTAL : 0);
    u32 hw_ = L_H1RED + w * 128 + (lane & 31) * 4, hr_ = L_H1RED + w * 128 + 16 * g4 + 64 * dh, qw_ = L_H1RED + 1024 + w * 272 + lane * 2, qr_ = L_H1RED + 1024 + col * 272 + 64 * g4;
    PIN(qpo); PIN(hw_); PIN(hr_); PIN(qw_); PIN(qr_);
    u32 ka = w * 16384 + off_b(col, 4 * g4), hr2 = hr_ ^ 64;
    PIN(ka); PIN(hr2);
    Q_LOAD(4, 8);
    asm volatile("s_waitcnt vmcnt(4)" : "+v"(xv), "+v"(x2_), "+v"(e2)::"memory");
    TAG_OF(e2.x, e2.y);
    float ln0, ln1;
    {
      float mb, rsd;
      ln_stats(xv, mb, rsd);
      ln0 = rbf(rbf(lo16(x2_) - mb) * rsd);
      ln1 = rbf(rbf(hi16(x2_) - mb) * rsd);
    }
    PIN(ln0); PIN(ln1);
    u32 t0_ = tlo << 24, t1_ = (tlo >> 8) | (thi << 24), f0_ = w ? 0u : tlo, f1_ = w ? 0u : thi;
    PIN(t0_); PIN(t1_); PIN(f0_); PIN(f1_);
    Q_LOAD(8, 8 + QW);
    waitvm<QW>();
#pragma unroll
    for (int c = 0; c < 8; ++c) wf[c] = tr_frag(lds, ta0 ^ (c << 5), ta1 ^ (c << 5));
    Q_LOAD(8 + QW, 16);
#pragma unroll
    for (int c = 0; c < 8; ++c) PIN(wf[c]);
    Q_LOAD(16, 16 + QW);
    waitvm<QW>();
#pragma unroll
    for (int c = 0; c < 8; ++c) wf[8 + c] = tr_frag(lds, (ta0 ^ (c << 5)) + 8192, (ta1 ^ (c << 5)) + 8192);
    Q_LOAD(16 + QW, 24);
    STAMP(12);
    until(t0, E1_T);
    STAMP(23);
    u32x4 g[4];
    u32x2 fl;
#define E1_LOAD(C)                                                                                                       \
  asm volatile("buffer_load_dwordx4 %0, %5, %7, 0 offen sc1\n buffer_load_dwordx4 %1, %5, %7, %8 offen sc1\n"             \
               "buffer_load_dwordx4 %2, %5, %7, %9 offen sc1\n buffer_load_dwordx4 %3, %5, %7, %10 offen sc1\n buffer_load_dwordx2 %4, %6, %7, 0 offen sc1" \
               : C(g[0]), C(g[1]), C(g[2]), C(g[3]), C(fl) : "v"(so), "v"(fo), "s"(rs), "s"(4096), "s"(8192), "s"(12288) : "memory")
    E1_LOAD("=&v");
    waitvm<5>();
#pragma unroll
    for (int i = 0; i < 8; ++i) wk[i] = *(const bf16x8*)(lds + (ka ^ ((i & 3) << 4)) + (i >> 2) * 4096);
#pragma unroll
    for (int c = 0; c < 8; ++c) { PIN(wf[8 + c]); PIN(wk[c]); }
    STAMP(4);
    _Pragma("unroll 1") for (u32 spins = 0; spins < SPIN_CAP; ++spins) {
      asm volatile("s_waitcnt vmcnt(0)" : "+v"(g[0]), "+v"(g[1]), "+v"(g[2]), "+v"(g[3]), "+v"(fl)::"memory");
      u32 bl = g[0].x ^ t0_, bh = (fl.x ^ f0_) | (fl.y ^ f1_);
#pragma unroll
      for (int k = 0; k < 8; ++k) {
        if (k) bl = __builtin_amdgcn_bitop3_b32(bl, g[k >> 1][2 * (k & 1)], t0_, 0xF6);
        bh = __builtin_amdgcn_bitop3_b32(bh, g[k >> 1][2 * (k & 1) + 1], t1_, 0xF6);
      }
      if (!RARE(__any(((bl >> 24) | bh) != 0u))) break;
      STAMP(29);
      __builtin_amdgcn_s_sleep(1);
      E1_LOAD("+v");
    }
    STAMP(1);
    {
      const float m0 = rbf((gran_val(g[0].x) + gran_val(g[1].x)) + (gran_val(g[2].x) + gran_val(g[3].x)));
      const float m1 = rbf((gran_val(g[0].z) + gran_val(g[1].z)) + (gran_val(g[2].z) + gran_val(g[3].z)));
      const auto s0 = __builtin_amdgcn_permlane32_swap(__float_as_uint(m0), __float_as_uint(m0), false, false);
      const auto s1 = __builtin_amdgcn_permlane32_swap(__float_as_uint(m1), __float_as_uint(m1), false, false);
      *(u32*)(lds + hw_) = pk2bf(rbf(ln0 * rbf(1.f + __uint_as_float(s0[1]))) + __uint_as_float(s0[0]),
                                 rbf(ln1 * rbf(1.f + __uint_as_float(s1[1]))) + __uint_as_float(s1[0]));
    }
    WAVE_LDS_SYNC();
    {
      const bf16x8 h0 = *(const bf16x8*)(lds + hr_), h1 = *(const bf16x8*)(lds + hr2);
      f32x4 d[8];
      SB();
#pragma unroll
      for (int c = 0; c < 8; ++c) { d[c] = __builtin_amdgcn_mfma_f32_16x16x32_bf16(h0, wf[c], f32x4{0.f, 0.f, 0.f, 0.f}, 0, 0, 0); SB(); }
#pragma unroll
      for (int c = 0; c < 8; ++c) { d[c] = __builtin_amdgcn_mfma_f32_16x16x32_bf16(h1, wf[8 + c], d[c], 0, 0, 0); SB(); }
#pragma unroll
      for (int k = 0; k < 2; ++k) {
        const float v = (g4 & 2) ? ((g4 & 1) ? d[4 * k + 3][0] : d[4 * k + 2][0]) : ((g4 & 1) ? d[4 * k + 1][0] : d[4 * k][0]);
        const u32 vh = f2bf(v);
        *(u16*)(lds + qw_ + 128 * k) = (u16)vh;
        *(u16*)(lds + qw_ + 128 * k + 8 * 272) = (u16)f2bf(v - bf2f(vh));
      }
    }
    STAMP(2);
    BAR();
    {
      bf16x8 a[4];
#pragma unroll
      for (int s = 0; s < 4; ++s) a[s] = *(const bf16x8*)(lds + qr_ + 16 * s);
      SB();
      f32x4 d[2] = {f32x4{0.f, 0.f, 0.f, 0.f}, f32x4{0.f, 0.f, 0.f, 0.f}};
#pragma unroll
      for (int s = 0; s < 4; ++s)
#pragma unroll
        for (int c = 0; c < 2; ++c) d[c] = __builtin_amdgcn_mfma_f32_16x16x32_bf16(a[s], wk[4 * c + s], d[c], 0, 0, 0);
      constexpr float SCALE = 0.08838834764831845f * 1.4426950408889634f;
      const float v = SCALE * x16_sum(rs32(sum4(d[0]), sum4(d[1])));
      const float p0 = __uint_as_float(nopois(pk2bf(dppf<0x00>(v), dppf<0x55>(v)))), p1 = __uint_as_float(nopois(pk2bf(dppf<0xAA>(v), dppf<0xFF>(v))));
      st128(rs, qpo, u32x4{__float_as_uint(p0), __float_as_uint(p1), __float_as_uint(dppf<0x104>(p0)), __float_as_uint(dppf<0x104>(p1))});
    }
    STAMP(5);
    SB();
    {
      st128(rs, lane < 50 ? WS_PCTX + (j * 8 + w) * 800 + lane * 16 : WS_TOTAL, P4);
      const int pp = __builtin_amdgcn_readfirstlane(WS_PROBE + il((j * 8 + w) * 128, w & 1));
      u64 ta, tb, tc;
      u32 dm;
      asm volatile("s_load_dword %3, %4, 0x0\n s_waitcnt lgkmcnt(0)\n"
                   "s_memtime %0\n s_waitcnt lgkmcnt(0)\n s_load_dword %3, %4, %5 glc\n s_waitcnt lgkmcnt(0)\n"
                   "s_memtime %1\n s_waitcnt lgkmcnt(0)\n s_load_dword %3, %4, %6 glc\n s_waitcnt lgkmcnt(0)\n"
                   "s_memtime %2\n s_waitcnt vmcnt(0) lgkmcnt(0)"
                   : "=&s"(ta), "=&s"(tb), "=&s"(tc), "=&s"(dm) : "s"(ws), "s"(pp), "s"(pp ^ 8192) : "memory");
      int df = min(max(((int)(u32)(tb - ta) - (int)(u32)(tc - tb)) >> 3, -VCLIP), VCLIP);
      df = ((w ^ myh) & 1) ? -df : df;
      u32 old = 0u;
      if (lane == 0) old = __hip_atomic_fetch_add((LAS u32*)(lds + L_CTR) + C_VOTE, (u32)(65536 + 64 + df), __ATOMIC_RELAXED, __HIP_MEMORY_SCOPE_WORKGROUP);
      old = __builtin_amdgcn_readfirstlane(old) + 65536 + 64 + df;
      if (old >= 524288u && lane < 8) st64(rs, WS_VOTE + lane * 2048 + j * 8, (old & 1023u) | (tlo << 24), (tlo >> 8) | (thi << 24));
      STAMP(6);
    }
    {
      const int r0 = 128 * (mu & 3) + 32 * (w & 3) + 4 * (lane >> 3);
      const u32x2 cvb = *(const u32x2*)(cvec + r0);
      u32x4 wsc[4];
#pragma unroll
      for (int i = 0; i < 4; ++i) wsc[i] = ldnt(w_mod + (size_t)(r0 + i) * 3072 + 1024 + (mu >> 2) * 64 + (lane & 7) * 8);
      float a[8];
#pragma unroll
      for (int k = 0; k < 8; ++k) a[k] = 0.f;
#pragma unroll
      for (int i = 0; i < 4; ++i) fma8b(a, f2bf(silu_bf((i & 1) ? hi16(i < 2 ? cvb.x : cvb.y) : lo16(i < 2 ? cvb.x : cvb.y))), wsc[i]);
      float r[4];
#pragma unroll
      for (int k = 0; k < 4; ++k) r[k] = rs32(a[k], a[k + 4]);
      const float o0 = b3_sum(rs16(r[0], r[2])), o1 = b3_sum(rs16(r[1], r[3]));
      float* red = (float*)(lds + L_H1RED + 8192);
      if ((lane & 8) == 0) *(f32x2*)(red + w * 64 + (lane & 7) * 8 + (lane >> 5) * 4 + ((lane >> 4) & 1) * 2) = f32x2{o0, o1};
      BAR();
      if ((w & 3) == 3 && lane < 16) {
        red += (w >> 2) * 256 + lane * 4;
        const f32x4 sm = (*(const f32x4*)(red) + *(const f32x4*)(red + 64)) + (*(const f32x4*)(red + 128) + *(const f32x4*)(red + 192));
        st128(rs, WS_MODB + ((mu & 3) * 2048 + (mu >> 2) * 64 + lane * 4) * 4, u32x4{fbits(sm.x), fbits(sm.y), fbits(sm.z), fbits(sm.w)});
      }
    }
    gx = 0;
  }

  RELANE();
  const int r16 = lane >> 2, ch = lane & 3;
  if (ii >= 16) {
    int mo = lane < 24 ? (WS_LSE + il((24 * w + lane) * 512 + c4 * 128 + (h >> 1) * 8, h & 1)) ^ gx : WS_TOTAL;
    PIN(mo);
    lds_spin(ctr + C_PD, 4u);
    STAMP(23);
    u32x2 mt = ld64(rs, mo);
    SB();
    const u32 pb = (w >> 1) * PS + (w & 1) * 6144;
    u32 po[6], pa[16], ea[8];
    {
      u32 t_ = g4 * 256 + ((col ^ (g4 << 2)) << 4) + ((WS_PART + il21(((h >> 1) * 4 + c4) * 65536, h & 1)) ^ gx);
      PIN(t_);
#pragma unroll
      for (int k = 0; k < 6; ++k) { po[k] = (t_ ^ ((k & 3) << 4)) + il((6 * w + k) * 1024, 0); PIN(po[k]); }
    }
    bf16x8 wk[4];
    {
      u32 wa = tq * PS + 16384 + off_b(g4, 2 * w + (tp >> 1)) + 8 * (tp & 1);
      PIN(wa);
#pragma unroll
      for (int s = 0; s < 4; ++s) wk[s] = tr_frag(lds, (wa ^ (s << 4)) + s * 1024, (wa ^ (s << 4)) + s * 1024 + 4096);
    }
    pa[0] = pb + off_b(8 * (g4 == 3 ? 0 : g4) + tq, tp >> 1) + 8 * (tp & 1);
    PIN(pa[0]);
#pragma unroll
    for (int i = 1; i < 16; ++i) { pa[i] = pa[0] ^ (i << 4); PIN(pa[i]); }
    u32 bo = L_QOFF + col * 272 + g4 * 16, mlo = L_QOFF + 4352 + (col & 7) * 8, ro = L_QOFF + (w + (col & 8)) * 272 + (col & 7) * 32 + g4 * 8;
    int so1 = (WS_PCTX + h * 2048 + c4 * 512 + (w >> 2) * 256 + (w & 1) * 128 + (w & 2) * 16 + (g4 >> 1) * 64 + (g4 & 1) * 8 + (col < 2 ? col * 16 : WS_TOTAL)) ^ gx;
    u32 eo = L_QOFF + 4416 + w * 128 + lane * 2, mw_ = L_QOFF + 4352 + w * 8;
#pragma unroll
    for (int i = 0; i < 8; ++i) { ea[i] = L_QOFF + 4416 + w * 128 + ((col & 7) == i ? 16 * g4 : 64); PIN(ea[i]); }
    float pad = lane < 24 ? 0.f : -__builtin_inff();
    PIN(bo); PIN(mlo); PIN(ro); PIN(so1); PIN(eo); PIN(mw_); PIN(pad);
    SB();
    u32 done = 0u;
    _Pragma("unroll 1") for (u32 spins = 0; spins < SPIN_CAP; ++spins) {
      const u32 ok = ~(u32)__ballot(max(mt.x, mt.y) == POISON);
      u32 nw = ok & (ok >> 1);
      nw = nw & (nw >> 2) & 0x111111u & ~done;
      done |= nw;
#define T1F(k, b) "s_bitcmp0_b32 %[nw], " #b "\n s_cbranch_scc1 1f\n global_load_lds_dwordx4 %[p" #k "], %[ws] sc1\n 1: s_add_u32 m0, m0, 0x400\n"
      asm volatile("s_mov_b32 m0, %[m]\n" T1F(0, 0) T1F(1, 4) T1F(2, 8) T1F(3, 12) T1F(4, 16) T1F(5, 20)
                   ::[m] "s"(pb), [nw] "s"(nw), [ws] "s"(ws), [p0] "v"(po[0]), [p1] "v"(po[1]), [p2] "v"(po[2]), [p3] "v"(po[3]), [p4] "v"(po[4]), [p5] "v"(po[5]) : "memory", "scc");
      if (done == 0x111111u) break;
      __builtin_amdgcn_s_sleep(1);
      if (max(mt.x, mt.y) == POISON) mt = ld64(rs, mo);
      PIN(mt);
    }
    STAMP(16);
    {
      const float m = __uint_as_float(mt.x) + pad;
      float M = m;
#define MX(C) "s_nop 1\n v_max_f32_dpp %0, %0, %0 " C " row_mask:0xf bank_mask:0xf\n"
      asm(MX("quad_perm:[1,0,3,2]") MX("quad_perm:[2,3,0,1]") MX("row_ror:8") MX("row_ror:4") "s_nop 1" : "+v"(M));
      M = fmaxf(__uint_as_float(__builtin_amdgcn_readlane(__float_as_uint(M), 0)), __uint_as_float(__builtin_amdgcn_readlane(__float_as_uint(M), 16)));
      const float e = __builtin_amdgcn_exp2f(m - M);
      *(u16*)(lds + eo) = (u16)f2bf(e);
      WAVE_LDS_SYNC();
      bf16x8 wf[8], pt[8];
#pragma unroll
      for (int i = 0; i < 8; ++i) wf[i] = *(const bf16x8*)(lds + ea[i]);
      SB();
      const float L = x16_sum(row16_sum(e * __uint_as_float(mt.y)));
      if (lane == 0) *(f32x2*)(lds + mw_) = f32x2{M, L};
      SB();
      waitvm<0>();
      STAMP(17);
#pragma unroll
      for (int i = 0; i < 8; ++i) pt[i] = tr_frag(lds, pa[2 * i], pa[2 * i + 1] + 1024);
      SB();
      f32x4 d = {0.f, 0.f, 0.f, 0.f};
#pragma unroll
      for (int i = 0; i < 8; ++i) d = __builtin_amdgcn_mfma_f32_16x16x32_bf16(pt[i], wf[i], d, 0, 0, 0);
      const u32 h0 = pk2bf(d[0], d[1]), h1 = pk2bf(d[2], d[3]);
      const u32 l0 = pk2bf(d[0] - lo16(h0), d[1] - hi16(h0)), l1 = pk2bf(d[2] - lo16(h1), d[3] - hi16(h1));
      *(u32x2*)(lds + ro) = (col & 8) ? u32x2{l0, l1} : u32x2{h0, h1};
    }
    BAR();
    STAMP(14);
    const f32x2 ml = *(const f32x2*)(lds + mlo);
    bf16x8 lf[4];
#pragma unroll
    for (int s = 0; s < 4; ++s) lf[s] = *(const bf16x8*)(lds + bo + 64 * s);
    SB();
    f32x4 d = {0.f, 0.f, 0.f, 0.f};
#pragma unroll
    for (int s = 0; s < 4; ++s) d = __builtin_amdgcn_mfma_f32_16x16x32_bf16(wk[s], lf[s], d, 0, 0, 0);
    SB();
    float Mg = fmaxf(ml.x, dppf<0xB1>(ml.x));
    Mg = fmaxf(Mg, dppf<0x4E>(Mg)); Mg = fmaxf(Mg, dppf<0x141>(Mg));
    const float e = __builtin_amdgcn_exp2f(ml.x - Mg);
    float Lg = quad_sum(e * ml.y);
    Lg += dppf<0x141>(Lg);
    const float sc = e * __builtin_amdgcn_rcpf(Lg);
    {
      float d0 = d[0] * sc, d1 = d[1] * sc, d2 = d[2] * sc, d3 = d[3] * sc;
      row16_sum4(d0, d1, d2, d3);
      d = f32x4{d0, d1, d2, d3};
    }
    const u32 h0 = pk2bf(d[0], d[1]), h1 = pk2bf(d[2], d[3]);
    const u32 l0 = pk2bf(d[0] - lo16(h0), d[1] - hi16(h0)), l1 = pk2bf(d[2] - lo16(h1), d[3] - hi16(h1));
    st64(rs, so1, nopois(col ? l0 : h0), nopois(col ? l1 : h1));
    STAMP(15);
    STAMP_FLUSH();
    return;
  }
  if (tl2) {
    BAR0();
    if (w < 4) {
#pragma unroll
      for (int i = 0; i < 8; ++i) dma<true>(owno + i * 1024, (ks * 512 + 64 * w + 8 * i + (lane >> 3)) * 1024 + cg * 128 + (lane & 7) * 16, w_o);
      waitvm<0>();
    } else {
#pragma unroll
      for (int i = 0; i < 8; ++i) *(u32x4*)(lds + owno + i * 1024 + lane * 16) = wv[i];
      WAVE_LDS_SYNC();
    }
    STAMP(26);
    bf16x8 wf[8];
#pragma unroll
    for (int i = 0; i < 8; ++i) {
      const u32 a = owno + (32 * (i >> 2) + 8 * g4 + tq) * 128 + (i & 3) * 32 + tp * 8;
      wf[i] = tr_frag(lds, a, a + 512);
    }
#pragma unroll
    for (int i = 0; i < 8; ++i) PIN(wf[i]);
    STAMP(23);
    int off[1] = {(WS_PCTX + (ks * 4 + (w >> 1)) * 2048 + tq * 512 + (w & 1) * 256 + g4 * 64 + tp * 16) ^ gx};
    u32 rw = L_RED + lane * 32 + w * 4;
    PIN(rw);
    int so2 = tp < 2 ? WS_PATTN + il((ks * 512 + cg * 64 + (lane & 60)) * 4, tp) : WS_TOTAL;
    PIN(so2);
    u32x4 g[1];
    gather128<1, 4>(rs, off, g);
    STAMP(24);
    const bf16x8 a0 = __builtin_bit_cast(bf16x8, (col & 2) ? Z4 : g[0]), a1 = __builtin_bit_cast(bf16x8, (col & 2) ? g[0] : Z4);
    f32x4 d[4];
#pragma unroll
    for (int c = 0; c < 4; ++c) d[c] = __builtin_amdgcn_mfma_f32_16x16x32_bf16(a0, wf[c], f32x4{0.f, 0.f, 0.f, 0.f}, 0, 0, 0);
#pragma unroll
    for (int c = 0; c < 4; ++c) d[c] = __builtin_amdgcn_mfma_f32_16x16x32_bf16(a1, wf[4 + c], d[c], 0, 0, 0);
    *(float*)(lds + rw) = rs16(rs32(sum4(d[0]), sum4(d[2])), rs32(sum4(d[1]), sum4(d[3])));
    BAR();
    if (w == 0) {
      const float v = sum4(*(const f32x4*)(lds + L_RED + lane * 32) + *(const f32x4*)(lds + L_RED + lane * 32 + 16));
      const float vn = __uint_as_float(fbits(v));
      st128(rs, so2, u32x4{__float_as_uint(dppf<0x00>(vn)), __float_as_uint(dppf<0x55>(vn)), __float_as_uint(dppf<0xAA>(vn)), __float_as_uint(dppf<0xFF>(vn))});
    }
    STAMP(17);
  } else if (t3 >= 0) {
    STAMP(20);
    if (w < 4) {
      const int fr = ((128 * w + r16) * DF + t3 * 32) * 2, ca_ = ch ^ ((r16 >> 2) & 2) ^ (t3 & 1);
      const u16* wa = (t3 & 1) ? w_up : w_gate;
      const u16* wb = (t3 & 1) ? w_gate : w_up;
      const u32 d = w * 32768 + 8192;
#pragma unroll
      for (int i = 0; i < 8; ++i) { waitvm<WING - 1>(); dma<false>(d + (i >> 2) * 12288 + (t3 & 1) * 4096 + (i & 3) * 1024, fr + ca_ * 16 + 32 * i * DF, wa); }
#pragma unroll
      for (int i = 0; i < 8; ++i) { waitvm<WING - 1>(); dma<false>(d + (i >> 2) * 12288 + 4096 - (t3 & 1) * 4096 + (i & 3) * 1024, fr + (ca_ ^ 1) * 16 + 32 * i * DF, wb); }
    } else {
#pragma unroll
      for (int i = 0; i < 8; ++i) {
        waitvm<WING - 1>();
        dma<true>(lw * 32768 + 16384 + (i >> 2) * 12288 + (i & 3) * 1024, (32 * t3 + 8 * (i & 3) + (lane >> 3)) * 1024 + 256 * lw + 128 * (i >> 2) + (lane & 7) * 16, w_down);
      }
      const u32 x2 = *(const u32*)(x + 128 * lw + 2 * lane);
      u32x4 m6[6];
      int mo[6];
#pragma unroll
      for (int i = 0; i < 6; ++i) mo[i] = MB_OFF(i);
      gather128<6, 8>(rs, mo, m6);
#pragma unroll
      for (int i = 0; i < 6; ++i) {
        const f32x4 f = __builtin_bit_cast(f32x4, m6[i]);
        const float v = rbf(rs16(rs32(f.x, f.z), rs32(f.y, f.w)));
        *(u16*)(lds + L_Z + (i < 2 ? 5 : i < 4 ? 3 : 2) * 1088 + (128 * lw + 64 * (i & 1) + 4 * col + g4) * 2) = (u16)f2bf(i < 4 ? v : 1.f + v);
      }
      *(u32*)(lds + L_Z + 6 * 1088 + lw * 256 + lane * 4) = x2;
      if (w == 4 && lane < 32) *(u32*)(lds + L_ZERO + lane * 4) = lane < 16 ? 0u : POISON;
      if (w == 6) COUNT_VOTES(false);
    }
    waitvm<0>();
    STAMP(26);
    BAR();
    gx = __builtin_amdgcn_readfirstlane(ctr[C_PAR]);
    u32 zw = L_Z + (64 * w + 4 * col + g4) * 2;
    u32 hr[4];
#pragma unroll
    for (int i = 0; i < 4; ++i) { hr[i] = (col == i ? L_HH : L_ZERO) + 16 * g4; PIN(hr[i]); }
    int off[2];
#pragma unroll
    for (int i = 0; i < 2; ++i) { off[i] = (WS_PATTN + il(((4 * i + g4) * 512 + 64 * w + col * 4) * 4, myh)) ^ gx; PIN(off[i]); }
    int so = col < 4 ? (WS_PDOWN + il((t3 * 256 + 64 * (w & 3) + 16 * col + 4 * g4) * 4, w >> 2)) ^ gx : WS_TOTAL;
    u32 zr = L_Z + tp * 1088 + 16 * g4, yr = L_Z + 4 * 1088 + lane * 16, hw = L_HH + (w & 3) * 16 + (g4 & 1) * 8 + tp * 2, cr = L_CF + (w & 3) * 16 + tp * 4;
    PIN(cr); PIN(so); PIN(zw); PIN(zr); PIN(yr); PIN(hw);
    float ga = bf2f(*(const u16*)(lds + zw + 5 * 1088)), cb1 = bf2f(*(const u16*)(lds + zw + 2 * 1088)), xe = bf2f(*(const u16*)(lds + zw + 6 * 1088));
    bf16x8 wf[16], wd[4];
    if (w < 4) {
#pragma unroll
      for (int k = 0; k < 16; ++k) {
        const u32 a = (k >> 2) * 32768 + 8192 + ((k >> 1) & 1) * 12288 + (tp >> 1) * 4096 + ((k & 1) * 32 + 8 * g4 + tq) * 64 + (w ^ ((g4 & 1) * 2) ^ (tp >> 1)) * 16 + (tp & 1) * 8;
        wf[k] = tr_frag(lds, a, a + 256);
      }
#pragma unroll
      for (int k = 0; k < 16; ++k) PIN(wf[k]);
    }
#pragma unroll
    for (int i = 0; i < 4; ++i) {
      const u32 a = owno + 8192 + (8 * g4 + tq) * 128 + i * 32 + tp * 8;
      wd[i] = tr_frag(lds, a, a + 512);
    }
#pragma unroll
    for (int i = 0; i < 4; ++i) PIN(wd[i]);
    PIN(ga); PIN(cb1); PIN(xe);
    STAMP(23);
    until(t0, E6_T);
    u32x4 pa2[2];
    gather128<2, 4>(rs, off, pa2);
    STAMP(18);
    {
      const f32x4 s4 = __builtin_bit_cast(f32x4, pa2[0]) + __builtin_bit_cast(f32x4, pa2[1]);
      const u32 yb = f2bf(xe + rbf(ga * rbf(rs16(rs32(s4.x, s4.z), rs32(s4.y, s4.w)))));
      const float z = bf2f(yb) * cb1;
      const u32 zh = f2bf(z);
      *(u16*)(lds + zw + 4 * 1088) = (u16)yb;
      *(u16*)(lds + zw) = (u16)zh;
      *(u16*)(lds + zw + 1088) = (u16)f2bf(z - bf2f(zh));
    }
    BAR();
    if (w < 4) {
      bf16x8 zf[16];
#pragma unroll
      for (int k = 0; k < 16; ++k) zf[k] = *(const bf16x8*)(lds + zr + 64 * k);
      __builtin_amdgcn_sched_barrier(0);
      f32x4 d = {0.f, 0.f, 0.f, 0.f};
#pragma unroll
      for (int k = 0; k < 16; ++k) d = __builtin_amdgcn_mfma_f32_16x16x32_bf16(wf[k], zf[k], d, 0, 0, 0);
      PIN(d);
      u32 cfb;
      do { cfb = *(volatile LAS u32*)(lds + cr); } while (__any(cfb == POISON));
      d *= __uint_as_float(cfb);
      const float a0 = (tp & 1) ? d[1] : d[0], b0 = (tp & 1) ? d[0] : d[1], a1 = (tp & 1) ? d[3] : d[2], b1 = (tp & 1) ? d[2] : d[3];
      const float e0 = a0 + dppf<0xB1>(b0), e1 = a1 + dppf<0xB1>(b1);
      const float v = rbf(((tp & 2) ? e1 : e0) + dppf<0x4E>((tp & 2) ? e0 : e1));
      const auto sw = __builtin_amdgcn_permlane32_swap(__float_as_uint(v), __float_as_uint(v), false, false);
      const float g = __uint_as_float(sw[0]), u = __uint_as_float(sw[1]);
      const float e2 = __builtin_amdgcn_exp2f(g * (-2.3022081981f - 0.1029432396f * g * g));
      *(u16*)(lds + hw) = (u16)f2bf(rbf(g * __builtin_amdgcn_rcpf(e2 + 1.f)) * u);
    } else {
      float mb, rsd;
      ln_stats(*(const u32x4*)(lds + yr), mb, rsd);
      *(u32*)(lds + cr) = fbits(tp == 3 ? 1.f : rsd * (tp == 2 ? -mb : 1.f));
    }
    BAR();
    {
      bf16x8 hf[4];
#pragma unroll
      for (int i = 0; i < 4; ++i) hf[i] = *(const bf16x8*)(lds + hr[i]);
      __builtin_amdgcn_sched_barrier(0);
      f32x4 d = {0.f, 0.f, 0.f, 0.f};
#pragma unroll
      for (int i = 0; i < 4; ++i) d = __builtin_amdgcn_mfma_f32_16x16x32_bf16(wd[i], hf[i], d, 0, 0, 0);
      st128(rs, so, u32x4{fbits(d[0]), fbits(d[1]), fbits(d[2]), fbits(d[3])});
    }
    STAMP(19);
  }
  if (t4 >= 0 && lw == 0) {
#define RED8(f) b3_sum(rs16(rs32(f.x, f.z), rs32(f.y, f.w)))
    const int oc = t4 * 32 + (lane & 7) * 4 + g4;
    const u32 xb = x[oc];
    float x1, gb;
    {
      int off[2];
      off[0] = lane < 32 ? WS_MODB + ((lane >> 3) * 2048 + t4 * 32 + (lane & 7) * 4) * 4 : WS_TOTAL;
      off[1] = lane < 32 ? WS_MODB + ((lane >> 3) * 2048 + 1536 + t4 * 32 + (lane & 7) * 4) * 4 : WS_TOTAL;
      u32x4 mg[2];
      gather128<2>(rs, off, mg);
      const f32x4 fa = __builtin_bit_cast(f32x4, mg[0]), fb = __builtin_bit_cast(f32x4, mg[1]);
      const float ga = rbf(RED8(fa));
      gb = rbf(RED8(fb));
      int of1[1] = {(WS_PATTN + il(((lane >> 3) * 512 + t4 * 32 + (lane & 7) * 4) * 4, myh)) ^ gx};
      u32x4 g[1];
      gather128<1, 4>(rs, of1, g);
      const f32x4 gf = __builtin_bit_cast(f32x4, g[0]);
      x1 = rbf(bf2f(xb) + rbf(ga * rbf(RED8(gf))));
    }
    STAMP(20);
    if (t4 == 0 && lane == 0) st64(rs, WS_EPOCH, tlo, thi);
    int off[7];
#pragma unroll
    for (int i = 0; i < 7; ++i) { off[i] = (WS_PDOWN + il(((7 * (lane >> 3) + i) * 256 + (t4 & 7) * 32 + (lane & 7) * 4) * 4, myh)) ^ gx; PIN(off[i]); }
    const rsrc_t ro = __builtin_amdgcn_make_buffer_rsrc((void*)out, 0, 1024, 0x00020000);
    int oo = (lane & 8) ? 1024 : oc * 2;
    PIN(x1); PIN(gb); PIN(oo);
    u32x4 g[7];
    __builtin_amdgcn_s_sleep(T4_DELAY);
#define F4(i) __builtin_bit_cast(f32x4, g[i])
    float s;
#pragma unroll
    for (int i = 0; i < 7; ++i) g[i] = ld128(rs, off[i]);
    _Pragma("unroll 1") for (u32 spins = 0; spins < SPIN_CAP; ++spins) {
      asm volatile("s_waitcnt vmcnt(0)" : "+v"(g[0]), "+v"(g[1]), "+v"(g[2]), "+v"(g[3]), "+v"(g[4]), "+v"(g[5]), "+v"(g[6])::"memory");
      const f32x4 s4 = ((F4(0) + F4(1)) + (F4(2) + F4(3))) + ((F4(4) + F4(5)) + F4(6));
      s = RED8(s4);
      if (!__any(s != s)) break;
      u64 bad = 0ull;
#pragma unroll
      for (int i = 0; i < 7; ++i) {
        const u64 m = __ballot(pois4(g[i]));
        bad |= m;
        asm volatile("s_mov_b64 exec, %2\n buffer_load_dwordx4 %0, %1, %3, 0 offen sc1\n s_mov_b64 exec, -1" : "+v"(g[i]) : "v"(off[i]), "s"(m), "s"(rs) : "memory");
      }
      if (!bad) break;
    }
    STAMP(21);
    __builtin_amdgcn_raw_buffer_store_b16((u16)f2bf(x1 + rbf(gb * rbf(s))), ro, oo, 0, 0);
    STAMP(22);
  }
  STAMP_FLUSH();
}

extern "C" void kernel_launch(void* const* d_in, const int* in_sizes, int n_in, void* d_out, int out_size,
                              void* d_ws, size_t ws_size, hipStream_t stream) {
  k_main<<<NWG, 512, 0, stream>>>((const u16*)d_in[0], (const u16*)d_in[1], (const u16*)d_in[2], (const u16*)d_in[3],
                                  (const u16*)d_in[4], (const u16*)d_in[5], (const u16*)d_in[7], (const u16*)d_in[8],
                                  (const u16*)d_in[9], (const u16*)d_in[10], (const u16*)d_in[11], (u16*)d_out,
                                  (unsigned char*)d_ws);
}
```

```cpp
#include <hip/hip_runtime.h>
#include <hip/hip_bf16.h>
#include <stdint.h>

typedef unsigned short u16;
typedef unsigned int u32;
typedef unsigned long long u64;
typedef float f32x2 __attribute__((ext_vector_type(2)));
typedef float f32x4 __attribute__((ext_vector_type(4)));
typedef float f32x16 __attribute__((ext_vector_type(16)));
typedef u32 u32x2 __attribute__((ext_vector_type(2)));
typedef u32 u32x4 __attribute__((ext_vector_type(4)));
typedef short bf16x8 __attribute__((ext_vector_type(8)));
typedef short s16x4 __attribute__((ext_vector_type(4)));
#define LAS __attribute__((address_space(3)))
#define DI __device__ __forceinline__

constexpr int DF = 1792;
constexpr int NWG = 256;
constexpr int AUX_SC1 = 16;

constexpr int WS_EPOCH = 0;
constexpr int WS_SFLAG = 4096;
constexpr int WS_QABS = 32768;
constexpr int WS_PCTX = WS_QABS + 65536;
constexpr int WS_PATTN = WS_PCTX + 65536;
constexpr int WS_PDOWN = WS_PATTN + 32768;
constexpr int WS_LSE = WS_PDOWN + 114688;
constexpr int WS_MODB = WS_LSE + 196608;
constexpr int WS_SC = WS_MODB + 32768;
constexpr int WS_PROBE = WS_SC + 32768;
constexpr int WS_VOTE = WS_PROBE + 262144;
constexpr int WS_PART = 2097152;
constexpr int PART_BYTES = 32 * 8 * 256 * 128;
constexpr int WS_TOTAL = WS_PART + PART_BYTES;

constexpr int il(int o, int hf) { return (o >> 13) * 16384 + hf * 8192 + (o & 8191); }
constexpr int il21(int o, int hf) { return il(o, hf ^ ((o >> 20) & 1) ^ 1); }
constexpr u32 SPIN_CAP = 1u << 16;
#define WIN 7
#define WA0 5
#define QW 4
#define WING 3
#define QF_T 300
#define E1_T 250
#define SCP_T 215
#define E3_T 470
#define E6_T 1200
#define T4_DELAY 30
#define TAU 32.f
#define VCLIP 40
#define PF_LEN 33100
#define G_OF(v) ((v) > 32768.f ? 8192u : 0u)
#define VOTE_STAMP(v)
#define STAMP_DECL u32 pt1 = 0; const u32 pt0 = (u32)__builtin_amdgcn_s_memrealtime()
#define STAMP(i) do { if ((i) == 25 && (1)) pt1 = (u32)__builtin_amdgcn_s_memrealtime() - pt0; } while (0)
#define STAMP_FLUSH() do { const u32 dl_ = __builtin_amdgcn_readfirstlane(pt1) * 4 + 2000; while ((u32)__builtin_amdgcn_s_memrealtime() - pt0 < dl_) __builtin_amdgcn_s_sleep(4); } while (0)

DI float bf2f(u32 b) { return __uint_as_float(b << 16); }
DI float rbf(float f) { return (float)(__bf16)f; }
DI u32 f2bf(float f) { return (u32)__builtin_bit_cast(u16, (__bf16)f); }
typedef __bf16 bf16x2_t __attribute__((ext_vector_type(2)));
DI u32 pk2bf(float a, float b) { return __builtin_bit_cast(u32, __builtin_convertvector(f32x2{a, b}, bf16x2_t)); }
DI float lo16(u32 v) { return __uint_as_float(v << 16); }
DI float hi16(u32 v) { return __uint_as_float(v & 0xFFFF0000u); }
DI u32x4 ldnt(const u16* p) { return __builtin_nontemporal_load((const u32x4*)p); }

DI float silu_bf(float a) { return rbf(a * __builtin_amdgcn_rcpf(1.f + __expf(-a))); }

template <int CTRL> DI float dppf(float v) {
  return __int_as_float(__builtin_amdgcn_update_dpp(0, __float_as_int(v), CTRL, 0xF, 0xF, true));
}
DI float quad_sum(float v) { v += dppf<0xB1>(v); v += dppf<0x4E>(v); return v; }
DI float b23_sum(float v) { v += dppf<0x128>(v); v += dppf<0x124>(v); return v; }
DI float b3_sum(float v) { return v + dppf<0x128>(v); }
DI float x16_sum(float v) {
  const auto r = __builtin_amdgcn_permlane16_swap(__float_as_uint(v), __float_as_uint(v), false, false);
  return __uint_as_float(r[0]) + __uint_as_float(r[1]);
}
DI float x32_sum(float v) {
  const auto r = __builtin_amdgcn_permlane32_swap(__float_as_uint(v), __float_as_uint(v), false, false);
  return __uint_as_float(r[0]) + __uint_as_float(r[1]);
}
DI float rs32(float a, float b) {
  const auto r = __builtin_amdgcn_permlane32_swap(__float_as_uint(a), __float_as_uint(b), false, false);
  return __uint_as_float(r[0]) + __uint_as_float(r[1]);
}
DI float rs16(float a, float b) {
  const auto r = __builtin_amdgcn_permlane16_swap(__float_as_uint(a), __float_as_uint(b), false, false);
  return __uint_as_float(r[0]) + __uint_as_float(r[1]);
}
DI float row16_sum(float v) { return b23_sum(quad_sum(v)); }
DI float wave_sum(float v) { return x32_sum(x16_sum(row16_sum(v))); }
DI void row16_sum4(float& a, float& b, float& c, float& d) {
#define R4(C) "v_add_f32_dpp %0, %0, %0 " C " row_mask:0xf bank_mask:0xf\n v_add_f32_dpp %1, %1, %1 " C " row_mask:0xf bank_mask:0xf\n" \
              "v_add_f32_dpp %2, %2, %2 " C " row_mask:0xf bank_mask:0xf\n v_add_f32_dpp %3, %3, %3 " C " row_mask:0xf bank_mask:0xf\n"
  asm("s_nop 1\n" R4("quad_perm:[1,0,3,2]") R4("quad_perm:[2,3,0,1]") R4("row_ror:8") R4("row_ror:4") : "+v"(a), "+v"(b), "+v"(c), "+v"(d));
#undef R4
}
DI float row16_max(float v) {
  v = fmaxf(v, dppf<0xB1>(v)); v = fmaxf(v, dppf<0x4E>(v));
  v = fmaxf(v, dppf<0x128>(v)); v = fmaxf(v, dppf<0x124>(v));
  return v;
}
DI float x16_max(float v) {
  const auto r = __builtin_amdgcn_permlane16_swap(__float_as_uint(v), __float_as_uint(v), false, false);
  return fmaxf(__uint_as_float(r[0]), __uint_as_float(r[1]));
}
DI float x32_max(float v) {
  const auto r = __builtin_amdgcn_permlane32_swap(__float_as_uint(v), __float_as_uint(v), false, false);
  return fmaxf(__uint_as_float(r[0]), __uint_as_float(r[1]));
}
DI float wave_max(float v) { return x32_max(x16_max(row16_max(v))); }

DI float dot2(u32 a, u32 b, float c) {
  return __builtin_amdgcn_fdot2_f32_bf16(__builtin_bit_cast(bf16x2_t, a), __builtin_bit_cast(bf16x2_t, b), c, false);
}
DI void fma8b(float* acc, u32 sbits, u32x4 w) {
  const u32 sh = sbits << 16;
  acc[0] = dot2(w.x, sbits, acc[0]); acc[1] = dot2(w.x, sh, acc[1]);
  acc[2] = dot2(w.y, sbits, acc[2]); acc[3] = dot2(w.y, sh, acc[3]);
  acc[4] = dot2(w.z, sbits, acc[4]); acc[5] = dot2(w.z, sh, acc[5]);
  acc[6] = dot2(w.w, sbits, acc[6]); acc[7] = dot2(w.w, sh, acc[7]);
}

DI unsigned fsw(unsigned row) { return ((row & 3) << 2) | ((row >> 2) & 3); }
DI unsigned off_b(unsigned row, unsigned ch) { return 256u * row + 16u * (ch ^ fsw(row)); }
DI unsigned tr_addr(unsigned lane, unsigned c, unsigned ks, unsigned t) {
  const unsigned h = lane >> 5, blk = (lane >> 4) & 1, q = (lane & 15) >> 2, p = lane & 3;
  return off_b(16 * ks + 8 * h + 4 * t + q, 4 * c + 2 * blk + (p >> 1)) + 8 * (p & 1);
}

typedef __amdgpu_buffer_rsrc_t rsrc_t;
DI u32x4 ld128(rsrc_t rs, int off) { return __builtin_amdgcn_raw_buffer_load_b128(rs, off, 0, AUX_SC1); }
DI u32x2 ld64(rsrc_t rs, int off) { return __builtin_amdgcn_raw_buffer_load_b64(rs, off, 0, AUX_SC1); }
DI void st64(rsrc_t rs, int off, u32 lo, u32 hi) { u32x2 v; v.x = lo; v.y = hi; __builtin_amdgcn_raw_buffer_store_b64(v, rs, off, 0, AUX_SC1); }
DI void st128(rsrc_t rs, int off, u32x4 v) { __builtin_amdgcn_raw_buffer_store_b128(v, rs, off, 0, AUX_SC1); }
#define BAR() do { asm volatile("s_waitcnt lgkmcnt(0)" ::: "memory"); __builtin_amdgcn_s_barrier(); asm volatile("" ::: "memory"); } while (0)
#define BAR0() do { __builtin_amdgcn_sched_barrier(0); asm volatile("" ::: "memory"); __builtin_amdgcn_s_barrier(); asm volatile("" ::: "memory"); __builtin_amdgcn_sched_barrier(0); } while (0)
#define PIN(x) asm volatile("" : "+v"(x))
constexpr u32 POISON = 0xFFFFFFFFu;
DI bool pois4(u32x4 v) { return max(max(v.x, v.y), max(v.z, v.w)) == POISON; }
DI u32 nopois(u32 v) { return min(v, 0xFFFFFFFEu); }
DI u32 fbits(float v) { return nopois(__float_as_uint(v)); }

DI u32x2 mk_gran(float v, u32 tlo, u32 thi) {
  u32 b = __float_as_uint(v);
  if ((b & 0x7F800000u) != 0x7F800000u) b += 0x80u;
  u32x2 g; g.x = (b >> 8) | (tlo << 24); g.y = (tlo >> 8) | (thi << 24);
  return g;
}
DI void st_gran2(rsrc_t rs, int off, float v0, float v1, u32 tlo, u32 thi) {
  const u32x2 a = mk_gran(v0, tlo, thi), b = mk_gran(v1, tlo, thi);
  u32x4 v; v.x = a.x; v.y = a.y; v.z = b.x; v.w = b.y;
  st128(rs, off, v);
}
DI bool gran_ok(u32 lo, u32 hi, u32 tlo, u32 thi) { return (((lo >> 24) ^ (tlo & 0xFFu)) | (hi ^ ((tlo >> 8) | (thi << 24)))) == 0u; }
DI float gran_val(u32 lo) { return __uint_as_float(lo << 8); }

template <int N, int SLP = 1> DI void gather128(rsrc_t rs, const int (&off)[N], u32x4 (&v)[N]) {
#pragma unroll
  for (int i = 0; i < N; ++i) v[i] = ld128(rs, off[i]);
  _Pragma("unroll 1") for (u32 spins = 0; spins < SPIN_CAP; ++spins) {
    bool need[N], bad = false;
#pragma unroll
    for (int i = 0; i < N; ++i) { need[i] = pois4(v[i]); bad = bad || need[i]; }
    if (!__any(bad)) break;
    __builtin_amdgcn_s_sleep(SLP);
    asm volatile("" ::: "memory");
    __builtin_amdgcn_sched_barrier(0);
#pragma unroll
    for (int i = 0; i < N; ++i)
      if (need[i]) v[i] = ld128(rs, off[i]);
  }
}
template <int N> DI void waitvm() { asm volatile("s_waitcnt vmcnt(%0)" ::"n"(N) : "memory"); }
constexpr u32 LSPIN_CAP = 1u << 22;
DI void lds_wait(volatile LAS u32* p, u32 val) {
  _Pragma("unroll 1") for (u32 n = 0; *p < val && n < LSPIN_CAP; ++n) __builtin_amdgcn_s_sleep(1);
  asm volatile("" ::: "memory");
}
#define WAVE_LDS_SYNC() do { __builtin_amdgcn_fence(__ATOMIC_RELEASE, "wavefront"); __builtin_amdgcn_wave_barrier(); __builtin_amdgcn_fence(__ATOMIC_ACQUIRE, "wavefront"); } while (0)
#define SB() __builtin_amdgcn_sched_barrier(0)
#define RARE(c) __builtin_expect(!!(c), 0)

constexpr int PS = 28672;
constexpr int L_QI = 4 * PS;
constexpr int L_P = L_QI + 32768;
constexpr int L_REF = L_P + 11264;
constexpr int L_MX = L_REF + 1408;
constexpr int L_ALW = L_MX + 128;
constexpr int L_DUMP = L_ALW + 512;
constexpr int L_QOFF = 131072;
constexpr int L_H1RED = L_QOFF;
constexpr int L_RED = L_QOFF + 16384;
constexpr int L_Z = L_RED, L_HH = L_RED + 7 * 1088, L_ZERO = L_HH + 64, L_CF = L_ZERO + 64;
constexpr int L_MISC = L_QOFF + 30720;
constexpr int L_CTR = L_MISC;
constexpr int L_WMAX = L_MISC + 256;
constexpr int L_ML = L_MISC + 512;

#define Z4 (u32x4{0u, 0u, 0u, 0u})
DI float max5(f32x4 v, float e) { return fmaxf(fmaxf(fmaxf(fmaxf(v.x, v.y), v.z), v.w), e); }

template <bool NT> DI void dma(u32 m, u32 voff, const void* sb) {
  if (NT) asm volatile("s_mov_b32 m0, %0\n s_nop 0\n global_load_lds_dwordx4 %1, %2 nt" ::"s"(m), "v"(voff), "s"(sb) : "memory");
  else asm volatile("s_mov_b32 m0, %0\n s_nop 0\n global_load_lds_dwordx4 %1, %2" ::"s"(m), "v"(voff), "s"(sb) : "memory");
}
DI void dma_sc1(u32 m, u32 voff, const void* sb) {
  asm volatile("s_mov_b32 m0, %0\n s_nop 0\n global_load_lds_dwordx4 %1, %2 sc1" ::"s"(m), "v"(voff), "s"(sb) : "memory");
}
DI void lds_add(int idx) {
  SB();
  asm volatile("s_mov_b64 exec, 1\n ds_add_u32 %0, %1 offset:%2\n s_mov_b64 exec, -1" ::"v"((u32)L_CTR), "v"(1u), "n"(idx * 4) : "memory");
  SB();
}
DI u32 spin_at(u32 a, u32 val) {
  u32 t, c, m, cap;
  asm volatile("s_mov_b32 %3, %6\n 1: ds_read_b32 %0, %4\n s_waitcnt lgkmcnt(0)\n v_readfirstlane_b32 %1, %0\n s_and_b32 %2, %1, 255\n s_cmp_ge_u32 %2, %5\n s_cbranch_scc1 2f\n"
               "s_sub_u32 %3, %3, 1\n s_cbranch_scc0 1b\n 2:"
               : "=&v"(t), "=&s"(c), "=&s"(m), "=&s"(cap) : "v"(a), "s"(val), "n"(LSPIN_CAP) : "memory", "scc");
  return c;
}
DI void lds_spin(volatile LAS u32* p, u32 val) { spin_at((u32)(uintptr_t)p, val); }
enum { C_SOK = 0, C_BH = 4, C_PD = 12, C_H1 = 18, C_PAR = 19, C_VOTE = 20, C_FREE = 22, C_QRDY = 23, C_MXC = 25, C_SD = 26, C_GX = 27, C_ML = 28, C_MK = 32, C_P = 44 };

DI u32 kv_mark(int nm, int skip) { return L_CTR + 4 * C_MK + __builtin_amdgcn_readfirstlane(4 * nm + (nm >= skip ? 4 : 0)); }
#define KV_TS
#define KV_TSO
#define KV_TSA
#define KV_TSP
#define KV_TRY(l)                                                                                                        \
  "7: s_getreg_b32 %[h], hwreg(HW_REG_IB_STS)\n s_and_b32 %[h], %[h], 0xC0000F\n s_max_i32 %[h], %[h], %[x]\n s_sub_i32 %[h], %[h], %[x]\n s_cmp_gt_i32 %[h], %[k4]\n s_cbranch_scc1 " l "\n" \
  "s_waitcnt vmcnt(%[x4])\n s_cmp_eq_u32 %[k4], 4\n s_cbranch_scc1 8f\n s_waitcnt vmcnt(%[x])\n"                        \
  "8: v_subrev_u32 %[t], %[k4], %[ml]\n s_mov_b64 exec, 1\n ds_add_u32 %[t], %[one] offset:12\n s_mov_b64 exec, -1\n"   \
  "s_sub_i32 %[k4], %[k4], 4\n" KV_TS "s_branch 7b\n"
template <int X> DI void kv_try(int& k4, u32 ml, u32 one KV_TSA) {
  u32 t, h;
  asm volatile(KV_TRY("9f") "9:" : [t] "=&v"(t), [h] "=&s"(h), [k4] "+s"(k4) KV_TSO : [ml] "v"(ml), [one] "v"(one), [x] "n"(X), [x4] "n"(X + 4) : "memory", "scc");
}
template <int X> DI u32 spin_try(int idx, u32 val, int& k4, u32 ml, u32 one KV_TSA) {
  u32 t, h, c, cap;
  asm volatile("s_mov_b32 %[cap], %[n]\n 1: ds_read_b32 %[t], %[ml] offset:%[o]\n s_waitcnt lgkmcnt(0)\n v_readfirstlane_b32 %[c], %[t]\n s_and_b32 %[h], %[c], 255\n"
               "s_cmp_ge_u32 %[h], %[v]\n s_cbranch_scc1 2f\n" KV_TRY("9f") "9: s_sub_u32 %[cap], %[cap], 1\n s_cbranch_scc0 1b\n 2:"
               : [t] "=&v"(t), [h] "=&s"(h), [c] "=&s"(c), [cap] "=&s"(cap), [k4] "+s"(k4) KV_TSO
               : [ml] "v"(ml), [one] "v"(one), [o] "n"(4 * idx), [v] "n"(val), [n] "n"(LSPIN_CAP), [x] "n"(X), [x4] "n"(X + 4) : "memory", "scc");
  return c;
}
template <int W> DI void kv_load(const u16* ckv, u32 vl, int lw, u32 g0, u32 g1, u32 m, int n0, int n1, int& nm, int skip) {
#pragma unroll 1
  for (int n = n0; n < n1; ++n) {
#define KV_POST(k) ".if (%[wm] | 4) == " #k "\n s_cmp_gt_i32 %[nm], %[n2]\n s_cbranch_scc1 20f\n s_mov_b64 exec, 1\n ds_add_u32 %[ma], %[one]\n s_mov_b64 exec, -1\n s_add_i32 %[nm], %[nm], 1\n 20:\n .endif\n"
#define KV_W(o, k) "s_waitcnt vmcnt(%[wm])\n" KV_POST(k) "global_load_lds_dwordx4 %[vo], %[p] offset:" #o " nt\n"
#define KV_M "s_add_u32 m0, m0, %[ms]\n"
    asm volatile("s_mov_b32 m0, %[m]\n" KV_W(0, 4) KV_M KV_W(256, 5) KV_M KV_W(512, 6) KV_M KV_W(768, 7)
                 : [nm] "+s"(nm)
                 : [m] "s"(m), [vo] "v"(vl + g0 + ((lw + n) & 3) * 4096), [p] "s"(ckv), [ma] "v"(kv_mark(nm, skip)),
                   [one] "v"(1u), [n2] "s"(__builtin_amdgcn_readfirstlane(n - (W > 4 ? 2 : 1))), [wm] "n"(W - 1), [ms] "n"(PS - 256)
                 : "memory", "scc");
    g0 = g1;
    g1 += 192 * 16384;
    m += n + 1 == skip ? 8192 : 4096;
  }
}
DI void until(u32 t0, u32 dt) {
  while ((u32)__builtin_amdgcn_s_memrealtime() - t0 < dt) __builtin_amdgcn_s_sleep(2);
}
DI bf16x8 tr_frag(unsigned char* lds, u32 a0, u32 a1) {
  const s16x4 b0 = __builtin_amdgcn_ds_read_tr16_b64_v4i16((LAS s16x4*)(lds + a0));
  const s16x4 b1 = __builtin_amdgcn_ds_read_tr16_b64_v4i16((LAS s16x4*)(lds + a1));
  bf16x8 v;
  v[0] = b0[0]; v[1] = b0[1]; v[2] = b0[2]; v[3] = b0[3]; v[4] = b1[0]; v[5] = b1[1]; v[6] = b1[2]; v[7] = b1[3];
  return v;
}
DI float sum4(f32x4 v) { const f32x2 t = f32x2{v.x, v.y} + f32x2{v.z, v.w}; return t.x + t.y; }

DI void ln_stats(u32x4 yv, float& mb, float& rsd) {
  const float a1 = dot2(yv.x, 0x3F803F80u, dot2(yv.y, 0x3F803F80u, 0.f)) + dot2(yv.z, 0x3F803F80u, dot2(yv.w, 0x3F803F80u, 0.f));
  const float a2 = dot2(yv.x, yv.x, dot2(yv.y, yv.y, 0.f)) + dot2(yv.z, yv.z, dot2(yv.w, yv.w, 0.f));
  const float sr = x16_sum(row16_sum(rs32(a1, a2))) * (1.f / 512.f);
  const float s1 = __uint_as_float(__builtin_amdgcn_readlane(__float_as_uint(sr), 0)), s2 = __uint_as_float(__builtin_amdgcn_readlane(__float_as_uint(sr), 32));
  const float var = rbf(fmaxf(s2 - s1 * s1, 0.f));
  mb = rbf(s1);
  const float sd = rbf(__builtin_amdgcn_sqrtf(rbf(var + 1e-5f)));
  rsd = __builtin_amdgcn_rcpf(sd);
  rsd = rsd * (2.f - sd * rsd);
}

__global__ __launch_bounds__(512, 2) void k_main(const u16* __restrict__ x, const u16* __restrict__ ckv,
                                                 const u16* __restrict__ w_q, const u16* __restrict__ w_uk,
                                                 const u16* __restrict__ w_uv, const u16* __restrict__ w_o,
                                                 const u16* __restrict__ w_gate, const u16* __restrict__ w_up,
                                                 const u16* __restrict__ w_down, const u16* __restrict__ cvec,
                                                 const u16* __restrict__ w_mod, u16* __restrict__ out,
                                                 unsigned char* ws) {
  __shared__ __attribute__((aligned(16))) unsigned char lds[163840];
  const int w = __builtin_amdgcn_readfirstlane(threadIdx.x >> 6), j = blockIdx.x;
  int lane = threadIdx.x & 63;
  const int xc = j & 7, ii = j >> 3;
  u64 tk0_, tk_, dmy_;
  asm volatile("s_memrealtime %0\n s_load_dwordx2 %2, %3, 0x10\n s_waitcnt lgkmcnt(0)\n s_load_dwordx2 %2, %3, 0x60\n s_waitcnt lgkmcnt(0)\n s_memrealtime %1\n s_waitcnt lgkmcnt(0)" : "=&s"(tk0_), "=&s"(tk_), "=&s"(dmy_) : "s"(__builtin_amdgcn_kernarg_segment_ptr()) : "memory");
  STAMP_DECL;
  STAMP(0);
  u32x2 ev;
  u32 xm, vh;
  {
    u64 p, pc, pw;
    u32 a, b, c, d, f, t;
    asm volatile("s_lshr_b32 %[f], %[j], 6\n s_cmp_eq_u32 %[f], 1\n s_cselect_b32 %[a], 0x50, 16\n"
                 "s_load_dwordx2 %[p], %[ka], %[a]\n s_load_dwordx2 %[pw], %[ka], 0x60\n"
                 "s_lshr_b32 %[d], %[j], 3\n s_cmp_lg_u32 %[w], 7\n s_cbranch_scc1 2f\n"
                 "s_and_b32 %[a], %[d], 15\n s_lshl_b32 %[a], %[a], 12\n v_lshl_add_u32 %[t], %[lane], 6, %[a]\n v_min_u32 %[t], %[pfl], %[t]\n"
                 "s_getpc_b64 %[pc]\n s_mov_b32 m0, %[dump]\n s_nop 0\n global_load_lds_dword %[t], %[pc]\n"
                 "2: s_load_dwordx2 %[pc], %[ka], 0x48\n s_and_b32 %[c], %[j], 7\n s_lshl_b32 %[c], %[c], 10\n s_cmp_eq_u32 %[f], 0\n s_cbranch_scc0 3f\n"
                 "s_and_b32 %[a], %[d], 1\n s_lshl1_add_u32 %[a], %[w], %[a]\n s_lshl_b32 %[a], %[a], 18\n s_add_u32 %[c], %[c], %[a]\n"
                 "s_lshr_b32 %[a], %[d], 1\n s_lshl_b32 %[a], %[a], 8\n s_add_u32 %[c], %[c], %[a]\n s_lshl_b32 m0, %[w], 14\n"
                 "v_lshrrev_b32 %[t], 4, %[lane]\n v_mul_u32_u24 %[t], 0x204, %[t]\n v_and_b32 %[vh], 15, %[lane]\n v_xor_b32 %[vh], %[vh], %[t]\n"
                 "v_lshl_add_u32 %[vh], %[vh], 4, %[c]\n v_xor_b32 %[t], 0x8010, %[vh]\n s_waitcnt lgkmcnt(0)\n"
                 "global_load_lds_dwordx4 %[vh], %[p]\n s_add_u32 m0, m0, 0x400\n s_nop 0\n"
                 "global_load_lds_dwordx4 %[t], %[p]\n s_add_u32 m0, m0, 0x400\n v_xor_b32 %[t], 0x10020, %[vh]\n"
                 "global_load_lds_dwordx4 %[t], %[p]\n s_add_u32 m0, m0, 0x400\n v_xor_b32 %[t], 0x18030, %[vh]\n"
                 "global_load_lds_dwordx4 %[t], %[p]\n s_branch 1f\n"
                 "3: s_lshl2_add_u32 %[d], %[d], %[w]\n s_sub_u32 %[d], %[d], 68\n s_lshl_b32 %[d], %[d], 16\n s_add_u32 %[d], %[d], %[c]\n"
                 "s_and_b32 %[a], %[j], 3\n s_lshl2_add_u32 %[b], %[a], %[w]\n s_mul_i32 %[b], %[b], 0x30000\n s_lshl_b32 %[a], %[a], 8\n"
                 "v_lshl_add_u32 %[xm], %[lane], 2, %[a]\n"
                 "s_and_b32 %[a], %[j], 60\n s_lshl_b32 %[a], %[a], 5\n s_add_u32 %[b], %[b], %[a]\n"
                 "s_lshl_b32 %[c], %[w], 12\n s_add_u32 %[c], %[c], %[mq]\n s_lshl_b32 %[a], %[w], 10\n s_add_u32 %[a], %[a], %[mh]\n"
                 "s_cmp_eq_u32 %[f], 1\n s_cselect_b32 %[d], %[b], %[d]\n s_cselect_b32 m0, %[c], %[a]\n s_cselect_b32 %[a], 48, 1\n s_cselect_b32 %[c], 0, 4\n"
                 "v_lshrrev_b32 %[t], 3, %[lane]\n v_mul_u32_u24 %[t], %[a], %[t]\n v_and_b32 %[vh], 7, %[lane]\n v_lshl_add_u32 %[vh], %[t], 3, %[vh]\n"
                 "v_lshl_add_u32 %[vh], %[vh], 4, %[d]\n"
                 "s_cselect_b32 %[b], 6, 1\n s_cselect_b32 %[a], 0x400, 0\n s_lshl_b32 %[b], %[b], 13\n"
                 "s_xor_b32 %[c], %[c], %[w]\n s_bitcmp1_b32 %[c], 2\n v_mov_b32 %[t], 0\n"
                 "s_waitcnt lgkmcnt(0)\n s_cbranch_scc1 1f\n s_cmp_eq_u32 %[f], 1\n s_cbranch_scc0 4f\n"
                 "global_load_dword %[xm], %[xm], %[pc]\n global_load_dwordx2 %[ev], %[t], %[pw] sc1\n"
                 "4: global_load_lds_dwordx4 %[vh], %[p]\n s_add_u32 m0, m0, %[a]\n v_add_u32 %[t], %[b], %[vh]\n"
                 "global_load_lds_dwordx4 %[t], %[p]\n s_add_u32 m0, m0, %[a]\n v_add_u32 %[t], %[b], %[t]\n"
                 "global_load_lds_dwordx4 %[t], %[p]\n s_add_u32 m0, m0, %[a]\n v_add_u32 %[t], %[b], %[t]\n"
                 "global_load_lds_dwordx4 %[t], %[p]\n"
                 "1:\n"
                 : [xm] "=&v"(xm), [ev] "=&v"(ev), [vh] "=&v"(vh), [t] "=&v"(t), [p] "=&s"(p), [pc] "=&s"(pc), [pw] "=&s"(pw), [a] "=&s"(a), [b] "=&s"(b), [c] "=&s"(c),
                   [d] "=&s"(d), [f] "=&s"(f)
                 : [j] "s"(j), [w] "s"(w), [lane] "v"(lane), [ka] "s"(__builtin_amdgcn_kernarg_segment_ptr()), [pfl] "n"(PF_LEN), [dump] "n"(L_DUMP),
                   [mq] "n"(L_QOFF + 8192), [mh] "n"(L_P)
                 : "memory", "scc");
  }
  SB();
  const u32 t0 = (u32)__builtin_amdgcn_s_memrealtime() - 35u;
  const bool isQ = j < 64, isS0 = j >= 64 && j < 128;
  const int hq = xc * 4 + (ii >> 1), dh = ii & 1, sj = j - 64;
  const int lw = w - 4, myh = xc >> 2;
  int col = lane & 15, g4 = lane >> 4, tq = col >> 2, tp = lane & 3;
  const rsrc_t rs = __builtin_amdgcn_make_buffer_rsrc((void*)ws, 0, WS_TOTAL, 0x00020000);
  volatile LAS u32* ctr = (volatile LAS u32*)(lds + L_CTR);
  SB();
  pt1 = (u32)__builtin_amdgcn_s_memrealtime() - (u32)tk_;
  if (w < 2) *(u32*)(lds + (w == 0 ? L_CTR + lane * 4 : L_MX + (lane & 31) * 4)) = w == 0 ? 0u : 0xFF800000u;
  BAR();
  const bool tl2 = isS0;
  const int t3 = xc < 4 ? (ii < 8 ? xc * 8 + ii : -1) : (ii < 6 ? 8 + xc * 6 + ii : -1);
  const int t4 = (ii >= 8 && ii < 10) ? xc * 2 + (ii - 8) : -1;
  const u32 owno = (w >> 1) * 32768 + 8192 + (w & 1) * 12288;
  const int ks = (xc & 3) * 2 + myh, cg = ii & 7;
  const int h = (j >> 2) & 31, c4 = j & 3;
  u32x4 wv[8];
  u32 tlo, thi;
  int gx;
#define RELANE() do { asm volatile("v_mbcnt_lo_u32_b32 %0, -1, 0\n v_mbcnt_hi_u32_b32 %0, -1, %0" : "=v"(lane)); col = lane & 15; g4 = lane >> 4; tq = col >> 2; tp = lane & 3; } while (0)
#define TAG_OF(e0, e1)                                                                                                  \
  do {                                                                                                                  \
    const u64 tag = (((u64)(e1) << 32) | (e0)) + 1ull;                                                                  \
    tlo = __builtin_amdgcn_readfirstlane((u32)tag); thi = __builtin_amdgcn_readfirstlane((u32)(tag >> 32));             \
  } while (0)
  const u32x4 P4 = {POISON, POISON, POISON, POISON};
#define COUNT_VOTES(PRE)                                                                                                \
  do {                                                                                                                  \
    u32x2 a_ = {0u, 0u};                                                                                                \
    if (PRE) a_ = *(const u32x2*)(lds + L_QI + lane * 8);                                                               \
    _Pragma("unroll 1") for (u32 spins = 0; spins < SPIN_CAP; ++spins) {                                                \
      if (((PRE) || spins) && __all(gran_ok(a_.x, a_.y, tlo, thi))) break;                                              \
      if (spins) __builtin_amdgcn_s_sleep(8);                                                                           \
      asm volatile("" ::: "memory");                                                                                    \
      a_ = ld64(rs, WS_VOTE + xc * 2048 + lane * 8);                                                                    \
    }                                                                                                                   \
    const float vt_ = wave_sum((float)(a_.x & 1023u));                                                                  \
    if (lane == 0) ctr[C_PAR] = G_OF(vt_);                                                                              \
    VOTE_STAMP(vt_);                                                                                                    \
    STAMP(8);                                                                                                           \
  } while (0)
#define S_POLL(a_)                                                                                                      \
  _Pragma("unroll 1") for (u32 spins = 0; spins < SPIN_CAP && RARE(__any(((a_.x ^ tlo) | (a_.y ^ thi)) != 0u)); ++spins) { \
    __builtin_amdgcn_s_sleep(8);                                                                                        \
    asm volatile("" ::: "memory");                                                                                      \
    a_ = ld64(rs, WS_SFLAG + xc * 2048 + lane * 8);                                                                     \
  }
#define MB_OFF(i) (WS_MODB + ((lane >> 4) * 2048 + ((i) >> 1) * 512 + 128 * lw + 64 * ((i) & 1) + (lane & 15) * 4) * 4)

  if (!isQ && w >= 4) {
    if (isS0) {
      if (w == 4) {
        const u32x2 e4 = ld64(rs, WS_EPOCH);
        st128(rs, WS_QABS + sj * 1024 + lane * 16, P4);
        st128(rs, lane < 32 ? WS_MODB + sj * 512 + lane * 16 : WS_TOTAL, P4);
        st128(rs, WS_PROBE + il(sj * 1024 + lane * 16, 0), Z4);
        st128(rs, WS_PROBE + il(sj * 1024 + lane * 16, 1), Z4);
        waitvm<0>();
        TAG_OF(e4.x, e4.y);
        if (lane < 8) st64(rs, WS_SFLAG + lane * 2048 + sj * 8, tlo, thi);
        STAMP(27);
      }
      lds_wait(ctr + C_H1, 4u);
    } else {
#pragma unroll
      for (int i = 4; i < 16; ++i) { waitvm<7>(); dma<false>(L_P + w * 1024, vh + (i & 7) * 8192, i < 8 ? w_q : w_uk); }
    }
    const u32 vl = g4 * 1024 + ((col ^ ((g4 << 2) | lw)) << 4);
    int nm = 0, ns = isS0 ? 6 : 7;
    const int skip = isS0 ? 4 : 99;
    if (isS0) kv_load<WA0>(ckv, vl, lw, sj * 16384, (sj + 192) * 16384, lw * 1024, 0, 2, nm, skip);
    else kv_load<WIN>(ckv, vl, lw, (1792 + j) * 16384, sj * 16384, lw * 1024, 0, 2, nm, skip);
    const rsrc_t rw = __builtin_amdgcn_make_buffer_rsrc((void*)(isS0 ? w_o : w_uv), 0, 4194304, 0x00020000);
    int wo = isS0 ? (ks * 512 + 64 * w + (lane >> 3)) * 1024 + cg * 128 + (lane & 7) * 16 : (c4 * 128 + 32 * lw + g4) * 8192 + h * 256 + col * 16;
    const int wst = isS0 ? 8192 : 32768;
    PIN(wo);
#define WV_LD(i) do { SB(); wv[i] = __builtin_amdgcn_raw_buffer_load_b128(rw, wo, (i) * wst, 2); wv[(i) + 1] = __builtin_amdgcn_raw_buffer_load_b128(rw, wo, ((i) + 1) * wst, 2); SB(); } while (0)
    u32 va[8];
#pragma unroll
    for (int i = 0; i < 8; ++i) { va[i] = lw * PS + tr_addr(lane, i >> 1, 0, i & 1); PIN(va[i]); }
    int eg[8];
#pragma unroll
    for (int u = 0; u < 8; ++u) { eg[u] = WS_PART + il21(((4 * (u >> 1) + 2 * (g4 >> 1) + (u & 1)) * 4 + lw) * 65536 + sj * 256 + col * 16, g4 & 1); PIN(eg[u]); }
    u32 pr_ = L_P + lane * 16;
    PIN(pr_);
    u32 ml = L_CTR + 4 * (C_MK + 7), one = 1u;
    PIN(ml); PIN(one);
    bf16x8 vf[4][4];
#define V_READ(i, slot) do { asm volatile("" ::: "memory"); SB(); _Pragma("unroll") for (int c = 0; c < 4; ++c) vf[i][c] = tr_frag(lds, va[2 * c] + (slot) * 4096, va[2 * c + 1] + (slot) * 4096); SB(); } while (0)
    SB();
    kv_load<WIN>(ckv, vl, lw, (sj + (isS0 ? 384 : 192)) * 16384, (sj + (isS0 ? 576 : 384)) * 16384, lw * 1024 + 8192, 2, ns, nm, skip);
    lds_wait(ctr + C_MK + 3, 4u);
    V_READ(0, 0); V_READ(1, 1); V_READ(2, 2); V_READ(3, 3);
    lds_add(C_FREE);
#pragma unroll
    for (int i = 0; i < 16; ++i) PIN(vf[i >> 2][i & 3]);
    STAMP(2);
    lds_wait(ctr + C_FREE, 8u);
    STAMP(5);
    kv_load<WIN>(ckv, vl, lw, (sj + 6 * 192) * 16384, (sj + 7 * 192) * 16384, lw * 1024, ns, ns + 4, nm, skip);
    int k4 = 4;
    STAMP(3);
    f32x16 acc[4];
#pragma unroll
    for (int c = 0; c < 4; ++c)
#pragma unroll
      for (int i = 0; i < 16; ++i) acc[c][i] = 0.f;
    bf16x8 pf[4], pn[4];
    u32 c_;
#define P_SPIN(g, n) c_ = spin_try<(g) ? 8 : 0>(C_P + (g) - C_MK - 7, n, k4, ml, one KV_TSP)
#define TRY() kv_try<8>(k4, ml, one KV_TSP)
#define P_READ(d, i0, i1, T0) _Pragma("unroll") for (int i = (i0); i < (i1); ++i) d[i] = *(volatile LAS bf16x8*)(lds + pr_ + ((T0) + i) * 1024)
#define PV(p, i) do { SB(); _Pragma("unroll") for (int c = 0; c < 4; ++c) acc[c] = __builtin_amdgcn_mfma_f32_32x32x16_bf16(p[i], vf[i][c], acc[c], 0, 0, 0); SB(); } while (0)
#define GX_USE(v) do { gx = __builtin_amdgcn_readfirstlane(v); _Pragma("unroll") for (int u = 0; u < 8; ++u) { eg[u] ^= gx; PIN(eg[u]); } } while (0)
#define MK_WAIT(T) spin_try<8>((T) - 7, 4u, k4, ml, one KV_TSP)
#define PVV(p, i, d, slot)                                                                                               \
  _Pragma("unroll") for (int c = 0; c < 4; ++c) {                                                                        \
    SB();                                                                                                                \
    acc[c] = __builtin_amdgcn_mfma_f32_32x32x16_bf16(p[i], vf[i][c], acc[c], 0, 0, 0);                                   \
    SB();                                                                                                                \
    vf[d][c] = tr_frag(lds, va[2 * c] + (slot) * 4096, va[2 * c + 1] + (slot) * 4096);                                   \
    SB();                                                                                                                \
  }
#define CT(i) (*(volatile LAS u32*)(lds + ml + 4 * ((i) - C_MK - 7)))
    P_SPIN(0, 4u);
    P_READ(pf, 0, 4, 0);
    const u32 k7 = CT(C_MK + 7);
    asm volatile("" ::: "memory");
    STAMP(6);
    PVV(pf, 0, 0, 4); kv_try<0>(k4, ml, one KV_TSP); WV_LD(0);
    PVV(pf, 1, 1, 5); kv_try<2>(k4, ml, one KV_TSP); WV_LD(2);
    PVV(pf, 2, 2, 6); WV_LD(4);
    u32 cv = CT(C_P + 1);
    P_READ(pn, 0, 4, 4);
    PVV(pf, 3, 3, 0); WV_LD(6);
    if (RARE(__builtin_amdgcn_readfirstlane(k7) < 4u)) { MK_WAIT(7); V_READ(0, 4); V_READ(1, 5); V_READ(2, 6); V_READ(3, 0); }
    STAMP(9);
    c_ = __builtin_amdgcn_readfirstlane(cv);
    if (RARE((c_ & 255u) != 4u)) { P_SPIN(1, 4u); P_READ(pn, 0, 4, 4); }
    int slow = -1;
    if (RARE(c_ > 255u)) slow = 4;
    else {
      if (!isS0) PV(pn, 0);
      TRY();
      const u32 k9 = CT(C_MK + 9);
      asm volatile("" ::: "memory");
      PVV(pn, 1, 0, 1); TRY();
      PVV(pn, 2, 1, 2);
      cv = CT(C_P + 2);
      P_READ(pf, 0, 2, 8);
      const u32 k10 = CT(C_MK + 10);
      asm volatile("" ::: "memory");
      PVV(pn, 3, 2, 3);
      if (RARE(__builtin_amdgcn_readfirstlane(k9) < 4u)) { MK_WAIT(9); V_READ(0, 1); V_READ(1, 2); }
      STAMP(10);
      c_ = __builtin_amdgcn_readfirstlane(cv);
      if (RARE((c_ & 255u) != 4u)) { P_SPIN(2, 4u); P_READ(pf, 0, 2, 8); }
      if (RARE(c_ > 255u)) slow = 8;
      else {
        TRY();
        PV(pf, 0);
        const u32 g0 = ctr[C_GX];
        u32 g1 = ctr[C_PAR];
        cv = CT(C_P + 3);
        P_READ(pf, 2, 3, 8);
        PV(pf, 1);
        STAMP(4);
        if (RARE(__builtin_amdgcn_readfirstlane(k10) < 4u)) { waitvm<8>(); TRY(); lds_spin(ctr + C_MK + 10, 4u); V_READ(2, 3); }
        if (RARE(__builtin_amdgcn_readfirstlane(g0) == 0u)) { lds_spin(ctr + C_GX, 1u); g1 = ctr[C_PAR]; }
        GX_USE(g1);
        STAMP(11);
        c_ = __builtin_amdgcn_readfirstlane(cv);
        _Pragma("unroll 1") for (u32 n_ = 0; RARE((c_ & 255u) != 2u) && n_ < LSPIN_CAP; ++n_) { cv = CT(C_P + 3); P_READ(pf, 2, 3, 8); c_ = __builtin_amdgcn_readfirstlane(cv); }
        if (RARE(c_ > 255u)) slow = 10;
        else PV(pf, 2);
      }
    }
    if (RARE(slow >= 0)) {
      MK_WAIT(10);
      float R = ceilf(*(const float*)(lds + L_MX + (lane & 31) * 4));
#pragma unroll 1
      for (int T = slow; T < 11; ++T) {
        if (T == 4 && isS0) continue;
        spin_at(L_CTR + 4 * (C_P + (T < 8 ? 1 : T < 10 ? 2 : 3)), T < 10 ? 4u : 2u);
        const float rf = *(const float*)(lds + L_REF + T * 128 + (lane & 31) * 4);
        u32x4 p = *(const u32x4*)(lds + pr_ + T * 1024);
        const u32 so_ = (T < 7 ? T : T - 7) * 4096;
#pragma unroll
        for (int c = 0; c < 4; ++c) vf[0][c] = tr_frag(lds, va[2 * c] + so_, va[2 * c + 1] + so_);
        const float Rn = fmaxf(R, rf), ps = __builtin_amdgcn_exp2f(rf - Rn);
        *(float*)(lds + L_ALW + lw * 128 + (lane & 31) * 4) = __builtin_amdgcn_exp2f(R - Rn);
        R = Rn;
        WAVE_LDS_SYNC();
#pragma unroll
        for (int i = 0; i < 4; ++i) {
          const f32x4 al = *(const f32x4*)(lds + L_ALW + lw * 128 + (lane >> 5) * 16 + i * 32);
#pragma unroll
          for (int c = 0; c < 4; ++c)
#pragma unroll
            for (int r = 0; r < 4; ++r) acc[c][4 * i + r] *= al[r];
          p[i] = pk2bf(lo16(p[i]) * ps, hi16(p[i]) * ps);
        }
        WAVE_LDS_SYNC();
        pf[0] = __builtin_bit_cast(bf16x8, p);
        PV(pf, 0);
      }
      if (slow != 10) { lds_spin(ctr + C_GX, 1u); GX_USE(ctr[C_PAR]); }
    }
    STAMP(30);
#pragma unroll
    for (int u = 0; u < 8; u += 2) {
      u32 c[8];
#pragma unroll
      for (int k = 0; k < 8; ++k) c[k] = pk2bf(acc[k & 2][2 * u + (k & 1) + (k >> 2) * 2], acc[(k & 2) + 1][2 * u + (k & 1) + (k >> 2) * 2]);
      SB();
#pragma unroll
      for (int k = 0; k < 2; ++k) {
        const auto r0 = __builtin_amdgcn_permlane16_swap(c[4 * k], c[4 * k + 1], false, false);
        const auto r1 = __builtin_amdgcn_permlane16_swap(c[4 * k + 2], c[4 * k + 3], false, false);
        st128(rs, eg[u + k], u32x4{r0[0], r1[0], r0[1], r1[1]});
      }
      SB();
    }
    STAMP(31);
    if (!isS0) {
#pragma unroll
      for (int i = 0; i < 8; ++i) *(u32x4*)(lds + lw * PS + 16384 + off_b(4 * i + g4, col)) = wv[i];
    }
    {
      int fo = lane < 16 ? (WS_LSE + il(sj * 512 + lw * 128 + (lane & 7) * 16, lane >> 3)) ^ gx : WS_TOTAL;
      lds_spin(ctr + C_ML, 1u);
      u32x4 ml4 = *(const u32x4*)(lds + L_WMAX + col * 16);
      PIN(fo); PIN(ml4);
      waitvm<0>();
      __builtin_amdgcn_s_waitcnt(0x0F70);
      st128(rs, fo, ml4);
      lds_add(C_PD);
    }
    STAMP(13);
  } else if (!isQ) {
    if (isS0) {
      u32 ta = L_QOFF + 8192 + (8 * g4 + tq) * 128 + w * 32 + tp * 8;
      u32 sa = L_H1RED + w * 256;
      int so = col < 2 ? WS_SC + (((sj >> 5) * 4 + (sj & 3)) * 512 + ((sj >> 2) & 7) * 64 + 16 * w + 4 * g4 + 2 * col) * 8 : WS_TOTAL;
      PIN(ta); PIN(so); PIN(sa);
      u32 c2, e0, e1;
      asm volatile("s_waitcnt vmcnt(4) ; HEAD_WAIT\n v_mov_b32 %0, %3\n v_mov_b32 %1, %4\n v_mov_b32 %2, %5" : "=&v"(c2), "=&v"(e0), "=&v"(e1) : "v"(xm), "v"(ev.x), "v"(ev.y) : "memory");
      TAG_OF(e0, e1);
      bf16x8 sf[4];
      u32x2 wt[8];
      asm volatile("ds_write_b32 %4, %5\n"
                   "ds_read_b128 %0, %6\n ds_read_b128 %1, %6 offset:64\n ds_read_b128 %2, %6 offset:128\n ds_read_b128 %3, %6 offset:192\n s_waitcnt lgkmcnt(0)"
                   : "=&v"(sf[0]), "=&v"(sf[1]), "=&v"(sf[2]), "=&v"(sf[3])
                   : "v"(sa + lane * 4), "v"(pk2bf(silu_bf(lo16(c2)), silu_bf(hi16(c2)))), "v"(sa + 16 * g4) : "memory");
      waitvm<0>();
      lds_add(C_BH);
      lds_spin(ctr + C_BH, 4u);
      STAMP(1);
      asm volatile("ds_read_b64_tr_b16 %0, %8\n ds_read_b64_tr_b16 %1, %8 offset:512\n ds_read_b64_tr_b16 %2, %8 offset:4096\n ds_read_b64_tr_b16 %3, %8 offset:4608\n"
                   "ds_read_b64_tr_b16 %4, %8 offset:8192\n ds_read_b64_tr_b16 %5, %8 offset:8704\n ds_read_b64_tr_b16 %6, %8 offset:12288\n ds_read_b64_tr_b16 %7, %8 offset:12800\n"
                   "s_waitcnt lgkmcnt(0)"
                   : "=&v"(wt[0]), "=&v"(wt[1]), "=&v"(wt[2]), "=&v"(wt[3]), "=&v"(wt[4]), "=&v"(wt[5]), "=&v"(wt[6]), "=&v"(wt[7]) : "v"(ta) : "memory");
      f32x4 d = {0.f, 0.f, 0.f, 0.f};
#pragma unroll
      for (int k = 0; k < 4; ++k)
        d = __builtin_amdgcn_mfma_f32_16x16x32_bf16(__builtin_bit_cast(bf16x8, u32x4{wt[2 * k].x, wt[2 * k].y, wt[2 * k + 1].x, wt[2 * k + 1].y}), sf[k], d, 0, 0, 0);
      st_gran2(rs, so, (col & 1) ? d[2] : d[0], (col & 1) ? d[3] : d[1], tlo, thi);
      lds_add(C_H1);
      STAMP(2);
      until(t0, SCP_T);
      dma_sc1(L_P + w * 1024, WS_SC + ((ii & 7) * 4 + w) * 1024 + lane * 16, ws);
    }
    u32 ab = off_b(col, 4 * g4), qv = WS_QABS + myh * 8192 + g4 * 1024 + w * 256 + ((col ^ (g4 << 2)) << 4);
    const int hb = 16 * (w & 1) + col;
    u32 pw = L_P + (g4 >> 1) * 512 + hb * 16 + (g4 & 1) * 8, mw = hb * 4;
    PIN(ab); PIN(qv); PIN(pw); PIN(mw);
    if (w == 2) {
      until(t0, QF_T);
      const u32x2 e2 = ld64(rs, WS_EPOCH);
      u32x2 f2 = ld64(rs, WS_SFLAG + xc * 2048 + lane * 8);
      TAG_OF(e2.x, e2.y);
      S_POLL(f2);
      lds_add(C_SOK);
      STAMP(3);
    }
    bf16x8 af[16], qf[16][2];
    u32 aa[16], qa[32];
#pragma unroll
    for (int i = 0; i < 16; ++i) {
      aa[i] = (ab ^ ((i & 3) << 4)) + (((w + (i >> 2)) & 3) * PS + w * 4096); PIN(aa[i]);
#pragma unroll
      for (int u = 0; u < 2; ++u) { qa[2 * i + u] = (ab ^ ((i & 3) << 4)) + (L_QI + ((w + (i >> 2)) & 3) * 8192 + ((u ^ w) & 1) * 4096); PIN(qa[2 * i + u]); }
    }
    const int sl1 = w == 3 ? 0 : 4 + w, sl2 = 1 + (w >> 1);
#define A_RD(i, o) af[i] = *(const bf16x8*)(lds + aa[i] + (o))
#define A_READ(i0, i1, o) _Pragma("unroll") for (int i = (i0); i < (i1); ++i) A_RD(i, o)
#define A_MOVE(i0, i1, d) _Pragma("unroll") for (int i = (i0); i < (i1); ++i) { aa[i] += (d) * 4096; PIN(aa[i]); }
#define Q_RD(i, u) qf[i][u] = *(const bf16x8*)(lds + qa[2 * (i) + (u)])
#define A_FIRST() do { A_READ(0, 16, 0); lds_add(C_FREE); _Pragma("unroll") for (int i = 0; i < 16; ++i) PIN(af[i]); STAMP(12); } while (0)
    u32 qd[8];
#pragma unroll
    for (int i = 0; i < 8; ++i) { qd[i] = (qv ^ ((i & 3) << 4)) + (i >> 1) * 16384 + (i & 1) * 4096; PIN(qd[i]); }
    bool have = false;
    _Pragma("unroll 1") while ((u32)__builtin_amdgcn_s_memrealtime() - t0 < E3_T) {
      if (__builtin_amdgcn_readfirstlane(ctr[C_MK + w]) >= 4u) { have = true; break; }
      __builtin_amdgcn_s_sleep(1);
    }
    asm volatile("" ::: "memory");
    if (have) A_FIRST();
    lds_wait(ctr + C_SOK, 1u);
    if (isS0) lds_wait(ctr + C_H1, 4u);
    until(t0, E3_T - 10);
    _Pragma("unroll 1") while ((u32)__builtin_amdgcn_s_memrealtime() - t0 < E3_T) {}
    STAMP(6);
#define QD(i) "global_load_lds_dwordx4 %[q" #i "], %[ws] sc1\n s_add_u32 m0, m0, 0x400\n s_nop 0\n"
    asm volatile("s_mov_b32 m0, %[m]\n s_nop 0\n" QD(0) QD(1) QD(2) QD(3) QD(4) QD(5) QD(6) QD(7)
                 ::[m] "s"(L_QI + w * 8192), [ws] "s"(ws), [q0] "v"(qd[0]), [q1] "v"(qd[1]), [q2] "v"(qd[2]), [q3] "v"(qd[3]), [q4] "v"(qd[4]), [q5] "v"(qd[5]), [q6] "v"(qd[6]), [q7] "v"(qd[7]) : "memory", "scc");
    if (RARE(!have)) { lds_wait(ctr + C_MK + w, 4u); A_FIRST(); }
    A_MOVE(0, 16, sl1 - w);
    u32 cb = L_CTR, cw = L_CTR + 4 * w, mx0 = mw + L_MX, mx1 = (mw ^ 64) + L_MX, pw0 = pw + w * 1024, pw1 = (pw ^ 256) + w * 1024;
    float ninf = -__builtin_inff();
    u32 one = 1u;
    PIN(ninf); PIN(one); PIN(cb); PIN(cw); PIN(mx0); PIN(mx1); PIN(pw0); PIN(pw1);
#define CTS(b, i) (*(volatile LAS u32*)(lds + (b) + 4 * (i)))
    _Pragma("unroll 1") for (u32 spins = 0; spins < SPIN_CAP; ++spins) {
      waitvm<0>();
#pragma unroll
      for (int i = 0; i < 8; ++i) Q_RD(i >> 1, i & 1);
      u32 mxu[2] = {0u, 0u};
#pragma unroll
      for (int i = 0; i < 8; ++i) { const u32x4 t_ = __builtin_bit_cast(u32x4, qf[i >> 1][i & 1]); mxu[i & 1] = max(max(mxu[i & 1], t_.x), t_.y); mxu[i & 1] = max(max(mxu[i & 1], t_.z), t_.w); }
      if (!RARE(__any(max(mxu[0], mxu[1]) == POISON))) break;
      STAMP(29);
      __builtin_amdgcn_s_sleep(2);
      const u64 b0 = __ballot(mxu[w & 1] == POISON), b1 = __ballot(mxu[~w & 1] == POISON);
#pragma unroll 1
      for (int i = 0; i < 8; ++i)
        if (((i < 4 ? b0 : b1) >> (4 * (i & 3))) & 0x000F000F000F000Full) dma_sc1(L_QI + w * 8192 + i * 1024, (qv ^ ((i & 3) << 4)) + (i >> 1) * 16384 + (i & 1) * 4096, ws);
    }
    lds_add(C_QRDY);
    STAMP(7);
    f32x4 sc[2], sn[2];
    sc[0] = sc[1] = sn[0] = sn[1] = f32x4{0.f, 0.f, 0.f, 0.f};
#define QK(d, i0, i1, NU)                                                                                                \
  do {                                                                                                                   \
    SB();                                                                                                                \
    _Pragma("unroll") for (int i = (i0); i < (i1); ++i) _Pragma("unroll") for (int u = 0; u < (NU); ++u)                 \
      d[u] = __builtin_amdgcn_mfma_f32_16x16x32_bf16(af[i], qf[i][u], d[u], 0, 0, 0);                                    \
    SB();                                                                                                                \
  } while (0)
#define MF(d, i, u) do { SB(); d[u] = __builtin_amdgcn_mfma_f32_16x16x32_bf16(af[i], qf[i][u], d[u], 0, 0, 0); SB(); } while (0)
    const bool idle1 = isS0 && w == 0;
    u32 c1;
    {
      const u32 c0 = CTS(cb, C_QRDY);
      QK(sc, 0, 2, 2);
      if (RARE(__builtin_amdgcn_readfirstlane(c0) < 4u)) lds_spin(ctr + C_QRDY, 4u);
      asm volatile("" ::: "memory");
#pragma unroll
      for (int i = 2; i < 4; ++i) { MF(sc, i, 0); Q_RD(2 * i, 0); Q_RD(2 * i, 1); MF(sc, i, 1); Q_RD(2 * i + 1, 0); Q_RD(2 * i + 1, 1); }
#pragma unroll
      for (int i = 4; i < 12; ++i) { MF(sc, i, 0); Q_RD(i + 4, 0); MF(sc, i, 1); Q_RD(i + 4, 1); }
      c1 = CTS(cw, C_MK + 4);
      asm volatile("" ::: "memory");
#pragma unroll
      for (int i = 12; i < 16; ++i) { MF(sc, i, 0); A_RD(2 * i - 24, 0); MF(sc, i, 1); A_RD(2 * i - 23, 0); }
    }
    STAMP(9);
    float mref[2], lsum[2] = {0.f, 0.f};
#define ADDC(i, v) do { SB(); asm volatile("s_mov_b64 exec, 1\n ds_add_u32 %0, %1 offset:%2\n s_mov_b64 exec, -1" ::"v"(cb), "v"(v), "n"(4 * (i)) : "memory"); SB(); } while (0)
#define SM(d, T, NU, G, P0, P1)                                                                                          \
  do {                                                                                                                   \
    SB();                                                                                                                \
    if (RARE(__any(max5(d[0], (NU) > 1 ? max5(d[1], ninf) : ninf) > TAU))) {                                             \
      sig = 257u;                                                                                                        \
      _Pragma("unroll") for (int u = 0; u < (NU); ++u) {                                                                 \
        const float up = ceilf(x32_max(x16_max(max5(d[u], 0.f))));                                                       \
        lsum[u] *= __builtin_amdgcn_exp2f(-up);                                                                          \
        mref[u] += up;                                                                                                   \
        d[u] -= up;                                                                                                      \
        *(float*)(lds + (mw ^ (u * 64)) + L_REF + (T) * 128) = mref[u];                                                  \
      }                                                                                                                  \
    }                                                                                                                    \
    u32x2 pk[2];                                                                                                         \
    _Pragma("unroll") for (int u = 0; u < (NU); ++u) {                                                                   \
      pk[u].x = pk2bf(__builtin_amdgcn_exp2f(d[u].x), __builtin_amdgcn_exp2f(d[u].y));                                   \
      pk[u].y = pk2bf(__builtin_amdgcn_exp2f(d[u].z), __builtin_amdgcn_exp2f(d[u].w));                                   \
      *(u32x2*)(lds + (u ? (P1) : (P0))) = pk[u];                                                                        \
    }                                                                                                                    \
    ADDC(C_P + (G), sig);                                                                                                \
    _Pragma("unroll") for (int u = 0; u < (NU); ++u) { lsum[u] = dot2(pk[u].x, 0x3F803F80u, dot2(pk[u].y, 0x3F803F80u, lsum[u])); PIN(lsum[u]); } \
    SB();                                                                                                                \
  } while (0)
#define REF_WRITE(T, NU) _Pragma("unroll") for (int u = 0; u < (NU); ++u) *(float*)(lds + (mw ^ (u * 64)) + L_REF + (T) * 128) = mref[u]
    u32 sig = one;
    {
      MF(sn, 0, 0); A_RD(8, 0); MF(sn, 0, 1);
      const float m0 = max5(sc[0], ninf), m1 = max5(sc[1], ninf);
      MF(sn, 1, 0);
      asm volatile("ds_max_f32 %0, %2\n ds_max_f32 %1, %3" ::"v"(mx0), "v"(mx1), "v"(m0), "v"(m1) : "memory");
      ADDC(C_MXC, one);
      MF(sn, 1, 1); A_RD(9, 0);
#pragma unroll
      for (int i = 2; i < 4; ++i) { MF(sn, i, 0); A_RD(2 * i + 6, 0); MF(sn, i, 1); A_RD(2 * i + 7, 0); }
      MF(sn, 4, 0);
      const u32 c0 = CTS(cb, C_MXC);
      mref[0] = *(volatile LAS float*)(lds + mx0);
      mref[1] = *(volatile LAS float*)(lds + mx1);
      MF(sn, 4, 1); MF(sn, 5, 0); A_RD(14, 0); MF(sn, 5, 1); A_RD(15, 0); MF(sn, 6, 0); MF(sn, 6, 1);
      if (RARE(__builtin_amdgcn_readfirstlane(c0) < 4u)) {
        lds_spin(ctr + C_MXC, 4u);
        mref[0] = *(volatile LAS float*)(lds + mx0);
        mref[1] = *(volatile LAS float*)(lds + mx1);
      }
      u32x2 pk[2];
#pragma unroll
      for (int u = 0; u < 2; ++u) {
        MF(sn, 7 + 2 * u, 0);
        mref[u] = ceilf(mref[u]);
        float e0 = __builtin_amdgcn_exp2f(sc[u].x - mref[u]); PIN(e0);
        MF(sn, 7 + 2 * u, 1);
        pk[u].x = pk2bf(e0, __builtin_amdgcn_exp2f(sc[u].y - mref[u])); PIN(pk[u].x);
        MF(sn, 8 + 2 * u, 0);
        e0 = __builtin_amdgcn_exp2f(sc[u].z - mref[u]); PIN(e0);
        MF(sn, 8 + 2 * u, 1);
        pk[u].y = pk2bf(e0, __builtin_amdgcn_exp2f(sc[u].w - mref[u]));
        *(u32x2*)(lds + (u ? pw1 : pw0)) = pk[u];
      }
      ADDC(C_P, one);
      MF(sn, 11, 0);
      lsum[0] = dot2(pk[0].x, 0x3F803F80u, dot2(pk[0].y, 0x3F803F80u, 0.f)); PIN(lsum[0]);
      MF(sn, 11, 1);
      lsum[1] = dot2(pk[1].x, 0x3F803F80u, dot2(pk[1].y, 0x3F803F80u, 0.f)); PIN(lsum[1]);
      MF(sn, 12, 0);
      REF_WRITE(4 + w, 2);
      MF(sn, 12, 1);
      QK(sn, 13, 16, 2);
    }
    STAMP(10);
#define QK1(N0, slot)                                                                                                    \
  do {                                                                                                                   \
    _Pragma("unroll") for (int i = 0; i < 16; ++i) { MF(sc, i, 0); if (i + (N0) < 16) A_RD(i + (N0), slot); }            \
  } while (0)
    if (RARE(__builtin_amdgcn_readfirstlane(c1) < 4u && !idle1)) {
      lds_spin(ctr + C_MK + 4 + w, 4u);
      A_READ(0, 16, 0);
      sn[0] = sn[1] = f32x4{0.f, 0.f, 0.f, 0.f};
      QK(sn, 0, 16, 2);
    }
    if (RARE(idle1)) lds_add(C_P + 1);
    else { sn[0] -= mref[0]; sn[1] -= mref[1]; SM(sn, 4 + w, 2, 1, pw0 + 4096, pw1 + 4096); }
    if (RARE(w == 2)) {
      RELANE();
      dma_sc1(L_QI, WS_VOTE + xc * 2048 + lane * 16, ws);
    }
    REF_WRITE(7 + sl2, 1);
    A_MOVE(0, 16, sl2 - sl1);
    pw0 = pw + sl2 * 1024;
    sc[0] = f32x4{-mref[0], -mref[0], -mref[0], -mref[0]};
    PIN(pw0); PIN(sc[0]);
    lds_spin(ctr + C_MK + 7 + sl2, 4u);
    A_READ(0, 6, 0);
    STAMP(4);
    QK1(6, 0);
    SM(sc, 7 + sl2, 1, 2, pw0 + 7168, 0);
    STAMP(11);
    if (!RARE(w >= 2)) {
      REF_WRITE(10, 1);
      sc[0] = f32x4{-mref[0], -mref[0], -mref[0], -mref[0]};
      PIN(sc[0]);
      _Pragma("unroll 1") for (u32 n_ = 0; n_ < LSPIN_CAP; ++n_) {
        const u32 k_ = CTS(cb, C_MK + 10);
        asm volatile("" ::: "memory");
        A_READ(0, 6, 8192);
        SB();
        if (__builtin_amdgcn_readfirstlane(k_) >= 4u) break;
      }
      STAMP(5);
      QK1(6, 8192);
      SM(sc, 10, 1, 3, pw0 + 9216, 0);
    } else if (w == 2) {
      RELANE();
      waitvm<0>();
      COUNT_VOTES(true);
      lds_add(C_GX);
    }
    STAMP(30);
    RELANE();
    {
#pragma unroll
      for (int u = 0; u < 2; ++u) {
        *(float*)(lds + L_ML + (mw ^ (u * 64)) * 4 + w * 4) = mref[u];
        *(float*)(lds + L_ML + 512 + (mw ^ (u * 64)) * 4 + w * 4) = x32_sum(x16_sum(lsum[u]));
      }
      u32 old;
      f32x4 m4, l4;
      asm volatile("s_mov_b64 exec, 1\n ds_add_rtn_u32 %0, %3, %4 offset:%6\n s_mov_b64 exec, -1\n ds_read_b128 %1, %5\n ds_read_b128 %2, %5 offset:512\n s_waitcnt lgkmcnt(0)"
                   : "=&v"(old), "=&v"(m4), "=&v"(l4) : "v"((u32)L_CTR), "v"(1u), "v"(L_ML + (lane & 31) * 16), "n"(4 * C_SD) : "memory");
      if (__builtin_amdgcn_readfirstlane(old) == 3u) {
        const float M = fmaxf(fmaxf(m4.x, m4.y), fmaxf(m4.z, m4.w));
        const float l = (l4.x * __builtin_amdgcn_exp2f(m4.x - M) + l4.y * __builtin_amdgcn_exp2f(m4.y - M)) + (l4.z * __builtin_amdgcn_exp2f(m4.z - M) + l4.w * __builtin_amdgcn_exp2f(m4.w - M));
        *(u32x2*)(lds + L_WMAX + (lane & 1) * 128 + (lane & 30) * 4) = u32x2{fbits(M), fbits(l)};
        lds_add(C_ML);
      }
    }
    lds_wait(ctr + C_GX, 1u);
    gx = __builtin_amdgcn_readfirstlane(ctr[C_PAR]);
    STAMP(13);
  } else {
    const int mu = 2 * j + (w >> 2);
#define Q_LOAD(i0, i1)                                                                                                   \
  _Pragma("unroll") for (int i = (i0); i < (i1); ++i) {                                                                  \
    waitvm<QW - 1>();                                                                                                    \
    dma<false>(w * 16384 + (i & 15) * 1024, (vq ^ ((i & 3) << 4)) + ((i < 8 ? 64 * w + 32 * dh : i < 16 ? 64 * w + 32 - 32 * dh : 256 * dh + 32 * w) + 4 * (i & 7)) * 8192, i < 16 ? w_q : w_uk); \
  }
    const u32 vq = g4 * 8192 + hq * 256 + ((col ^ (g4 << 2)) << 4);
    u32x4 xv;
    u32x2 e2;
    u32 x2_;
    asm volatile("global_load_dwordx4 %0, %3, %5\n global_load_dword %1, %4, %5\n buffer_load_dwordx2 %2, off, %6, 0 sc1"
                 : "=&v"(xv), "=&v"(x2_), "=&v"(e2) : "v"(lane * 16), "v"(128 * w + 4 * (lane & 31)), "s"(x), "s"(rs) : "memory");
    bf16x8 wf[16], wk[8];
    u32 ta0 = w * 16384 + off_b(8 * g4 + tq, tp >> 1) + 8 * (tp & 1), ta1 = w * 16384 + off_b(8 * g4 + 4 + tq, tp >> 1) + 8 * (tp & 1);
    int so = WS_SC + ((lane >> 5) * 2048 + 64 * w + 2 * (lane & 31)) * 8;
    int fo = w == 0 ? WS_SFLAG + xc * 2048 + lane * 8 : WS_TOTAL;
    PIN(ta0); PIN(ta1); PIN(so); PIN(fo);
    int qpo = WS_QABS + il(hq * 1024 + dh * 512 + w * 64, 0) + (lane & 32) + (lane & 8) * 2 + (g4 & 1) * 8192 + ((lane & 7) ? WS_TOTAL : 0);
    u32 hw_ = L_H1RED + w * 128 + (lane & 31) * 4, hr_ = L_H1RED + w * 128 + 16 * g4 + 64 * dh, qw_ = L_H1RED + 1024 + w * 272 + lane * 2, qr_ = L_H1RED + 1024 + col * 272 + 64 * g4;
    PIN(qpo); PIN(hw_); PIN(hr_); PIN(qw_); PIN(qr_);
    u32 ka = w * 16384 + off_b(col, 4 * g4), hr2 = hr_ ^ 64;
    PIN(ka); PIN(hr2);
    Q_LOAD(4, 8);
    asm volatile("s_waitcnt vmcnt(4)" : "+v"(xv), "+v"(x2_), "+v"(e2)::"memory");
    TAG_OF(e2.x, e2.y);
    float ln0, ln1;
    {
      float mb, rsd;
      ln_stats(xv, mb, rsd);
      ln0 = rbf(rbf(lo16(x2_) - mb) * rsd);
      ln1 = rbf(rbf(hi16(x2_) - mb) * rsd);
    }
    PIN(ln0); PIN(ln1);
    u32 t0_ = tlo << 24, t1_ = (tlo >> 8) | (thi << 24), f0_ = w ? 0u : tlo, f1_ = w ? 0u : thi;
    PIN(t0_); PIN(t1_); PIN(f0_); PIN(f1_);
    Q_LOAD(8, 8 + QW);
    waitvm<QW>();
#pragma unroll
    for (int c = 0; c < 8; ++c) wf[c] = tr_frag(lds, ta0 ^ (c << 5), ta1 ^ (c << 5));
    Q_LOAD(8 + QW, 16);
#pragma unroll
    for (int c = 0; c < 8; ++c) PIN(wf[c]);
    Q_LOAD(16, 16 + QW);
    waitvm<QW>();
#pragma unroll
    for (int c = 0; c < 8; ++c) wf[8 + c] = tr_frag(lds, (ta0 ^ (c << 5)) + 8192, (ta1 ^ (c << 5)) + 8192);
    Q_LOAD(16 + QW, 24);
    STAMP(12);
    until(t0, E1_T);
    STAMP(23);
    u32x4 g[4];
    u32x2 fl;
#define E1_LOAD(C)                                                                                                       \
  asm volatile("buffer_load_dwordx4 %0, %5, %7, 0 offen sc1\n buffer_load_dwordx4 %1, %5, %7, %8 offen sc1\n"             \
               "buffer_load_dwordx4 %2, %5, %7, %9 offen sc1\n buffer_load_dwordx4 %3, %5, %7, %10 offen sc1\n buffer_load_dwordx2 %4, %6, %7, 0 offen sc1" \
               : C(g[0]), C(g[1]), C(g[2]), C(g[3]), C(fl) : "v"(so), "v"(fo), "s"(rs), "s"(4096), "s"(8192), "s"(12288) : "memory")
    E1_LOAD("=&v");
    waitvm<5>();
#pragma unroll
    for (int i = 0; i < 8; ++i) wk[i] = *(const bf16x8*)(lds + (ka ^ ((i & 3) << 4)) + (i >> 2) * 4096);
#pragma unroll
    for (int c = 0; c < 8; ++c) { PIN(wf[8 + c]); PIN(wk[c]); }
    STAMP(4);
    _Pragma("unroll 1") for (u32 spins = 0; spins < SPIN_CAP; ++spins) {
      asm volatile("s_waitcnt vmcnt(0)" : "+v"(g[0]), "+v"(g[1]), "+v"(g[2]), "+v"(g[3]), "+v"(fl)::"memory");
      u32 bl = g[0].x ^ t0_, bh = (fl.x ^ f0_) | (fl.y ^ f1_);
#pragma unroll
      for (int k = 0; k < 8; ++k) {
        if (k) bl = __builtin_amdgcn_bitop3_b32(bl, g[k >> 1][2 * (k & 1)], t0_, 0xF6);
        bh = __builtin_amdgcn_bitop3_b32(bh, g[k >> 1][2 * (k & 1) + 1], t1_, 0xF6);
      }
      if (!RARE(__any(((bl >> 24) | bh) != 0u))) break;
      STAMP(29);
      __builtin_amdgcn_s_sleep(1);
      E1_LOAD("+v");
    }
    STAMP(1);
    {
      const float m0 = rbf((gran_val(g[0].x) + gran_val(g[1].x)) + (gran_val(g[2].x) + gran_val(g[3].x)));
      const float m1 = rbf((gran_val(g[0].z) + gran_val(g[1].z)) + (gran_val(g[2].z) + gran_val(g[3].z)));
      const auto s0 = __builtin_amdgcn_permlane32_swap(__float_as_uint(m0), __float_as_uint(m0), false, false);
      const auto s1 = __builtin_amdgcn_permlane32_swap(__float_as_uint(m1), __float_as_uint(m1), false, false);
      *(u32*)(lds + hw_) = pk2bf(rbf(ln0 * rbf(1.f + __uint_as_float(s0[1]))) + __uint_as_float(s0[0]),
                                 rbf(ln1 * rbf(1.f + __uint_as_float(s1[1]))) + __uint_as_float(s1[0]));
    }
    WAVE_LDS_SYNC();
    {
      const bf16x8 h0 = *(const bf16x8*)(lds + hr_), h1 = *(const bf16x8*)(lds + hr2);
      f32x4 d[8];
      SB();
#pragma unroll
      for (int c = 0; c < 8; ++c) { d[c] = __builtin_amdgcn_mfma_f32_16x16x32_bf16(h0, wf[c], f32x4{0.f, 0.f, 0.f, 0.f}, 0, 0, 0); SB(); }
#pragma unroll
      for (int c = 0; c < 8; ++c) { d[c] = __builtin_amdgcn_mfma_f32_16x16x32_bf16(h1, wf[8 + c], d[c], 0, 0, 0); SB(); }
#pragma unroll
      for (int k = 0; k < 2; ++k) {
        const float v = (g4 & 2) ? ((g4 & 1) ? d[4 * k + 3][0] : d[4 * k + 2][0]) : ((g4 & 1) ? d[4 * k + 1][0] : d[4 * k][0]);
        const u32 vh = f2bf(v);
        *(u16*)(lds + qw_ + 128 * k) = (u16)vh;
        *(u16*)(lds + qw_ + 128 * k + 8 * 272) = (u16)f2bf(v - bf2f(vh));
      }
    }
    STAMP(2);
    BAR();
    {
      bf16x8 a[4];
#pragma unroll
      for (int s = 0; s < 4; ++s) a[s] = *(const bf16x8*)(lds + qr_ + 16 * s);
      SB();
      f32x4 d[2] = {f32x4{0.f, 0.f, 0.f, 0.f}, f32x4{0.f, 0.f, 0.f, 0.f}};
#pragma unroll
      for (int s = 0; s < 4; ++s)
#pragma unroll
        for (int c = 0; c < 2; ++c) d[c] = __builtin_amdgcn_mfma_f32_16x16x32_bf16(a[s], wk[4 * c + s], d[c], 0, 0, 0);
      constexpr float SCALE = 0.08838834764831845f * 1.4426950408889634f;
      const float v = SCALE * x16_sum(rs32(sum4(d[0]), sum4(d[1])));
      const float p0 = __uint_as_float(nopois(pk2bf(dppf<0x00>(v), dppf<0x55>(v)))), p1 = __uint_as_float(nopois(pk2bf(dppf<0xAA>(v), dppf<0xFF>(v))));
      st128(rs, qpo, u32x4{__float_as_uint(p0), __float_as_uint(p1), __float_as_uint(dppf<0x104>(p0)), __float_as_uint(dppf<0x104>(p1))});
    }
    STAMP(5);
    SB();
    {
      st128(rs, lane < 50 ? WS_PCTX + (j * 8 + w) * 800 + lane * 16 : WS_TOTAL, P4);
      const int pp = __builtin_amdgcn_readfirstlane(WS_PROBE + il((j * 8 + w) * 128, w & 1));
      u64 ta, tb, tc;
      u32 dm;
      asm volatile("s_load_dword %3, %4, 0x0\n s_waitcnt lgkmcnt(0)\n"
                   "s_memtime %0\n s_waitcnt lgkmcnt(0)\n s_load_dword %3, %4, %5 glc\n s_waitcnt lgkmcnt(0)\n"
                   "s_memtime %1\n s_waitcnt lgkmcnt(0)\n s_load_dword %3, %4, %6 glc\n s_waitcnt lgkmcnt(0)\n"
                   "s_memtime %2\n s_waitcnt vmcnt(0) lgkmcnt(0)"
                   : "=&s"(ta), "=&s"(tb), "=&s"(tc), "=&s"(dm) : "s"(ws), "s"(pp), "s"(pp ^ 8192) : "memory");
      int df = min(max(((int)(u32)(tb - ta) - (int)(u32)(tc - tb)) >> 3, -VCLIP), VCLIP);
      df = ((w ^ myh) & 1) ? -df : df;
      u32 old = 0u;
      if (lane == 0) old = __hip_atomic_fetch_add((LAS u32*)(lds + L_CTR) + C_VOTE, (u32)(65536 + 64 + df), __ATOMIC_RELAXED, __HIP_MEMORY_SCOPE_WORKGROUP);
      old = __builtin_amdgcn_readfirstlane(old) + 65536 + 64 + df;
      if (old >= 524288u && lane < 8) st64(rs, WS_VOTE + lane * 2048 + j * 8, (old & 1023u) | (tlo << 24), (tlo >> 8) | (thi << 24));
      STAMP(6);
    }
    {
      const int r0 = 128 * (mu & 3) + 32 * (w & 3) + 4 * (lane >> 3);
      const u32x2 cvb = *(const u32x2*)(cvec + r0);
      u32x4 wsc[4];
#pragma unroll
      for (int i = 0; i < 4; ++i) wsc[i] = ldnt(w_mod + (size_t)(r0 + i) * 3072 + 1024 + (mu >> 2) * 64 + (lane & 7) * 8);
      float a[8];
#pragma unroll
      for (int k = 0; k < 8; ++k) a[k] = 0.f;
#pragma unroll
      for (int i = 0; i < 4; ++i) fma8b(a, f2bf(silu_bf((i & 1) ? hi16(i < 2 ? cvb.x : cvb.y) : lo16(i < 2 ? cvb.x : cvb.y))), wsc[i]);
      float r[4];
#pragma unroll
      for (int k = 0; k < 4; ++k) r[k] = rs32(a[k], a[k + 4]);
      const float o0 = b3_sum(rs16(r[0], r[2])), o1 = b3_sum(rs16(r[1], r[3]));
      float* red = (float*)(lds + L_H1RED + 8192);
      if ((lane & 8) == 0) *(f32x2*)(red + w * 64 + (lane & 7) * 8 + (lane >> 5) * 4 + ((lane >> 4) & 1) * 2) = f32x2{o0, o1};
      BAR();
      if ((w & 3) == 3 && lane < 16) {
        red += (w >> 2) * 256 + lane * 4;
        const f32x4 sm = (*(const f32x4*)(red) + *(const f32x4*)(red + 64)) + (*(const f32x4*)(red + 128) + *(const f32x4*)(red + 192));
        st128(rs, WS_MODB + ((mu & 3) * 2048 + (mu >> 2) * 64 + lane * 4) * 4, u32x4{fbits(sm.x), fbits(sm.y), fbits(sm.z), fbits(sm.w)});
      }
    }
    gx = 0;
  }

  RELANE();
  const int r16 = lane >> 2, ch = lane & 3;
  if (ii >= 16) {
    int mo = lane < 24 ? (WS_LSE + il((24 * w + lane) * 512 + c4 * 128 + (h >> 1) * 8, h & 1)) ^ gx : WS_TOTAL;
    PIN(mo);
    lds_spin(ctr + C_PD, 4u);
    STAMP(23);
    u32x2 mt = ld64(rs, mo);
    SB();
    const u32 pb = (w >> 1) * PS + (w & 1) * 6144;
    u32 po[6], pa[16], ea[8];
    {
      u32 t_ = g4 * 256 + ((col ^ (g4 << 2)) << 4) + ((WS_PART + il21(((h >> 1) * 4 + c4) * 65536, h & 1)) ^ gx);
      PIN(t_);
#pragma unroll
      for (int k = 0; k < 6; ++k) { po[k] = (t_ ^ ((k & 3) << 4)) + il((6 * w + k) * 1024, 0); PIN(po[k]); }
    }
    bf16x8 wk[4];
    {
      u32 wa = tq * PS + 16384 + off_b(g4, 2 * w + (tp >> 1)) + 8 * (tp & 1);
      PIN(wa);
#pragma unroll
      for (int s = 0; s < 4; ++s) wk[s] = tr_frag(lds, (wa ^ (s << 4)) + s * 1024, (wa ^ (s << 4)) + s * 1024 + 4096);
    }
    pa[0] = pb + off_b(8 * (g4 == 3 ? 0 : g4) + tq, tp >> 1) + 8 * (tp & 1);
    PIN(pa[0]);
#pragma unroll
    for (int i = 1; i < 16; ++i) { pa[i] = pa[0] ^ (i << 4); PIN(pa[i]); }
    u32 bo = L_QOFF + col * 272 + g4 * 16, mlo = L_QOFF + 4352 + (col & 7) * 8, ro = L_QOFF + (w + (col & 8)) * 272 + (col & 7) * 32 + g4 * 8;
    int so1 = (WS_PCTX + h * 2048 + c4 * 512 + (w >> 2) * 256 + (w & 1) * 128 + (w & 2) * 16 + (g4 >> 1) * 64 + (g4 & 1) * 8 + (col < 2 ? col * 16 : WS_TOTAL)) ^ gx;
    u32 eo = L_QOFF + 4416 + w * 128 + lane * 2, mw_ = L_QOFF + 4352 + w * 8;
#pragma unroll
    for (int i = 0; i < 8; ++i) { ea[i] = L_QOFF + 4416 + w * 128 + ((col & 7) == i ? 16 * g4 : 64); PIN(ea[i]); }
    float pad = lane < 24 ? 0.f : -__builtin_inff();
    PIN(bo); PIN(mlo); PIN(ro); PIN(so1); PIN(eo); PIN(mw_); PIN(pad);
    SB();
    u32 done = 0u;
    _Pragma("unroll 1") for (u32 spins = 0; spins < SPIN_CAP; ++spins) {
      const u32 ok = ~(u32)__ballot(max(mt.x, mt.y) == POISON);
      u32 nw = ok & (ok >> 1);
      nw = nw & (nw >> 2) & 0x111111u & ~done;
      done |= nw;
#define T1F(k, b) "s_bitcmp0_b32 %[nw], " #b "\n s_cbranch_scc1 1f\n global_load_lds_dwordx4 %[p" #k "], %[ws] sc1\n 1: s_add_u32 m0, m0, 0x400\n"
      asm volatile("s_mov_b32 m0, %[m]\n" T1F(0, 0) T1F(1, 4) T1F(2, 8) T1F(3, 12) T1F(4, 16) T1F(5, 20)
                   ::[m] "s"(pb), [nw] "s"(nw), [ws] "s"(ws), [p0] "v"(po[0]), [p1] "v"(po[1]), [p2] "v"(po[2]), [p3] "v"(po[3]), [p4] "v"(po[4]), [p5] "v"(po[5]) : "memory", "scc");
      if (done == 0x111111u) break;
      __builtin_amdgcn_s_sleep(1);
      if (max(mt.x, mt.y) == POISON) mt = ld64(rs, mo);
      PIN(mt);
    }
    STAMP(16);
    {
      const float m = __uint_as_float(mt.x) + pad;
      float M = m;
#define MX(C) "s_nop 1\n v_max_f32_dpp %0, %0, %0 " C " row_mask:0xf bank_mask:0xf\n"
      asm(MX("quad_perm:[1,0,3,2]") MX("quad_perm:[2,3,0,1]") MX("row_ror:8") MX("row_ror:4") "s_nop 1" : "+v"(M));
      M = fmaxf(__uint_as_float(__builtin_amdgcn_readlane(__float_as_uint(M), 0)), __uint_as_float(__builtin_amdgcn_readlane(__float_as_uint(M), 16)));
      const float e = __builtin_amdgcn_exp2f(m - M);
      *(u16*)(lds + eo) = (u16)f2bf(e);
      WAVE_LDS_SYNC();
      bf16x8 wf[8], pt[8];
#pragma unroll
      for (int i = 0; i < 8; ++i) wf[i] = *(const bf16x8*)(lds + ea[i]);
      SB();
      const float L = x16_sum(row16_sum(e * __uint_as_float(mt.y)));
      if (lane == 0) *(f32x2*)(lds + mw_) = f32x2{M, L};
      SB();
      waitvm<0>();
      STAMP(17);
#pragma unroll
      for (int i = 0; i < 8; ++i) pt[i] = tr_frag(lds, pa[2 * i], pa[2 * i + 1] + 1024);
      SB();
      f32x4 d = {0.f, 0.f, 0.f, 0.f};
#pragma unroll
      for (int i = 0; i < 8; ++i) d = __builtin_amdgcn_mfma_f32_16x16x32_bf16(pt[i], wf[i], d, 0, 0, 0);
      const u32 h0 = pk2bf(d[0], d[1]), h1 = pk2bf(d[2], d[3]);
      const u32 l0 = pk2bf(d[0] - lo16(h0), d[1] - hi16(h0)), l1 = pk2bf(d[2] - lo16(h1), d[3] - hi16(h1));
      *(u32x2*)(lds + ro) = (col & 8) ? u32x2{l0, l1} : u32x2{h0, h1};
    }
    BAR();
    STAMP(14);
    const f32x2 ml = *(const f32x2*)(lds + mlo);
    bf16x8 lf[4];
#pragma unroll
    for (int s = 0; s < 4; ++s) lf[s] = *(const bf16x8*)(lds + bo + 64 * s);
    SB();
    f32x4 d = {0.f, 0.f, 0.f, 0.f};
#pragma unroll
    for (int s = 0; s < 4; ++s) d = __builtin_amdgcn_mfma_f32_16x16x32_bf16(wk[s], lf[s], d, 0, 0, 0);
    SB();
    float Mg = fmaxf(ml.x, dppf<0xB1>(ml.x));
    Mg = fmaxf(Mg, dppf<0x4E>(Mg)); Mg = fmaxf(Mg, dppf<0x141>(Mg));
    const float e = __builtin_amdgcn_exp2f(ml.x - Mg);
    float Lg = quad_sum(e * ml.y);
    Lg += dppf<0x141>(Lg);
    const float sc = e * __builtin_amdgcn_rcpf(Lg);
    {
      float d0 = d[0] * sc, d1 = d[1] * sc, d2 = d[2] * sc, d3 = d[3] * sc;
      row16_sum4(d0, d1, d2, d3);
      d = f32x4{d0, d1, d2, d3};
    }
    const u32 h0 = pk2bf(d[0], d[1]), h1 = pk2bf(d[2], d[3]);
    const u32 l0 = pk2bf(d[0] - lo16(h0), d[1] - hi16(h0)), l1 = pk2bf(d[2] - lo16(h1), d[3] - hi16(h1));
    st64(rs, so1, nopois(col ? l0 : h0), nopois(col ? l1 : h1));
    STAMP(15);
    STAMP_FLUSH();
    return;
  }
  if (tl2) {
    BAR0();
    if (w < 4) {
#pragma unroll
      for (int i = 0; i < 8; ++i) dma<true>(owno + i * 1024, (ks * 512 + 64 * w + 8 * i + (lane >> 3)) * 1024 + cg * 128 + (lane & 7) * 16, w_o);
      waitvm<0>();
    } else {
#pragma unroll
      for (int i = 0; i < 8; ++i) *(u32x4*)(lds + owno + i * 1024 + lane * 16) = wv[i];
      WAVE_LDS_SYNC();
    }
    STAMP(26);
    bf16x8 wf[8];
#pragma unroll
    for (int i = 0; i < 8; ++i) {
      const u32 a = owno + (32 * (i >> 2) + 8 * g4 + tq) * 128 + (i & 3) * 32 + tp * 8;
      wf[i] = tr_frag(lds, a, a + 512);
    }
#pragma unroll
    for (int i = 0; i < 8; ++i) PIN(wf[i]);
    STAMP(23);
    int off[1] = {(WS_PCTX + (ks * 4 + (w >> 1)) * 2048 + tq * 512 + (w & 1) * 256 + g4 * 64 + tp * 16) ^ gx};
    u32 rw = L_RED + lane * 32 + w * 4;
    PIN(rw);
    int so2 = tp < 2 ? WS_PATTN + il((ks * 512 + cg * 64 + (lane & 60)) * 4, tp) : WS_TOTAL;
    PIN(so2);
    u32x4 g[1];
    gather128<1, 4>(rs, off, g);
    STAMP(24);
    const bf16x8 a0 = __builtin_bit_cast(bf16x8, (col & 2) ? Z4 : g[0]), a1 = __builtin_bit_cast(bf16x8, (col & 2) ? g[0] : Z4);
    f32x4 d[4];
#pragma unroll
    for (int c = 0; c < 4; ++c) d[c] = __builtin_amdgcn_mfma_f32_16x16x32_bf16(a0, wf[c], f32x4{0.f, 0.f, 0.f, 0.f}, 0, 0, 0);
#pragma unroll
    for (int c = 0; c < 4; ++c) d[c] = __builtin_amdgcn_mfma_f32_16x16x32_bf16(a1, wf[4 + c], d[c], 0, 0, 0);
    *(float*)(lds + rw) = rs16(rs32(sum4(d[0]), sum4(d[2])), rs32(sum4(d[1]), sum4(d[3])));
    BAR();
    if (w == 0) {
      const float v = sum4(*(const f32x4*)(lds + L_RED + lane * 32) + *(const f32x4*)(lds + L_RED + lane * 32 + 16));
      const float vn = __uint_as_float(fbits(v));
      st128(rs, so2, u32x4{__float_as_uint(dppf<0x00>(vn)), __float_as_uint(dppf<0x55>(vn)), __float_as_uint(dppf<0xAA>(vn)), __float_as_uint(dppf<0xFF>(vn))});
    }
    STAMP(17);
  } else if (t3 >= 0) {
    STAMP(20);
    if (w < 4) {
      const int fr = ((128 * w + r16) * DF + t3 * 32) * 2, ca_ = ch ^ ((r16 >> 2) & 2) ^ (t3 & 1);
      const u16* wa = (t3 & 1) ? w_up : w_gate;
      const u16* wb = (t3 & 1) ? w_gate : w_up;
      const u32 d = w * 32768 + 8192;
#pragma unroll
      for (int i = 0; i < 8; ++i) { waitvm<WING - 1>(); dma<false>(d + (i >> 2) * 12288 + (t3 & 1) * 4096 + (i & 3) * 1024, fr + ca_ * 16 + 32 * i * DF, wa); }
#pragma unroll
      for (int i = 0; i < 8; ++i) { waitvm<WING - 1>(); dma<false>(d + (i >> 2) * 12288 + 4096 - (t3 & 1) * 4096 + (i & 3) * 1024, fr + (ca_ ^ 1) * 16 + 32 * i * DF, wb); }
    } else {
#pragma unroll
      for (int i = 0; i < 8; ++i) {
        waitvm<WING - 1>();
        dma<true>(lw * 32768 + 16384 + (i >> 2) * 12288 + (i & 3) * 1024, (32 * t3 + 8 * (i & 3) + (lane >> 3)) * 1024 + 256 * lw + 128 * (i >> 2) + (lane & 7) * 16, w_down);
      }
      const u32 x2 = *(const u32*)(x + 128 * lw + 2 * lane);
      u32x4 m6[6];
      int mo[6];
#pragma unroll
      for (int i = 0; i < 6; ++i) mo[i] = MB_OFF(i);
      gather128<6, 8>(rs, mo, m6);
#pragma unroll
      for (int i = 0; i < 6; ++i) {
        const f32x4 f = __builtin_bit_cast(f32x4, m6[i]);
        const float v = rbf(rs16(rs32(f.x, f.z), rs32(f.y, f.w)));
        *(u16*)(lds + L_Z + (i < 2 ? 5 : i < 4 ? 3 : 2) * 1088 + (128 * lw + 64 * (i & 1) + 4 * col + g4) * 2) = (u16)f2bf(i < 4 ? v : 1.f + v);
      }
      *(u32*)(lds + L_Z + 6 * 1088 + lw * 256 + lane * 4) = x2;
      if (w == 4 && lane < 32) *(u32*)(lds + L_ZERO + lane * 4) = lane < 16 ? 0u : POISON;
      if (w == 6) COUNT_VOTES(false);
    }
    waitvm<0>();
    STAMP(26);
    BAR();
    gx = __builtin_amdgcn_readfirstlane(ctr[C_PAR]);
    u32 zw = L_Z + (64 * w + 4 * col + g4) * 2;
    u32 hr[4];
#pragma unroll
    for (int i = 0; i < 4; ++i) { hr[i] = (col == i ? L_HH : L_ZERO) + 16 * g4; PIN(hr[i]); }
    int off[2];
#pragma unroll
    for (int i = 0; i < 2; ++i) { off[i] = (WS_PATTN + il(((4 * i + g4) * 512 + 64 * w + col * 4) * 4, myh)) ^ gx; PIN(off[i]); }
    int so = col < 4 ? (WS_PDOWN + il((t3 * 256 + 64 * (w & 3) + 16 * col + 4 * g4) * 4, w >> 2)) ^ gx : WS_TOTAL;
    u32 zr = L_Z + tp * 1088 + 16 * g4, yr = L_Z + 4 * 1088 + lane * 16, hw = L_HH + (w & 3) * 16 + (g4 & 1) * 8 + tp * 2, cr = L_CF + (w & 3) * 16 + tp * 4;
    PIN(cr); PIN(so); PIN(zw); PIN(zr); PIN(yr); PIN(hw);
    float ga = bf2f(*(const u16*)(lds + zw + 5 * 1088)), cb1 = bf2f(*(const u16*)(lds + zw + 2 * 1088)), xe = bf2f(*(const u16*)(lds + zw + 6 * 1088));
    bf16x8 wf[16], wd[4];
    if (w < 4) {
#pragma unroll
      for (int k = 0; k < 16; ++k) {
        const u32 a = (k >> 2) * 32768 + 8192 + ((k >> 1) & 1) * 12288 + (tp >> 1) * 4096 + ((k & 1) * 32 + 8 * g4 + tq) * 64 + (w ^ ((g4 & 1) * 2) ^ (tp >> 1)) * 16 + (tp & 1) * 8;
        wf[k] = tr_frag(lds, a, a + 256);
      }
#pragma unroll
      for (int k = 0; k < 16; ++k) PIN(wf[k]);
    }
#pragma unroll
    for (int i = 0; i < 4; ++i) {
      const u32 a = owno + 8192 + (8 * g4 + tq) * 128 + i * 32 + tp * 8;
      wd[i] = tr_frag(lds, a, a + 512);
    }
#pragma unroll
    for (int i = 0; i < 4; ++i) PIN(wd[i]);
    PIN(ga); PIN(cb1); PIN(xe);
    STAMP(23);
    until(t0, E6_T);
    u32x4 pa2[2];
    gather128<2, 4>(rs, off, pa2);
    STAMP(18);
    {
      const f32x4 s4 = __builtin_bit_cast(f32x4, pa2[0]) + __builtin_bit_cast(f32x4, pa2[1]);
      const u32 yb = f2bf(xe + rbf(ga * rbf(rs16(rs32(s4.x, s4.z), rs32(s4.y, s4.w)))));
      const float z = bf2f(yb) * cb1;
      const u32 zh = f2bf(z);
      *(u16*)(lds + zw + 4 * 1088) = (u16)yb;
      *(u16*)(lds + zw) = (u16)zh;
      *(u16*)(lds + zw + 1088) = (u16)f2bf(z - bf2f(zh));
    }
    BAR();
    if (w < 4) {
      bf16x8 zf[16];
#pragma unroll
      for (int k = 0; k < 16; ++k) zf[k] = *(const bf16x8*)(lds + zr + 64 * k);
      __builtin_amdgcn_sched_barrier(0);
      f32x4 d = {0.f, 0.f, 0.f, 0.f};
#pragma unroll
      for (int k = 0; k < 16; ++k) d = __builtin_amdgcn_mfma_f32_16x16x32_bf16(wf[k], zf[k], d, 0, 0, 0);
      PIN(d);
      u32 cfb;
      do { cfb = *(volatile LAS u32*)(lds + cr); } while (__any(cfb == POISON));
      d *= __uint_as_float(cfb);
      const float a0 = (tp & 1) ? d[1] : d[0], b0 = (tp & 1) ? d[0] : d[1], a1 = (tp & 1) ? d[3] : d[2], b1 = (tp & 1) ? d[2] : d[3];
      const float e0 = a0 + dppf<0xB1>(b0), e1 = a1 + dppf<0xB1>(b1);
      const float v = rbf(((tp & 2) ? e1 : e0) + dppf<0x4E>((tp & 2) ? e0 : e1));
      const auto sw = __builtin_amdgcn_permlane32_swap(__float_as_uint(v), __float_as_uint(v), false, false);
      const float g = __uint_as_float(sw[0]), u = __uint_as_float(sw[1]);
      const float e2 = __builtin_amdgcn_exp2f(g * (-2.3022081981f - 0.1029432396f * g * g));
      *(u16*)(lds + hw) = (u16)f2bf(rbf(g * __builtin_amdgcn_rcpf(e2 + 1.f)) * u);
    } else {
      float mb, rsd;
      ln_stats(*(const u32x4*)(lds + yr), mb, rsd);
      *(u32*)(lds + cr) = fbits(tp == 3 ? 1.f : rsd * (tp == 2 ? -mb : 1.f));
    }
    BAR();
    {
      bf16x8 hf[4];
#pragma unroll
      for (int i = 0; i < 4; ++i) hf[i] = *(const bf16x8*)(lds + hr[i]);
      __builtin_amdgcn_sched_barrier(0);
      f32x4 d = {0.f, 0.f, 0.f, 0.f};
#pragma unroll
      for (int i = 0; i < 4; ++i) d = __builtin_amdgcn_mfma_f32_16x16x32_bf16(wd[i], hf[i], d, 0, 0, 0);
      st128(rs, so, u32x4{fbits(d[0]), fbits(d[1]), fbits(d[2]), fbits(d[3])});
    }
    STAMP(19);
  }
  if (t4 >= 0 && lw == 0) {
#define RED8(f) b3_sum(rs16(rs32(f.x, f.z), rs32(f.y, f.w)))
    const int oc = t4 * 32 + (lane & 7) * 4 + g4;
    const u32 xb = x[oc];
    float x1, gb;
    {
      int off[2];
      off[0] = lane < 32 ? WS_MODB + ((lane >> 3) * 2048 + t4 * 32 + (lane & 7) * 4) * 4 : WS_TOTAL;
      off[1] = lane < 32 ? WS_MODB + ((lane >> 3) * 2048 + 1536 + t4 * 32 + (lane & 7) * 4) * 4 : WS_TOTAL;
      u32x4 mg[2];
      gather128<2>(rs, off, mg);
      const f32x4 fa = __builtin_bit_cast(f32x4, mg[0]), fb = __builtin_bit_cast(f32x4, mg[1]);
      const float ga = rbf(RED8(fa));
      gb = rbf(RED8(fb));
      int of1[1] = {(WS_PATTN + il(((lane >> 3) * 512 + t4 * 32 + (lane & 7) * 4) * 4, myh)) ^ gx};
      u32x4 g[1];
      gather128<1, 4>(rs, of1, g);
      const f32x4 gf = __builtin_bit_cast(f32x4, g[0]);
      x1 = rbf(bf2f(xb) + rbf(ga * rbf(RED8(gf))));
    }
    STAMP(20);
    if (t4 == 0 && lane == 0) st64(rs, WS_EPOCH, tlo, thi);
    int off[7];
#pragma unroll
    for (int i = 0; i < 7; ++i) { off[i] = (WS_PDOWN + il(((7 * (lane >> 3) + i) * 256 + (t4 & 7) * 32 + (lane & 7) * 4) * 4, myh)) ^ gx; PIN(off[i]); }
    const rsrc_t ro = __builtin_amdgcn_make_buffer_rsrc((void*)out, 0, 1024, 0x00020000);
    int oo = (lane & 8) ? 1024 : oc * 2;
    PIN(x1); PIN(gb); PIN(oo);
    u32x4 g[7];
    __builtin_amdgcn_s_sleep(T4_DELAY);
#define F4(i) __builtin_bit_cast(f32x4, g[i])
    float s;
#pragma unroll
    for (int i = 0; i < 7; ++i) g[i] = ld128(rs, off[i]);
    _Pragma("unroll 1") for (u32 spins = 0; spins < SPIN_CAP; ++spins) {
      asm volatile("s_waitcnt vmcnt(0)" : "+v"(g[0]), "+v"(g[1]), "+v"(g[2]), "+v"(g[3]), "+v"(g[4]), "+v"(g[5]), "+v"(g[6])::"memory");
      const f32x4 s4 = ((F4(0) + F4(1)) + (F4(2) + F4(3))) + ((F4(4) + F4(5)) + F4(6));
      s = RED8(s4);
      if (!__any(s != s)) break;
      u64 bad = 0ull;
#pragma unroll
      for (int i = 0; i < 7; ++i) {
        const u64 m = __ballot(pois4(g[i]));
        bad |= m;
        asm volatile("s_mov_b64 exec, %2\n buffer_load_dwordx4 %0, %1, %3, 0 offen sc1\n s_mov_b64 exec, -1" : "+v"(g[i]) : "v"(off[i]), "s"(m), "s"(rs) : "memory");
      }
      if (!bad) break;
    }
    STAMP(21);
    __builtin_amdgcn_raw_buffer_store_b16((u16)f2bf(x1 + rbf(gb * rbf(s))), ro, oo, 0, 0);
    STAMP(22);
  }
  STAMP_FLUSH();
}

extern "C" void kernel_launch(void* const* d_in, const int* in_sizes, int n_in, void* d_out, int out_size,
                              void* d_ws, size_t ws_size, hipStream_t stream) {
  k_main<<<NWG, 512, 0, stream>>>((const u16*)d_in[0], (const u16*)d_in[1], (const u16*)d_in[2], (const u16*)d_in[3],
                                  (const u16*)d_in[4], (const u16*)d_in[5], (const u16*)d_in[7], (const u16*)d_in[8],
                                  (const u16*)d_in[9], (const u16*)d_in[10], (const u16*)d_in[11], (u16*)d_out,
                                  (unsigned char*)d_ws);
}
```

```cpp
#include <hip/hip_runtime.h>
#include <hip/hip_bf16.h>
#include <stdint.h>

typedef unsigned short u16;
typedef unsigned int u32;
typedef unsigned long long u64;
typedef float f32x2 __attribute__((ext_vector_type(2)));
typedef float f32x4 __attribute__((ext_vector_type(4)));
typedef float f32x16 __attribute__((ext_vector_type(16)));
typedef u32 u32x2 __attribute__((ext_vector_type(2)));
typedef u32 u32x4 __attribute__((ext_vector_type(4)));
typedef short bf16x8 __attribute__((ext_vector_type(8)));
typedef short s16x4 __attribute__((ext_vector_type(4)));
#define LAS __attribute__((address_space(3)))
#define DI __device__ __forceinline__

constexpr int DF = 1792;
constexpr int NWG = 256;
constexpr int AUX_SC1 = 16;

constexpr int WS_EPOCH = 0;
constexpr int WS_SFLAG = 4096;
constexpr int WS_QABS = 32768;
constexpr int WS_PCTX = WS_QABS + 65536;
constexpr int WS_PATTN = WS_PCTX + 65536;
constexpr int WS_PDOWN = WS_PATTN + 32768;
constexpr int WS_LSE = WS_PDOWN + 114688;
constexpr int WS_MODB = WS_LSE + 196608;
constexpr int WS_SC = WS_MODB + 32768;
constexpr int WS_PROBE = WS_SC + 32768;
constexpr int WS_VOTE = WS_PROBE + 262144;
constexpr int WS_PART = 2097152;
constexpr int PART_BYTES = 32 * 8 * 256 * 128;
constexpr int WS_TOTAL = WS_PART + PART_BYTES;

constexpr int il(int o, int hf) { return (o >> 13) * 16384 + hf * 8192 + (o & 8191); }
constexpr int il21(int o, int hf) { return il(o, hf ^ ((o >> 20) & 1) ^ 1); }
constexpr u32 SPIN_CAP = 1u << 16;
#define WIN 7
#define WA0 5
#define QW 4
#define WING 3
#define QF_T 300
#define E1_T 250
#define SCP_T 215
#define E3_T 470
#define E6_T 1200
#define T4_DELAY 30
#define TAU 32.f
#define VCLIP 40
#define PF_LEN 33400
#define G_OF(v) ((v) > 32768.f ? 8192u : 0u)
#define VOTE_STAMP(v)
#define STAMP_DECL
#define STAMP(i)
#define STAMP_FLUSH()

DI float bf2f(u32 b) { return __uint_as_float(b << 16); }
DI float rbf(float f) { return (float)(__bf16)f; }
DI u32 f2bf(float f) { return (u32)__builtin_bit_cast(u16, (__bf16)f); }
typedef __bf16 bf16x2_t __attribute__((ext_vector_type(2)));
DI u32 pk2bf(float a, float b) { return __builtin_bit_cast(u32, __builtin_convertvector(f32x2{a, b}, bf16x2_t)); }
DI float lo16(u32 v) { return __uint_as_float(v << 16); }
DI float hi16(u32 v) { return __uint_as_float(v & 0xFFFF0000u); }
DI u32x4 ldnt(const u16* p) { return __builtin_nontemporal_load((const u32x4*)p); }

DI float silu_bf(float a) { return rbf(a * __builtin_amdgcn_rcpf(1.f + __expf(-a))); }

template <int CTRL> DI float dppf(float v) {
  return __int_as_float(__builtin_amdgcn_update_dpp(0, __float_as_int(v), CTRL, 0xF, 0xF, true));
}
DI float quad_sum(float v) { v += dppf<0xB1>(v); v += dppf<0x4E>(v); return v; }
DI float b23_sum(float v) { v += dppf<0x128>(v); v += dppf<0x124>(v); return v; }
DI float b3_sum(float v) { return v + dppf<0x128>(v); }
DI float x16_sum(float v) {
  const auto r = __builtin_amdgcn_permlane16_swap(__float_as_uint(v), __float_as_uint(v), false, false);
  return __uint_as_float(r[0]) + __uint_as_float(r[1]);
}
DI float x32_sum(float v) {
  const auto r = __builtin_amdgcn_permlane32_swap(__float_as_uint(v), __float_as_uint(v), false, false);
  return __uint_as_float(r[0]) + __uint_as_float(r[1]);
}
DI float rs32(float a, float b) {
  const auto r = __builtin_amdgcn_permlane32_swap(__float_as_uint(a), __float_as_uint(b), false, false);
  return __uint_as_float(r[0]) + __uint_as_float(r[1]);
}
DI float rs16(float a, float b) {
  const auto r = __builtin_amdgcn_permlane16_swap(__float_as_uint(a), __float_as_uint(b), false, false);
  return __uint_as_float(r[0]) + __uint_as_float(r[1]);
}
DI float row16_sum(float v) { return b23_sum(quad_sum(v)); }
DI float wave_sum(float v) { return x32_sum(x16_sum(row16_sum(v))); }
DI void row8_sum4(float& a, float& b, float& c, float& d) {
#define R4(C) "v_add_f32_dpp %0, %0, %0 " C " row_mask:0xf bank_mask:0xf\n v_add_f32_dpp %1, %1, %1 " C " row_mask:0xf bank_mask:0xf\n" \
              "v_add_f32_dpp %2, %2, %2 " C " row_mask:0xf bank_mask:0xf\n v_add_f32_dpp %3, %3, %3 " C " row_mask:0xf bank_mask:0xf\n"
  asm("s_nop 1\n" R4("quad_perm:[1,0,3,2]") R4("quad_perm:[2,3,0,1]") R4("row_half_mirror") : "+v"(a), "+v"(b), "+v"(c), "+v"(d));
#undef R4
}
DI float row16_max(float v) {
  v = fmaxf(v, dppf<0xB1>(v)); v = fmaxf(v, dppf<0x4E>(v));
  v = fmaxf(v, dppf<0x128>(v)); v = fmaxf(v, dppf<0x124>(v));
  return v;
}
DI float x16_max(float v) {
  const auto r = __builtin_amdgcn_permlane16_swap(__float_as_uint(v), __float_as_uint(v), false, false);
  return fmaxf(__uint_as_float(r[0]), __uint_as_float(r[1]));
}
DI float x32_max(float v) {
  const auto r = __builtin_amdgcn_permlane32_swap(__float_as_uint(v), __float_as_uint(v), false, false);
  return fmaxf(__uint_as_float(r[0]), __uint_as_float(r[1]));
}
DI float wave_max(float v) { return x32_max(x16_max(row16_max(v))); }

DI float dot2(u32 a, u32 b, float c) {
  return __builtin_amdgcn_fdot2_f32_bf16(__builtin_bit_cast(bf16x2_t, a), __builtin_bit_cast(bf16x2_t, b), c, false);
}
DI void fma8b(float* acc, u32 sbits, u32x4 w) {
  const u32 sh = sbits << 16;
  acc[0] = dot2(w.x, sbits, acc[0]); acc[1] = dot2(w.x, sh, acc[1]);
  acc[2] = dot2(w.y, sbits, acc[2]); acc[3] = dot2(w.y, sh, acc[3]);
  acc[4] = dot2(w.z, sbits, acc[4]); acc[5] = dot2(w.z, sh, acc[5]);
  acc[6] = dot2(w.w, sbits, acc[6]); acc[7] = dot2(w.w, sh, acc[7]);
}

DI unsigned fsw(unsigned row) { return ((row & 3) << 2) | ((row >> 2) & 3); }
DI unsigned off_b(unsigned row, unsigned ch) { return 256u * row + 16u * (ch ^ fsw(row)); }
DI unsigned tr_addr(unsigned lane, unsigned c, unsigned ks, unsigned t) {
  const unsigned h = lane >> 5, blk = (lane >> 4) & 1, q = (lane & 15) >> 2, p = lane & 3;
  return off_b(16 * ks + 8 * h + 4 * t + q, 4 * c + 2 * blk + (p >> 1)) + 8 * (p & 1);
}

typedef __amdgpu_buffer_rsrc_t rsrc_t;
DI u32x4 ld128(rsrc_t rs, int off) { return __builtin_amdgcn_raw_buffer_load_b128(rs, off, 0, AUX_SC1); }
DI u32x2 ld64(rsrc_t rs, int off) { return __builtin_amdgcn_raw_buffer_load_b64(rs, off, 0, AUX_SC1); }
DI void st64(rsrc_t rs, int off, u32 lo, u32 hi) { u32x2 v; v.x = lo; v.y = hi; __builtin_amdgcn_raw_buffer_store_b64(v, rs, off, 0, AUX_SC1); }
DI void st128(rsrc_t rs, int off, u32x4 v) { __builtin_amdgcn_raw_buffer_store_b128(v, rs, off, 0, AUX_SC1); }
#define BAR() do { asm volatile("s_waitcnt lgkmcnt(0)" ::: "memory"); __builtin_amdgcn_s_barrier(); asm volatile("" ::: "memory"); } while (0)
#define BAR0() do { __builtin_amdgcn_sched_barrier(0); asm volatile("" ::: "memory"); __builtin_amdgcn_s_barrier(); asm volatile("" ::: "memory"); __builtin_amdgcn_sched_barrier(0); } while (0)
#define PIN(x) asm volatile("" : "+v"(x))
constexpr u32 POISON = 0xFFFFFFFFu;
DI bool pois4(u32x4 v) { return max(max(v.x, v.y), max(v.z, v.w)) == POISON; }
DI u32 nopois(u32 v) { return min(v, 0xFFFFFFFEu); }
DI u32 fbits(float v) { return nopois(__float_as_uint(v)); }

DI u32x2 mk_gran(float v, u32 tlo, u32 thi) {
  u32 b = __float_as_uint(v);
  if ((b & 0x7F800000u) != 0x7F800000u) b += 0x80u;
  u32x2 g; g.x = (b >> 8) | (tlo << 24); g.y = (tlo >> 8) | (thi << 24);
  return g;
}
DI void st_gran2(rsrc_t rs, int off, float v0, float v1, u32 tlo, u32 thi) {
  const u32x2 a = mk_gran(v0, tlo, thi), b = mk_gran(v1, tlo, thi);
  u32x4 v; v.x = a.x; v.y = a.y; v.z = b.x; v.w = b.y;
  st128(rs, off, v);
}
DI bool gran_ok(u32 lo, u32 hi, u32 tlo, u32 thi) { return (((lo >> 24) ^ (tlo & 0xFFu)) | (hi ^ ((tlo >> 8) | (thi << 24)))) == 0u; }
DI float gran_val(u32 lo) { return __uint_as_float(lo << 8); }

template <int N, int SLP = 1> DI void gather128(rsrc_t rs, const int (&off)[N], u32x4 (&v)[N]) {
#pragma unroll
  for (int i = 0; i < N; ++i) v[i] = ld128(rs, off[i]);
  _Pragma("unroll 1") for (u32 spins = 0; spins < SPIN_CAP; ++spins) {
    bool need[N], bad = false;
#pragma unroll
    for (int i = 0; i < N; ++i) { need[i] = pois4(v[i]); bad = bad || need[i]; }
    if (!__any(bad)) break;
    __builtin_amdgcn_s_sleep(SLP);
    asm volatile("" ::: "memory");
    __builtin_amdgcn_sched_barrier(0);
#pragma unroll
    for (int i = 0; i < N; ++i)
      if (need[i]) v[i] = ld128(rs, off[i]);
  }
}
template <int N> DI void waitvm() { asm volatile("s_waitcnt vmcnt(%0)" ::"n"(N) : "memory"); }
constexpr u32 LSPIN_CAP = 1u << 22;
DI void lds_wait(volatile LAS u32* p, u32 val) {
  _Pragma("unroll 1") for (u32 n = 0; *p < val && n < LSPIN_CAP; ++n) __builtin_amdgcn_s_sleep(1);
  asm volatile("" ::: "memory");
}
#define WAVE_LDS_SYNC() do { __builtin_amdgcn_fence(__ATOMIC_RELEASE, "wavefront"); __builtin_amdgcn_wave_barrier(); __builtin_amdgcn_fence(__ATOMIC_ACQUIRE, "wavefront"); } while (0)
#define SB() __builtin_amdgcn_sched_barrier(0)
#define RARE(c) __builtin_expect(!!(c), 0)

constexpr int PS = 28672;
constexpr int L_QI = 4 * PS;
constexpr int L_P = L_QI + 32768;
constexpr int L_REF = L_P + 11264;
constexpr int L_MX = L_REF + 1408;
constexpr int L_ALW = L_MX + 128;
constexpr int L_DUMP = L_ALW + 512;
constexpr int L_QOFF = 131072;
constexpr int L_H1RED = L_QOFF;
constexpr int L_RED = L_QOFF + 16384;
constexpr int L_Z = L_RED, L_HH = L_RED + 7 * 1088, L_ZERO = L_HH + 64, L_CF = L_ZERO + 64;
constexpr int L_MISC = L_QOFF + 30720;
constexpr int L_CTR = L_MISC;
constexpr int L_WMAX = L_MISC + 256;
constexpr int L_ML = L_MISC + 512;

#define Z4 (u32x4{0u, 0u, 0u, 0u})
DI float max5(f32x4 v, float e) { return fmaxf(fmaxf(fmaxf(fmaxf(v.x, v.y), v.z), v.w), e); }

template <bool NT> DI void dma(u32 m, u32 voff, const void* sb) {
  if (NT) asm volatile("s_mov_b32 m0, %0\n s_nop 0\n global_load_lds_dwordx4 %1, %2 nt" ::"s"(m), "v"(voff), "s"(sb) : "memory");
  else asm volatile("s_mov_b32 m0, %0\n s_nop 0\n global_load_lds_dwordx4 %1, %2" ::"s"(m), "v"(voff), "s"(sb) : "memory");
}
DI void dma_sc1(u32 m, u32 voff, const void* sb) {
  asm volatile("s_mov_b32 m0, %0\n s_nop 0\n global_load_lds_dwordx4 %1, %2 sc1" ::"s"(m), "v"(voff), "s"(sb) : "memory");
}
DI void lds_add(int idx) {
  SB();
  asm volatile("s_mov_b64 exec, 1\n ds_add_u32 %0, %1 offset:%2\n s_mov_b64 exec, -1" ::"v"((u32)L_CTR), "v"(1u), "n"(idx * 4) : "memory");
  SB();
}
DI u32 spin_at(u32 a, u32 val) {
  u32 t, c, m, cap;
  asm volatile("s_mov_b32 %3, %6\n 1: ds_read_b32 %0, %4\n s_waitcnt lgkmcnt(0)\n v_readfirstlane_b32 %1, %0\n s_and_b32 %2, %1, 255\n s_cmp_ge_u32 %2, %5\n s_cbranch_scc1 2f\n"
               "s_sub_u32 %3, %3, 1\n s_cbranch_scc0 1b\n 2:"
               : "=&v"(t), "=&s"(c), "=&s"(m), "=&s"(cap) : "v"(a), "s"(val), "n"(LSPIN_CAP) : "memory", "scc");
  return c;
}
DI void lds_spin(volatile LAS u32* p, u32 val) { spin_at((u32)(uintptr_t)p, val); }
enum { C_SOK = 0, C_BH = 4, C_PD = 12, C_H1 = 18, C_PAR = 19, C_VOTE = 20, C_FREE = 22, C_QRDY = 23, C_MXC = 25, C_SD = 26, C_GX = 27, C_ML = 28, C_MK = 32, C_P = 44 };

DI u32 kv_mark(int nm, int skip) { return L_CTR + 4 * C_MK + __builtin_amdgcn_readfirstlane(4 * nm + (nm >= skip ? 4 : 0)); }
#define KV_TS
#define KV_TSO
#define KV_TSA
#define KV_TSP
#define KV_TRY(l)                                                                                                        \
  "7: s_getreg_b32 %[h], hwreg(HW_REG_IB_STS)\n s_and_b32 %[h], %[h], 0xC0000F\n s_max_i32 %[h], %[h], %[x]\n s_sub_i32 %[h], %[h], %[x]\n s_cmp_gt_i32 %[h], %[k4]\n s_cbranch_scc1 " l "\n" \
  "s_waitcnt vmcnt(%[x4])\n s_cmp_eq_u32 %[k4], 4\n s_cbranch_scc1 8f\n s_waitcnt vmcnt(%[x])\n"                        \
  "8: v_subrev_u32 %[t], %[k4], %[ml]\n s_mov_b64 exec, 1\n ds_add_u32 %[t], %[one] offset:12\n s_mov_b64 exec, -1\n"   \
  "s_sub_i32 %[k4], %[k4], 4\n" KV_TS "s_branch 7b\n"
template <int X> DI void kv_try(int& k4, u32 ml, u32 one KV_TSA) {
  u32 t, h;
  asm volatile(KV_TRY("9f") "9:" : [t] "=&v"(t), [h] "=&s"(h), [k4] "+s"(k4) KV_TSO : [ml] "v"(ml), [one] "v"(one), [x] "n"(X), [x4] "n"(X + 4) : "memory", "scc");
}
template <int X> DI u32 spin_try(int idx, u32 val, int& k4, u32 ml, u32 one KV_TSA) {
  u32 t, h, c, cap;
  asm volatile("s_mov_b32 %[cap], %[n]\n 1: ds_read_b32 %[t], %[ml] offset:%[o]\n s_waitcnt lgkmcnt(0)\n v_readfirstlane_b32 %[c], %[t]\n s_and_b32 %[h], %[c], 255\n"
               "s_cmp_ge_u32 %[h], %[v]\n s_cbranch_scc1 2f\n" KV_TRY("9f") "9: s_sub_u32 %[cap], %[cap], 1\n s_cbranch_scc0 1b\n 2:"
               : [t] "=&v"(t), [h] "=&s"(h), [c] "=&s"(c), [cap] "=&s"(cap), [k4] "+s"(k4) KV_TSO
               : [ml] "v"(ml), [one] "v"(one), [o] "n"(4 * idx), [v] "n"(val), [n] "n"(LSPIN_CAP), [x] "n"(X), [x4] "n"(X + 4) : "memory", "scc");
  return c;
}
template <int W> DI void kv_load(const u16* ckv, u32 vl, int lw, u32 g0, u32 g1, u32 m, int n0, int n1, int& nm, int skip) {
#pragma unroll 1
  for (int n = n0; n < n1; ++n) {
#define KV_POST(k) ".if (%[wm] | 4) == " #k "\n s_cmp_gt_i32 %[nm], %[n2]\n s_cbranch_scc1 20f\n s_mov_b64 exec, 1\n ds_add_u32 %[ma], %[one]\n s_mov_b64 exec, -1\n s_add_i32 %[nm], %[nm], 1\n 20:\n .endif\n"
#define KV_W(o, k) "s_waitcnt vmcnt(%[wm])\n" KV_POST(k) "global_load_lds_dwordx4 %[vo], %[p] offset:" #o " nt\n"
#define KV_M "s_add_u32 m0, m0, %[ms]\n"
    asm volatile("s_mov_b32 m0, %[m]\n" KV_W(0, 4) KV_M KV_W(256, 5) KV_M KV_W(512, 6) KV_M KV_W(768, 7)
                 : [nm] "+s"(nm)
                 : [m] "s"(m), [vo] "v"(vl + g0 + ((lw + n) & 3) * 4096), [p] "s"(ckv), [ma] "v"(kv_mark(nm, skip)),
                   [one] "v"(1u), [n2] "s"(__builtin_amdgcn_readfirstlane(n - (W > 4 ? 2 : 1))), [wm] "n"(W - 1), [ms] "n"(PS - 256)
                 : "memory", "scc");
    g0 = g1;
    g1 += 192 * 16384;
    m += n + 1 == skip ? 8192 : 4096;
  }
}
DI void until(u32 t0, u32 dt) {
  while ((u32)__builtin_amdgcn_s_memrealtime() - t0 < dt) __builtin_amdgcn_s_sleep(2);
}
DI bf16x8 tr_frag(unsigned char* lds, u32 a0, u32 a1) {
  const s16x4 b0 = __builtin_amdgcn_ds_read_tr16_b64_v4i16((LAS s16x4*)(lds + a0));
  const s16x4 b1 = __builtin_amdgcn_ds_read_tr16_b64_v4i16((LAS s16x4*)(lds + a1));
  bf16x8 v;
  v[0] = b0[0]; v[1] = b0[1]; v[2] = b0[2]; v[3] = b0[3]; v[4] = b1[0]; v[5] = b1[1]; v[6] = b1[2]; v[7] = b1[3];
  return v;
}
DI float sum4(f32x4 v) { const f32x2 t = f32x2{v.x, v.y} + f32x2{v.z, v.w}; return t.x + t.y; }

DI void ln_stats(u32x4 yv, float& mb, float& rsd) {
  const float a1 = dot2(yv.x, 0x3F803F80u, dot2(yv.y, 0x3F803F80u, 0.f)) + dot2(yv.z, 0x3F803F80u, dot2(yv.w, 0x3F803F80u, 0.f));
  const float a2 = dot2(yv.x, yv.x, dot2(yv.y, yv.y, 0.f)) + dot2(yv.z, yv.z, dot2(yv.w, yv.w, 0.f));
  const float sr = x16_sum(row16_sum(rs32(a1, a2))) * (1.f / 512.f);
  const float s1 = __uint_as_float(__builtin_amdgcn_readlane(__float_as_uint(sr), 0)), s2 = __uint_as_float(__builtin_amdgcn_readlane(__float_as_uint(sr), 32));
  const float var = rbf(fmaxf(s2 - s1 * s1, 0.f));
  mb = rbf(s1);
  const float sd = rbf(__builtin_amdgcn_sqrtf(rbf(var + 1e-5f)));
  rsd = __builtin_amdgcn_rcpf(sd);
  rsd = rsd * (2.f - sd * rsd);
}

__global__ __launch_bounds__(512, 2) void k_main(const u16* __restrict__ x, const u16* __restrict__ ckv,
                                                 const u16* __restrict__ w_q, const u16* __restrict__ w_uk,
                                                 const u16* __restrict__ w_uv, const u16* __restrict__ w_o,
                                                 const u16* __restrict__ w_gate, const u16* __restrict__ w_up,
                                                 const u16* __restrict__ w_down, const u16* __restrict__ cvec,
                                                 const u16* __restrict__ w_mod, u16* __restrict__ out,
                                                 unsigned char* ws) {
  __shared__ __attribute__((aligned(16))) unsigned char lds[163840];
  const int w = __builtin_amdgcn_readfirstlane(threadIdx.x >> 6), j = blockIdx.x;
  int lane = threadIdx.x & 63;
  const int xc = j & 7, ii = j >> 3;
  STAMP_DECL;
  STAMP(0);
  u32x2 ev;
  u32 xm, vh;
  {
    u64 p, pc, pw;
    u32 a, b, c, d, f, t;
    asm volatile("s_lshr_b32 %[f], %[j], 6\n s_cmp_eq_u32 %[f], 1\n s_cselect_b32 %[a], 0x50, 16\n"
                 "s_load_dwordx2 %[p], %[ka], %[a]\n s_load_dwordx2 %[pw], %[ka], 0x60\n"
                 "s_lshr_b32 %[d], %[j], 3\n s_cmp_lg_u32 %[w], 7\n s_cbranch_scc1 2f\n"
                 "s_and_b32 %[a], %[d], 15\n s_lshl_b32 %[a], %[a], 12\n v_lshl_add_u32 %[t], %[lane], 6, %[a]\n v_min_u32 %[t], %[pfl], %[t]\n"
                 "s_getpc_b64 %[pc]\n s_mov_b32 m0, %[dump]\n s_nop 0\n global_load_lds_dword %[t], %[pc]\n"
                 "2: s_load_dwordx2 %[pc], %[ka], 0x48\n s_and_b32 %[c], %[j], 7\n s_lshl_b32 %[c], %[c], 10\n s_cmp_eq_u32 %[f], 0\n s_cbranch_scc0 3f\n"
                 "s_and_b32 %[a], %[d], 1\n s_lshl1_add_u32 %[a], %[w], %[a]\n s_lshl_b32 %[a], %[a], 18\n s_add_u32 %[c], %[c], %[a]\n"
                 "s_lshr_b32 %[a], %[d], 1\n s_lshl_b32 %[a], %[a], 8\n s_add_u32 %[c], %[c], %[a]\n s_lshl_b32 m0, %[w], 14\n"
                 "v_lshrrev_b32 %[t], 4, %[lane]\n v_mul_u32_u24 %[t], 0x204, %[t]\n v_and_b32 %[vh], 15, %[lane]\n v_xor_b32 %[vh], %[vh], %[t]\n"
                 "v_lshl_add_u32 %[vh], %[vh], 4, %[c]\n v_xor_b32 %[t], 0x8010, %[vh]\n s_waitcnt lgkmcnt(0)\n"
                 "global_load_lds_dwordx4 %[vh], %[p]\n s_add_u32 m0, m0, 0x400\n s_nop 0\n"
                 "global_load_lds_dwordx4 %[t], %[p]\n s_add_u32 m0, m0, 0x400\n v_xor_b32 %[t], 0x10020, %[vh]\n"
                 "global_load_lds_dwordx4 %[t], %[p]\n s_add_u32 m0, m0, 0x400\n v_xor_b32 %[t], 0x18030, %[vh]\n"
                 "global_load_lds_dwordx4 %[t], %[p]\n s_branch 1f\n"
                 "3: s_lshl2_add_u32 %[d], %[d], %[w]\n s_sub_u32 %[d], %[d], 68\n s_lshl_b32 %[d], %[d], 16\n s_add_u32 %[d], %[d], %[c]\n"
                 "s_and_b32 %[a], %[j], 3\n s_lshl2_add_u32 %[b], %[a], %[w]\n s_mul_i32 %[b], %[b], 0x30000\n s_lshl_b32 %[a], %[a], 8\n"
                 "v_lshl_add_u32 %[xm], %[lane], 2, %[a]\n"
                 "s_and_b32 %[a], %[j], 60\n s_lshl_b32 %[a], %[a], 5\n s_add_u32 %[b], %[b], %[a]\n"
                 "s_lshl_b32 %[c], %[w], 12\n s_add_u32 %[c], %[c], %[mq]\n s_lshl_b32 %[a], %[w], 10\n s_add_u32 %[a], %[a], %[mh]\n"
                 "s_cmp_eq_u32 %[f], 1\n s_cselect_b32 %[d], %[b], %[d]\n s_cselect_b32 m0, %[c], %[a]\n s_cselect_b32 %[a], 48, 1\n s_cselect_b32 %[c], 0, 4\n"
                 "v_lshrrev_b32 %[t], 3, %[lane]\n v_mul_u32_u24 %[t], %[a], %[t]\n v_and_b32 %[vh], 7, %[lane]\n v_lshl_add_u32 %[vh], %[t], 3, %[vh]\n"
                 "v_lshl_add_u32 %[vh], %[vh], 4, %[d]\n"
                 "s_cselect_b32 %[b], 6, 1\n s_cselect_b32 %[a], 0x400, 0\n s_lshl_b32 %[b], %[b], 13\n"
                 "s_xor_b32 %[c], %[c], %[w]\n s_bitcmp1_b32 %[c], 2\n v_mov_b32 %[t], 0\n"
                 "s_waitcnt lgkmcnt(0)\n s_cbranch_scc1 1f\n s_cmp_eq_u32 %[f], 1\n s_cbranch_scc0 4f\n"
                 "global_load_dword %[xm], %[xm], %[pc]\n global_load_dwordx2 %[ev], %[t], %[pw] sc1\n"
                 "4: global_load_lds_dwordx4 %[vh], %[p]\n s_add_u32 m0, m0, %[a]\n v_add_u32 %[t], %[b], %[vh]\n"
                 "global_load_lds_dwordx4 %[t], %[p]\n s_add_u32 m0, m0, %[a]\n v_add_u32 %[t], %[b], %[t]\n"
                 "global_load_lds_dwordx4 %[t], %[p]\n s_add_u32 m0, m0, %[a]\n v_add_u32 %[t], %[b], %[t]\n"
                 "global_load_lds_dwordx4 %[t], %[p]\n"
                 "1:\n"
                 : [xm] "=&v"(xm), [ev] "=&v"(ev), [vh] "=&v"(vh), [t] "=&v"(t), [p] "=&s"(p), [pc] "=&s"(pc), [pw] "=&s"(pw), [a] "=&s"(a), [b] "=&s"(b), [c] "=&s"(c),
                   [d] "=&s"(d), [f] "=&s"(f)
                 : [j] "s"(j), [w] "s"(w), [lane] "v"(lane), [ka] "s"(__builtin_amdgcn_kernarg_segment_ptr()), [pfl] "n"(PF_LEN), [dump] "n"(L_DUMP),
                   [mq] "n"(L_QOFF + 8192), [mh] "n"(L_P)
                 : "memory", "scc");
  }
  SB();
  const u32 t0 = (u32)__builtin_amdgcn_s_memrealtime() - 35u;
  const bool isQ = j < 64, isS0 = j >= 64 && j < 128;
  const int hq = xc * 4 + (ii >> 1), dh = ii & 1, sj = j - 64;
  const int lw = w - 4, myh = xc >> 2;
  int col = lane & 15, g4 = lane >> 4, tq = col >> 2, tp = lane & 3;
  const rsrc_t rs = __builtin_amdgcn_make_buffer_rsrc((void*)ws, 0, WS_TOTAL, 0x00020000);
  volatile LAS u32* ctr = (volatile LAS u32*)(lds + L_CTR);
  SB();
  STAMP(25);
  if (w < 2) *(u32*)(lds + (w == 0 ? L_CTR + lane * 4 : L_MX + (lane & 31) * 4)) = w == 0 ? 0u : 0xFF800000u;
  BAR();
  const bool tl2 = isS0;
  const int t3 = xc < 4 ? (ii < 8 ? xc * 8 + ii : -1) : (ii < 6 ? 8 + xc * 6 + ii : -1);
  const int t4 = (ii >= 8 && ii < 10) ? xc * 2 + (ii - 8) : -1;
  const u32 owno = (w >> 1) * 32768 + 8192 + (w & 1) * 12288;
  const int ks = (xc & 3) * 2 + myh, cg = ii & 7;
  const int h = (j >> 2) & 31, c4 = j & 3;
  u32x4 wv[8];
  u32 tlo, thi;
  int gx;
#define RELANE() do { asm volatile("v_mbcnt_lo_u32_b32 %0, -1, 0\n v_mbcnt_hi_u32_b32 %0, -1, %0" : "=v"(lane)); col = lane & 15; g4 = lane >> 4; tq = col >> 2; tp = lane & 3; } while (0)
#define TAG_OF(e0, e1)                                                                                                  \
  do {                                                                                                                  \
    const u64 tag = (((u64)(e1) << 32) | (e0)) + 1ull;                                                                  \
    tlo = __builtin_amdgcn_readfirstlane((u32)tag); thi = __builtin_amdgcn_readfirstlane((u32)(tag >> 32));             \
  } while (0)
  const u32x4 P4 = {POISON, POISON, POISON, POISON};
#define COUNT_VOTES(PRE)                                                                                                \
  do {                                                                                                                  \
    u32x2 a_ = {0u, 0u};                                                                                                \
    if (PRE) a_ = *(const u32x2*)(lds + L_QI + lane * 8);                                                               \
    _Pragma("unroll 1") for (u32 spins = 0; spins < SPIN_CAP; ++spins) {                                                \
      if (((PRE) || spins) && __all(gran_ok(a_.x, a_.y, tlo, thi))) break;                                              \
      if (spins) __builtin_amdgcn_s_sleep(8);                                                                           \
      asm volatile("" ::: "memory");                                                                                    \
      a_ = ld64(rs, WS_VOTE + xc * 2048 + lane * 8);                                                                    \
    }                                                                                                                   \
    const float vt_ = wave_sum((float)(a_.x & 1023u));                                                                  \
    if (lane == 0) ctr[C_PAR] = G_OF(vt_);                                                                              \
    VOTE_STAMP(vt_);                                                                                                    \
    STAMP(8);                                                                                                           \
  } while (0)
#define S_POLL(a_)                                                                                                      \
  _Pragma("unroll 1") for (u32 spins = 0; spins < SPIN_CAP && RARE(__any(((a_.x ^ tlo) | (a_.y ^ thi)) != 0u)); ++spins) { \
    __builtin_amdgcn_s_sleep(8);                                                                                        \
    asm volatile("" ::: "memory");                                                                                      \
    a_ = ld64(rs, WS_SFLAG + xc * 2048 + lane * 8);                                                                     \
  }
#define MB_OFF(i) (WS_MODB + ((lane >> 4) * 2048 + ((i) >> 1) * 512 + 128 * lw + 64 * ((i) & 1) + (lane & 15) * 4) * 4)

  if (!isQ && w >= 4) {
    if (isS0) {
      if (w == 4) {
        const u32x2 e4 = ld64(rs, WS_EPOCH);
        st128(rs, WS_QABS + sj * 1024 + lane * 16, P4);
        st128(rs, lane < 32 ? WS_MODB + sj * 512 + lane * 16 : WS_TOTAL, P4);
        st128(rs, WS_PROBE + il(sj * 1024 + lane * 16, 0), Z4);
        st128(rs, WS_PROBE + il(sj * 1024 + lane * 16, 1), Z4);
        waitvm<0>();
        TAG_OF(e4.x, e4.y);
        if (lane < 8) st64(rs, WS_SFLAG + lane * 2048 + sj * 8, tlo, thi);
        STAMP(27);
      }
      lds_wait(ctr + C_H1, 4u);
    } else {
#pragma unroll
      for (int i = 4; i < 16; ++i) { waitvm<7>(); dma<false>(L_P + w * 1024, vh + (i & 7) * 8192, i < 8 ? w_q : w_uk); }
    }
    const u32 vl = g4 * 1024 + ((col ^ ((g4 << 2) | lw)) << 4);
    int nm = 0, ns = isS0 ? 6 : 7;
    const int skip = isS0 ? 4 : 99;
    if (isS0) kv_load<WA0>(ckv, vl, lw, sj * 16384, (sj + 192) * 16384, lw * 1024, 0, 2, nm, skip);
    else kv_load<WIN>(ckv, vl, lw, (1792 + j) * 16384, sj * 16384, lw * 1024, 0, 2, nm, skip);
    const rsrc_t rw = __builtin_amdgcn_make_buffer_rsrc((void*)(isS0 ? w_o : w_uv), 0, 4194304, 0x00020000);
    int wo = isS0 ? (ks * 512 + 64 * w + (lane >> 3)) * 1024 + cg * 128 + (lane & 7) * 16 : (c4 * 128 + 32 * lw + g4) * 8192 + h * 256 + col * 16;
    const int wst = isS0 ? 8192 : 32768;
    PIN(wo);
#define WV_LD(i) do { SB(); wv[i] = __builtin_amdgcn_raw_buffer_load_b128(rw, wo, (i) * wst, 2); wv[(i) + 1] = __builtin_amdgcn_raw_buffer_load_b128(rw, wo, ((i) + 1) * wst, 2); SB(); } while (0)
    u32 va[8];
#pragma unroll
    for (int i = 0; i < 8; ++i) { va[i] = lw * PS + tr_addr(lane, i >> 1, 0, i & 1); PIN(va[i]); }
    int eg[8];
#pragma unroll
    for (int u = 0; u < 8; ++u) { eg[u] = WS_PART + il21(((4 * (u >> 1) + 2 * (g4 >> 1) + (u & 1)) * 4 + lw) * 65536 + sj * 256 + col * 16, g4 & 1); PIN(eg[u]); }
    u32 pr_ = L_P + lane * 16;
    PIN(pr_);
    u32 ml = L_CTR + 4 * (C_MK + 7), one = 1u;
    PIN(ml); PIN(one);
    bf16x8 vf[4][4];
#define V_READ(i, slot) do { asm volatile("" ::: "memory"); SB(); _Pragma("unroll") for (int c = 0; c < 4; ++c) vf[i][c] = tr_frag(lds, va[2 * c] + (slot) * 4096, va[2 * c + 1] + (slot) * 4096); SB(); } while (0)
    SB();
    kv_load<WIN>(ckv, vl, lw, (sj + (isS0 ? 384 : 192)) * 16384, (sj + (isS0 ? 576 : 384)) * 16384, lw * 1024 + 8192, 2, ns, nm, skip);
    lds_wait(ctr + C_MK + 3, 4u);
    V_READ(0, 0); V_READ(1, 1); V_READ(2, 2); V_READ(3, 3);
    lds_add(C_FREE);
#pragma unroll
    for (int i = 0; i < 16; ++i) PIN(vf[i >> 2][i & 3]);
    STAMP(2);
    lds_wait(ctr + C_FREE, 8u);
    STAMP(5);
    kv_load<WIN>(ckv, vl, lw, (sj + 6 * 192) * 16384, (sj + 7 * 192) * 16384, lw * 1024, ns, ns + 4, nm, skip);
    int k4 = 4;
    STAMP(3);
    f32x16 acc[4];
#pragma unroll
    for (int c = 0; c < 4; ++c)
#pragma unroll
      for (int i = 0; i < 16; ++i) acc[c][i] = 0.f;
    bf16x8 pf[4], pn[4];
    u32 c_;
#define P_SPIN(g, n) c_ = spin_try<(g) ? 8 : 0>(C_P + (g) - C_MK - 7, n, k4, ml, one KV_TSP)
#define TRY() kv_try<8>(k4, ml, one KV_TSP)
#define P_READ(d, i0, i1, T0) _Pragma("unroll") for (int i = (i0); i < (i1); ++i) d[i] = *(volatile LAS bf16x8*)(lds + pr_ + ((T0) + i) * 1024)
#define PV(p, i) do { SB(); _Pragma("unroll") for (int c = 0; c < 4; ++c) acc[c] = __builtin_amdgcn_mfma_f32_32x32x16_bf16(p[i], vf[i][c], acc[c], 0, 0, 0); SB(); } while (0)
#define GX_USE(v) do { gx = __builtin_amdgcn_readfirstlane(v); _Pragma("unroll") for (int u = 0; u < 8; ++u) { eg[u] ^= gx; PIN(eg[u]); } } while (0)
#define MK_WAIT(T) spin_try<8>((T) - 7, 4u, k4, ml, one KV_TSP)
#define PVV(p, i, d, slot)                                                                                               \
  _Pragma("unroll") for (int c = 0; c < 4; ++c) {                                                                        \
    SB();                                                                                                                \
    acc[c] = __builtin_amdgcn_mfma_f32_32x32x16_bf16(p[i], vf[i][c], acc[c], 0, 0, 0);                                   \
    SB();                                                                                                                \
    vf[d][c] = tr_frag(lds, va[2 * c] + (slot) * 4096, va[2 * c + 1] + (slot) * 4096);                                   \
    SB();                                                                                                                \
  }
#define CT(i) (*(volatile LAS u32*)(lds + ml + 4 * ((i) - C_MK - 7)))
    P_SPIN(0, 4u);
    P_READ(pf, 0, 4, 0);
    const u32 k7 = CT(C_MK + 7);
    asm volatile("" ::: "memory");
    STAMP(6);
    PVV(pf, 0, 0, 4); kv_try<0>(k4, ml, one KV_TSP); WV_LD(0);
    PVV(pf, 1, 1, 5); kv_try<2>(k4, ml, one KV_TSP); WV_LD(2);
    PVV(pf, 2, 2, 6); WV_LD(4);
    u32 cv = CT(C_P + 1);
    P_READ(pn, 0, 4, 4);
    PVV(pf, 3, 3, 0); WV_LD(6);
    if (RARE(__builtin_amdgcn_readfirstlane(k7) < 4u)) { MK_WAIT(7); V_READ(0, 4); V_READ(1, 5); V_READ(2, 6); V_READ(3, 0); }
    STAMP(9);
    c_ = __builtin_amdgcn_readfirstlane(cv);
    if (RARE((c_ & 255u) != 4u)) { P_SPIN(1, 4u); P_READ(pn, 0, 4, 4); }
    int slow = -1;
    if (RARE(c_ > 255u)) slow = 4;
    else {
      if (!isS0) PV(pn, 0);
      TRY();
      const u32 k9 = CT(C_MK + 9);
      asm volatile("" ::: "memory");
      PVV(pn, 1, 0, 1); TRY();
      PVV(pn, 2, 1, 2);
      cv = CT(C_P + 2);
      P_READ(pf, 0, 2, 8);
      const u32 k10 = CT(C_MK + 10);
      asm volatile("" ::: "memory");
      PVV(pn, 3, 2, 3);
      if (RARE(__builtin_amdgcn_readfirstlane(k9) < 4u)) { MK_WAIT(9); V_READ(0, 1); V_READ(1, 2); }
      STAMP(10);
      c_ = __builtin_amdgcn_readfirstlane(cv);
      if (RARE((c_ & 255u) != 4u)) { P_SPIN(2, 4u); P_READ(pf, 0, 2, 8); }
      if (RARE(c_ > 255u)) slow = 8;
      else {
        TRY();
        PV(pf, 0);
        const u32 g0 = ctr[C_GX];
        u32 g1 = ctr[C_PAR];
        cv = CT(C_P + 3);
        P_READ(pf, 2, 3, 8);
        PV(pf, 1);
        STAMP(4);
        if (RARE(__builtin_amdgcn_readfirstlane(k10) < 4u)) { waitvm<8>(); TRY(); lds_spin(ctr + C_MK + 10, 4u); V_READ(2, 3); }
        if (RARE(__builtin_amdgcn_readfirstlane(g0) == 0u)) { lds_spin(ctr + C_GX, 1u); g1 = ctr[C_PAR]; }
        GX_USE(g1);
        STAMP(11);
        c_ = __builtin_amdgcn_readfirstlane(cv);
        _Pragma("unroll 1") for (u32 n_ = 0; RARE((c_ & 255u) != 2u) && n_ < LSPIN_CAP; ++n_) { cv = CT(C_P + 3); P_READ(pf, 2, 3, 8); c_ = __builtin_amdgcn_readfirstlane(cv); }
        if (RARE(c_ > 255u)) slow = 10;
        else PV(pf, 2);
      }
    }
    if (RARE(slow >= 0)) {
      MK_WAIT(10);
      float R = ceilf(*(const float*)(lds + L_MX + (lane & 31) * 4));
#pragma unroll 1
      for (int T = slow; T < 11; ++T) {
        if (T == 4 && isS0) continue;
        spin_at(L_CTR + 4 * (C_P + (T < 8 ? 1 : T < 10 ? 2 : 3)), T < 10 ? 4u : 2u);
        const float rf = *(const float*)(lds + L_REF + T * 128 + (lane & 31) * 4);
        u32x4 p = *(const u32x4*)(lds + pr_ + T * 1024);
        const u32 so_ = (T < 7 ? T : T - 7) * 4096;
#pragma unroll
        for (int c = 0; c < 4; ++c) vf[0][c] = tr_frag(lds, va[2 * c] + so_, va[2 * c + 1] + so_);
        const float Rn = fmaxf(R, rf), ps = __builtin_amdgcn_exp2f(rf - Rn);
        *(float*)(lds + L_ALW + lw * 128 + (lane & 31) * 4) = __builtin_amdgcn_exp2f(R - Rn);
        R = Rn;
        WAVE_LDS_SYNC();
#pragma unroll
        for (int i = 0; i < 4; ++i) {
          const f32x4 al = *(const f32x4*)(lds + L_ALW + lw * 128 + (lane >> 5) * 16 + i * 32);
#pragma unroll
          for (int c = 0; c < 4; ++c)
#pragma unroll
            for (int r = 0; r < 4; ++r) acc[c][4 * i + r] *= al[r];
          p[i] = pk2bf(lo16(p[i]) * ps, hi16(p[i]) * ps);
        }
        WAVE_LDS_SYNC();
        pf[0] = __builtin_bit_cast(bf16x8, p);
        PV(pf, 0);
      }
      if (slow != 10) { lds_spin(ctr + C_GX, 1u); GX_USE(ctr[C_PAR]); }
    }
    STAMP(30);
#pragma unroll
    for (int u = 0; u < 8; u += 2) {
      u32 c[8];
#pragma unroll
      for (int k = 0; k < 8; ++k) c[k] = pk2bf(acc[k & 2][2 * u + (k & 1) + (k >> 2) * 2], acc[(k & 2) + 1][2 * u + (k & 1) + (k >> 2) * 2]);
      SB();
#pragma unroll
      for (int k = 0; k < 2; ++k) {
        const auto r0 = __builtin_amdgcn_permlane16_swap(c[4 * k], c[4 * k + 1], false, false);
        const auto r1 = __builtin_amdgcn_permlane16_swap(c[4 * k + 2], c[4 * k + 3], false, false);
        st128(rs, eg[u + k], u32x4{r0[0], r1[0], r0[1], r1[1]});
      }
      SB();
    }
    STAMP(31);
    if (!isS0) {
#pragma unroll
      for (int i = 0; i < 8; ++i) *(u32x4*)(lds + lw * PS + 16384 + off_b(4 * i + g4, col)) = wv[i];
    }
    {
      int fo = lane < 16 ? (WS_LSE + il(sj * 512 + lw * 128 + (lane & 7) * 16, lane >> 3)) ^ gx : WS_TOTAL;
      lds_spin(ctr + C_ML, 1u);
      u32x4 ml4 = *(const u32x4*)(lds + L_WMAX + col * 16);
      PIN(fo); PIN(ml4);
      waitvm<0>();
      __builtin_amdgcn_s_waitcnt(0x0F70);
      st128(rs, fo, ml4);
      lds_add(C_PD);
    }
    STAMP(13);
  } else if (!isQ) {
    if (isS0) {
      u32 ta = L_QOFF + 8192 + (8 * g4 + tq) * 128 + w * 32 + tp * 8;
      u32 sa = L_H1RED + w * 256;
      int so = col < 2 ? WS_SC + (((sj >> 5) * 4 + (sj & 3)) * 512 + ((sj >> 2) & 7) * 64 + 16 * w + 4 * g4 + 2 * col) * 8 : WS_TOTAL;
      PIN(ta); PIN(so); PIN(sa);
      u32 c2, e0, e1;
      asm volatile("s_waitcnt vmcnt(4) ; HEAD_WAIT\n v_mov_b32 %0, %3\n v_mov_b32 %1, %4\n v_mov_b32 %2, %5" : "=&v"(c2), "=&v"(e0), "=&v"(e1) : "v"(xm), "v"(ev.x), "v"(ev.y) : "memory");
      TAG_OF(e0, e1);
      bf16x8 sf[4];
      u32x2 wt[8];
      asm volatile("ds_write_b32 %4, %5\n"
                   "ds_read_b128 %0, %6\n ds_read_b128 %1, %6 offset:64\n ds_read_b128 %2, %6 offset:128\n ds_read_b128 %3, %6 offset:192\n s_waitcnt lgkmcnt(0)"
                   : "=&v"(sf[0]), "=&v"(sf[1]), "=&v"(sf[2]), "=&v"(sf[3])
                   : "v"(sa + lane * 4), "v"(pk2bf(silu_bf(lo16(c2)), silu_bf(hi16(c2)))), "v"(sa + 16 * g4) : "memory");
      waitvm<0>();
      lds_add(C_BH);
      lds_spin(ctr + C_BH, 4u);
      STAMP(1);
      asm volatile("ds_read_b64_tr_b16 %0, %8\n ds_read_b64_tr_b16 %1, %8 offset:512\n ds_read_b64_tr_b16 %2, %8 offset:4096\n ds_read_b64_tr_b16 %3, %8 offset:4608\n"
                   "ds_read_b64_tr_b16 %4, %8 offset:8192\n ds_read_b64_tr_b16 %5, %8 offset:8704\n ds_read_b64_tr_b16 %6, %8 offset:12288\n ds_read_b64_tr_b16 %7, %8 offset:12800\n"
                   "s_waitcnt lgkmcnt(0)"
                   : "=&v"(wt[0]), "=&v"(wt[1]), "=&v"(wt[2]), "=&v"(wt[3]), "=&v"(wt[4]), "=&v"(wt[5]), "=&v"(wt[6]), "=&v"(wt[7]) : "v"(ta) : "memory");
      f32x4 d = {0.f, 0.f, 0.f, 0.f};
#pragma unroll
      for (int k = 0; k < 4; ++k)
        d = __builtin_amdgcn_mfma_f32_16x16x32_bf16(__builtin_bit_cast(bf16x8, u32x4{wt[2 * k].x, wt[2 * k].y, wt[2 * k + 1].x, wt[2 * k + 1].y}), sf[k], d, 0, 0, 0);
      st_gran2(rs, so, (col & 1) ? d[2] : d[0], (col & 1) ? d[3] : d[1], tlo, thi);
      lds_add(C_H1);
      STAMP(2);
      until(t0, SCP_T);
      dma_sc1(L_P + w * 1024, WS_SC + ((ii & 7) * 4 + w) * 1024 + lane * 16, ws);
    }
    u32 ab = off_b(col, 4 * g4), qv = WS_QABS + myh * 8192 + g4 * 1024 + w * 256 + ((col ^ (g4 << 2)) << 4);
    const int hb = 16 * (w & 1) + col;
    u32 pw = L_P + (g4 >> 1) * 512 + hb * 16 + (g4 & 1) * 8, mw = hb * 4;
    PIN(ab); PIN(qv); PIN(pw); PIN(mw);
    if (w == 2) {
      until(t0, QF_T);
      const u32x2 e2 = ld64(rs, WS_EPOCH);
      u32x2 f2 = ld64(rs, WS_SFLAG + xc * 2048 + lane * 8);
      TAG_OF(e2.x, e2.y);
      S_POLL(f2);
      lds_add(C_SOK);
      STAMP(3);
    }
    bf16x8 af[16], qf[16][2];
    u32 aa[16], qa[32];
#pragma unroll
    for (int i = 0; i < 16; ++i) {
      aa[i] = (ab ^ ((i & 3) << 4)) + (((w + (i >> 2)) & 3) * PS + w * 4096); PIN(aa[i]);
#pragma unroll
      for (int u = 0; u < 2; ++u) { qa[2 * i + u] = (ab ^ ((i & 3) << 4)) + (L_QI + ((w + (i >> 2)) & 3) * 8192 + ((u ^ w) & 1) * 4096); PIN(qa[2 * i + u]); }
    }
    const int sl1 = w == 3 ? 0 : 4 + w, sl2 = 1 + (w >> 1);
#define A_RD(i, o) af[i] = *(const bf16x8*)(lds + aa[i] + (o))
#define A_READ(i0, i1, o) _Pragma("unroll") for (int i = (i0); i < (i1); ++i) A_RD(i, o)
#define A_MOVE(i0, i1, d) _Pragma("unroll") for (int i = (i0); i < (i1); ++i) { aa[i] += (d) * 4096; PIN(aa[i]); }
#define Q_RD(i, u) qf[i][u] = *(const bf16x8*)(lds + qa[2 * (i) + (u)])
#define A_FIRST() do { A_READ(0, 16, 0); lds_add(C_FREE); _Pragma("unroll") for (int i = 0; i < 16; ++i) PIN(af[i]); STAMP(12); } while (0)
    u32 qd[8];
#pragma unroll
    for (int i = 0; i < 8; ++i) { qd[i] = (qv ^ ((i & 3) << 4)) + (i >> 1) * 16384 + (i & 1) * 4096; PIN(qd[i]); }
    bool have = false;
    _Pragma("unroll 1") while ((u32)__builtin_amdgcn_s_memrealtime() - t0 < E3_T) {
      if (__builtin_amdgcn_readfirstlane(ctr[C_MK + w]) >= 4u) { have = true; break; }
      __builtin_amdgcn_s_sleep(1);
    }
    asm volatile("" ::: "memory");
    if (have) A_FIRST();
    lds_wait(ctr + C_SOK, 1u);
    if (isS0) lds_wait(ctr + C_H1, 4u);
    until(t0, E3_T - 10);
    _Pragma("unroll 1") while ((u32)__builtin_amdgcn_s_memrealtime() - t0 < E3_T) {}
    STAMP(6);
#define QD(i) "global_load_lds_dwordx4 %[q" #i "], %[ws] sc1\n s_add_u32 m0, m0, 0x400\n s_nop 0\n"
    asm volatile("s_mov_b32 m0, %[m]\n s_nop 0\n" QD(0) QD(1) QD(2) QD(3) QD(4) QD(5) QD(6) QD(7)
                 ::[m] "s"(L_QI + w * 8192), [ws] "s"(ws), [q0] "v"(qd[0]), [q1] "v"(qd[1]), [q2] "v"(qd[2]), [q3] "v"(qd[3]), [q4] "v"(qd[4]), [q5] "v"(qd[5]), [q6] "v"(qd[6]), [q7] "v"(qd[7]) : "memory", "scc");
    if (RARE(!have)) { lds_wait(ctr + C_MK + w, 4u); A_FIRST(); }
    A_MOVE(0, 16, sl1 - w);
    u32 cb = L_CTR, cw = L_CTR + 4 * w, mx0 = mw + L_MX, mx1 = (mw ^ 64) + L_MX, pw0 = pw + w * 1024, pw1 = (pw ^ 256) + w * 1024;
    float ninf = -__builtin_inff();
    u32 one = 1u;
    PIN(ninf); PIN(one); PIN(cb); PIN(cw); PIN(mx0); PIN(mx1); PIN(pw0); PIN(pw1);
#define CTS(b, i) (*(volatile LAS u32*)(lds + (b) + 4 * (i)))
    _Pragma("unroll 1") for (u32 spins = 0; spins < SPIN_CAP; ++spins) {
      waitvm<0>();
#pragma unroll
      for (int i = 0; i < 8; ++i) Q_RD(i >> 1, i & 1);
      u32 mxu[2] = {0u, 0u};
#pragma unroll
      for (int i = 0; i < 8; ++i) { const u32x4 t_ = __builtin_bit_cast(u32x4, qf[i >> 1][i & 1]); mxu[i & 1] = max(max(mxu[i & 1], t_.x), t_.y); mxu[i & 1] = max(max(mxu[i & 1], t_.z), t_.w); }
      if (!RARE(__any(max(mxu[0], mxu[1]) == POISON))) break;
      STAMP(29);
      __builtin_amdgcn_s_sleep(2);
      const u64 b0 = __ballot(mxu[w & 1] == POISON), b1 = __ballot(mxu[~w & 1] == POISON);
#pragma unroll 1
      for (int i = 0; i < 8; ++i)
        if (((i < 4 ? b0 : b1) >> (4 * (i & 3))) & 0x000F000F000F000Full) dma_sc1(L_QI + w * 8192 + i * 1024, (qv ^ ((i & 3) << 4)) + (i >> 1) * 16384 + (i & 1) * 4096, ws);
    }
    lds_add(C_QRDY);
    STAMP(7);
    f32x4 sc[2], sn[2];
    sc[0] = sc[1] = sn[0] = sn[1] = f32x4{0.f, 0.f, 0.f, 0.f};
#define QK(d, i0, i1, NU)                                                                                                \
  do {                                                                                                                   \
    SB();                                                                                                                \
    _Pragma("unroll") for (int i = (i0); i < (i1); ++i) _Pragma("unroll") for (int u = 0; u < (NU); ++u)                 \
      d[u] = __builtin_amdgcn_mfma_f32_16x16x32_bf16(af[i], qf[i][u], d[u], 0, 0, 0);                                    \
    SB();                                                                                                                \
  } while (0)
#define MF(d, i, u) do { SB(); d[u] = __builtin_amdgcn_mfma_f32_16x16x32_bf16(af[i], qf[i][u], d[u], 0, 0, 0); SB(); } while (0)
    const bool idle1 = isS0 && w == 0;
    u32 c1;
    {
      const u32 c0 = CTS(cb, C_QRDY);
      QK(sc, 0, 2, 2);
      if (RARE(__builtin_amdgcn_readfirstlane(c0) < 4u)) lds_spin(ctr + C_QRDY, 4u);
      asm volatile("" ::: "memory");
#pragma unroll
      for (int i = 2; i < 4; ++i) { MF(sc, i, 0); Q_RD(2 * i, 0); Q_RD(2 * i, 1); MF(sc, i, 1); Q_RD(2 * i + 1, 0); Q_RD(2 * i + 1, 1); }
#pragma unroll
      for (int i = 4; i < 12; ++i) { MF(sc, i, 0); Q_RD(i + 4, 0); MF(sc, i, 1); Q_RD(i + 4, 1); }
      c1 = CTS(cw, C_MK + 4);
      asm volatile("" ::: "memory");
#pragma unroll
      for (int i = 12; i < 16; ++i) { MF(sc, i, 0); A_RD(2 * i - 24, 0); MF(sc, i, 1); A_RD(2 * i - 23, 0); }
    }
    STAMP(9);
    float mref[2], lsum[2] = {0.f, 0.f};
#define ADDC(i, v) do { SB(); asm volatile("s_mov_b64 exec, 1\n ds_add_u32 %0, %1 offset:%2\n s_mov_b64 exec, -1" ::"v"(cb), "v"(v), "n"(4 * (i)) : "memory"); SB(); } while (0)
#define SM(d, T, NU, G, P0, P1)                                                                                          \
  do {                                                                                                                   \
    SB();                                                                                                                \
    if (RARE(__any(max5(d[0], (NU) > 1 ? max5(d[1], ninf) : ninf) > TAU))) {                                             \
      sig = 257u;                                                                                                        \
      _Pragma("unroll") for (int u = 0; u < (NU); ++u) {                                                                 \
        const float up = ceilf(x32_max(x16_max(max5(d[u], 0.f))));                                                       \
        lsum[u] *= __builtin_amdgcn_exp2f(-up);                                                                          \
        mref[u] += up;                                                                                                   \
        d[u] -= up;                                                                                                      \
        *(float*)(lds + (mw ^ (u * 64)) + L_REF + (T) * 128) = mref[u];                                                  \
      }                                                                                                                  \
    }                                                                                                                    \
    u32x2 pk[2];                                                                                                         \
    _Pragma("unroll") for (int u = 0; u < (NU); ++u) {                                                                   \
      pk[u].x = pk2bf(__builtin_amdgcn_exp2f(d[u].x), __builtin_amdgcn_exp2f(d[u].y));                                   \
      pk[u].y = pk2bf(__builtin_amdgcn_exp2f(d[u].z), __builtin_amdgcn_exp2f(d[u].w));                                   \
      *(u32x2*)(lds + (u ? (P1) : (P0))) = pk[u];                                                                        \
    }                                                                                                                    \
    ADDC(C_P + (G), sig);                                                                                                \
    _Pragma("unroll") for (int u = 0; u < (NU); ++u) { lsum[u] = dot2(pk[u].x, 0x3F803F80u, dot2(pk[u].y, 0x3F803F80u, lsum[u])); PIN(lsum[u]); } \
    SB();                                                                                                                \
  } while (0)
#define REF_WRITE(T, NU) _Pragma("unroll") for (int u = 0; u < (NU); ++u) *(float*)(lds + (mw ^ (u * 64)) + L_REF + (T) * 128) = mref[u]
    u32 sig = one;
    {
      MF(sn, 0, 0); A_RD(8, 0); MF(sn, 0, 1);
      const float m0 = max5(sc[0], ninf), m1 = max5(sc[1], ninf);
      MF(sn, 1, 0);
      asm volatile("ds_max_f32 %0, %2\n ds_max_f32 %1, %3" ::"v"(mx0), "v"(mx1), "v"(m0), "v"(m1) : "memory");
      ADDC(C_MXC, one);
      MF(sn, 1, 1); A_RD(9, 0);
#pragma unroll
      for (int i = 2; i < 4; ++i) { MF(sn, i, 0); A_RD(2 * i + 6, 0); MF(sn, i, 1); A_RD(2 * i + 7, 0); }
      MF(sn, 4, 0);
      const u32 c0 = CTS(cb, C_MXC);
      mref[0] = *(volatile LAS float*)(lds + mx0);
      mref[1] = *(volatile LAS float*)(lds + mx1);
      MF(sn, 4, 1); MF(sn, 5, 0); A_RD(14, 0); MF(sn, 5, 1); A_RD(15, 0); MF(sn, 6, 0); MF(sn, 6, 1);
      if (RARE(__builtin_amdgcn_readfirstlane(c0) < 4u)) {
        lds_spin(ctr + C_MXC, 4u);
        mref[0] = *(volatile LAS float*)(lds + mx0);
        mref[1] = *(volatile LAS float*)(lds + mx1);
      }
      u32x2 pk[2];
#pragma unroll
      for (int u = 0; u < 2; ++u) {
        MF(sn, 7 + 2 * u, 0);
        mref[u] = ceilf(mref[u]);
        float e0 = __builtin_amdgcn_exp2f(sc[u].x - mref[u]); PIN(e0);
        MF(sn, 7 + 2 * u, 1);
        pk[u].x = pk2bf(e0, __builtin_amdgcn_exp2f(sc[u].y - mref[u])); PIN(pk[u].x);
        MF(sn, 8 + 2 * u, 0);
        e0 = __builtin_amdgcn_exp2f(sc[u].z - mref[u]); PIN(e0);
        MF(sn, 8 + 2 * u, 1);
        pk[u].y = pk2bf(e0, __builtin_amdgcn_exp2f(sc[u].w - mref[u]));
        *(u32x2*)(lds + (u ? pw1 : pw0)) = pk[u];
      }
      ADDC(C_P, one);
      MF(sn, 11, 0);
      lsum[0] = dot2(pk[0].x, 0x3F803F80u, dot2(pk[0].y, 0x3F803F80u, 0.f)); PIN(lsum[0]);
      MF(sn, 11, 1);
      lsum[1] = dot2(pk[1].x, 0x3F803F80u, dot2(pk[1].y, 0x3F803F80u, 0.f)); PIN(lsum[1]);
      MF(sn, 12, 0);
      REF_WRITE(4 + w, 2);
      MF(sn, 12, 1);
      QK(sn, 13, 16, 2);
    }
    STAMP(10);
#define QK1(N0, slot)                                                                                                    \
  do {                                                                                                                   \
    _Pragma("unroll") for (int i = 0; i < 16; ++i) { MF(sc, i, 0); if (i + (N0) < 16) A_RD(i + (N0), slot); }            \
  } while (0)
    if (RARE(__builtin_amdgcn_readfirstlane(c1) < 4u && !idle1)) {
      lds_spin(ctr + C_MK + 4 + w, 4u);
      A_READ(0, 16, 0);
      sn[0] = sn[1] = f32x4{0.f, 0.f, 0.f, 0.f};
      QK(sn, 0, 16, 2);
    }
    if (RARE(idle1)) lds_add(C_P + 1);
    else { sn[0] -= mref[0]; sn[1] -= mref[1]; SM(sn, 4 + w, 2, 1, pw0 + 4096, pw1 + 4096); }
    if (RARE(w == 2)) {
      RELANE();
      dma_sc1(L_QI, WS_VOTE + xc * 2048 + lane * 16, ws);
    }
    REF_WRITE(7 + sl2, 1);
    A_MOVE(0, 16, sl2 - sl1);
    pw0 = pw + sl2 * 1024;
    sc[0] = f32x4{-mref[0], -mref[0], -mref[0], -mref[0]};
    PIN(pw0); PIN(sc[0]);
    lds_spin(ctr + C_MK + 7 + sl2, 4u);
    A_READ(0, 6, 0);
    STAMP(4);
    QK1(6, 0);
    SM(sc, 7 + sl2, 1, 2, pw0 + 7168, 0);
    STAMP(11);
    if (!RARE(w >= 2)) {
      REF_WRITE(10, 1);
      sc[0] = f32x4{-mref[0], -mref[0], -mref[0], -mref[0]};
      PIN(sc[0]);
      _Pragma("unroll 1") for (u32 n_ = 0; n_ < LSPIN_CAP; ++n_) {
        const u32 k_ = CTS(cb, C_MK + 10);
        asm volatile("" ::: "memory");
        A_READ(0, 6, 8192);
        SB();
        if (__builtin_amdgcn_readfirstlane(k_) >= 4u) break;
      }
      STAMP(5);
      QK1(6, 8192);
      SM(sc, 10, 1, 3, pw0 + 9216, 0);
    } else if (w == 2) {
      RELANE();
      waitvm<0>();
      COUNT_VOTES(true);
      lds_add(C_GX);
    }
    STAMP(30);
    RELANE();
    {
#pragma unroll
      for (int u = 0; u < 2; ++u) {
        *(float*)(lds + L_ML + (mw ^ (u * 64)) * 4 + w * 4) = mref[u];
        *(float*)(lds + L_ML + 512 + (mw ^ (u * 64)) * 4 + w * 4) = x32_sum(x16_sum(lsum[u]));
      }
      u32 old;
      f32x4 m4, l4;
      asm volatile("s_mov_b64 exec, 1\n ds_add_rtn_u32 %0, %3, %4 offset:%6\n s_mov_b64 exec, -1\n ds_read_b128 %1, %5\n ds_read_b128 %2, %5 offset:512\n s_waitcnt lgkmcnt(0)"
                   : "=&v"(old), "=&v"(m4), "=&v"(l4) : "v"((u32)L_CTR), "v"(1u), "v"(L_ML + (lane & 31) * 16), "n"(4 * C_SD) : "memory");
      if (__builtin_amdgcn_readfirstlane(old) == 3u) {
        const float M = fmaxf(fmaxf(m4.x, m4.y), fmaxf(m4.z, m4.w));
        const float l = (l4.x * __builtin_amdgcn_exp2f(m4.x - M) + l4.y * __builtin_amdgcn_exp2f(m4.y - M)) + (l4.z * __builtin_amdgcn_exp2f(m4.z - M) + l4.w * __builtin_amdgcn_exp2f(m4.w - M));
        *(u32x2*)(lds + L_WMAX + (lane & 1) * 128 + (lane & 30) * 4) = u32x2{fbits(M), fbits(l)};
        lds_add(C_ML);
      }
    }
    lds_wait(ctr + C_GX, 1u);
    gx = __builtin_amdgcn_readfirstlane(ctr[C_PAR]);
    STAMP(13);
  } else {
    const int mu = 2 * j + (w >> 2);
#define Q_LOAD(i0, i1)                                                                                                   \
  _Pragma("unroll") for (int i = (i0); i < (i1); ++i) {                                                                  \
    waitvm<QW - 1>();                                                                                                    \
    dma<false>(w * 16384 + (i & 15) * 1024, (vq ^ ((i & 3) << 4)) + ((i < 8 ? 64 * w + 32 * dh : i < 16 ? 64 * w + 32 - 32 * dh : 256 * dh + 32 * w) + 4 * (i & 7)) * 8192, i < 16 ? w_q : w_uk); \
  }
    const u32 vq = g4 * 8192 + hq * 256 + ((col ^ (g4 << 2)) << 4);
    u32x4 xv;
    u32x2 e2;
    u32 x2_;
    asm volatile("global_load_dwordx4 %0, %3, %5\n global_load_dword %1, %4, %5\n buffer_load_dwordx2 %2, off, %6, 0 sc1"
                 : "=&v"(xv), "=&v"(x2_), "=&v"(e2) : "v"(lane * 16), "v"(128 * w + 4 * (lane & 31)), "s"(x), "s"(rs) : "memory");
    bf16x8 wf[16], wk[8];
    u32 ta0 = w * 16384 + off_b(8 * g4 + tq, tp >> 1) + 8 * (tp & 1), ta1 = w * 16384 + off_b(8 * g4 + 4 + tq, tp >> 1) + 8 * (tp & 1);
    int so = WS_SC + ((lane >> 5) * 2048 + 64 * w + 2 * (lane & 31)) * 8;
    int fo = w == 0 ? WS_SFLAG + xc * 2048 + lane * 8 : WS_TOTAL;
    PIN(ta0); PIN(ta1); PIN(so); PIN(fo);
    int qpo = WS_QABS + il(hq * 1024 + dh * 512 + w * 64, 0) + (lane & 32) + (lane & 8) * 2 + (g4 & 1) * 8192 + ((lane & 7) ? WS_TOTAL : 0);
    u32 hw_ = L_H1RED + w * 128 + (lane & 31) * 4, hr_ = L_H1RED + w * 128 + 16 * g4 + 64 * dh, qw_ = L_H1RED + 1024 + w * 272 + lane * 2, qr_ = L_H1RED + 1024 + col * 272 + 64 * g4;
    PIN(qpo); PIN(hw_); PIN(hr_); PIN(qw_); PIN(qr_);
    u32 ka = w * 16384 + off_b(col, 4 * g4), hr2 = hr_ ^ 64;
    PIN(ka); PIN(hr2);
    Q_LOAD(4, 8);
    asm volatile("s_waitcnt vmcnt(4)" : "+v"(xv), "+v"(x2_), "+v"(e2)::"memory");
    TAG_OF(e2.x, e2.y);
    float ln0, ln1;
    {
      float mb, rsd;
      ln_stats(xv, mb, rsd);
      ln0 = rbf(rbf(lo16(x2_) - mb) * rsd);
      ln1 = rbf(rbf(hi16(x2_) - mb) * rsd);
    }
    PIN(ln0); PIN(ln1);
    u32 t0_ = tlo << 24, t1_ = (tlo >> 8) | (thi << 24), f0_ = w ? 0u : tlo, f1_ = w ? 0u : thi;
    PIN(t0_); PIN(t1_); PIN(f0_); PIN(f1_);
    Q_LOAD(8, 8 + QW);
    waitvm<QW>();
#pragma unroll
    for (int c = 0; c < 8; ++c) wf[c] = tr_frag(lds, ta0 ^ (c << 5), ta1 ^ (c << 5));
    Q_LOAD(8 + QW, 16);
#pragma unroll
    for (int c = 0; c < 8; ++c) PIN(wf[c]);
    Q_LOAD(16, 16 + QW);
    waitvm<QW>();
#pragma unroll
    for (int c = 0; c < 8; ++c) wf[8 + c] = tr_frag(lds, (ta0 ^ (c << 5)) + 8192, (ta1 ^ (c << 5)) + 8192);
    Q_LOAD(16 + QW, 24);
    STAMP(12);
    until(t0, E1_T);
    STAMP(23);
    u32x4 g[4];
    u32x2 fl;
#define E1_LOAD(C)                                                                                                       \
  asm volatile("buffer_load_dwordx4 %0, %5, %7, 0 offen sc1\n buffer_load_dwordx4 %1, %5, %7, %8 offen sc1\n"             \
               "buffer_load_dwordx4 %2, %5, %7, %9 offen sc1\n buffer_load_dwordx4 %3, %5, %7, %10 offen sc1\n buffer_load_dwordx2 %4, %6, %7, 0 offen sc1" \
               : C(g[0]), C(g[1]), C(g[2]), C(g[3]), C(fl) : "v"(so), "v"(fo), "s"(rs), "s"(4096), "s"(8192), "s"(12288) : "memory")
    E1_LOAD("=&v");
    waitvm<5>();
#pragma unroll
    for (int i = 0; i < 8; ++i) wk[i] = *(const bf16x8*)(lds + (ka ^ ((i & 3) << 4)) + (i >> 2) * 4096);
#pragma unroll
    for (int c = 0; c < 8; ++c) { PIN(wf[8 + c]); PIN(wk[c]); }
    STAMP(4);
    _Pragma("unroll 1") for (u32 spins = 0; spins < SPIN_CAP; ++spins) {
      asm volatile("s_waitcnt vmcnt(0)" : "+v"(g[0]), "+v"(g[1]), "+v"(g[2]), "+v"(g[3]), "+v"(fl)::"memory");
      u32 bl = g[0].x ^ t0_, bh = (fl.x ^ f0_) | (fl.y ^ f1_);
#pragma unroll
      for (int k = 0; k < 8; ++k) {
        if (k) bl = __builtin_amdgcn_bitop3_b32(bl, g[k >> 1][2 * (k & 1)], t0_, 0xF6);
        bh = __builtin_amdgcn_bitop3_b32(bh, g[k >> 1][2 * (k & 1) + 1], t1_, 0xF6);
      }
      if (!RARE(__any(((bl >> 24) | bh) != 0u))) break;
      STAMP(29);
      __builtin_amdgcn_s_sleep(1);
      E1_LOAD("+v");
    }
    STAMP(1);
    {
      const float m0 = rbf((gran_val(g[0].x) + gran_val(g[1].x)) + (gran_val(g[2].x) + gran_val(g[3].x)));
      const float m1 = rbf((gran_val(g[0].z) + gran_val(g[1].z)) + (gran_val(g[2].z) + gran_val(g[3].z)));
      const auto s0 = __builtin_amdgcn_permlane32_swap(__float_as_uint(m0), __float_as_uint(m0), false, false);
      const auto s1 = __builtin_amdgcn_permlane32_swap(__float_as_uint(m1), __float_as_uint(m1), false, false);
      *(u32*)(lds + hw_) = pk2bf(rbf(ln0 * rbf(1.f + __uint_as_float(s0[1]))) + __uint_as_float(s0[0]),
                                 rbf(ln1 * rbf(1.f + __uint_as_float(s1[1]))) + __uint_as_float(s1[0]));
    }
    WAVE_LDS_SYNC();
    {
      const bf16x8 h0 = *(const bf16x8*)(lds + hr_), h1 = *(const bf16x8*)(lds + hr2);
      f32x4 d[8];
      SB();
#pragma unroll
      for (int c = 0; c < 8; ++c) { d[c] = __builtin_amdgcn_mfma_f32_16x16x32_bf16(h0, wf[c], f32x4{0.f, 0.f, 0.f, 0.f}, 0, 0, 0); SB(); }
#pragma unroll
      for (int c = 0; c < 8; ++c) { d[c] = __builtin_amdgcn_mfma_f32_16x16x32_bf16(h1, wf[8 + c], d[c], 0, 0, 0); SB(); }
#pragma unroll
      for (int k = 0; k < 2; ++k) {
        const float v = (g4 & 2) ? ((g4 & 1) ? d[4 * k + 3][0] : d[4 * k + 2][0]) : ((g4 & 1) ? d[4 * k + 1][0] : d[4 * k][0]);
        const u32 vh = f2bf(v);
        *(u16*)(lds + qw_ + 128 * k) = (u16)vh;
        *(u16*)(lds + qw_ + 128 * k + 8 * 272) = (u16)f2bf(v - bf2f(vh));
      }
    }
    STAMP(2);
    BAR();
    {
      bf16x8 a[4];
#pragma unroll
      for (int s = 0; s < 4; ++s) a[s] = *(const bf16x8*)(lds + qr_ + 16 * s);
      SB();
      f32x4 d[2] = {f32x4{0.f, 0.f, 0.f, 0.f}, f32x4{0.f, 0.f, 0.f, 0.f}};
#pragma unroll
      for (int s = 0; s < 4; ++s)
#pragma unroll
        for (int c = 0; c < 2; ++c) d[c] = __builtin_amdgcn_mfma_f32_16x16x32_bf16(a[s], wk[4 * c + s], d[c], 0, 0, 0);
      constexpr float SCALE = 0.08838834764831845f * 1.4426950408889634f;
      const float v = SCALE * x16_sum(rs32(sum4(d[0]), sum4(d[1])));
      const float p0 = __uint_as_float(nopois(pk2bf(dppf<0x00>(v), dppf<0x55>(v)))), p1 = __uint_as_float(nopois(pk2bf(dppf<0xAA>(v), dppf<0xFF>(v))));
      st128(rs, qpo, u32x4{__float_as_uint(p0), __float_as_uint(p1), __float_as_uint(dppf<0x104>(p0)), __float_as_uint(dppf<0x104>(p1))});
    }
    STAMP(5);
    SB();
    {
      st128(rs, lane < 50 ? WS_PCTX + (j * 8 + w) * 800 + lane * 16 : WS_TOTAL, P4);
      const int pp = __builtin_amdgcn_readfirstlane(WS_PROBE + il((j * 8 + w) * 128, w & 1));
      u64 ta, tb, tc;
      u32 dm;
      asm volatile("s_load_dword %3, %4, 0x0\n s_waitcnt lgkmcnt(0)\n"
                   "s_memtime %0\n s_waitcnt lgkmcnt(0)\n s_load_dword %3, %4, %5 glc\n s_waitcnt lgkmcnt(0)\n"
                   "s_memtime %1\n s_waitcnt lgkmcnt(0)\n s_load_dword %3, %4, %6 glc\n s_waitcnt lgkmcnt(0)\n"
                   "s_memtime %2\n s_waitcnt vmcnt(0) lgkmcnt(0)"
                   : "=&s"(ta), "=&s"(tb), "=&s"(tc), "=&s"(dm) : "s"(ws), "s"(pp), "s"(pp ^ 8192) : "memory");
      int df = min(max(((int)(u32)(tb - ta) - (int)(u32)(tc - tb)) >> 3, -VCLIP), VCLIP);
      df = ((w ^ myh) & 1) ? -df : df;
      u32 old = 0u;
      if (lane == 0) old = __hip_atomic_fetch_add((LAS u32*)(lds + L_CTR) + C_VOTE, (u32)(65536 + 64 + df), __ATOMIC_RELAXED, __HIP_MEMORY_SCOPE_WORKGROUP);
      old = __builtin_amdgcn_readfirstlane(old) + 65536 + 64 + df;
      if (old >= 524288u && lane < 8) st64(rs, WS_VOTE + lane * 2048 + j * 8, (old & 1023u) | (tlo << 24), (tlo >> 8) | (thi << 24));
      STAMP(6);
    }
    {
      const int r0 = 128 * (mu & 3) + 32 * (w & 3) + 4 * (lane >> 3);
      const u32x2 cvb = *(const u32x2*)(cvec + r0);
      u32x4 wsc[4];
#pragma unroll
      for (int i = 0; i < 4; ++i) wsc[i] = ldnt(w_mod + (size_t)(r0 + i) * 3072 + 1024 + (mu >> 2) * 64 + (lane & 7) * 8);
      float a[8];
#pragma unroll
      for (int k = 0; k < 8; ++k) a[k] = 0.f;
#pragma unroll
      for (int i = 0; i < 4; ++i) fma8b(a, f2bf(silu_bf((i & 1) ? hi16(i < 2 ? cvb.x : cvb.y) : lo16(i < 2 ? cvb.x : cvb.y))), wsc[i]);
      float r[4];
#pragma unroll
      for (int k = 0; k < 4; ++k) r[k] = rs32(a[k], a[k + 4]);
      const float o0 = b3_sum(rs16(r[0], r[2])), o1 = b3_sum(rs16(r[1], r[3]));
      float* red = (float*)(lds + L_H1RED + 8192);
      if ((lane & 8) == 0) *(f32x2*)(red + w * 64 + (lane & 7) * 8 + (lane >> 5) * 4 + ((lane >> 4) & 1) * 2) = f32x2{o0, o1};
      BAR();
      if ((w & 3) == 3 && lane < 16) {
        red += (w >> 2) * 256 + lane * 4;
        const f32x4 sm = (*(const f32x4*)(red) + *(const f32x4*)(red + 64)) + (*(const f32x4*)(red + 128) + *(const f32x4*)(red + 192));
        st128(rs, WS_MODB + ((mu & 3) * 2048 + (mu >> 2) * 64 + lane * 4) * 4, u32x4{fbits(sm.x), fbits(sm.y), fbits(sm.z), fbits(sm.w)});
      }
    }
    gx = 0;
  }

  RELANE();
  const int r16 = lane >> 2, ch = lane & 3;
  if (ii >= 16) {
    int mo = lane < 24 ? (WS_LSE + il((24 * w + lane) * 512 + c4 * 128 + (h >> 1) * 8, h & 1)) ^ gx : WS_TOTAL;
    PIN(mo);
    lds_spin(ctr + C_PD, 4u);
    STAMP(23);
    u32x2 mt = ld64(rs, mo);
    SB();
    const u32 pb = (w >> 1) * PS + (w & 1) * 6144;
    u32 po[6], pa[16], ea[8];
    {
      u32 t_ = g4 * 256 + ((col ^ (g4 << 2)) << 4) + ((WS_PART + il21(((h >> 1) * 4 + c4) * 65536, h & 1)) ^ gx);
      PIN(t_);
#pragma unroll
      for (int k = 0; k < 6; ++k) { po[k] = (t_ ^ ((k & 3) << 4)) + il((6 * w + k) * 1024, 0); PIN(po[k]); }
    }
    bf16x8 wk[8];
    if (w < 4) {
      u32 wa = tq * PS + 16384 + off_b(g4, 2 * w + (tp >> 1)) + 8 * (tp & 1);
      PIN(wa);
#pragma unroll
      for (int s = 0; s < 8; ++s) { const u32 a = (wa ^ ((s & 3) << 4) ^ ((s >> 2) << 7)) + (s & 3) * 1024; wk[s] = tr_frag(lds, a, a + 4096); }
    }
    pa[0] = pb + off_b(8 * (g4 == 3 ? 0 : g4) + tq, tp >> 1) + 8 * (tp & 1);
    PIN(pa[0]);
#pragma unroll
    for (int i = 1; i < 16; ++i) { pa[i] = pa[0] ^ (i << 4); PIN(pa[i]); }
    u32 bo = L_QOFF + col * 272 + g4 * 16, mlo = L_QOFF + 4352 + (col & 7) * 8, ro = L_QOFF + (w + (col & 8)) * 272 + (col & 7) * 32 + g4 * 8;
    int so1 = (WS_PCTX + h * 2048 + c4 * 512 + (col & 8) * 32 + (w & 1) * 128 + (w & 2) * 16 + (g4 >> 1) * 64 + (g4 & 1) * 8 + ((col & 6) ? WS_TOTAL : (col & 1) * 16)) ^ gx;
    u32 eo = L_QOFF + 4416 + w * 128 + lane * 2, mw_ = L_QOFF + 4352 + w * 8;
#pragma unroll
    for (int i = 0; i < 8; ++i) { ea[i] = L_QOFF + 4416 + w * 128 + ((col & 7) == i ? 16 * g4 : 64); PIN(ea[i]); }
    float pad = lane < 24 ? 0.f : -__builtin_inff();
    PIN(bo); PIN(mlo); PIN(ro); PIN(so1); PIN(eo); PIN(mw_); PIN(pad);
    SB();
    u32 done = 0u;
    _Pragma("unroll 1") for (u32 spins = 0; spins < SPIN_CAP; ++spins) {
      const u32 ok = ~(u32)__ballot(max(mt.x, mt.y) == POISON);
      u32 nw = ok & (ok >> 1);
      nw = nw & (nw >> 2) & 0x111111u & ~done;
      done |= nw;
#define T1F(k, b) "s_bitcmp0_b32 %[nw], " #b "\n s_cbranch_scc1 1f\n global_load_lds_dwordx4 %[p" #k "], %[ws] sc1\n 1: s_add_u32 m0, m0, 0x400\n"
      asm volatile("s_mov_b32 m0, %[m]\n" T1F(0, 0) T1F(1, 4) T1F(2, 8) T1F(3, 12) T1F(4, 16) T1F(5, 20)
                   ::[m] "s"(pb), [nw] "s"(nw), [ws] "s"(ws), [p0] "v"(po[0]), [p1] "v"(po[1]), [p2] "v"(po[2]), [p3] "v"(po[3]), [p4] "v"(po[4]), [p5] "v"(po[5]) : "memory", "scc");
      if (done == 0x111111u) break;
      __builtin_amdgcn_s_sleep(1);
      if (max(mt.x, mt.y) == POISON) mt = ld64(rs, mo);
      PIN(mt);
    }
    STAMP(16);
    {
      const float m = __uint_as_float(mt.x) + pad;
      float M = m;
#define MX(C) "s_nop 1\n v_max_f32_dpp %0, %0, %0 " C " row_mask:0xf bank_mask:0xf\n"
      asm(MX("quad_perm:[1,0,3,2]") MX("quad_perm:[2,3,0,1]") MX("row_ror:8") MX("row_ror:4") "s_nop 1" : "+v"(M));
      M = fmaxf(__uint_as_float(__builtin_amdgcn_readlane(__float_as_uint(M), 0)), __uint_as_float(__builtin_amdgcn_readlane(__float_as_uint(M), 16)));
      const float e = __builtin_amdgcn_exp2f(m - M);
      *(u16*)(lds + eo) = (u16)f2bf(e);
      WAVE_LDS_SYNC();
      bf16x8 wf[8], pt[8];
#pragma unroll
      for (int i = 0; i < 8; ++i) wf[i] = *(const bf16x8*)(lds + ea[i]);
      SB();
      const float L = x16_sum(row16_sum(e * __uint_as_float(mt.y)));
      if (lane == 0) *(f32x2*)(lds + mw_) = f32x2{M, L};
      SB();
      waitvm<0>();
      STAMP(17);
#pragma unroll
      for (int i = 0; i < 8; ++i) pt[i] = tr_frag(lds, pa[2 * i], pa[2 * i + 1] + 1024);
      SB();
      f32x4 d = {0.f, 0.f, 0.f, 0.f};
#pragma unroll
      for (int i = 0; i < 8; ++i) d = __builtin_amdgcn_mfma_f32_16x16x32_bf16(pt[i], wf[i], d, 0, 0, 0);
      const u32 h0 = pk2bf(d[0], d[1]), h1 = pk2bf(d[2], d[3]);
      const u32 l0 = pk2bf(d[0] - lo16(h0), d[1] - hi16(h0)), l1 = pk2bf(d[2] - lo16(h1), d[3] - hi16(h1));
      *(u32x2*)(lds + ro) = (col & 8) ? u32x2{l0, l1} : u32x2{h0, h1};
    }
    BAR();
    STAMP(14);
    if (w < 4) {
      const f32x2 ml = *(const f32x2*)(lds + mlo);
      bf16x8 lf[4];
#pragma unroll
      for (int s = 0; s < 4; ++s) lf[s] = *(const bf16x8*)(lds + bo + 64 * s);
      SB();
      float Mg = ml.x;
      asm(MX("quad_perm:[1,0,3,2]") MX("quad_perm:[2,3,0,1]") MX("row_half_mirror") : "+v"(Mg));
      f32x4 da = {0.f, 0.f, 0.f, 0.f}, db = da;
#define T1M(s) do { SB(); da = __builtin_amdgcn_mfma_f32_16x16x32_bf16(wk[s], lf[s], da, 0, 0, 0); db = __builtin_amdgcn_mfma_f32_16x16x32_bf16(wk[4 + (s)], lf[s], db, 0, 0, 0); SB(); } while (0)
      T1M(0);
      float e = __builtin_amdgcn_exp2f(ml.x - Mg), Lg = e * ml.y;
      PIN(e); PIN(Lg);
      T1M(1);
      Lg = quad_sum(Lg);
      PIN(Lg);
      T1M(2);
      Lg += dppf<0x141>(Lg);
      float sc = e * __builtin_amdgcn_rcpf(Lg);
      PIN(sc);
      T1M(3);
      const bool hi8 = (col & 8) != 0;
      float d0 = ((hi8 ? db[0] : da[0]) + dppf<0x128>(hi8 ? da[0] : db[0])) * sc, d1 = ((hi8 ? db[1] : da[1]) + dppf<0x128>(hi8 ? da[1] : db[1])) * sc;
      float d2 = ((hi8 ? db[2] : da[2]) + dppf<0x128>(hi8 ? da[2] : db[2])) * sc, d3 = ((hi8 ? db[3] : da[3]) + dppf<0x128>(hi8 ? da[3] : db[3])) * sc;
      row8_sum4(d0, d1, d2, d3);
      const u32 h0 = pk2bf(d0, d1), h1 = pk2bf(d2, d3);
      const u32 l0 = pk2bf(d0 - lo16(h0), d1 - hi16(h0)), l1 = pk2bf(d2 - lo16(h1), d3 - hi16(h1));
      st64(rs, so1, nopois((col & 1) ? l0 : h0), nopois((col & 1) ? l1 : h1));
    }
    STAMP(15);
    STAMP_FLUSH();
    return;
  }
  if (tl2) {
    BAR0();
    if (w < 4) {
#pragma unroll
      for (int i = 0; i < 8; ++i) dma<true>(owno + i * 1024, (ks * 512 + 64 * w + 8 * i + (lane >> 3)) * 1024 + cg * 128 + (lane & 7) * 16, w_o);
      waitvm<0>();
    } else {
#pragma unroll
      for (int i = 0; i < 8; ++i) *(u32x4*)(lds + owno + i * 1024 + lane * 16) = wv[i];
      WAVE_LDS_SYNC();
    }
    STAMP(26);
    bf16x8 wf[8];
#pragma unroll
    for (int i = 0; i < 8; ++i) {
      const u32 a = owno + (32 * (i >> 2) + 8 * g4 + tq) * 128 + (i & 3) * 32 + tp * 8;
      wf[i] = tr_frag(lds, a, a + 512);
    }
#pragma unroll
    for (int i = 0; i < 8; ++i) PIN(wf[i]);
    STAMP(23);
    int off[1] = {(WS_PCTX + (ks * 4 + (w >> 1)) * 2048 + tq * 512 + (w & 1) * 256 + g4 * 64 + tp * 16) ^ gx};
    u32 rw = L_RED + lane * 32 + w * 4;
    PIN(rw);
    int so2 = tp < 2 ? WS_PATTN + il((ks * 512 + cg * 64 + (lane & 60)) * 4, tp) : WS_TOTAL;
    PIN(so2);
    u32x4 g[1];
    gather128<1, 4>(rs, off, g);
    STAMP(24);
    const bf16x8 a0 = __builtin_bit_cast(bf16x8, (col & 2) ? Z4 : g[0]), a1 = __builtin_bit_cast(bf16x8, (col & 2) ? g[0] : Z4);
    f32x4 d[4];
#pragma unroll
    for (int c = 0; c < 4; ++c) d[c] = __builtin_amdgcn_mfma_f32_16x16x32_bf16(a0, wf[c], f32x4{0.f, 0.f, 0.f, 0.f}, 0, 0, 0);
#pragma unroll
    for (int c = 0; c < 4; ++c) d[c] = __builtin_amdgcn_mfma_f32_16x16x32_bf16(a1, wf[4 + c], d[c], 0, 0, 0);
    *(float*)(lds + rw) = rs16(rs32(sum4(d[0]), sum4(d[2])), rs32(sum4(d[1]), sum4(d[3])));
    BAR();
    if (w == 0) {
      const float v = sum4(*(const f32x4*)(lds + L_RED + lane * 32) + *(const f32x4*)(lds + L_RED + lane * 32 + 16));
      const float vn = __uint_as_float(fbits(v));
      st128(rs, so2, u32x4{__float_as_uint(dppf<0x00>(vn)), __float_as_uint(dppf<0x55>(vn)), __float_as_uint(dppf<0xAA>(vn)), __float_as_uint(dppf<0xFF>(vn))});
    }
    STAMP(17);
  } else if (t3 >= 0) {
    STAMP(20);
    if (w < 4) {
      const int fr = ((128 * w + r16) * DF + t3 * 32) * 2, ca_ = ch ^ ((r16 >> 2) & 2) ^ (t3 & 1);
      const u16* wa = (t3 & 1) ? w_up : w_gate;
      const u16* wb = (t3 & 1) ? w_gate : w_up;
      const u32 d = w * 32768 + 8192;
#pragma unroll
      for (int i = 0; i < 8; ++i) { waitvm<WING - 1>(); dma<false>(d + (i >> 2) * 12288 + (t3 & 1) * 4096 + (i & 3) * 1024, fr + ca_ * 16 + 32 * i * DF, wa); }
#pragma unroll
      for (int i = 0; i < 8; ++i) { waitvm<WING - 1>(); dma<false>(d + (i >> 2) * 12288 + 4096 - (t3 & 1) * 4096 + (i & 3) * 1024, fr + (ca_ ^ 1) * 16 + 32 * i * DF, wb); }
    } else {
#pragma unroll
      for (int i = 0; i < 8; ++i) {
        waitvm<WING - 1>();
        dma<true>(lw * 32768 + 16384 + (i >> 2) * 12288 + (i & 3) * 1024, (32 * t3 + 8 * (i & 3) + (lane >> 3)) * 1024 + 256 * lw + 128 * (i >> 2) + (lane & 7) * 16, w_down);
      }
      const u32 x2 = *(const u32*)(x + 128 * lw + 2 * lane);
      u32x4 m6[6];
      int mo[6];
#pragma unroll
      for (int i = 0; i < 6; ++i) mo[i] = MB_OFF(i);
      gather128<6, 8>(rs, mo, m6);
#pragma unroll
      for (int i = 0; i < 6; ++i) {
        const f32x4 f = __builtin_bit_cast(f32x4, m6[i]);
        const float v = rbf(rs16(rs32(f.x, f.z), rs32(f.y, f.w)));
        *(u16*)(lds + L_Z + (i < 2 ? 5 : i < 4 ? 3 : 2) * 1088 + (128 * lw + 64 * (i & 1) + 4 * col + g4) * 2) = (u16)f2bf(i < 4 ? v : 1.f + v);
      }
      *(u32*)(lds + L_Z + 6 * 1088 + lw * 256 + lane * 4) = x2;
      if (w == 4 && lane < 32) *(u32*)(lds + L_ZERO + lane * 4) = lane < 16 ? 0u : POISON;
      if (w == 6) COUNT_VOTES(false);
    }
    waitvm<0>();
    STAMP(26);
    BAR();
    gx = __builtin_amdgcn_readfirstlane(ctr[C_PAR]);
    u32 zw = L_Z + (64 * w + 4 * col + g4) * 2;
    u32 hr[4];
#pragma unroll
    for (int i = 0; i < 4; ++i) { hr[i] = (col == i ? L_HH : L_ZERO) + 16 * g4; PIN(hr[i]); }
    int off[2];
#pragma unroll
    for (int i = 0; i < 2; ++i) { off[i] = (WS_PATTN + il(((4 * i + g4) * 512 + 64 * w + col * 4) * 4, myh)) ^ gx; PIN(off[i]); }
    int so = col < 4 ? (WS_PDOWN + il((t3 * 256 + 64 * (w & 3) + 16 * col + 4 * g4) * 4, w >> 2)) ^ gx : WS_TOTAL;
    u32 zr = L_Z + tp * 1088 + 16 * g4, yr = L_Z + 4 * 1088 + lane * 16, hw = L_HH + (w & 3) * 16 + (g4 & 1) * 8 + tp * 2, cr = L_CF + (w & 3) * 16 + tp * 4;
    PIN(cr); PIN(so); PIN(zw); PIN(zr); PIN(yr); PIN(hw);
    float ga = bf2f(*(const u16*)(lds + zw + 5 * 1088)), cb1 = bf2f(*(const u16*)(lds + zw + 2 * 1088)), xe = bf2f(*(const u16*)(lds + zw + 6 * 1088));
    bf16x8 wf[16], wd[4];
    if (w < 4) {
#pragma unroll
      for (int k = 0; k < 16; ++k) {
        const u32 a = (k >> 2) * 32768 + 8192 + ((k >> 1) & 1) * 12288 + (tp >> 1) * 4096 + ((k & 1) * 32 + 8 * g4 + tq) * 64 + (w ^ ((g4 & 1) * 2) ^ (tp >> 1)) * 16 + (tp & 1) * 8;
        wf[k] = tr_frag(lds, a, a + 256);
      }
#pragma unroll
      for (int k = 0; k < 16; ++k) PIN(wf[k]);
    }
#pragma unroll
    for (int i = 0; i < 4; ++i) {
      const u32 a = owno + 8192 + (8 * g4 + tq) * 128 + i * 32 + tp * 8;
      wd[i] = tr_frag(lds, a, a + 512);
    }
#pragma unroll
    for (int i = 0; i < 4; ++i) PIN(wd[i]);
    PIN(ga); PIN(cb1); PIN(xe);
    STAMP(23);
    until(t0, E6_T);
    u32x4 pa2[2];
    gather128<2, 4>(rs, off, pa2);
    STAMP(18);
    {
      const f32x4 s4 = __builtin_bit_cast(f32x4, pa2[0]) + __builtin_bit_cast(f32x4, pa2[1]);
      const u32 yb = f2bf(xe + rbf(ga * rbf(rs16(rs32(s4.x, s4.z), rs32(s4.y, s4.w)))));
      const float z = bf2f(yb) * cb1;
      const u32 zh = f2bf(z);
      *(u16*)(lds + zw + 4 * 1088) = (u16)yb;
      *(u16*)(lds + zw) = (u16)zh;
      *(u16*)(lds + zw + 1088) = (u16)f2bf(z - bf2f(zh));
    }
    BAR();
    if (w < 4) {
      bf16x8 zf[16];
#pragma unroll
      for (int k = 0; k < 16; ++k) zf[k] = *(const bf16x8*)(lds + zr + 64 * k);
      __builtin_amdgcn_sched_barrier(0);
      f32x4 d = {0.f, 0.f, 0.f, 0.f};
#pragma unroll
      for (int k = 0; k < 16; ++k) d = __builtin_amdgcn_mfma_f32_16x16x32_bf16(wf[k], zf[k], d, 0, 0, 0);
      PIN(d);
      u32 cfb;
      do { cfb = *(volatile LAS u32*)(lds + cr); } while (__any(cfb == POISON));
      d *= __uint_as_float(cfb);
      const float a0 = (tp & 1) ? d[1] : d[0], b0 = (tp & 1) ? d[0] : d[1], a1 = (tp & 1) ? d[3] : d[2], b1 = (tp & 1) ? d[2] : d[3];
      const float e0 = a0 + dppf<0xB1>(b0), e1 = a1 + dppf<0xB1>(b1);
      const float v = rbf(((tp & 2) ? e1 : e0) + dppf<0x4E>((tp & 2) ? e0 : e1));
      const auto sw = __builtin_amdgcn_permlane32_swap(__float_as_uint(v), __float_as_uint(v), false, false);
      const float g = __uint_as_float(sw[0]), u = __uint_as_float(sw[1]);
      const float e2 = __builtin_amdgcn_exp2f(g * (-2.3022081981f - 0.1029432396f * g * g));
      *(u16*)(lds + hw) = (u16)f2bf(rbf(g * __builtin_amdgcn_rcpf(e2 + 1.f)) * u);
    } else {
      float mb, rsd;
      ln_stats(*(const u32x4*)(lds + yr), mb, rsd);
      *(u32*)(lds + cr) = fbits(tp == 3 ? 1.f : rsd * (tp == 2 ? -mb : 1.f));
    }
    BAR();
    {
      bf16x8 hf[4];
#pragma unroll
      for (int i = 0; i < 4; ++i) hf[i] = *(const bf16x8*)(lds + hr[i]);
      __builtin_amdgcn_sched_barrier(0);
      f32x4 d = {0.f, 0.f, 0.f, 0.f};
#pragma unroll
      for (int i = 0; i < 4; ++i) d = __builtin_amdgcn_mfma_f32_16x16x32_bf16(wd[i], hf[i], d, 0, 0, 0);
      st128(rs, so, u32x4{fbits(d[0]), fbits(d[1]), fbits(d[2]), fbits(d[3])});
    }
    STAMP(19);
  }
  if (t4 >= 0 && lw == 0) {
#define RED8(f) b3_sum(rs16(rs32(f.x, f.z), rs32(f.y, f.w)))
    const int oc = t4 * 32 + (lane & 7) * 4 + g4;
    const u32 xb = x[oc];
    float x1, gb;
    {
      int off[2];
      off[0] = lane < 32 ? WS_MODB + ((lane >> 3) * 2048 + t4 * 32 + (lane & 7) * 4) * 4 : WS_TOTAL;
      off[1] = lane < 32 ? WS_MODB + ((lane >> 3) * 2048 + 1536 + t4 * 32 + (lane & 7) * 4) * 4 : WS_TOTAL;
      u32x4 mg[2];
      gather128<2>(rs, off, mg);
      const f32x4 fa = __builtin_bit_cast(f32x4, mg[0]), fb = __builtin_bit_cast(f32x4, mg[1]);
      const float ga = rbf(RED8(fa));
      gb = rbf(RED8(fb));
      int of1[1] = {(WS_PATTN + il(((lane >> 3) * 512 + t4 * 32 + (lane & 7) * 4) * 4, myh)) ^ gx};
      u32x4 g[1];
      gather128<1, 4>(rs, of1, g);
      const f32x4 gf = __builtin_bit_cast(f32x4, g[0]);
      x1 = rbf(bf2f(xb) + rbf(ga * rbf(RED8(gf))));
    }
    STAMP(20);
    if (t4 == 0 && lane == 0) st64(rs, WS_EPOCH, tlo, thi);
    int off[7];
#pragma unroll
    for (int i = 0; i < 7; ++i) { off[i] = (WS_PDOWN + il(((7 * (lane >> 3) + i) * 256 + (t4 & 7) * 32 + (lane & 7) * 4) * 4, myh)) ^ gx; PIN(off[i]); }
    const rsrc_t ro = __builtin_amdgcn_make_buffer_rsrc((void*)out, 0, 1024, 0x00020000);
    int oo = (lane & 8) ? 1024 : oc * 2;
    PIN(x1); PIN(gb); PIN(oo);
    u32x4 g[7];
    __builtin_amdgcn_s_sleep(T4_DELAY);
#define F4(i) __builtin_bit_cast(f32x4, g[i])
    float s;
#pragma unroll
    for (int i = 0; i < 7; ++i) g[i] = ld128(rs, off[i]);
    _Pragma("unroll 1") for (u32 spins = 0; spins < SPIN_CAP; ++spins) {
      asm volatile("s_waitcnt vmcnt(0)" : "+v"(g[0]), "+v"(g[1]), "+v"(g[2]), "+v"(g[3]), "+v"(g[4]), "+v"(g[5]), "+v"(g[6])::"memory");
      const f32x4 s4 = ((F4(0) + F4(1)) + (F4(2) + F4(3))) + ((F4(4) + F4(5)) + F4(6));
      s = RED8(s4);
      if (!__any(s != s)) break;
      u64 bad = 0ull;
#pragma unroll
      for (int i = 0; i < 7; ++i) {
        const u64 m = __ballot(pois4(g[i]));
        bad |= m;
        asm volatile("s_mov_b64 exec, %2\n buffer_load_dwordx4 %0, %1, %3, 0 offen sc1\n s_mov_b64 exec, -1" : "+v"(g[i]) : "v"(off[i]), "s"(m), "s"(rs) : "memory");
      }
      if (!bad) break;
    }
    STAMP(21);
    __builtin_amdgcn_raw_buffer_store_b16((u16)f2bf(x1 + rbf(gb * rbf(s))), ro, oo, 0, 0);
    STAMP(22);
  }
  STAMP_FLUSH();
}

extern "C" void kernel_launch(void* const* d_in, const int* in_sizes, int n_in, void* d_out, int out_size,
                              void* d_ws, size_t ws_size, hipStream_t stream) {
  k_main<<<NWG, 512, 0, stream>>>((const u16*)d_in[0], (const u16*)d_in[1], (const u16*)d_in[2], (const u16*)d_in[3],
                                  (const u16*)d_in[4], (const u16*)d_in[5], (const u16*)d_in[7], (const u16*)d_in[8],
                                  (const u16*)d_in[9], (const u16*)d_in[10], (const u16*)d_in[11], (u16*)d_out,
                                  (unsigned char*)d_ws);
}
```

```cpp
#include <hip/hip_runtime.h>
#include <hip/hip_bf16.h>
#include <stdint.h>

typedef unsigned short u16;
typedef unsigned int u32;
typedef unsigned long long u64;
typedef float f32x2 __attribute__((ext_vector_type(2)));
typedef float f32x4 __attribute__((ext_vector_type(4)));
typedef float f32x16 __attribute__((ext_vector_type(16)));
typedef u32 u32x2 __attribute__((ext_vector_type(2)));
typedef u32 u32x4 __attribute__((ext_vector_type(4)));
typedef short bf16x8 __attribute__((ext_vector_type(8)));
typedef short s16x4 __attribute__((ext_vector_type(4)));
#define LAS __attribute__((address_space(3)))
#define DI __device__ __forceinline__

constexpr int DF = 1792;
constexpr int NWG = 256;
constexpr int AUX_SC1 = 16;

constexpr int WS_EPOCH = 0;
constexpr int WS_SFLAG = 4096;
constexpr int WS_QABS = 32768;
constexpr int WS_PCTX = WS_QABS + 65536;
constexpr int WS_PATTN = WS_PCTX + 65536;
constexpr int WS_PDOWN = WS_PATTN + 32768;
constexpr int WS_LSE = WS_PDOWN + 114688;
constexpr int WS_MODB = WS_LSE + 196608;
constexpr int WS_SC = WS_MODB + 32768;
constexpr int WS_PROBE = WS_SC + 32768;
constexpr int WS_VOTE = WS_PROBE + 262144;
constexpr int WS_PART = 2097152;
constexpr int PART_BYTES = 32 * 8 * 256 * 128;
constexpr int WS_TOTAL = WS_PART + PART_BYTES;

constexpr int il(int o, int hf) { return (o >> 13) * 16384 + hf * 8192 + (o & 8191); }
constexpr int il21(int o, int hf) { return il(o, hf ^ ((o >> 20) & 1) ^ 1); }
constexpr u32 SPIN_CAP = 1u << 16;
#define WIN 8
#define QW 4
#define WING 3
#define QF_T 300
#define E1_T 250
#define SCP_T 215
#define E3_T 480
#define E6_T 1200
#define T4_DELAY 30
#define TAU 32.f
#define VCLIP 40
#define PF_LEN 32900
#define G_OF(v) ((v) > 32768.f ? 8192u : 0u)
#define VOTE_STAMP(v)
#define STAMP_DECL
#define STAMP(i)
#define STAMP_FLUSH()

DI float bf2f(u32 b) { return __uint_as_float(b << 16); }
DI float rbf(float f) { return (float)(__bf16)f; }
DI u32 f2bf(float f) { return (u32)__builtin_bit_cast(u16, (__bf16)f); }
typedef __bf16 bf16x2_t __attribute__((ext_vector_type(2)));
DI u32 pk2bf(float a, float b) { return __builtin_bit_cast(u32, __builtin_convertvector(f32x2{a, b}, bf16x2_t)); }
DI float lo16(u32 v) { return __uint_as_float(v << 16); }
DI float hi16(u32 v) { return __uint_as_float(v & 0xFFFF0000u); }
DI u32x4 ldnt(const u16* p) { return __builtin_nontemporal_load((const u32x4*)p); }

DI float silu_bf(float a) { return rbf(a * __builtin_amdgcn_rcpf(1.f + __expf(-a))); }

template <int CTRL> DI float dppf(float v) {
  return __int_as_float(__builtin_amdgcn_update_dpp(0, __float_as_int(v), CTRL, 0xF, 0xF, true));
}
DI float quad_sum(float v) { v += dppf<0xB1>(v); v += dppf<0x4E>(v); return v; }
DI float b23_sum(float v) { v += dppf<0x128>(v); v += dppf<0x124>(v); return v; }
DI float b3_sum(float v) { return v + dppf<0x128>(v); }
DI float x16_sum(float v) {
  const auto r = __builtin_amdgcn_permlane16_swap(__float_as_uint(v), __float_as_uint(v), false, false);
  return __uint_as_float(r[0]) + __uint_as_float(r[1]);
}
DI float x32_sum(float v) {
  const auto r = __builtin_amdgcn_permlane32_swap(__float_as_uint(v), __float_as_uint(v), false, false);
  return __uint_as_float(r[0]) + __uint_as_float(r[1]);
}
DI float rs32(float a, float b) {
  const auto r = __builtin_amdgcn_permlane32_swap(__float_as_uint(a), __float_as_uint(b), false, false);
  return __uint_as_float(r[0]) + __uint_as_float(r[1]);
}
DI float rs16(float a, float b) {
  const auto r = __builtin_amdgcn_permlane16_swap(__float_as_uint(a), __float_as_uint(b), false, false);
  return __uint_as_float(r[0]) + __uint_as_float(r[1]);
}
DI float row16_sum(float v) { return b23_sum(quad_sum(v)); }
DI float wave_sum(float v) { return x32_sum(x16_sum(row16_sum(v))); }
DI void row8_sum4(float& a, float& b, float& c, float& d) {
#define R4(C) "v_add_f32_dpp %0, %0, %0 " C " row_mask:0xf bank_mask:0xf\n v_add_f32_dpp %1, %1, %1 " C " row_mask:0xf bank_mask:0xf\n" \
              "v_add_f32_dpp %2, %2, %2 " C " row_mask:0xf bank_mask:0xf\n v_add_f32_dpp %3, %3, %3 " C " row_mask:0xf bank_mask:0xf\n"
  asm("s_nop 1\n" R4("quad_perm:[1,0,3,2]") R4("quad_perm:[2,3,0,1]") R4("row_half_mirror") : "+v"(a), "+v"(b), "+v"(c), "+v"(d));
#undef R4
}
DI float row16_max(float v) {
  v = fmaxf(v, dppf<0xB1>(v)); v = fmaxf(v, dppf<0x4E>(v));
  v = fmaxf(v, dppf<0x128>(v)); v = fmaxf(v, dppf<0x124>(v));
  return v;
}
DI float x16_max(float v) {
  const auto r = __builtin_amdgcn_permlane16_swap(__float_as_uint(v), __float_as_uint(v), false, false);
  return fmaxf(__uint_as_float(r[0]), __uint_as_float(r[1]));
}
DI float x32_max(float v) {
  const auto r = __builtin_amdgcn_permlane32_swap(__float_as_uint(v), __float_as_uint(v), false, false);
  return fmaxf(__uint_as_float(r[0]), __uint_as_float(r[1]));
}
DI float wave_max(float v) { return x32_max(x16_max(row16_max(v))); }

DI float dot2(u32 a, u32 b, float c) {
  return __builtin_amdgcn_fdot2_f32_bf16(__builtin_bit_cast(bf16x2_t, a), __builtin_bit_cast(bf16x2_t, b), c, false);
}
DI void fma8b(float* acc, u32 sbits, u32x4 w) {
  const u32 sh = sbits << 16;
  acc[0] = dot2(w.x, sbits, acc[0]); acc[1] = dot2(w.x, sh, acc[1]);
  acc[2] = dot2(w.y, sbits, acc[2]); acc[3] = dot2(w.y, sh, acc[3]);
  acc[4] = dot2(w.z, sbits, acc[4]); acc[5] = dot2(w.z, sh, acc[5]);
  acc[6] = dot2(w.w, sbits, acc[6]); acc[7] = dot2(w.w, sh, acc[7]);
}

DI unsigned fsw(unsigned row) { return ((row & 3) << 2) | ((row >> 2) & 3); }
DI unsigned off_b(unsigned row, unsigned ch) { return 256u * row + 16u * (ch ^ fsw(row)); }
DI unsigned tr_addr(unsigned lane, unsigned c, unsigned ks, unsigned t) {
  const unsigned h = lane >> 5, blk = (lane >> 4) & 1, q = (lane & 15) >> 2, p = lane & 3;
  return off_b(16 * ks + 8 * h + 4 * t + q, 4 * c + 2 * blk + (p >> 1)) + 8 * (p & 1);
}

typedef __amdgpu_buffer_rsrc_t rsrc_t;
DI u32x4 ld128(rsrc_t rs, int off) { return __builtin_amdgcn_raw_buffer_load_b128(rs, off, 0, AUX_SC1); }
DI u32x2 ld64(rsrc_t rs, int off) { return __builtin_amdgcn_raw_buffer_load_b64(rs, off, 0, AUX_SC1); }
DI void st64(rsrc_t rs, int off, u32 lo, u32 hi) { u32x2 v; v.x = lo; v.y = hi; __builtin_amdgcn_raw_buffer_store_b64(v, rs, off, 0, AUX_SC1); }
DI void st128(rsrc_t rs, int off, u32x4 v) { __builtin_amdgcn_raw_buffer_store_b128(v, rs, off, 0, AUX_SC1); }
#define BAR() do { asm volatile("s_waitcnt lgkmcnt(0)" ::: "memory"); __builtin_amdgcn_s_barrier(); asm volatile("" ::: "memory"); } while (0)
#define BAR0() do { __builtin_amdgcn_sched_barrier(0); asm volatile("" ::: "memory"); __builtin_amdgcn_s_barrier(); asm volatile("" ::: "memory"); __builtin_amdgcn_sched_barrier(0); } while (0)
#define PIN(x) asm volatile("" : "+v"(x))
constexpr u32 POISON = 0xFFFFFFFFu;
DI bool pois4(u32x4 v) { return max(max(v.x, v.y), max(v.z, v.w)) == POISON; }
DI u32 nopois(u32 v) { return min(v, 0xFFFFFFFEu); }
DI u32 fbits(float v) { return nopois(__float_as_uint(v)); }

DI u32x2 mk_gran(float v, u32 tlo, u32 thi) {
  u32 b = __float_as_uint(v);
  if ((b & 0x7F800000u) != 0x7F800000u) b += 0x80u;
  u32x2 g; g.x = (b >> 8) | (tlo << 24); g.y = (tlo >> 8) | (thi << 24);
  return g;
}
DI void st_gran2(rsrc_t rs, int off, float v0, float v1, u32 tlo, u32 thi) {
  const u32x2 a = mk_gran(v0, tlo, thi), b = mk_gran(v1, tlo, thi);
  u32x4 v; v.x = a.x; v.y = a.y; v.z = b.x; v.w = b.y;
  st128(rs, off, v);
}
DI bool gran_ok(u32 lo, u32 hi, u32 tlo, u32 thi) { return (((lo >> 24) ^ (tlo & 0xFFu)) | (hi ^ ((tlo >> 8) | (thi << 24)))) == 0u; }
DI float gran_val(u32 lo) { return __uint_as_float(lo << 8); }

template <int N, int SLP = 1> DI void gather128(rsrc_t rs, const int (&off)[N], u32x4 (&v)[N]) {
#pragma unroll
  for (int i = 0; i < N; ++i) v[i] = ld128(rs, off[i]);
  _Pragma("unroll 1") for (u32 spins = 0; spins < SPIN_CAP; ++spins) {
    bool need[N], bad = false;
#pragma unroll
    for (int i = 0; i < N; ++i) { need[i] = pois4(v[i]); bad = bad || need[i]; }
    if (!__any(bad)) break;
    __builtin_amdgcn_s_sleep(SLP);
    asm volatile("" ::: "memory");
    __builtin_amdgcn_sched_barrier(0);
#pragma unroll
    for (int i = 0; i < N; ++i)
      if (need[i]) v[i] = ld128(rs, off[i]);
  }
}
template <int N> DI void waitvm() { asm volatile("s_waitcnt vmcnt(%0)" ::"n"(N) : "memory"); }
constexpr u32 LSPIN_CAP = 1u << 22;
DI void lds_wait(volatile LAS u32* p, u32 val) {
  _Pragma("unroll 1") for (u32 n = 0; *p < val && n < LSPIN_CAP; ++n) __builtin_amdgcn_s_sleep(1);
  asm volatile("" ::: "memory");
}
#define WAVE_LDS_SYNC() do { __builtin_amdgcn_fence(__ATOMIC_RELEASE, "wavefront"); __builtin_amdgcn_wave_barrier(); __builtin_amdgcn_fence(__ATOMIC_ACQUIRE, "wavefront"); } while (0)
#define SB() __builtin_amdgcn_sched_barrier(0)
#define RARE(c) __builtin_expect(!!(c), 0)

constexpr int PS = 28672;
constexpr int L_QI = 4 * PS;
constexpr int L_P = L_QI + 32768;
constexpr int L_REF = L_P + 11264;
constexpr int L_MX = L_REF + 1408;
constexpr int L_ALW = L_MX + 128;
constexpr int L_DUMP = L_ALW + 512;
constexpr int L_QOFF = 131072;
constexpr int L_H1RED = L_QOFF;
constexpr int L_RED = L_QOFF + 16384;
constexpr int L_Z = L_RED, L_HH = L_RED + 7 * 1088, L_ZERO = L_HH + 64, L_CF = L_ZERO + 64;
constexpr int L_MISC = L_QOFF + 30720;
constexpr int L_CTR = L_MISC;
constexpr int L_WMAX = L_MISC + 256;
constexpr int L_ML = L_MISC + 512;

#define Z4 (u32x4{0u, 0u, 0u, 0u})
DI float max5(f32x4 v, float e) { return fmaxf(fmaxf(fmaxf(fmaxf(v.x, v.y), v.z), v.w), e); }

template <bool NT> DI void dma(u32 m, u32 voff, const void* sb) {
  if (NT) asm volatile("s_mov_b32 m0, %0\n s_nop 0\n global_load_lds_dwordx4 %1, %2 nt" ::"s"(m), "v"(voff), "s"(sb) : "memory");
  else asm volatile("s_mov_b32 m0, %0\n s_nop 0\n global_load_lds_dwordx4 %1, %2" ::"s"(m), "v"(voff), "s"(sb) : "memory");
}
DI void dma_sc1(u32 m, u32 voff, const void* sb) {
  asm volatile("s_mov_b32 m0, %0\n s_nop 0\n global_load_lds_dwordx4 %1, %2 sc1" ::"s"(m), "v"(voff), "s"(sb) : "memory");
}
DI void lds_add(int idx) {
  SB();
  asm volatile("s_mov_b64 exec, 1\n ds_add_u32 %0, %1 offset:%2\n s_mov_b64 exec, -1" ::"v"((u32)L_CTR), "v"(1u), "n"(idx * 4) : "memory");
  SB();
}
DI u32 spin_at(u32 a, u32 val) {
  u32 t, c, m, cap;
  asm volatile("s_mov_b32 %3, %6\n 1: ds_read_b32 %0, %4\n s_waitcnt lgkmcnt(0)\n v_readfirstlane_b32 %1, %0\n s_and_b32 %2, %1, 255\n s_cmp_ge_u32 %2, %5\n s_cbranch_scc1 2f\n"
               "s_sub_u32 %3, %3, 1\n s_cbranch_scc0 1b\n 2:"
               : "=&v"(t), "=&s"(c), "=&s"(m), "=&s"(cap) : "v"(a), "s"(val), "n"(LSPIN_CAP) : "memory", "scc");
  return c;
}
DI void lds_spin(volatile LAS u32* p, u32 val) { spin_at((u32)(uintptr_t)p, val); }
enum { C_SOK = 0, C_BH = 4, C_PD = 12, C_H1 = 18, C_PAR = 19, C_VOTE = 20, C_FREE = 22, C_QRDY = 23, C_MXC = 25, C_SD = 26, C_GX = 27, C_ML = 28, C_MK = 32, C_P = 44 };

DI u32 kv_mark(int nm, int skip) { return L_CTR + 4 * C_MK + __builtin_amdgcn_readfirstlane(4 * nm + (nm >= skip ? 4 : 0)); }
#define KV_TS
#define KV_TSO
#define KV_TSA
#define KV_TSP
#define KV_TRY(l)                                                                                                        \
  "7: s_getreg_b32 %[h], hwreg(HW_REG_IB_STS)\n s_and_b32 %[h], %[h], 0xC0000F\n s_max_i32 %[h], %[h], %[x]\n s_sub_i32 %[h], %[h], %[x]\n s_cmp_gt_i32 %[h], %[k4]\n s_cbranch_scc1 " l "\n" \
  "s_waitcnt vmcnt(%[x4])\n s_cmp_eq_u32 %[k4], 4\n s_cbranch_scc1 8f\n s_waitcnt vmcnt(%[x])\n"                        \
  "8: v_subrev_u32 %[t], %[k4], %[ml]\n s_mov_b64 exec, 1\n ds_add_u32 %[t], %[one] offset:12\n s_mov_b64 exec, -1\n"   \
  "s_sub_i32 %[k4], %[k4], 4\n" KV_TS "s_branch 7b\n"
template <int X> DI void kv_try(int& k4, u32 ml, u32 one KV_TSA) {
  u32 t, h;
  asm volatile(KV_TRY("9f") "9:" : [t] "=&v"(t), [h] "=&s"(h), [k4] "+s"(k4) KV_TSO : [ml] "v"(ml), [one] "v"(one), [x] "n"(X), [x4] "n"(X + 4) : "memory", "scc");
}
template <int X> DI u32 spin_try(int idx, u32 val, int& k4, u32 ml, u32 one KV_TSA) {
  u32 t, h, c, cap;
  asm volatile("s_mov_b32 %[cap], %[n]\n 1: ds_read_b32 %[t], %[ml] offset:%[o]\n s_waitcnt lgkmcnt(0)\n v_readfirstlane_b32 %[c], %[t]\n s_and_b32 %[h], %[c], 255\n"
               "s_cmp_ge_u32 %[h], %[v]\n s_cbranch_scc1 2f\n" KV_TRY("9f") "9: s_sub_u32 %[cap], %[cap], 1\n s_cbranch_scc0 1b\n 2:"
               : [t] "=&v"(t), [h] "=&s"(h), [c] "=&s"(c), [cap] "=&s"(cap), [k4] "+s"(k4) KV_TSO
               : [ml] "v"(ml), [one] "v"(one), [o] "n"(4 * idx), [v] "n"(val), [n] "n"(LSPIN_CAP), [x] "n"(X), [x4] "n"(X + 4) : "memory", "scc");
  return c;
}
DI void kv_load(const u16* ckv, u32 vl, int lw, u32 g0, u32 g1, u32 m, int n0, int n1, int& nm, int skip) {
#pragma unroll 1
  for (int n = n0; n < n1; ++n) {
#define KV_POST(k) ".if %[wm] == " #k "\n s_cmp_gt_i32 %[nm], %[n2]\n s_cbranch_scc1 20f\n s_mov_b64 exec, 1\n ds_add_u32 %[ma], %[one]\n s_mov_b64 exec, -1\n s_add_i32 %[nm], %[nm], 1\n 20:\n .endif\n"
#define KV_W(o, k) "s_waitcnt vmcnt(%[wm])\n" KV_POST(k) "global_load_lds_dwordx4 %[vo], %[p] offset:" #o " nt\n"
#define KV_M "s_add_u32 m0, m0, %[ms]\n"
    asm volatile("s_mov_b32 m0, %[m]\n" KV_W(0, 4) KV_M KV_W(256, 5) KV_M KV_W(512, 6) KV_M KV_W(768, 7)
                 : [nm] "+s"(nm)
                 : [m] "s"(m), [vo] "v"(vl + g0 + ((lw + n) & 3) * 4096), [p] "s"(ckv), [ma] "v"(kv_mark(nm, skip)),
                   [one] "v"(1u), [n2] "s"(__builtin_amdgcn_readfirstlane(n - 2)), [wm] "n"(WIN - 1), [ms] "n"(PS - 256)
                 : "memory", "scc");
    g0 = g1;
    g1 += 192 * 16384;
    m += n + 1 == skip ? 8192 : 4096;
  }
}
DI void until(u32 t0, u32 dt) {
  while ((u32)__builtin_amdgcn_s_memrealtime() - t0 < dt) __builtin_amdgcn_s_sleep(2);
}
DI bf16x8 tr_frag(unsigned char* lds, u32 a0, u32 a1) {
  const s16x4 b0 = __builtin_amdgcn_ds_read_tr16_b64_v4i16((LAS s16x4*)(lds + a0));
  const s16x4 b1 = __builtin_amdgcn_ds_read_tr16_b64_v4i16((LAS s16x4*)(lds + a1));
  bf16x8 v;
  v[0] = b0[0]; v[1] = b0[1]; v[2] = b0[2]; v[3] = b0[3]; v[4] = b1[0]; v[5] = b1[1]; v[6] = b1[2]; v[7] = b1[3];
  return v;
}
DI float sum4(f32x4 v) { const f32x2 t = f32x2{v.x, v.y} + f32x2{v.z, v.w}; return t.x + t.y; }

DI void ln_stats(u32x4 yv, float& mb, float& rsd) {
  const float a1 = dot2(yv.x, 0x3F803F80u, dot2(yv.y, 0x3F803F80u, 0.f)) + dot2(yv.z, 0x3F803F80u, dot2(yv.w, 0x3F803F80u, 0.f));
  const float a2 = dot2(yv.x, yv.x, dot2(yv.y, yv.y, 0.f)) + dot2(yv.z, yv.z, dot2(yv.w, yv.w, 0.f));
  const float sr = x16_sum(row16_sum(rs32(a1, a2))) * (1.f / 512.f);
  const float s1 = __uint_as_float(__builtin_amdgcn_readlane(__float_as_uint(sr), 0)), s2 = __uint_as_float(__builtin_amdgcn_readlane(__float_as_uint(sr), 32));
  const float var = rbf(fmaxf(s2 - s1 * s1, 0.f));
  mb = rbf(s1);
  const float sd = rbf(__builtin_amdgcn_sqrtf(rbf(var + 1e-5f)));
  rsd = __builtin_amdgcn_rcpf(sd);
  rsd = rsd * (2.f - sd * rsd);
}

__global__ __launch_bounds__(512, 2) void k_main(const u16* __restrict__ x, const u16* __restrict__ ckv,
                                                 const u16* __restrict__ w_q, const u16* __restrict__ w_uk,
                                                 const u16* __restrict__ w_uv, const u16* __restrict__ w_o,
                                                 const u16* __restrict__ w_gate, const u16* __restrict__ w_up,
                                                 const u16* __restrict__ w_down, const u16* __restrict__ cvec,
                                                 const u16* __restrict__ w_mod, u16* __restrict__ out,
                                                 unsigned char* ws) {
  __shared__ __attribute__((aligned(16))) unsigned char lds[163840];
  const int w = __builtin_amdgcn_readfirstlane(threadIdx.x >> 6), j = blockIdx.x;
  int lane = threadIdx.x & 63;
  const int xc = j & 7, ii = j >> 3;
  STAMP_DECL;
  STAMP(0);
  u32x2 ev;
  u32 xm, vh;
  {
    u64 p, pc, pw;
    u32 a, b, c, d, f, t;
    asm volatile("s_lshr_b32 %[f], %[j], 6\n s_cmp_eq_u32 %[f], 1\n s_cselect_b32 %[a], 0x50, 16\n"
                 "s_load_dwordx2 %[p], %[ka], %[a]\n s_load_dwordx2 %[pw], %[ka], 0x60\n"
                 "s_lshr_b32 %[d], %[j], 3\n s_cmp_lg_u32 %[w], 7\n s_cbranch_scc1 2f\n"
                 "s_and_b32 %[a], %[d], 15\n s_lshl_b32 %[a], %[a], 12\n v_lshl_add_u32 %[t], %[lane], 6, %[a]\n v_min_u32 %[t], %[pfl], %[t]\n"
                 "s_getpc_b64 %[pc]\n s_mov_b32 m0, %[dump]\n s_nop 0\n global_load_lds_dword %[t], %[pc]\n"
                 "2: s_load_dwordx2 %[pc], %[ka], 0x48\n s_and_b32 %[c], %[j], 7\n s_lshl_b32 %[c], %[c], 10\n s_cmp_eq_u32 %[f], 0\n s_cbranch_scc0 3f\n"
                 "s_and_b32 %[a], %[d], 1\n s_lshl1_add_u32 %[a], %[w], %[a]\n s_lshl_b32 %[a], %[a], 18\n s_add_u32 %[c], %[c], %[a]\n"
                 "s_lshr_b32 %[a], %[d], 1\n s_lshl_b32 %[a], %[a], 8\n s_add_u32 %[c], %[c], %[a]\n s_lshl_b32 m0, %[w], 14\n"
                 "v_lshrrev_b32 %[t], 4, %[lane]\n v_mul_u32_u24 %[t], 0x204, %[t]\n v_and_b32 %[vh], 15, %[lane]\n v_xor_b32 %[vh], %[vh], %[t]\n"
                 "v_lshl_add_u32 %[vh], %[vh], 4, %[c]\n v_xor_b32 %[t], 0x8010, %[vh]\n s_waitcnt lgkmcnt(0)\n"
                 "global_load_lds_dwordx4 %[vh], %[p]\n s_add_u32 m0, m0, 0x400\n s_nop 0\n"
                 "global_load_lds_dwordx4 %[t], %[p]\n s_add_u32 m0, m0, 0x400\n v_xor_b32 %[t], 0x10020, %[vh]\n"
                 "global_load_lds_dwordx4 %[t], %[p]\n s_add_u32 m0, m0, 0x400\n v_xor_b32 %[t], 0x18030, %[vh]\n"
                 "global_load_lds_dwordx4 %[t], %[p]\n s_branch 1f\n"
                 "3: s_lshl2_add_u32 %[d], %[d], %[w]\n s_sub_u32 %[d], %[d], 68\n s_lshl_b32 %[d], %[d], 16\n s_add_u32 %[d], %[d], %[c]\n"
                 "s_and_b32 %[a], %[j], 3\n s_lshl2_add_u32 %[b], %[a], %[w]\n s_mul_i32 %[b], %[b], 0x30000\n s_lshl_b32 %[a], %[a], 8\n"
                 "v_lshl_add_u32 %[xm], %[lane], 2, %[a]\n"
                 "s_and_b32 %[a], %[j], 60\n s_lshl_b32 %[a], %[a], 5\n s_add_u32 %[b], %[b], %[a]\n"
                 "s_lshl_b32 %[c], %[w], 12\n s_add_u32 %[c], %[c], %[mq]\n s_lshl_b32 %[a], %[w], 10\n s_add_u32 %[a], %[a], %[mh]\n"
                 "s_cmp_eq_u32 %[f], 1\n s_cselect_b32 %[d], %[b], %[d]\n s_cselect_b32 m0, %[c], %[a]\n s_cselect_b32 %[a], 48, 1\n s_cselect_b32 %[c], 0, 4\n"
                 "v_lshrrev_b32 %[t], 3, %[lane]\n v_mul_u32_u24 %[t], %[a], %[t]\n v_and_b32 %[vh], 7, %[lane]\n v_lshl_add_u32 %[vh], %[t], 3, %[vh]\n"
                 "v_lshl_add_u32 %[vh], %[vh], 4, %[d]\n"
                 "s_cselect_b32 %[b], 6, 1\n s_cselect_b32 %[a], 0x400, 0\n s_lshl_b32 %[b], %[b], 13\n"
                 "s_xor_b32 %[c], %[c], %[w]\n s_bitcmp1_b32 %[c], 2\n v_mov_b32 %[t], 0\n"
                 "s_waitcnt lgkmcnt(0)\n s_cbranch_scc1 1f\n s_cmp_eq_u32 %[f], 1\n s_cbranch_scc0 4f\n"
                 "global_load_dword %[xm], %[xm], %[pc]\n global_load_dwordx2 %[ev], %[t], %[pw] sc1\n"
                 "4: global_load_lds_dwordx4 %[vh], %[p]\n s_add_u32 m0, m0, %[a]\n v_add_u32 %[t], %[b], %[vh]\n"
                 "global_load_lds_dwordx4 %[t], %[p]\n s_add_u32 m0, m0, %[a]\n v_add_u32 %[t], %[b], %[t]\n"
                 "global_load_lds_dwordx4 %[t], %[p]\n s_add_u32 m0, m0, %[a]\n v_add_u32 %[t], %[b], %[t]\n"
                 "global_load_lds_dwordx4 %[t], %[p]\n"
                 "1:\n"
                 : [xm] "=&v"(xm), [ev] "=&v"(ev), [vh] "=&v"(vh), [t] "=&v"(t), [p] "=&s"(p), [pc] "=&s"(pc), [pw] "=&s"(pw), [a] "=&s"(a), [b] "=&s"(b), [c] "=&s"(c),
                   [d] "=&s"(d), [f] "=&s"(f)
                 : [j] "s"(j), [w] "s"(w), [lane] "v"(lane), [ka] "s"(__builtin_amdgcn_kernarg_segment_ptr()), [pfl] "n"(PF_LEN), [dump] "n"(L_DUMP),
                   [mq] "n"(L_QOFF + 8192), [mh] "n"(L_P)
                 : "memory", "scc");
  }
  SB();
  const u32 t0 = (u32)__builtin_amdgcn_s_memrealtime() - 35u;
  const bool isQ = j < 64, isS0 = j >= 64 && j < 128;
  const int hq = xc * 4 + (ii >> 1), dh = ii & 1, sj = j - 64;
  const int lw = w - 4, myh = xc >> 2;
  int col = lane & 15, g4 = lane >> 4, tq = col >> 2, tp = lane & 3;
  const rsrc_t rs = __builtin_amdgcn_make_buffer_rsrc((void*)ws, 0, WS_TOTAL, 0x00020000);
  volatile LAS u32* ctr = (volatile LAS u32*)(lds + L_CTR);
  SB();
  STAMP(25);
  if (w < 2) *(u32*)(lds + (w == 0 ? L_CTR + lane * 4 : L_MX + (lane & 31) * 4)) = w == 0 ? 0u : 0xFF800000u;
  BAR();
  const bool tl2 = isS0;
  const int t3 = xc < 4 ? (ii < 8 ? xc * 8 + ii : -1) : (ii < 6 ? 8 + xc * 6 + ii : -1);
  const int t4 = (ii >= 8 && ii < 10) ? xc * 2 + (ii - 8) : -1;
  const u32 owno = (w >> 1) * 32768 + 8192 + (w & 1) * 12288;
  const int ks = (xc & 3) * 2 + myh, cg = ii & 7;
  const int h = (j >> 2) & 31, c4 = j & 3;
  u32x4 wv[8];
  u32 tlo, thi;
  int gx;
#define RELANE() do { asm volatile("v_mbcnt_lo_u32_b32 %0, -1, 0\n v_mbcnt_hi_u32_b32 %0, -1, %0" : "=v"(lane)); col = lane & 15; g4 = lane >> 4; tq = col >> 2; tp = lane & 3; } while (0)
#define TAG_OF(e0, e1)                                                                                                  \
  do {                                                                                                                  \
    const u64 tag = (((u64)(e1) << 32) | (e0)) + 1ull;                                                                  \
    tlo = __builtin_amdgcn_readfirstlane((u32)tag); thi = __builtin_amdgcn_readfirstlane((u32)(tag >> 32));             \
  } while (0)
  const u32x4 P4 = {POISON, POISON, POISON, POISON};
#define COUNT_VOTES(PRE)                                                                                                \
  do {                                                                                                                  \
    u32x2 a_ = {0u, 0u};                                                                                                \
    if (PRE) a_ = *(const u32x2*)(lds + L_QI + lane * 8);                                                               \
    _Pragma("unroll 1") for (u32 spins = 0; spins < SPIN_CAP; ++spins) {                                                \
      if (((PRE) || spins) && __all(gran_ok(a_.x, a_.y, tlo, thi))) break;                                              \
      if (spins) __builtin_amdgcn_s_sleep(8);                                                                           \
      asm volatile("" ::: "memory");                                                                                    \
      a_ = ld64(rs, WS_VOTE + xc * 2048 + lane * 8);                                                                    \
    }                                                                                                                   \
    const float vt_ = wave_sum((float)(a_.x & 1023u));                                                                  \
    if (lane == 0) ctr[C_PAR] = G_OF(vt_);                                                                              \
    VOTE_STAMP(vt_);                                                                                                    \
    STAMP(8);                                                                                                           \
  } while (0)
#define S_POLL(a_)                                                                                                      \
  _Pragma("unroll 1") for (u32 spins = 0; spins < SPIN_CAP && RARE(__any(((a_.x ^ tlo) | (a_.y ^ thi)) != 0u)); ++spins) { \
    __builtin_amdgcn_s_sleep(8);                                                                                        \
    asm volatile("" ::: "memory");                                                                                      \
    a_ = ld64(rs, WS_SFLAG + xc * 2048 + lane * 8);                                                                     \
  }
#define MB_OFF(i) (WS_MODB + ((lane >> 4) * 2048 + ((i) >> 1) * 512 + 128 * lw + 64 * ((i) & 1) + (lane & 15) * 4) * 4)

  if (!isQ && w >= 4) {
    if (isS0) {
      if (w == 4) {
        const u32x2 e4 = ld64(rs, WS_EPOCH);
        st128(rs, WS_QABS + sj * 1024 + lane * 16, P4);
        st128(rs, lane < 32 ? WS_MODB + sj * 512 + lane * 16 : WS_TOTAL, P4);
        st128(rs, WS_PROBE + il(sj * 1024 + lane * 16, 0), Z4);
        st128(rs, WS_PROBE + il(sj * 1024 + lane * 16, 1), Z4);
        waitvm<0>();
        TAG_OF(e4.x, e4.y);
        if (lane < 8) st64(rs, WS_SFLAG + lane * 2048 + sj * 8, tlo, thi);
        STAMP(27);
      }
      lds_wait(ctr + C_H1, 4u);
    } else {
#pragma unroll
      for (int i = 4; i < 16; ++i) { waitvm<7>(); dma<false>(L_P + w * 1024, vh + (i & 7) * 8192, i < 8 ? w_q : w_uk); }
    }
    const u32 vl = g4 * 1024 + ((col ^ ((g4 << 2) | lw)) << 4);
    int nm = 0, ns = isS0 ? 6 : 7;
    const int skip = isS0 ? 4 : 99;
    kv_load(ckv, vl, lw, (isS0 ? sj : 1792 + j) * 16384, (isS0 ? sj + 192 : sj) * 16384, lw * 1024, 0, ns, nm, skip);
    u32 va[8];
    va[0] = lw * PS + tr_addr(lane, 0, 0, 0);
    PIN(va[0]);
#pragma unroll
    for (int i = 1; i < 8; ++i) { va[i] = (va[0] ^ ((i >> 1) << 6) ^ ((i & 1) << 4)) + (i & 1) * 1024; PIN(va[i]); }
    bf16x8 vf[4][4];
#define V_READ(i, slot) do { asm volatile("" ::: "memory"); SB(); _Pragma("unroll") for (int c = 0; c < 4; ++c) vf[i][c] = tr_frag(lds, va[2 * c] + (slot) * 4096, va[2 * c + 1] + (slot) * 4096); SB(); } while (0)
    lds_wait(ctr + C_MK + 3, 4u);
    V_READ(0, 0); V_READ(1, 1); V_READ(2, 2); V_READ(3, 3);
    lds_add(C_FREE);
    const rsrc_t rw = __builtin_amdgcn_make_buffer_rsrc((void*)(isS0 ? w_o : w_uv), 0, 4194304, 0x00020000);
    int wo = isS0 ? (ks * 512 + 64 * w + (lane >> 3)) * 1024 + cg * 128 + (lane & 7) * 16 : (c4 * 128 + 32 * lw + g4) * 8192 + h * 256 + col * 16;
    const int wst = isS0 ? 8192 : 32768;
    PIN(wo);
#define WV_LD(i) do { SB(); wv[i] = __builtin_amdgcn_raw_buffer_load_b128(rw, wo, (i) * wst, 2); wv[(i) + 1] = __builtin_amdgcn_raw_buffer_load_b128(rw, wo, ((i) + 1) * wst, 2); SB(); } while (0)
    int eg[8];
#pragma unroll
    for (int u = 0; u < 8; ++u) { eg[u] = WS_PART + il21(((4 * (u >> 1) + 2 * (g4 >> 1) + (u & 1)) * 4 + lw) * 65536 + sj * 256 + col * 16, g4 & 1); PIN(eg[u]); }
    u32 pr_ = L_P + lane * 16;
    PIN(pr_);
    u32 ml = L_CTR + 4 * (C_MK + 7), one = 1u;
    PIN(ml); PIN(one);
#pragma unroll
    for (int i = 0; i < 16; ++i) PIN(vf[i >> 2][i & 3]);
    STAMP(2);
    lds_wait(ctr + C_FREE, 8u);
    STAMP(5);
    kv_load(ckv, vl, lw, (sj + 6 * 192) * 16384, (sj + 7 * 192) * 16384, lw * 1024, ns, ns + 4, nm, skip);
    int k4 = 4;
    STAMP(3);
    f32x16 acc[4];
#pragma unroll
    for (int c = 0; c < 4; ++c)
#pragma unroll
      for (int i = 0; i < 16; ++i) acc[c][i] = 0.f;
    bf16x8 pf[4], pn[4];
    u32 c_;
#define P_SPIN(g, n) c_ = spin_try<(g) ? 8 : 0>(C_P + (g) - C_MK - 7, n, k4, ml, one KV_TSP)
#define TRY() kv_try<8>(k4, ml, one KV_TSP)
#define P_READ(d, i0, i1, T0) _Pragma("unroll") for (int i = (i0); i < (i1); ++i) d[i] = *(volatile LAS bf16x8*)(lds + pr_ + ((T0) + i) * 1024)
#define PV(p, i) do { SB(); _Pragma("unroll") for (int c = 0; c < 4; ++c) acc[c] = __builtin_amdgcn_mfma_f32_32x32x16_bf16(p[i], vf[i][c], acc[c], 0, 0, 0); SB(); } while (0)
#define GX_USE(v) do { gx = __builtin_amdgcn_readfirstlane(v); _Pragma("unroll") for (int u = 0; u < 8; ++u) { eg[u] ^= gx; PIN(eg[u]); } } while (0)
#define MK_WAIT(T) spin_try<8>((T) - 7, 4u, k4, ml, one KV_TSP)
#define PVV(p, i, d, slot)                                                                                               \
  _Pragma("unroll") for (int c = 0; c < 4; ++c) {                                                                        \
    SB();                                                                                                                \
    acc[c] = __builtin_amdgcn_mfma_f32_32x32x16_bf16(p[i], vf[i][c], acc[c], 0, 0, 0);                                   \
    SB();                                                                                                                \
    vf[d][c] = tr_frag(lds, va[2 * c] + (slot) * 4096, va[2 * c + 1] + (slot) * 4096);                                   \
    SB();                                                                                                                \
  }
#define CT(i) (*(volatile LAS u32*)(lds + ml + 4 * ((i) - C_MK - 7)))
    P_SPIN(0, 4u);
    P_READ(pf, 0, 4, 0);
    const u32 k7 = CT(C_MK + 7);
    asm volatile("" ::: "memory");
    STAMP(6);
    PVV(pf, 0, 0, 4); kv_try<0>(k4, ml, one KV_TSP); WV_LD(0);
    PVV(pf, 1, 1, 5); kv_try<2>(k4, ml, one KV_TSP); WV_LD(2);
    PVV(pf, 2, 2, 6); WV_LD(4);
    u32 cv = CT(C_P + 1);
    P_READ(pn, 0, 4, 4);
    PVV(pf, 3, 3, 0); WV_LD(6);
    if (RARE(__builtin_amdgcn_readfirstlane(k7) < 4u)) { MK_WAIT(7); V_READ(0, 4); V_READ(1, 5); V_READ(2, 6); V_READ(3, 0); }
    STAMP(9);
    c_ = __builtin_amdgcn_readfirstlane(cv);
    if (RARE((c_ & 255u) != 4u)) { P_SPIN(1, 4u); P_READ(pn, 0, 4, 4); }
    int slow = -1;
    if (RARE(c_ > 255u)) slow = 4;
    else {
      if (!isS0) PV(pn, 0);
      TRY();
      const u32 k9 = CT(C_MK + 9);
      asm volatile("" ::: "memory");
      PVV(pn, 1, 0, 1); TRY();
      PVV(pn, 2, 1, 2);
      cv = CT(C_P + 2);
      P_READ(pf, 0, 2, 8);
      const u32 k10 = CT(C_MK + 10);
      asm volatile("" ::: "memory");
      PVV(pn, 3, 2, 3);
      if (RARE(__builtin_amdgcn_readfirstlane(k9) < 4u)) { MK_WAIT(9); V_READ(0, 1); V_READ(1, 2); }
      STAMP(10);
      c_ = __builtin_amdgcn_readfirstlane(cv);
      if (RARE((c_ & 255u) != 4u)) { P_SPIN(2, 4u); P_READ(pf, 0, 2, 8); }
      if (RARE(c_ > 255u)) slow = 8;
      else {
        TRY();
        PV(pf, 0);
        const u32 g0 = ctr[C_GX];
        u32 g1 = ctr[C_PAR];
        cv = CT(C_P + 3);
        P_READ(pf, 2, 3, 8);
        PV(pf, 1);
        STAMP(4);
        if (RARE(__builtin_amdgcn_readfirstlane(k10) < 4u)) { waitvm<8>(); TRY(); lds_spin(ctr + C_MK + 10, 4u); V_READ(2, 3); }
        if (RARE(__builtin_amdgcn_readfirstlane(g0) == 0u)) { lds_spin(ctr + C_GX, 1u); g1 = ctr[C_PAR]; }
        GX_USE(g1);
        STAMP(11);
        c_ = __builtin_amdgcn_readfirstlane(cv);
        _Pragma("unroll 1") for (u32 n_ = 0; RARE((c_ & 255u) != 2u) && n_ < LSPIN_CAP; ++n_) { cv = CT(C_P + 3); P_READ(pf, 2, 3, 8); c_ = __builtin_amdgcn_readfirstlane(cv); }
        if (RARE(c_ > 255u)) slow = 10;
        else PV(pf, 2);
      }
    }
    if (RARE(slow >= 0)) {
      MK_WAIT(10);
      float R = ceilf(*(const float*)(lds + L_MX + (lane & 31) * 4));
#pragma unroll 1
      for (int T = slow; T < 11; ++T) {
        if (T == 4 && isS0) continue;
        spin_at(L_CTR + 4 * (C_P + (T < 8 ? 1 : T < 10 ? 2 : 3)), T < 10 ? 4u : 2u);
        const float rf = *(const float*)(lds + L_REF + T * 128 + (lane & 31) * 4);
        u32x4 p = *(const u32x4*)(lds + pr_ + T * 1024);
        const u32 so_ = (T < 7 ? T : T - 7) * 4096;
#pragma unroll
        for (int c = 0; c < 4; ++c) vf[0][c] = tr_frag(lds, va[2 * c] + so_, va[2 * c + 1] + so_);
        const float Rn = fmaxf(R, rf), ps = __builtin_amdgcn_exp2f(rf - Rn);
        *(float*)(lds + L_ALW + lw * 128 + (lane & 31) * 4) = __builtin_amdgcn_exp2f(R - Rn);
        R = Rn;
        WAVE_LDS_SYNC();
#pragma unroll
        for (int i = 0; i < 4; ++i) {
          const f32x4 al = *(const f32x4*)(lds + L_ALW + lw * 128 + (lane >> 5) * 16 + i * 32);
#pragma unroll
          for (int c = 0; c < 4; ++c)
#pragma unroll
            for (int r = 0; r < 4; ++r) acc[c][4 * i + r] *= al[r];
          p[i] = pk2bf(lo16(p[i]) * ps, hi16(p[i]) * ps);
        }
        WAVE_LDS_SYNC();
        pf[0] = __builtin_bit_cast(bf16x8, p);
        PV(pf, 0);
      }
      if (slow != 10) { lds_spin(ctr + C_GX, 1u); GX_USE(ctr[C_PAR]); }
    }
    STAMP(30);
#pragma unroll
    for (int u = 0; u < 8; u += 2) {
      u32 c[8];
#pragma unroll
      for (int k = 0; k < 8; ++k) c[k] = pk2bf(acc[k & 2][2 * u + (k & 1) + (k >> 2) * 2], acc[(k & 2) + 1][2 * u + (k & 1) + (k >> 2) * 2]);
      SB();
#pragma unroll
      for (int k = 0; k < 2; ++k) {
        const auto r0 = __builtin_amdgcn_permlane16_swap(c[4 * k], c[4 * k + 1], false, false);
        const auto r1 = __builtin_amdgcn_permlane16_swap(c[4 * k + 2], c[4 * k + 3], false, false);
        st128(rs, eg[u + k], u32x4{r0[0], r1[0], r0[1], r1[1]});
      }
      SB();
    }
    STAMP(31);
    if (!isS0) {
#pragma unroll
      for (int i = 0; i < 8; ++i) *(u32x4*)(lds + lw * PS + 16384 + off_b(4 * i + g4, col)) = wv[i];
    }
    {
      int fo = lane < 16 ? (WS_LSE + il(sj * 512 + lw * 128 + (lane & 7) * 16, lane >> 3)) ^ gx : WS_TOTAL;
      lds_spin(ctr + C_ML, 1u);
      u32x4 ml4 = *(const u32x4*)(lds + L_WMAX + col * 16);
      PIN(fo); PIN(ml4);
      waitvm<0>();
      __builtin_amdgcn_s_waitcnt(0x0F70);
      st128(rs, fo, ml4);
      lds_add(C_PD);
    }
    STAMP(13);
  } else if (!isQ) {
    if (isS0) {
      u32 ta = L_QOFF + 8192 + (8 * g4 + tq) * 128 + w * 32 + tp * 8;
      u32 sa = L_H1RED + w * 256;
      int so = col < 2 ? WS_SC + (((sj >> 5) * 4 + (sj & 3)) * 512 + ((sj >> 2) & 7) * 64 + 16 * w + 4 * g4 + 2 * col) * 8 : WS_TOTAL;
      PIN(ta); PIN(so); PIN(sa);
      u32 c2, e0, e1;
      asm volatile("s_waitcnt vmcnt(4) ; HEAD_WAIT\n v_mov_b32 %0, %3\n v_mov_b32 %1, %4\n v_mov_b32 %2, %5" : "=&v"(c2), "=&v"(e0), "=&v"(e1) : "v"(xm), "v"(ev.x), "v"(ev.y) : "memory");
      TAG_OF(e0, e1);
      bf16x8 sf[4];
      u32x2 wt[8];
      asm volatile("ds_write_b32 %4, %5\n"
                   "ds_read_b128 %0, %6\n ds_read_b128 %1, %6 offset:64\n ds_read_b128 %2, %6 offset:128\n ds_read_b128 %3, %6 offset:192\n s_waitcnt lgkmcnt(0)"
                   : "=&v"(sf[0]), "=&v"(sf[1]), "=&v"(sf[2]), "=&v"(sf[3])
                   : "v"(sa + lane * 4), "v"(pk2bf(silu_bf(lo16(c2)), silu_bf(hi16(c2)))), "v"(sa + 16 * g4) : "memory");
      waitvm<0>();
      lds_add(C_BH);
      lds_spin(ctr + C_BH, 4u);
      STAMP(1);
      asm volatile("ds_read_b64_tr_b16 %0, %8\n ds_read_b64_tr_b16 %1, %8 offset:512\n ds_read_b64_tr_b16 %2, %8 offset:4096\n ds_read_b64_tr_b16 %3, %8 offset:4608\n"
                   "ds_read_b64_tr_b16 %4, %8 offset:8192\n ds_read_b64_tr_b16 %5, %8 offset:8704\n ds_read_b64_tr_b16 %6, %8 offset:12288\n ds_read_b64_tr_b16 %7, %8 offset:12800\n"
                   "s_waitcnt lgkmcnt(0)"
                   : "=&v"(wt[0]), "=&v"(wt[1]), "=&v"(wt[2]), "=&v"(wt[3]), "=&v"(wt[4]), "=&v"(wt[5]), "=&v"(wt[6]), "=&v"(wt[7]) : "v"(ta) : "memory");
      f32x4 d = {0.f, 0.f, 0.f, 0.f};
#pragma unroll
      for (int k = 0; k < 4; ++k)
        d = __builtin_amdgcn_mfma_f32_16x16x32_bf16(__builtin_bit_cast(bf16x8, u32x4{wt[2 * k].x, wt[2 * k].y, wt[2 * k + 1].x, wt[2 * k + 1].y}), sf[k], d, 0, 0, 0);
      st_gran2(rs, so, (col & 1) ? d[2] : d[0], (col & 1) ? d[3] : d[1], tlo, thi);
      lds_add(C_H1);
      STAMP(2);
      lds_wait(ctr + C_H1, 4u);
      until(t0, SCP_T);
      dma_sc1(L_P + w * 1024, WS_SC + ((ii & 7) * 4 + w) * 1024 + lane * 16, ws);
    }
    u32 ab = off_b(col, 4 * g4), qv = WS_QABS + myh * 8192 + g4 * 1024 + w * 256 + ((col ^ (g4 << 2)) << 4);
    const int hb = 16 * (w & 1) + col;
    u32 pw = L_P + (g4 >> 1) * 512 + hb * 16 + (g4 & 1) * 8, mw = hb * 4;
    PIN(ab); PIN(qv); PIN(pw); PIN(mw);
    if (w == 2) {
      until(t0, QF_T);
      const u32x2 e2 = ld64(rs, WS_EPOCH);
      u32x2 f2 = ld64(rs, WS_SFLAG + xc * 2048 + lane * 8);
      TAG_OF(e2.x, e2.y);
      S_POLL(f2);
      lds_add(C_SOK);
      STAMP(3);
    }
    bf16x8 af[16], qf[16][2];
    u32 aa[16], qa[32];
#pragma unroll
    for (int i = 0; i < 16; ++i) {
      aa[i] = (ab ^ ((i & 3) << 4)) + (((w + (i >> 2)) & 3) * PS + w * 4096); PIN(aa[i]);
#pragma unroll
      for (int u = 0; u < 2; ++u) { qa[2 * i + u] = (ab ^ ((i & 3) << 4)) + (L_QI + ((w + (i >> 2)) & 3) * 8192 + ((u ^ w) & 1) * 4096); PIN(qa[2 * i + u]); }
    }
    const int sl1 = w == 3 ? 0 : 4 + w, sl2 = 1 + (w >> 1);
#define A_RD(i, o) af[i] = *(const bf16x8*)(lds + aa[i] + (o))
#define A_READ(i0, i1, o) _Pragma("unroll") for (int i = (i0); i < (i1); ++i) A_RD(i, o)
#define A_MOVE(i0, i1, d) _Pragma("unroll") for (int i = (i0); i < (i1); ++i) { aa[i] += (d) * 4096; PIN(aa[i]); }
#define Q_RD(i, u) qf[i][u] = *(const bf16x8*)(lds + qa[2 * (i) + (u)])
#define A_FIRST() do { A_READ(0, 16, 0); lds_add(C_FREE); _Pragma("unroll") for (int i = 0; i < 16; ++i) PIN(af[i]); STAMP(12); } while (0)
    u32 qd[8];
#pragma unroll
    for (int i = 0; i < 8; ++i) { qd[i] = (qv ^ ((i & 3) << 4)) + (i >> 1) * 16384 + (i & 1) * 4096; PIN(qd[i]); }
    bool have = false;
    _Pragma("unroll 1") while ((u32)__builtin_amdgcn_s_memrealtime() - t0 < E3_T) {
      if (__builtin_amdgcn_readfirstlane(ctr[C_MK + w]) >= 4u) { have = true; break; }
      __builtin_amdgcn_s_sleep(1);
    }
    asm volatile("" ::: "memory");
    if (have) A_FIRST();
    lds_wait(ctr + C_SOK, 1u);
    until(t0, E3_T - 10);
    _Pragma("unroll 1") while ((u32)__builtin_amdgcn_s_memrealtime() - t0 < E3_T) {}
    STAMP(6);
#define QD(i) "global_load_lds_dwordx4 %[q" #i "], %[ws] sc1\n s_add_u32 m0, m0, 0x400\n s_nop 0\n"
    asm volatile("s_mov_b32 m0, %[m]\n s_nop 0\n" QD(0) QD(1) QD(2) QD(3) QD(4) QD(5) QD(6) QD(7)
                 ::[m] "s"(L_QI + w * 8192), [ws] "s"(ws), [q0] "v"(qd[0]), [q1] "v"(qd[1]), [q2] "v"(qd[2]), [q3] "v"(qd[3]), [q4] "v"(qd[4]), [q5] "v"(qd[5]), [q6] "v"(qd[6]), [q7] "v"(qd[7]) : "memory", "scc");
    if (RARE(!have)) { lds_wait(ctr + C_MK + w, 4u); A_FIRST(); }
    A_MOVE(0, 16, sl1 - w);
    u32 cb = L_CTR, cw = L_CTR + 4 * w, mx0 = mw + L_MX, mx1 = (mw ^ 64) + L_MX, pw0 = pw + w * 1024, pw1 = (pw ^ 256) + w * 1024;
    float ninf = -__builtin_inff();
    u32 one = 1u;
    PIN(ninf); PIN(one); PIN(cb); PIN(cw); PIN(mx0); PIN(mx1); PIN(pw0); PIN(pw1);
#define CTS(b, i) (*(volatile LAS u32*)(lds + (b) + 4 * (i)))
    _Pragma("unroll 1") for (u32 spins = 0; spins < SPIN_CAP; ++spins) {
      waitvm<0>();
#pragma unroll
      for (int i = 0; i < 8; ++i) Q_RD(i >> 1, i & 1);
      u32 mxu[2] = {0u, 0u};
#pragma unroll
      for (int i = 0; i < 8; ++i) { const u32x4 t_ = __builtin_bit_cast(u32x4, qf[i >> 1][i & 1]); mxu[i & 1] = max(max(mxu[i & 1], t_.x), t_.y); mxu[i & 1] = max(max(mxu[i & 1], t_.z), t_.w); }
      if (!RARE(__any(max(mxu[0], mxu[1]) == POISON))) break;
      STAMP(29);
      __builtin_amdgcn_s_sleep(2);
      const u64 b0 = __ballot(mxu[w & 1] == POISON), b1 = __ballot(mxu[~w & 1] == POISON);
#pragma unroll 1
      for (int i = 0; i < 8; ++i)
        if (((i < 4 ? b0 : b1) >> (4 * (i & 3))) & 0x000F000F000F000Full) dma_sc1(L_QI + w * 8192 + i * 1024, (qv ^ ((i & 3) << 4)) + (i >> 1) * 16384 + (i & 1) * 4096, ws);
    }
    lds_add(C_QRDY);
    STAMP(7);
    f32x4 sc[2], sn[2];
    sc[0] = sc[1] = sn[0] = sn[1] = f32x4{0.f, 0.f, 0.f, 0.f};
#define QK(d, i0, i1, NU)                                                                                                \
  do {                                                                                                                   \
    SB();                                                                                                                \
    _Pragma("unroll") for (int i = (i0); i < (i1); ++i) _Pragma("unroll") for (int u = 0; u < (NU); ++u)                 \
      d[u] = __builtin_amdgcn_mfma_f32_16x16x32_bf16(af[i], qf[i][u], d[u], 0, 0, 0);                                    \
    SB();                                                                                                                \
  } while (0)
#define MF(d, i, u) do { SB(); d[u] = __builtin_amdgcn_mfma_f32_16x16x32_bf16(af[i], qf[i][u], d[u], 0, 0, 0); SB(); } while (0)
    const bool idle1 = isS0 && w == 0;
    u32 c1;
    {
      const u32 c0 = CTS(cb, C_QRDY);
      QK(sc, 0, 2, 2);
      if (RARE(__builtin_amdgcn_readfirstlane(c0) < 4u)) lds_spin(ctr + C_QRDY, 4u);
      asm volatile("" ::: "memory");
#pragma unroll
      for (int i = 2; i < 4; ++i) { MF(sc, i, 0); Q_RD(2 * i, 0); Q_RD(2 * i, 1); MF(sc, i, 1); Q_RD(2 * i + 1, 0); Q_RD(2 * i + 1, 1); }
#pragma unroll
      for (int i = 4; i < 12; ++i) { MF(sc, i, 0); Q_RD(i + 4, 0); MF(sc, i, 1); Q_RD(i + 4, 1); }
      c1 = CTS(cw, C_MK + 4);
      asm volatile("" ::: "memory");
#pragma unroll
      for (int i = 12; i < 16; ++i) { MF(sc, i, 0); A_RD(2 * i - 24, 0); MF(sc, i, 1); A_RD(2 * i - 23, 0); }
    }
    STAMP(9);
    float mref[2], lsum[2] = {0.f, 0.f};
#define ADDC(i, v) do { SB(); asm volatile("s_mov_b64 exec, 1\n ds_add_u32 %0, %1 offset:%2\n s_mov_b64 exec, -1" ::"v"(cb), "v"(v), "n"(4 * (i)) : "memory"); SB(); } while (0)
#define SM(d, T, NU, G, P0, P1)                                                                                          \
  do {                                                                                                                   \
    SB();                                                                                                                \
    if (RARE(__any(max5(d[0], (NU) > 1 ? max5(d[1], ninf) : ninf) > TAU))) {                                             \
      sig = 257u;                                                                                                        \
      _Pragma("unroll") for (int u = 0; u < (NU); ++u) {                                                                 \
        const float up = ceilf(x32_max(x16_max(max5(d[u], 0.f))));                                                       \
        lsum[u] *= __builtin_amdgcn_exp2f(-up);                                                                          \
        mref[u] += up;                                                                                                   \
        d[u] -= up;                                                                                                      \
        *(float*)(lds + (mw ^ (u * 64)) + L_REF + (T) * 128) = mref[u];                                                  \
      }                                                                                                                  \
    }                                                                                                                    \
    u32x2 pk[2];                                                                                                         \
    _Pragma("unroll") for (int u = 0; u < (NU); ++u) {                                                                   \
      pk[u].x = pk2bf(__builtin_amdgcn_exp2f(d[u].x), __builtin_amdgcn_exp2f(d[u].y));                                   \
      pk[u].y = pk2bf(__builtin_amdgcn_exp2f(d[u].z), __builtin_amdgcn_exp2f(d[u].w));                                   \
      *(u32x2*)(lds + (u ? (P1) : (P0))) = pk[u];                                                                        \
    }                                                                                                                    \
    ADDC(C_P + (G), sig);                                                                                                \
    _Pragma("unroll") for (int u = 0; u < (NU); ++u) { lsum[u] = dot2(pk[u].x, 0x3F803F80u, dot2(pk[u].y, 0x3F803F80u, lsum[u])); PIN(lsum[u]); } \
    SB();                                                                                                                \
  } while (0)
#define REF_WRITE(T, NU) _Pragma("unroll") for (int u = 0; u < (NU); ++u) *(float*)(lds + (mw ^ (u * 64)) + L_REF + (T) * 128) = mref[u]
    u32 sig = one;
    {
      MF(sn, 0, 0); A_RD(8, 0); MF(sn, 0, 1);
      const float m0 = max5(sc[0], ninf), m1 = max5(sc[1], ninf);
      MF(sn, 1, 0);
      asm volatile("ds_max_f32 %0, %2\n ds_max_f32 %1, %3" ::"v"(mx0), "v"(mx1), "v"(m0), "v"(m1) : "memory");
      ADDC(C_MXC, one);
      MF(sn, 1, 1); A_RD(9, 0);
#pragma unroll
      for (int i = 2; i < 4; ++i) { MF(sn, i, 0); A_RD(2 * i + 6, 0); MF(sn, i, 1); A_RD(2 * i + 7, 0); }
      MF(sn, 4, 0);
      const u32 c0 = CTS(cb, C_MXC);
      mref[0] = *(volatile LAS float*)(lds + mx0);
      mref[1] = *(volatile LAS float*)(lds + mx1);
      MF(sn, 4, 1); MF(sn, 5, 0); A_RD(14, 0); MF(sn, 5, 1); A_RD(15, 0); MF(sn, 6, 0); MF(sn, 6, 1);
      if (RARE(__builtin_amdgcn_readfirstlane(c0) < 4u)) {
        lds_spin(ctr + C_MXC, 4u);
        mref[0] = *(volatile LAS float*)(lds + mx0);
        mref[1] = *(volatile LAS float*)(lds + mx1);
      }
      u32x2 pk[2];
#pragma unroll
      for (int u = 0; u < 2; ++u) {
        MF(sn, 7 + 2 * u, 0);
        mref[u] = ceilf(mref[u]);
        float e0 = __builtin_amdgcn_exp2f(sc[u].x - mref[u]); PIN(e0);
        MF(sn, 7 + 2 * u, 1);
        pk[u].x = pk2bf(e0, __builtin_amdgcn_exp2f(sc[u].y - mref[u])); PIN(pk[u].x);
        MF(sn, 8 + 2 * u, 0);
        e0 = __builtin_amdgcn_exp2f(sc[u].z - mref[u]); PIN(e0);
        MF(sn, 8 + 2 * u, 1);
        pk[u].y = pk2bf(e0, __builtin_amdgcn_exp2f(sc[u].w - mref[u]));
        *(u32x2*)(lds + (u ? pw1 : pw0)) = pk[u];
      }
      ADDC(C_P, one);
      MF(sn, 11, 0);
      lsum[0] = dot2(pk[0].x, 0x3F803F80u, dot2(pk[0].y, 0x3F803F80u, 0.f)); PIN(lsum[0]);
      MF(sn, 11, 1);
      lsum[1] = dot2(pk[1].x, 0x3F803F80u, dot2(pk[1].y, 0x3F803F80u, 0.f)); PIN(lsum[1]);
      MF(sn, 12, 0);
      REF_WRITE(4 + w, 2);
      MF(sn, 12, 1);
      QK(sn, 13, 16, 2);
    }
    STAMP(10);
#define QK1(N0, slot)                                                                                                    \
  do {                                                                                                                   \
    _Pragma("unroll") for (int i = 0; i < 16; ++i) { MF(sc, i, 0); if (i + (N0) < 16) A_RD(i + (N0), slot); }            \
  } while (0)
    if (RARE(__builtin_amdgcn_readfirstlane(c1) < 4u && !idle1)) {
      lds_spin(ctr + C_MK + 4 + w, 4u);
      A_READ(0, 16, 0);
      sn[0] = sn[1] = f32x4{0.f, 0.f, 0.f, 0.f};
      QK(sn, 0, 16, 2);
    }
    if (RARE(idle1)) lds_add(C_P + 1);
    else { sn[0] -= mref[0]; sn[1] -= mref[1]; SM(sn, 4 + w, 2, 1, pw0 + 4096, pw1 + 4096); }
    if (RARE(w == 2)) {
      RELANE();
      dma_sc1(L_QI, WS_VOTE + xc * 2048 + lane * 16, ws);
    }
    REF_WRITE(7 + sl2, 1);
    A_MOVE(0, 16, sl2 - sl1);
    pw0 = pw + sl2 * 1024;
    sc[0] = f32x4{-mref[0], -mref[0], -mref[0], -mref[0]};
    PIN(pw0); PIN(sc[0]);
    lds_spin(ctr + C_MK + 7 + sl2, 4u);
    A_READ(0, 6, 0);
    STAMP(4);
    QK1(6, 0);
    SM(sc, 7 + sl2, 1, 2, pw0 + 7168, 0);
    STAMP(11);
    if (!RARE(w >= 2)) {
      REF_WRITE(10, 1);
      sc[0] = f32x4{-mref[0], -mref[0], -mref[0], -mref[0]};
      PIN(sc[0]);
      _Pragma("unroll 1") for (u32 n_ = 0; n_ < LSPIN_CAP; ++n_) {
        const u32 k_ = CTS(cb, C_MK + 10);
        asm volatile("" ::: "memory");
        A_READ(0, 6, 8192);
        SB();
        if (__builtin_amdgcn_readfirstlane(k_) >= 4u) break;
      }
      STAMP(5);
      QK1(6, 8192);
      SM(sc, 10, 1, 3, pw0 + 9216, 0);
    } else if (w == 2) {
      RELANE();
      waitvm<0>();
      COUNT_VOTES(true);
      lds_add(C_GX);
    }
    STAMP(30);
    RELANE();
    {
#pragma unroll
      for (int u = 0; u < 2; ++u) {
        *(float*)(lds + L_ML + (mw ^ (u * 64)) * 4 + w * 4) = mref[u];
        *(float*)(lds + L_ML + 512 + (mw ^ (u * 64)) * 4 + w * 4) = x32_sum(x16_sum(lsum[u]));
      }
      u32 old;
      f32x4 m4, l4;
      asm volatile("s_mov_b64 exec, 1\n ds_add_rtn_u32 %0, %3, %4 offset:%6\n s_mov_b64 exec, -1\n ds_read_b128 %1, %5\n ds_read_b128 %2, %5 offset:512\n s_waitcnt lgkmcnt(0)"
                   : "=&v"(old), "=&v"(m4), "=&v"(l4) : "v"((u32)L_CTR), "v"(1u), "v"(L_ML + (lane & 31) * 16), "n"(4 * C_SD) : "memory");
      if (__builtin_amdgcn_readfirstlane(old) == 3u) {
        const float M = fmaxf(fmaxf(m4.x, m4.y), fmaxf(m4.z, m4.w));
        const float l = (l4.x * __builtin_amdgcn_exp2f(m4.x - M) + l4.y * __builtin_amdgcn_exp2f(m4.y - M)) + (l4.z * __builtin_amdgcn_exp2f(m4.z - M) + l4.w * __builtin_amdgcn_exp2f(m4.w - M));
        *(u32x2*)(lds + L_WMAX + (lane & 1) * 128 + (lane & 30) * 4) = u32x2{fbits(M), fbits(l)};
        lds_add(C_ML);
      }
    }
    lds_wait(ctr + C_GX, 1u);
    gx = __builtin_amdgcn_readfirstlane(ctr[C_PAR]);
    STAMP(13);
  } else {
    const int mu = 2 * j + (w >> 2);
#define Q_LOAD(i0, i1)                                                                                                   \
  _Pragma("unroll") for (int i = (i0); i < (i1); ++i) {                                                                  \
    waitvm<QW - 1>();                                                                                                    \
    dma<false>(w * 16384 + (i & 15) * 1024, (vq ^ ((i & 3) << 4)) + ((i < 8 ? 64 * w + 32 * dh : i < 16 ? 64 * w + 32 - 32 * dh : 256 * dh + 32 * w) + 4 * (i & 7)) * 8192, i < 16 ? w_q : w_uk); \
  }
    const u32 vq = g4 * 8192 + hq * 256 + ((col ^ (g4 << 2)) << 4);
    u32x4 xv;
    u32x2 e2;
    u32 x2_;
    asm volatile("global_load_dwordx4 %0, %3, %5\n global_load_dword %1, %4, %5\n buffer_load_dwordx2 %2, off, %6, 0 sc1"
                 : "=&v"(xv), "=&v"(x2_), "=&v"(e2) : "v"(lane * 16), "v"(128 * w + 4 * (lane & 31)), "s"(x), "s"(rs) : "memory");
    bf16x8 wf[16], wk[8];
    u32 ta0 = w * 16384 + off_b(8 * g4 + tq, tp >> 1) + 8 * (tp & 1), ta1 = w * 16384 + off_b(8 * g4 + 4 + tq, tp >> 1) + 8 * (tp & 1);
    int so = WS_SC + ((lane >> 5) * 2048 + 64 * w + 2 * (lane & 31)) * 8;
    int fo = w == 0 ? WS_SFLAG + xc * 2048 + lane * 8 : WS_TOTAL;
    PIN(ta0); PIN(ta1); PIN(so); PIN(fo);
    int qpo = WS_QABS + il(hq * 1024 + dh * 512 + w * 64, 0) + (lane & 32) + (lane & 8) * 2 + (g4 & 1) * 8192 + ((lane & 7) ? WS_TOTAL : 0);
    u32 hw_ = L_H1RED + w * 128 + (lane & 31) * 4, hr_ = L_H1RED + w * 128 + 16 * g4 + 64 * dh, qw_ = L_H1RED + 1024 + w * 272 + lane * 2, qr_ = L_H1RED + 1024 + col * 272 + 64 * g4;
    PIN(qpo); PIN(hw_); PIN(hr_); PIN(qw_); PIN(qr_);
    u32 ka = w * 16384 + off_b(col, 4 * g4), hr2 = hr_ ^ 64;
    PIN(ka); PIN(hr2);
    Q_LOAD(4, 8);
    asm volatile("s_waitcnt vmcnt(4)" : "+v"(xv), "+v"(x2_), "+v"(e2)::"memory");
    TAG_OF(e2.x, e2.y);
    float ln0, ln1;
    {
      float mb, rsd;
      ln_stats(xv, mb, rsd);
      ln0 = rbf(rbf(lo16(x2_) - mb) * rsd);
      ln1 = rbf(rbf(hi16(x2_) - mb) * rsd);
    }
    PIN(ln0); PIN(ln1);
    u32 t0_ = tlo << 24, t1_ = (tlo >> 8) | (thi << 24), f0_ = w ? 0u : tlo, f1_ = w ? 0u : thi;
    PIN(t0_); PIN(t1_); PIN(f0_); PIN(f1_);
    Q_LOAD(8, 8 + QW);
    waitvm<QW>();
#pragma unroll
    for (int c = 0; c < 8; ++c) wf[c] = tr_frag(lds, ta0 ^ (c << 5), ta1 ^ (c << 5));
    Q_LOAD(8 + QW, 16);
#pragma unroll
    for (int c = 0; c < 8; ++c) PIN(wf[c]);
    Q_LOAD(16, 16 + QW);
    waitvm<QW>();
#pragma unroll
    for (int c = 0; c < 8; ++c) wf[8 + c] = tr_frag(lds, (ta0 ^ (c << 5)) + 8192, (ta1 ^ (c << 5)) + 8192);
    Q_LOAD(16 + QW, 24);
    STAMP(12);
    until(t0, E1_T);
    STAMP(23);
    u32x4 g[4];
    u32x2 fl;
#define E1_LOAD(C)                                                                                                       \
  asm volatile("buffer_load_dwordx4 %0, %5, %7, 0 offen sc1\n buffer_load_dwordx4 %1, %5, %7, %8 offen sc1\n"             \
               "buffer_load_dwordx4 %2, %5, %7, %9 offen sc1\n buffer_load_dwordx4 %3, %5, %7, %10 offen sc1\n buffer_load_dwordx2 %4, %6, %7, 0 offen sc1" \
               : C(g[0]), C(g[1]), C(g[2]), C(g[3]), C(fl) : "v"(so), "v"(fo), "s"(rs), "s"(4096), "s"(8192), "s"(12288) : "memory")
    E1_LOAD("=&v");
    waitvm<5>();
#pragma unroll
    for (int i = 0; i < 8; ++i) wk[i] = *(const bf16x8*)(lds + (ka ^ ((i & 3) << 4)) + (i >> 2) * 4096);
#pragma unroll
    for (int c = 0; c < 8; ++c) { PIN(wf[8 + c]); PIN(wk[c]); }
    STAMP(4);
    _Pragma("unroll 1") for (u32 spins = 0; spins < SPIN_CAP; ++spins) {
      asm volatile("s_waitcnt vmcnt(0)" : "+v"(g[0]), "+v"(g[1]), "+v"(g[2]), "+v"(g[3]), "+v"(fl)::"memory");
      u32 bl = g[0].x ^ t0_, bh = (fl.x ^ f0_) | (fl.y ^ f1_);
#pragma unroll
      for (int k = 0; k < 8; ++k) {
        if (k) bl = __builtin_amdgcn_bitop3_b32(bl, g[k >> 1][2 * (k & 1)], t0_, 0xF6);
        bh = __builtin_amdgcn_bitop3_b32(bh, g[k >> 1][2 * (k & 1) + 1], t1_, 0xF6);
      }
      if (!RARE(__any(((bl >> 24) | bh) != 0u))) break;
      STAMP(29);
      __builtin_amdgcn_s_sleep(1);
      E1_LOAD("+v");
    }
    STAMP(1);
    {
      const float m0 = rbf((gran_val(g[0].x) + gran_val(g[1].x)) + (gran_val(g[2].x) + gran_val(g[3].x)));
      const float m1 = rbf((gran_val(g[0].z) + gran_val(g[1].z)) + (gran_val(g[2].z) + gran_val(g[3].z)));
      const auto s0 = __builtin_amdgcn_permlane32_swap(__float_as_uint(m0), __float_as_uint(m0), false, false);
      const auto s1 = __builtin_amdgcn_permlane32_swap(__float_as_uint(m1), __float_as_uint(m1), false, false);
      *(u32*)(lds + hw_) = pk2bf(rbf(ln0 * rbf(1.f + __uint_as_float(s0[1]))) + __uint_as_float(s0[0]),
                                 rbf(ln1 * rbf(1.f + __uint_as_float(s1[1]))) + __uint_as_float(s1[0]));
    }
    WAVE_LDS_SYNC();
    {
      const bf16x8 h0 = *(const bf16x8*)(lds + hr_), h1 = *(const bf16x8*)(lds + hr2);
      f32x4 d[8];
      SB();
#pragma unroll
      for (int c = 0; c < 8; ++c) { d[c] = __builtin_amdgcn_mfma_f32_16x16x32_bf16(h0, wf[c], f32x4{0.f, 0.f, 0.f, 0.f}, 0, 0, 0); SB(); }
#pragma unroll
      for (int c = 0; c < 8; ++c) { d[c] = __builtin_amdgcn_mfma_f32_16x16x32_bf16(h1, wf[8 + c], d[c], 0, 0, 0); SB(); }
#pragma unroll
      for (int k = 0; k < 2; ++k) {
        const float v = (g4 & 2) ? ((g4 & 1) ? d[4 * k + 3][0] : d[4 * k + 2][0]) : ((g4 & 1) ? d[4 * k + 1][0] : d[4 * k][0]);
        const u32 vh = f2bf(v);
        *(u16*)(lds + qw_ + 128 * k) = (u16)vh;
        *(u16*)(lds + qw_ + 128 * k + 8 * 272) = (u16)f2bf(v - bf2f(vh));
      }
    }
    STAMP(2);
    BAR();
    {
      bf16x8 a[4];
#pragma unroll
      for (int s = 0; s < 4; ++s) a[s] = *(const bf16x8*)(lds + qr_ + 16 * s);
      SB();
      f32x4 d[2] = {f32x4{0.f, 0.f, 0.f, 0.f}, f32x4{0.f, 0.f, 0.f, 0.f}};
#pragma unroll
      for (int s = 0; s < 4; ++s)
#pragma unroll
        for (int c = 0; c < 2; ++c) d[c] = __builtin_amdgcn_mfma_f32_16x16x32_bf16(a[s], wk[4 * c + s], d[c], 0, 0, 0);
      constexpr float SCALE = 0.08838834764831845f * 1.4426950408889634f;
      const float v = SCALE * x16_sum(rs32(sum4(d[0]), sum4(d[1])));
      const float p0 = __uint_as_float(nopois(pk2bf(dppf<0x00>(v), dppf<0x55>(v)))), p1 = __uint_as_float(nopois(pk2bf(dppf<0xAA>(v), dppf<0xFF>(v))));
      st128(rs, qpo, u32x4{__float_as_uint(p0), __float_as_uint(p1), __float_as_uint(dppf<0x104>(p0)), __float_as_uint(dppf<0x104>(p1))});
    }
    STAMP(5);
    SB();
    {
      st128(rs, lane < 50 ? WS_PCTX + (j * 8 + w) * 800 + lane * 16 : WS_TOTAL, P4);
      const int pp = __builtin_amdgcn_readfirstlane(WS_PROBE + il((j * 8 + w) * 128, w & 1));
      u64 ta, tb, tc;
      u32 dm;
      asm volatile("s_load_dword %3, %4, 0x0\n s_waitcnt lgkmcnt(0)\n"
                   "s_memtime %0\n s_waitcnt lgkmcnt(0)\n s_load_dword %3, %4, %5 glc\n s_waitcnt lgkmcnt(0)\n"
                   "s_memtime %1\n s_waitcnt lgkmcnt(0)\n s_load_dword %3, %4, %6 glc\n s_waitcnt lgkmcnt(0)\n"
                   "s_memtime %2\n s_waitcnt vmcnt(0) lgkmcnt(0)"
                   : "=&s"(ta), "=&s"(tb), "=&s"(tc), "=&s"(dm) : "s"(ws), "s"(pp), "s"(pp ^ 8192) : "memory");
      int df = min(max(((int)(u32)(tb - ta) - (int)(u32)(tc - tb)) >> 3, -VCLIP), VCLIP);
      df = ((w ^ myh) & 1) ? -df : df;
      u32 old = 0u;
      if (lane == 0) old = __hip_atomic_fetch_add((LAS u32*)(lds + L_CTR) + C_VOTE, (u32)(65536 + 64 + df), __ATOMIC_RELAXED, __HIP_MEMORY_SCOPE_WORKGROUP);
      old = __builtin_amdgcn_readfirstlane(old) + 65536 + 64 + df;
      if (old >= 524288u && lane < 8) st64(rs, WS_VOTE + lane * 2048 + j * 8, (old & 1023u) | (tlo << 24), (tlo >> 8) | (thi << 24));
      STAMP(6);
    }
    {
      const int r0 = 128 * (mu & 3) + 32 * (w & 3) + 4 * (lane >> 3);
      const u32x2 cvb = *(const u32x2*)(cvec + r0);
      u32x4 wsc[4];
#pragma unroll
      for (int i = 0; i < 4; ++i) wsc[i] = ldnt(w_mod + (size_t)(r0 + i) * 3072 + 1024 + (mu >> 2) * 64 + (lane & 7) * 8);
      float a[8];
#pragma unroll
      for (int k = 0; k < 8; ++k) a[k] = 0.f;
#pragma unroll
      for (int i = 0; i < 4; ++i) fma8b(a, f2bf(silu_bf((i & 1) ? hi16(i < 2 ? cvb.x : cvb.y) : lo16(i < 2 ? cvb.x : cvb.y))), wsc[i]);
      float r[4];
#pragma unroll
      for (int k = 0; k < 4; ++k) r[k] = rs32(a[k], a[k + 4]);
      const float o0 = b3_sum(rs16(r[0], r[2])), o1 = b3_sum(rs16(r[1], r[3]));
      float* red = (float*)(lds + L_H1RED + 8192);
      if ((lane & 8) == 0) *(f32x2*)(red + w * 64 + (lane & 7) * 8 + (lane >> 5) * 4 + ((lane >> 4) & 1) * 2) = f32x2{o0, o1};
      BAR();
      if ((w & 3) == 3 && lane < 16) {
        red += (w >> 2) * 256 + lane * 4;
        const f32x4 sm = (*(const f32x4*)(red) + *(const f32x4*)(red + 64)) + (*(const f32x4*)(red + 128) + *(const f32x4*)(red + 192));
        st128(rs, WS_MODB + ((mu & 3) * 2048 + (mu >> 2) * 64 + lane * 4) * 4, u32x4{fbits(sm.x), fbits(sm.y), fbits(sm.z), fbits(sm.w)});
      }
    }
    gx = 0;
  }

  RELANE();
  const int r16 = lane >> 2, ch = lane & 3;
  if (ii >= 16) {
    int mo = lane < 24 ? (WS_LSE + il((24 * w + lane) * 512 + c4 * 128 + (h >> 1) * 8, h & 1)) ^ gx : WS_TOTAL;
    PIN(mo);
    lds_spin(ctr + C_PD, 4u);
    STAMP(23);
    u32x2 mt = ld64(rs, mo);
    SB();
    const u32 pb = (w >> 1) * PS + (w & 1) * 6144;
    u32 po[6], pa[16], ea[8];
    {
      u32 t_ = g4 * 256 + ((col ^ (g4 << 2)) << 4) + ((WS_PART + il21(((h >> 1) * 4 + c4) * 65536, h & 1)) ^ gx);
      PIN(t_);
#pragma unroll
      for (int k = 0; k < 6; ++k) { po[k] = (t_ ^ ((k & 3) << 4)) + il((6 * w + k) * 1024, 0); PIN(po[k]); }
    }
    bf16x8 wk[8];
    if (w < 4) {
      u32 wa = tq * PS + 16384 + off_b(g4, 2 * w + (tp >> 1)) + 8 * (tp & 1);
      PIN(wa);
#pragma unroll
      for (int s = 0; s < 8; ++s) { const u32 a = (wa ^ ((s & 3) << 4) ^ ((s >> 2) << 7)) + (s & 3) * 1024; wk[s] = tr_frag(lds, a, a + 4096); }
    }
    pa[0] = pb + off_b(8 * (g4 == 3 ? 0 : g4) + tq, tp >> 1) + 8 * (tp & 1);
    PIN(pa[0]);
#pragma unroll
    for (int i = 1; i < 16; ++i) { pa[i] = pa[0] ^ (i << 4); PIN(pa[i]); }
    u32 bo = L_QOFF + col * 272 + g4 * 16, mlo = L_QOFF + 4352 + (col & 7) * 8, ro = L_QOFF + (w + (col & 8)) * 272 + (col & 7) * 32 + g4 * 8;
    int so1 = (WS_PCTX + h * 2048 + c4 * 512 + (col & 8) * 32 + (w & 1) * 128 + (w & 2) * 16 + (g4 >> 1) * 64 + (g4 & 1) * 8 + ((col & 6) ? WS_TOTAL : (col & 1) * 16)) ^ gx;
    u32 eo = L_QOFF + 4416 + w * 128 + lane * 2, mw_ = L_QOFF + 4352 + w * 8;
#pragma unroll
    for (int i = 0; i < 8; ++i) { ea[i] = L_QOFF + 4416 + w * 128 + ((col & 7) == i ? 16 * g4 : 64); PIN(ea[i]); }
    float pad = lane < 24 ? 0.f : -__builtin_inff();
    PIN(bo); PIN(mlo); PIN(ro); PIN(so1); PIN(eo); PIN(mw_); PIN(pad);
    SB();
    u32 done = 0u;
    _Pragma("unroll 1") for (u32 spins = 0; spins < SPIN_CAP; ++spins) {
      const u32 ok = ~(u32)__ballot(max(mt.x, mt.y) == POISON);
      u32 nw = ok & (ok >> 1);
      nw = nw & (nw >> 2) & 0x111111u & ~done;
      done |= nw;
#define T1F(k, b) "s_bitcmp0_b32 %[nw], " #b "\n s_cbranch_scc1 1f\n global_load_lds_dwordx4 %[p" #k "], %[ws] sc1\n 1: s_add_u32 m0, m0, 0x400\n"
      asm volatile("s_mov_b32 m0, %[m]\n" T1F(0, 0) T1F(1, 4) T1F(2, 8) T1F(3, 12) T1F(4, 16) T1F(5, 20)
                   ::[m] "s"(pb), [nw] "s"(nw), [ws] "s"(ws), [p0] "v"(po[0]), [p1] "v"(po[1]), [p2] "v"(po[2]), [p3] "v"(po[3]), [p4] "v"(po[4]), [p5] "v"(po[5]) : "memory", "scc");
      if (done == 0x111111u) break;
      __builtin_amdgcn_s_sleep(1);
      if (max(mt.x, mt.y) == POISON) mt = ld64(rs, mo);
      PIN(mt);
    }
    STAMP(16);
    {
      const float m = __uint_as_float(mt.x) + pad;
      float M = m;
#define MX(C) "s_nop 1\n v_max_f32_dpp %0, %0, %0 " C " row_mask:0xf bank_mask:0xf\n"
      asm(MX("quad_perm:[1,0,3,2]") MX("quad_perm:[2,3,0,1]") MX("row_ror:8") MX("row_ror:4") "s_nop 1" : "+v"(M));
      M = fmaxf(__uint_as_float(__builtin_amdgcn_readlane(__float_as_uint(M), 0)), __uint_as_float(__builtin_amdgcn_readlane(__float_as_uint(M), 16)));
      const float e = __builtin_amdgcn_exp2f(m - M);
      *(u16*)(lds + eo) = (u16)f2bf(e);
      WAVE_LDS_SYNC();
      bf16x8 wf[8], pt[8];
#pragma unroll
      for (int i = 0; i < 8; ++i) wf[i] = *(const bf16x8*)(lds + ea[i]);
      SB();
      const float L = x16_sum(row16_sum(e * __uint_as_float(mt.y)));
      if (lane == 0) *(f32x2*)(lds + mw_) = f32x2{M, L};
      SB();
      waitvm<0>();
      STAMP(17);
#pragma unroll
      for (int i = 0; i < 8; ++i) pt[i] = tr_frag(lds, pa[2 * i], pa[2 * i + 1] + 1024);
      SB();
      f32x4 d = {0.f, 0.f, 0.f, 0.f};
#pragma unroll
      for (int i = 0; i < 8; ++i) d = __builtin_amdgcn_mfma_f32_16x16x32_bf16(pt[i], wf[i], d, 0, 0, 0);
      const u32 h0 = pk2bf(d[0], d[1]), h1 = pk2bf(d[2], d[3]);
      const u32 l0 = pk2bf(d[0] - lo16(h0), d[1] - hi16(h0)), l1 = pk2bf(d[2] - lo16(h1), d[3] - hi16(h1));
      *(u32x2*)(lds + ro) = (col & 8) ? u32x2{l0, l1} : u32x2{h0, h1};
    }
    BAR();
    STAMP(14);
    if (w < 4) {
      const f32x2 ml = *(const f32x2*)(lds + mlo);
      bf16x8 lf[4];
#pragma unroll
      for (int s = 0; s < 4; ++s) lf[s] = *(const bf16x8*)(lds + bo + 64 * s);
      SB();
      float Mg = ml.x;
      asm(MX("quad_perm:[1,0,3,2]") MX("quad_perm:[2,3,0,1]") MX("row_half_mirror") : "+v"(Mg));
      f32x4 da = {0.f, 0.f, 0.f, 0.f}, db = da;
#define T1M(s) do { SB(); da = __builtin_amdgcn_mfma_f32_16x16x32_bf16(wk[s], lf[s], da, 0, 0, 0); db = __builtin_amdgcn_mfma_f32_16x16x32_bf16(wk[4 + (s)], lf[s], db, 0, 0, 0); SB(); } while (0)
      T1M(0);
      float e = __builtin_amdgcn_exp2f(ml.x - Mg), Lg = e * ml.y;
      PIN(e); PIN(Lg);
      T1M(1);
      Lg = quad_sum(Lg);
      PIN(Lg);
      T1M(2);
      Lg += dppf<0x141>(Lg);
      float sc = e * __builtin_amdgcn_rcpf(Lg);
      PIN(sc);
      T1M(3);
      const bool hi8 = (col & 8) != 0;
      float d0 = ((hi8 ? db[0] : da[0]) + dppf<0x128>(hi8 ? da[0] : db[0])) * sc, d1 = ((hi8 ? db[1] : da[1]) + dppf<0x128>(hi8 ? da[1] : db[1])) * sc;
      float d2 = ((hi8 ? db[2] : da[2]) + dppf<0x128>(hi8 ? da[2] : db[2])) * sc, d3 = ((hi8 ? db[3] : da[3]) + dppf<0x128>(hi8 ? da[3] : db[3])) * sc;
      row8_sum4(d0, d1, d2, d3);
      const u32 h0 = pk2bf(d0, d1), h1 = pk2bf(d2, d3);
      const u32 l0 = pk2bf(d0 - lo16(h0), d1 - hi16(h0)), l1 = pk2bf(d2 - lo16(h1), d3 - hi16(h1));
      st64(rs, so1, nopois((col & 1) ? l0 : h0), nopois((col & 1) ? l1 : h1));
    }
    STAMP(15);
    STAMP_FLUSH();
    return;
  }
  if (tl2) {
    BAR0();
    if (w < 4) {
#pragma unroll
      for (int i = 0; i < 8; ++i) dma<true>(owno + i * 1024, (ks * 512 + 64 * w + 8 * i + (lane >> 3)) * 1024 + cg * 128 + (lane & 7) * 16, w_o);
      waitvm<0>();
    } else {
#pragma unroll
      for (int i = 0; i < 8; ++i) *(u32x4*)(lds + owno + i * 1024 + lane * 16) = wv[i];
      WAVE_LDS_SYNC();
    }
    STAMP(26);
    bf16x8 wf[8];
#pragma unroll
    for (int i = 0; i < 8; ++i) {
      const u32 a = owno + (32 * (i >> 2) + 8 * g4 + tq) * 128 + (i & 3) * 32 + tp * 8;
      wf[i] = tr_frag(lds, a, a + 512);
    }
#pragma unroll
    for (int i = 0; i < 8; ++i) PIN(wf[i]);
    STAMP(23);
    int off[1] = {(WS_PCTX + (ks * 4 + (w >> 1)) * 2048 + tq * 512 + (w & 1) * 256 + g4 * 64 + tp * 16) ^ gx};
    u32 rw = L_RED + lane * 32 + w * 4;
    PIN(rw);
    int so2 = tp < 2 ? WS_PATTN + il((ks * 512 + cg * 64 + (lane & 60)) * 4, tp) : WS_TOTAL;
    PIN(so2);
    u32x4 g[1];
    gather128<1, 4>(rs, off, g);
    STAMP(24);
    const bf16x8 a0 = __builtin_bit_cast(bf16x8, (col & 2) ? Z4 : g[0]), a1 = __builtin_bit_cast(bf16x8, (col & 2) ? g[0] : Z4);
    f32x4 d[4];
#pragma unroll
    for (int c = 0; c < 4; ++c) d[c] = __builtin_amdgcn_mfma_f32_16x16x32_bf16(a0, wf[c], f32x4{0.f, 0.f, 0.f, 0.f}, 0, 0, 0);
#pragma unroll
    for (int c = 0; c < 4; ++c) d[c] = __builtin_amdgcn_mfma_f32_16x16x32_bf16(a1, wf[4 + c], d[c], 0, 0, 0);
    *(float*)(lds + rw) = rs16(rs32(sum4(d[0]), sum4(d[2])), rs32(sum4(d[1]), sum4(d[3])));
    BAR();
    if (w == 0) {
      const float v = sum4(*(const f32x4*)(lds + L_RED + lane * 32) + *(const f32x4*)(lds + L_RED + lane * 32 + 16));
      const float vn = __uint_as_float(fbits(v));
      st128(rs, so2, u32x4{__float_as_uint(dppf<0x00>(vn)), __float_as_uint(dppf<0x55>(vn)), __float_as_uint(dppf<0xAA>(vn)), __float_as_uint(dppf<0xFF>(vn))});
    }
    STAMP(17);
  } else if (t3 >= 0) {
    STAMP(20);
    if (w < 4) {
      const int fr = ((128 * w + r16) * DF + t3 * 32) * 2, ca_ = ch ^ ((r16 >> 2) & 2) ^ (t3 & 1);
      const u16* wa = (t3 & 1) ? w_up : w_gate;
      const u16* wb = (t3 & 1) ? w_gate : w_up;
      const u32 d = w * 32768 + 8192;
#pragma unroll
      for (int i = 0; i < 8; ++i) { waitvm<WING - 1>(); dma<false>(d + (i >> 2) * 12288 + (t3 & 1) * 4096 + (i & 3) * 1024, fr + ca_ * 16 + 32 * i * DF, wa); }
#pragma unroll
      for (int i = 0; i < 8; ++i) { waitvm<WING - 1>(); dma<false>(d + (i >> 2) * 12288 + 4096 - (t3 & 1) * 4096 + (i & 3) * 1024, fr + (ca_ ^ 1) * 16 + 32 * i * DF, wb); }
    } else {
#pragma unroll
      for (int i = 0; i < 8; ++i) {
        waitvm<WING - 1>();
        dma<true>(lw * 32768 + 16384 + (i >> 2) * 12288 + (i & 3) * 1024, (32 * t3 + 8 * (i & 3) + (lane >> 3)) * 1024 + 256 * lw + 128 * (i >> 2) + (lane & 7) * 16, w_down);
      }
      const u32 x2 = *(const u32*)(x + 128 * lw + 2 * lane);
      u32x4 m6[6];
      int mo[6];
#pragma unroll
      for (int i = 0; i < 6; ++i) mo[i] = MB_OFF(i);
      gather128<6, 8>(rs, mo, m6);
#pragma unroll
      for (int i = 0; i < 6; ++i) {
        const f32x4 f = __builtin_bit_cast(f32x4, m6[i]);
        const float v = rbf(rs16(rs32(f.x, f.z), rs32(f.y, f.w)));
        *(u16*)(lds + L_Z + (i < 2 ? 5 : i < 4 ? 3 : 2) * 1088 + (128 * lw + 64 * (i & 1) + 4 * col + g4) * 2) = (u16)f2bf(i < 4 ? v : 1.f + v);
      }
      *(u32*)(lds + L_Z + 6 * 1088 + lw * 256 + lane * 4) = x2;
      if (w == 4 && lane < 32) *(u32*)(lds + L_ZERO + lane * 4) = lane < 16 ? 0u : POISON;
      if (w == 6) COUNT_VOTES(false);
    }
    waitvm<0>();
    STAMP(26);
    BAR();
    gx = __builtin_amdgcn_readfirstlane(ctr[C_PAR]);
    u32 zw = L_Z + (64 * w + 4 * col + g4) * 2;
    u32 hr[4];
#pragma unroll
    for (int i = 0; i < 4; ++i) { hr[i] = (col == i ? L_HH : L_ZERO) + 16 * g4; PIN(hr[i]); }
    int off[2];
#pragma unroll
    for (int i = 0; i < 2; ++i) { off[i] = (WS_PATTN + il(((4 * i + g4) * 512 + 64 * w + col * 4) * 4, myh)) ^ gx; PIN(off[i]); }
    int so = col < 4 ? (WS_PDOWN + il((t3 * 256 + 64 * (w & 3) + 16 * col + 4 * g4) * 4, w >> 2)) ^ gx : WS_TOTAL;
    u32 zr = L_Z + tp * 1088 + 16 * g4, yr = L_Z + 4 * 1088 + lane * 16, hw = L_HH + (w & 3) * 16 + (g4 & 1) * 8 + tp * 2, cr = L_CF + (w & 3) * 16 + tp * 4;
    PIN(cr); PIN(so); PIN(zw); PIN(zr); PIN(yr); PIN(hw);
    float ga = bf2f(*(const u16*)(lds + zw + 5 * 1088)), cb1 = bf2f(*(const u16*)(lds + zw + 2 * 1088)), xe = bf2f(*(const u16*)(lds + zw + 6 * 1088));
    bf16x8 wf[16], wd[4];
    if (w < 4) {
#pragma unroll
      for (int k = 0; k < 16; ++k) {
        const u32 a = (k >> 2) * 32768 + 8192 + ((k >> 1) & 1) * 12288 + (tp >> 1) * 4096 + ((k & 1) * 32 + 8 * g4 + tq) * 64 + (w ^ ((g4 & 1) * 2) ^ (tp >> 1)) * 16 + (tp & 1) * 8;
        wf[k] = tr_frag(lds, a, a + 256);
      }
#pragma unroll
      for (int k = 0; k < 16; ++k) PIN(wf[k]);
    }
#pragma unroll
    for (int i = 0; i < 4; ++i) {
      const u32 a = owno + 8192 + (8 * g4 + tq) * 128 + i * 32 + tp * 8;
      wd[i] = tr_frag(lds, a, a + 512);
    }
#pragma unroll
    for (int i = 0; i < 4; ++i) PIN(wd[i]);
    PIN(ga); PIN(cb1); PIN(xe);
    STAMP(23);
    until(t0, E6_T);
    u32x4 pa2[2];
    gather128<2, 4>(rs, off, pa2);
    STAMP(18);
    {
      const f32x4 s4 = __builtin_bit_cast(f32x4, pa2[0]) + __builtin_bit_cast(f32x4, pa2[1]);
      const u32 yb = f2bf(xe + rbf(ga * rbf(rs16(rs32(s4.x, s4.z), rs32(s4.y, s4.w)))));
      const float z = bf2f(yb) * cb1;
      const u32 zh = f2bf(z);
      *(u16*)(lds + zw + 4 * 1088) = (u16)yb;
      *(u16*)(lds + zw) = (u16)zh;
      *(u16*)(lds + zw + 1088) = (u16)f2bf(z - bf2f(zh));
    }
    BAR();
    if (w < 4) {
      bf16x8 zf[16];
#pragma unroll
      for (int k = 0; k < 16; ++k) zf[k] = *(const bf16x8*)(lds + zr + 64 * k);
      __builtin_amdgcn_sched_barrier(0);
      f32x4 d = {0.f, 0.f, 0.f, 0.f};
#pragma unroll
      for (int k = 0; k < 16; ++k) d = __builtin_amdgcn_mfma_f32_16x16x32_bf16(wf[k], zf[k], d, 0, 0, 0);
      PIN(d);
      u32 cfb;
      do { cfb = *(volatile LAS u32*)(lds + cr); } while (__any(cfb == POISON));
      d *= __uint_as_float(cfb);
      const float a0 = (tp & 1) ? d[1] : d[0], b0 = (tp & 1) ? d[0] : d[1], a1 = (tp & 1) ? d[3] : d[2], b1 = (tp & 1) ? d[2] : d[3];
      const float e0 = a0 + dppf<0xB1>(b0), e1 = a1 + dppf<0xB1>(b1);
      const float v = rbf(((tp & 2) ? e1 : e0) + dppf<0x4E>((tp & 2) ? e0 : e1));
      const auto sw = __builtin_amdgcn_permlane32_swap(__float_as_uint(v), __float_as_uint(v), false, false);
      const float g = __uint_as_float(sw[0]), u = __uint_as_float(sw[1]);
      const float e2 = __builtin_amdgcn_exp2f(g * (-2.3022081981f - 0.1029432396f * g * g));
      *(u16*)(lds + hw) = (u16)f2bf(rbf(g * __builtin_amdgcn_rcpf(e2 + 1.f)) * u);
    } else {
      float mb, rsd;
      ln_stats(*(const u32x4*)(lds + yr), mb, rsd);
      *(u32*)(lds + cr) = fbits(tp == 3 ? 1.f : rsd * (tp == 2 ? -mb : 1.f));
    }
    BAR();
    {
      bf16x8 hf[4];
#pragma unroll
      for (int i = 0; i < 4; ++i) hf[i] = *(const bf16x8*)(lds + hr[i]);
      __builtin_amdgcn_sched_barrier(0);
      f32x4 d = {0.f, 0.f, 0.f, 0.f};
#pragma unroll
      for (int i = 0; i < 4; ++i) d = __builtin_amdgcn_mfma_f32_16x16x32_bf16(wd[i], hf[i], d, 0, 0, 0);
      st128(rs, so, u32x4{fbits(d[0]), fbits(d[1]), fbits(d[2]), fbits(d[3])});
    }
    STAMP(19);
  }
  if (t4 >= 0 && lw == 0) {
#define RED8(f) b3_sum(rs16(rs32(f.x, f.z), rs32(f.y, f.w)))
    const int oc = t4 * 32 + (lane & 7) * 4 + g4;
    const u32 xb = x[oc];
    float x1, gb;
    {
      int off[2];
      off[0] = lane < 32 ? WS_MODB + ((lane >> 3) * 2048 + t4 * 32 + (lane & 7) * 4) * 4 : WS_TOTAL;
      off[1] = lane < 32 ? WS_MODB + ((lane >> 3) * 2048 + 1536 + t4 * 32 + (lane & 7) * 4) * 4 : WS_TOTAL;
      u32x4 mg[2];
      gather128<2>(rs, off, mg);
      const f32x4 fa = __builtin_bit_cast(f32x4, mg[0]), fb = __builtin_bit_cast(f32x4, mg[1]);
      const float ga = rbf(RED8(fa));
      gb = rbf(RED8(fb));
      int of1[1] = {(WS_PATTN + il(((lane >> 3) * 512 + t4 * 32 + (lane & 7) * 4) * 4, myh)) ^ gx};
      u32x4 g[1];
      gather128<1, 4>(rs, of1, g);
      const f32x4 gf = __builtin_bit_cast(f32x4, g[0]);
      x1 = rbf(bf2f(xb) + rbf(ga * rbf(RED8(gf))));
    }
    STAMP(20);
    if (t4 == 0 && lane == 0) st64(rs, WS_EPOCH, tlo, thi);
    int off[7];
#pragma unroll
    for (int i = 0; i < 7; ++i) { off[i] = (WS_PDOWN + il(((7 * (lane >> 3) + i) * 256 + (t4 & 7) * 32 + (lane & 7) * 4) * 4, myh)) ^ gx; PIN(off[i]); }
    const rsrc_t ro = __builtin_amdgcn_make_buffer_rsrc((void*)out, 0, 1024, 0x00020000);
    int oo = (lane & 8) ? 1024 : oc * 2;
    PIN(x1); PIN(gb); PIN(oo);
    u32x4 g[7];
    __builtin_amdgcn_s_sleep(T4_DELAY);
#define F4(i) __builtin_bit_cast(f32x4, g[i])
    float s;
#pragma unroll
    for (int i = 0; i < 7; ++i) g[i] = ld128(rs, off[i]);
    _Pragma("unroll 1") for (u32 spins = 0; spins < SPIN_CAP; ++spins) {
      asm volatile("s_waitcnt vmcnt(0)" : "+v"(g[0]), "+v"(g[1]), "+v"(g[2]), "+v"(g[3]), "+v"(g[4]), "+v"(g[5]), "+v"(g[6])::"memory");
      const f32x4 s4 = ((F4(0) + F4(1)) + (F4(2) + F4(3))) + ((F4(4) + F4(5)) + F4(6));
      s = RED8(s4);
      if (!__any(s != s)) break;
      u64 bad = 0ull;
#pragma unroll
      for (int i = 0; i < 7; ++i) {
        const u64 m = __ballot(pois4(g[i]));
        bad |= m;
        asm volatile("s_mov_b64 exec, %2\n buffer_load_dwordx4 %0, %1, %3, 0 offen sc1\n s_mov_b64 exec, -1" : "+v"(g[i]) : "v"(off[i]), "s"(m), "s"(rs) : "memory");
      }
      if (!bad) break;
    }
    STAMP(21);
    __builtin_amdgcn_raw_buffer_store_b16((u16)f2bf(x1 + rbf(gb * rbf(s))), ro, oo, 0, 0);
    STAMP(22);
  }
  STAMP_FLUSH();
}

extern "C" void kernel_launch(void* const* d_in, const int* in_sizes, int n_in, void* d_out, int out_size,
                              void* d_ws, size_t ws_size, hipStream_t stream) {
  k_main<<<NWG, 512, 0, stream>>>((const u16*)d_in[0], (const u16*)d_in[1], (const u16*)d_in[2], (const u16*)d_in[3],
                                  (const u16*)d_in[4], (const u16*)d_in[5], (const u16*)d_in[7], (const u16*)d_in[8],
                                  (const u16*)d_in[9], (const u16*)d_in[10], (const u16*)d_in[11], (u16*)d_out,
                                  (unsigned char*)d_ws);
}
```
